```python
import torch

def _softmax(x, axis=-1):
    return torch.softmax(x, dim=axis)
def _gelu(x):
    return torch.nn.functional.gelu(x, approximate="tanh")
def _silu(x):
    return torch.nn.functional.silu(x)
def _sigmoid(x):
    return torch.sigmoid(x)
def _layernorm(x, axis=-1, eps=1e-5):
    m = x.mean(dim=axis, keepdim=True)
    v = x.var(dim=axis, keepdim=True, unbiased=False)
    return (x - m) / torch.sqrt(v + eps)
def _scatter_add(tgt, idx, src, axis=0):
    return tgt.clone().index_add_(axis, idx, src)
def _segment_sum(src, seg, n):
    out = torch.zeros((n,) + src.shape[1:], dtype=src.dtype)
    return out.index_add_(0, seg, src)
def _pad(x, pw):
    flat = tuple(v for pair in reversed(pw) for v in pair)
    return torch.nn.functional.pad(x, flat)

kernel_name = 'parallel_sum__compressed_kv_cla__glu_gated_reglu__prompt'

def setup_inputs():

    _w = (lambda a: (lambda t: t.to(torch.bfloat16) if t.is_floating_point() else t)(torch.as_tensor(a)))
    def _A_setup():
        B, H, Sq, Sk, D, M = 1, 32, 2048, 2048, 128, 512
        Dc = 4 * D
        return {
            "x": _w((lambda *s: torch.randn(*s, dtype=torch.bfloat16) * 0.05)(B, Sq, M)),
            "c_kv": _w((lambda *s: torch.randn(*s, dtype=torch.bfloat16) * 0.4)(B, Sk, Dc)),
            "w_q": _w((lambda *s: torch.randn(*s, dtype=torch.bfloat16) * 1.6)(M, H, D) * (M ** -0.5)),
            "w_uk": _w((lambda *s: torch.randn(*s, dtype=torch.bfloat16) * 1.6)(Dc, H, D) * (Dc ** -0.5)),
            "w_uv": _w((lambda *s: torch.randn(*s, dtype=torch.bfloat16) * 0.4)(Dc, H, D) * (Dc ** -0.5)),
            "w_o": _w((lambda *s: torch.randn(*s, dtype=torch.bfloat16) * 0.4)(H, D, M) * ((H * D) ** -0.5)),
        }
    _A_in = _A_setup()

    def _B_setup():
        B, S, D, F = 1, 2048, 512, 1376
        return {
            "x": _w((lambda *s: torch.randn(*s, dtype=torch.bfloat16) * 0.05)(B, S, D)),
            "W_gate": _w((lambda *s: torch.randn(*s, dtype=torch.bfloat16) * 0.2)(D, F) * (2.0 / D) ** 0.5),
            "W_up": _w((lambda *s: torch.randn(*s, dtype=torch.bfloat16) * 0.2)(D, F) * (2.0 / D) ** 0.5),
            "W_down": _w((lambda *s: torch.randn(*s, dtype=torch.bfloat16) * 0.2)(F, D) * (1.0 / F) ** 0.5),
        }
    _B_in = _B_setup()

    return {
        "A_x": _A_in["x"],
        "A_c_kv": _A_in["c_kv"],
        "A_w_q": _A_in["w_q"],
        "A_w_uk": _A_in["w_uk"],
        "A_w_uv": _A_in["w_uv"],
        "A_w_o": _A_in["w_o"],
        "B_x": _B_in["x"],
        "B_W_gate": _B_in["W_gate"],
        "B_W_up": _B_in["W_up"],
        "B_W_down": _B_in["W_down"],
    }

def reference(A_x, A_c_kv, A_w_q, A_w_uk, A_w_uv, A_w_o, B_x, B_W_gate, B_W_up, B_W_down):
    def _A_ref(x, c_kv, w_q, w_uk, w_uv, w_o):
        B, Sq, M = x.shape[0], x.shape[1], x.shape[2]
        Sk = c_kv.shape[1]
        H, D = w_q.shape[1], w_q.shape[2]
        q = torch.einsum("bqm,mhd->bhqd", x, w_q)
        k = torch.einsum("bkc,chd->bhkd", c_kv, w_uk)
        v = torch.einsum("bkc,chd->bhkd", c_kv, w_uv)
        scores = torch.einsum("bhqd,bhkd->bhqk", q, k) * (D ** -0.5)
        off = Sk - Sq
        qi = torch.arange(Sq)[:, None]
        kj = torch.arange(Sk)[None, :]
        cmask = kj <= (qi + off)
        scores = torch.where(cmask[None, None], scores, -1e9)
        p = _softmax(scores, axis=-1)
        ctx = torch.einsum("bhqk,bhkd->bhqd", p, v)
        return torch.einsum("bhqd,hdm->bqm", ctx, w_o)

    def _B_ref(x, W_gate, W_up, W_down):
        gate = torch.einsum("bsd,df->bsf", x, W_gate)
        up = torch.einsum("bsd,df->bsf", x, W_up)
        gate = gate * (lambda x, dt: x.to(dt))(gate > 0, gate.dtype)
        h = gate * up
        return torch.einsum("bsf,fd->bsd", h, W_down)

    x = A_x
    xn = _layernorm(x, axis=-1)
    return x + _A_ref(xn, A_c_kv, A_w_q, A_w_uk, A_w_uv, A_w_o) + _B_ref(xn, B_W_gate, B_W_up, B_W_down)
```

```cpp
#include <hip/hip_runtime.h>
#include <hip/hip_bf16.h>
#include <stdint.h>
typedef __bf16 bf16x8 __attribute__((ext_vector_type(8)));
typedef float f32x16 __attribute__((ext_vector_type(16)));
typedef unsigned short u16;
typedef u16 u16x8 __attribute__((ext_vector_type(8)));
#define MFMA(a,b,c) __builtin_amdgcn_mfma_f32_32x32x16_bf16((a),(b),(c),0,0,0)
constexpr int S=2048, M=512, H=32, D=128, F=1376, KS_O=342;
constexpr size_t WS_XN=0, WS_KV=WS_XN+(size_t)S*M*2, WS_ACT=WS_KV+(size_t)H*S*D*2*2;
__device__ __forceinline__ float bf2f(u16 x){ return __uint_as_float(((unsigned)x)<<16); }
__device__ __forceinline__ u16 f2bf(float x){ __bf16 b=(__bf16)x; return __builtin_bit_cast(u16,b); }
__device__ __forceinline__ bf16x8 pack8(const f32x16&x,int s,float sc){ bf16x8 r;
#pragma unroll
  for(int j=0;j<8;++j) r[j]=(__bf16)(x[8*s+j]*sc); return r; }

__global__ __launch_bounds__(256) void ln_kernel(const u16* x, u16* xn){
  int row=blockIdx.x*4+(threadIdx.x>>6), lane=threadIdx.x&63;
  u16x8 v=*(const u16x8*)(x+(size_t)row*M+lane*8);
  float f[8],s=0; for(int j=0;j<8;++j){f[j]=bf2f(v[j]); s+=f[j];}
  for(int o=32;o;o>>=1) s+=__shfl_xor(s,o);
  float mean=s*(1.f/M), q=0; for(int j=0;j<8;++j){f[j]-=mean; q+=f[j]*f[j];}
  for(int o=32;o;o>>=1) q+=__shfl_xor(q,o);
  float r=1.f/sqrtf(q*(1.f/M)+1e-5f);
  u16x8 o; for(int j=0;j<8;++j) o[j]=f2bf(f[j]*r);
  int tb=row>>5, ks=lane>>1, hi=lane&1;
  *(u16x8*)(xn+(((size_t)tb*32+ks)*64+(row&31)+32*hi)*8)=o;
}
__device__ __forceinline__ bf16x8 wfrag(const u16* W,int ldw,int k0,int n,int hi){ u16x8 r;
#pragma unroll
  for(int j=0;j<8;++j) r[j]=W[(size_t)(k0+8*hi+j)*ldw+n]; return __builtin_bit_cast(bf16x8,r);}
__device__ __forceinline__ bf16x8 wfragp(const u16* W,int ldw,int k0,int n,int hi){ u16x8 r;
#pragma unroll
  for(int j=0;j<8;++j) r[j]=W[(size_t)(k0+8*(j>>2)+4*hi+(j&3))*ldw+n]; return __builtin_bit_cast(bf16x8,r);}

__global__ __launch_bounds__(256) void kv_kernel(const u16* ckv,const u16* wuk,const u16* wuv,u16* kv){
  int w=blockIdx.x*4+(threadIdx.x>>6), lane=threadIdx.x&63, r=lane&31, hi=lane>>5;
  int db=w&3, isv=(w>>2)&1, kb=(w>>3)&63, h=w>>9;
  const u16* W=isv?wuv:wuk;
  f32x16 acc={};
  for(int ks=0;ks<32;++ks){
    bf16x8 c=*(const bf16x8*)(ckv+(size_t)(kb*32+r)*M+ks*16+8*hi);
    bf16x8 wf=wfrag(W,H*D,ks*16,h*D+db*32+r,hi);
    acc = isv? MFMA(c,wf,acc) : MFMA(wf,c,acc);
  }
  u16* base=kv+((size_t)(h*32+(kb>>1))*2+isv)*8192+(size_t)((kb&1)*8+db*2)*512;
  *(bf16x8*)(base+lane*8)=pack8(acc,0,1.f);
  *(bf16x8*)(base+512+lane*8)=pack8(acc,1,1.f);
}
__global__ __launch_bounds__(256) void attn_kernel(const u16* xn,const u16* wq,const u16* kv,u16* act){
  int w=blockIdx.x*4+(threadIdx.x>>6), lane=threadIdx.x&63, r=lane&31, hi=lane>>5;
  int tb=w&63, h=w>>6;
  bf16x8 q[8];
  const float C=0.08838834764831845f*1.4426950408889634f;
#pragma unroll
  for(int db=0;db<4;++db){ f32x16 acc={};
    for(int ks=0;ks<32;++ks){
      bf16x8 xf=*(const bf16x8*)(xn+(((size_t)tb*32+ks)*64+lane)*8);
      bf16x8 wf=wfrag(wq,H*D,ks*16,h*D+db*32+r,hi);
      acc=MFMA(wf,xf,acc);
    }
    q[2*db]=pack8(acc,0,C); q[2*db+1]=pack8(acc,1,C);
  }
  f32x16 o[4]={}; float m=-1e30f,l=0;
  for(int kb=0;kb<=tb;++kb){
    const u16* kbase=kv+((size_t)(h*32+(kb>>1))*2)*8192+(size_t)((kb&1)*8)*512;
    const u16* vbase=kbase+8192;
    f32x16 s={};
#pragma unroll
    for(int i=0;i<8;++i) s=MFMA(*(const bf16x8*)(kbase+i*512+lane*8),q[i],s);
    if(kb==tb){
#pragma unroll
      for(int i=0;i<16;++i){ int key=(i&3)+8*(i>>2)+4*hi; if(key>r) s[i]=-INFINITY; }
    }
    float mx=s[0];
#pragma unroll
    for(int i=1;i<16;++i) mx=fmaxf(mx,s[i]);
    mx=fmaxf(mx,__shfl_xor(mx,32));
    float mn=fmaxf(m,mx), al=exp2f(m-mn); m=mn;
    float ps=0;
#pragma unroll
    for(int i=0;i<16;++i){ s[i]=exp2f(s[i]-mn); ps+=s[i]; }
    ps+=__shfl_xor(ps,32);
    l=l*al+ps;
    bf16x8 p0=pack8(s,0,1.f), p1=pack8(s,1,1.f);
#pragma unroll
    for(int db=0;db<4;++db){
#pragma unroll
      for(int i=0;i<16;++i) o[db][i]*=al;
      o[db]=MFMA(*(const bf16x8*)(vbase+(db*2)*512+lane*8),p0,o[db]);
      o[db]=MFMA(*(const bf16x8*)(vbase+(db*2+1)*512+lane*8),p1,o[db]);
    }
  }
  float il=1.f/l;
#pragma unroll
  for(int db=0;db<4;++db){
    u16* base=act+(((size_t)tb*KS_O+h*8+db*2)*64+lane)*8;
    *(bf16x8*)base=pack8(o[db],0,il); *(bf16x8*)(base+512)=pack8(o[db],1,il);
  }
}
__global__ __launch_bounds__(256) void ffn_kernel(const u16* xn,const u16* wg,const u16* wu,u16* act){
  int w=blockIdx.x*4+(threadIdx.x>>6), lane=threadIdx.x&63, r=lane&31, hi=lane>>5;
  int tb=w&63, fs=w>>6; if(fs>=86) return;
  const u16* W=(r<16)?wg:wu; int n=fs*16+(r&15);
  f32x16 acc={};
  for(int ks=0;ks<32;++ks){
    bf16x8 xf=*(const bf16x8*)(xn+(((size_t)tb*32+ks)*64+lane)*8);
    bf16x8 wf=wfrag(W,F,ks*16,n,hi);
    acc=MFMA(wf,xf,acc);
  }
  bf16x8 hv;
#pragma unroll
  for(int j=0;j<8;++j){ float g=bf2f(f2bf(acc[j])), u=bf2f(f2bf(acc[8+j])); g=g>0.f?g:0.f; hv[j]=(__bf16)(g*u);}
  *(bf16x8*)(act+(((size_t)tb*KS_O+256+fs)*64+lane)*8)=hv;
}
__global__ __launch_bounds__(256) void out_kernel(const u16* act,const u16* wo,const u16* wd,const u16* x,u16* out){
  int w=blockIdx.x*4+(threadIdx.x>>6), lane=threadIdx.x&63, r=lane&31, hi=lane>>5;
  int nt=w&15, tb=w>>4;
  f32x16 acc={};
  for(int ks=0;ks<KS_O;++ks){
    bf16x8 af=*(const bf16x8*)(act+(((size_t)tb*KS_O+ks)*64+lane)*8);
    bf16x8 wf= ks<256? wfragp(wo,M,ks*16,nt*32+r,hi) : wfragp(wd,M,(ks-256)*16,nt*32+r,hi);
    acc=MFMA(wf,af,acc);
  }
  int tok=tb*32+r;
#pragma unroll
  for(int i=0;i<16;++i){ int n=nt*32+(i&3)+8*(i>>2)+4*hi; out[(size_t)tok*M+n]=f2bf(acc[i]+bf2f(x[(size_t)tok*M+n])); }
}
extern "C" void kernel_launch(void* const* d_in,const int* in_sizes,int n_in,void* d_out,int out_size,void* d_ws,size_t ws_size,hipStream_t stream){
  const u16* x=(const u16*)d_in[0]; const u16* ckv=(const u16*)d_in[1]; const u16* wq=(const u16*)d_in[2];
  const u16* wuk=(const u16*)d_in[3]; const u16* wuv=(const u16*)d_in[4]; const u16* wo=(const u16*)d_in[5];
  const u16* wg=(const u16*)d_in[7]; const u16* wu=(const u16*)d_in[8]; const u16* wd=(const u16*)d_in[9];
  u16* xn=(u16*)((char*)d_ws+WS_XN); u16* kv=(u16*)((char*)d_ws+WS_KV); u16* act=(u16*)((char*)d_ws+WS_ACT);
  ln_kernel<<<S/4,256,0,stream>>>(x,xn);
  kv_kernel<<<H*64*8/4,256,0,stream>>>(ckv,wuk,wuv,kv);
  attn_kernel<<<H*64/4,256,0,stream>>>(xn,wq,kv,act);
  ffn_kernel<<<64*86/4,256,0,stream>>>(xn,wg,wu,act);
  out_kernel<<<64*16/4,256,0,stream>>>(act,wo,wd,x,(u16*)d_out);
}
```

```cpp
#include <hip/hip_runtime.h>
struct Params { const void* in[10]; void* out; void* ws; };
__global__ __launch_bounds__(256, 1) void fused_block(Params prm) {
  __shared__ __attribute__((aligned(16))) char lds[163840];
  asm volatile(
    "s_mov_b64 s[0:1], %0\n"
    "s_mov_b32 s2, %1\n"
    "v_mov_b32 v0, %2\n"
    "Lstart:\n"
    "s_load_dwordx16 s[32:47], s[0:1], 0\n"
    "s_load_dwordx8 s[48:55], s[0:1], 64\n"
    "v_and_b32 v1, 63, v0\n"
    "v_lshrrev_b32 v3, 6, v0\n"
    "v_lshlrev_b32 v2, 4, v1\n"
    "v_readfirstlane_b32 s58, v3\n"
    "s_and_b32 s60, s2, 7\n"
    "s_lshr_b32 s5, s2, 3\n"
    "s_and_b32 s6, s5, 3\n"
    "s_lshl_b32 s56, s60, 2\n"
    "s_add_u32 s56, s56, s6\n"
    "s_lshr_b32 s57, s5, 2\n"
    "s_lshl_b32 s59, s58, 12\n"
    "s_lshl_b32 s65, s58, 11\n"
    "s_lshl_b32 s91, s58, 13\n"
    "s_add_u32 s62, s57, s60\n"
    "s_lshl_b32 s62, s62, 1\n"
    "s_and_b32 s62, s62, 15\n"
    "s_mov_b32 s94, s56\n"
    "s_mov_b32 s95, s57\n"
    "s_mov_b32 s4, s62\n"
    "s_lshl_b32 s63, s4, 18\n"
    "s_lshl_b32 s64, s4, 6\n"
    "v_lshrrev_b32 v116, 3, v1\n"
    "v_bfe_u32 v3, v1, 4, 1\n"
    "v_lshlrev_b32 v3, 1, v3\n"
    "v_and_b32 v4, 7, v1\n"
    "v_xor_b32 v3, v3, v4\n"
    "v_lshlrev_b32 v117, 4, v3\n"
    "s_and_b32 s4, s58, 1\n"
    "s_lshl_b32 s4, s4, 6\n"
    "s_lshl_b32 s5, s58, 3\n"
    "v_xor_b32 v3, s4, v117\n"
    "v_add_u32 v4, s5, v116\n"
    "v_lshl_add_u32 v104, v4, 13, v3\n"
    "v_add_u32 v105, 0x80, v104\n"
    "s_lshl_b32 s4, s95, 18\n"
    "s_lshl_b32 s5, s58, 16\n"
    "s_add_u32 s4, s4, s5\n"
    "v_lshrrev_b32 v3, 4, v1\n"
    "v_and_b32 v4, 7, v1\n"
    "v_xor_b32 v3, v3, v4\n"
    "v_lshlrev_b32 v3, 4, v3\n"
    "v_lshl_add_u32 v118, v116, 10, v3\n"
    "v_add_u32 v110, s4, v118\n"
    "v_xor_b32 v111, 64, v110\n"
    "v_add_u32 v111, 0x2000, v111\n"
    "v_add_u32 v125, 0x8000, v110\n"
    "v_add_u32 v126, 0x8000, v111\n"
    "s_waitcnt lgkmcnt(0)\n"
    "s_lshl_b32 s6, s94, 8\n"
    "s_sub_u32 s7, 0x400000, s6\n"
    "s_add_u32 s16, s40, s6\n"
    "s_addc_u32 s17, s41, 0\n"
    "s_and_b32 s17, s17, 0xffff\n"
    "s_mov_b32 s18, s7\n"
    "s_mov_b32 s19, 0x20000\n"
    "s_add_u32 s12, s34, 0\n"
    "s_addc_u32 s13, s35, 0\n"
    "s_and_b32 s13, s13, 0xffff\n"
    "s_mov_b32 s14, 0x200000\n"
    "s_mov_b32 s15, 0x20000\n"
    "s_add_u32 m0, s65, 0\n"
    "s_nop 0\n"
    "buffer_load_dwordx4 v104, s[16:19], s63 offen lds\n"
    "s_add_u32 m0, s65, 0x400\n"
    "s_nop 0\n"
    "buffer_load_dwordx4 v105, s[16:19], s63 offen lds\n"
    "s_add_u32 s63, s63, 0x40000\n"
    "s_and_b32 s63, s63, 0x3fffff\n"
    "s_add_u32 m0, s91, 0x18000\n"
    "s_nop 0\n"
    "buffer_load_dwordx4 v110, s[12:15], s64 offen lds\n"
    "s_add_u32 m0, s91, 0x18400\n"
    "s_nop 0\n"
    "buffer_load_dwordx4 v111, s[12:15], s64 offen lds\n"
    "s_add_u32 m0, s91, 0x18800\n"
    "s_add_u32 s83, s64, 0x4000\n"
    "buffer_load_dwordx4 v110, s[12:15], s83 offen lds\n"
    "s_add_u32 m0, s91, 0x18c00\n"
    "s_nop 0\n"
    "buffer_load_dwordx4 v111, s[12:15], s83 offen lds\n"
    "s_add_u32 m0, s91, 0x19000\n"
    "s_nop 0\n"
    "buffer_load_dwordx4 v125, s[12:15], s64 offen lds\n"
    "s_add_u32 m0, s91, 0x19400\n"
    "s_nop 0\n"
    "buffer_load_dwordx4 v126, s[12:15], s64 offen lds\n"
    "s_add_u32 m0, s91, 0x19800\n"
    "s_add_u32 s83, s64, 0x4000\n"
    "buffer_load_dwordx4 v125, s[12:15], s83 offen lds\n"
    "s_add_u32 m0, s91, 0x19c00\n"
    "s_nop 0\n"
    "buffer_load_dwordx4 v126, s[12:15], s83 offen lds\n"
    "s_add_u32 s64, s64, 0x80\n"
    "s_and_b32 s64, s64, 0x3ff\n"
    "s_add_u32 m0, s65, 0x2000\n"
    "s_nop 0\n"
    "buffer_load_dwordx4 v104, s[16:19], s63 offen lds\n"
    "s_add_u32 m0, s65, 0x2400\n"
    "s_nop 0\n"
    "buffer_load_dwordx4 v105, s[16:19], s63 offen lds\n"
    "s_add_u32 s63, s63, 0x40000\n"
    "s_and_b32 s63, s63, 0x3fffff\n"
    "s_add_u32 s8, s54, 0\n"
    "s_addc_u32 s9, s55, 0\n"
    "s_and_b32 s9, s9, 0xffff\n"
    "s_mov_b32 s10, 0x4820000\n"
    "s_mov_b32 s11, 0x20000\n"
    "s_add_u32 s20, s32, 0\n"
    "s_addc_u32 s21, s33, 0\n"
    "s_and_b32 s21, s21, 0xffff\n"
    "s_mov_b32 s22, 0x200000\n"
    "s_mov_b32 s23, 0x20000\n"
    "s_add_u32 s28, s38, s6\n"
    "s_addc_u32 s29, s39, 0\n"
    "s_and_b32 s29, s29, 0xffff\n"
    "s_mov_b32 s30, s7\n"
    "s_mov_b32 s31, 0x20000\n"
    "v_mov_b32 v6, 0\n"
    "buffer_load_dword v7, v6, s[8:11], 0 offen sc1\n"
    "s_lshl_b32 s4, s57, 8\n"
    "s_lshl_b32 s5, s56, 3\n"
    "s_add_u32 s4, s4, s5\n"
    "s_lshl_b32 s5, s58, 1\n"
    "s_add_u32 s4, s4, s5\n"
    "s_lshl_b32 s68, s4, 10\n"
    "buffer_load_dwordx4 v[8:11], v2, s[20:23], s68 offen\n"
    "buffer_load_dwordx4 v[12:15], v2, s[20:23], s68 offen offset:1024\n"
    "s_getpc_b64 s[44:45]\n"
    "Lpc:\n"
    "s_sub_u32 s44, s44, Lpc-Lstart\n"
    "s_subb_u32 s45, s45, 0\n"
    "s_and_b32 s45, s45, 0xffff\n"
    "s_lshr_b32 s4, s2, 3\n"
    "s_lshl_b32 s4, s4, 12\n"
    "s_lshl_b32 s5, s58, 10\n"
    "s_add_u32 s32, s4, s5\n"
    "s_mov_b64 s[24:25], s[44:45]\n"
    "s_mov_b32 s26, Lcodeend-Lstart\n"
    "s_mov_b32 s27, 0x20000\n"
    "buffer_load_dword v124, v2, s[24:27], s32 offen\n"
    "v_lshrrev_b32 v3, 4, v1\n"
    "v_bfe_u32 v4, v1, 2, 2\n"
    "v_lshlrev_b32 v4, 7, v4\n"
    "v_and_b32 v5, 3, v1\n"
    "v_lshl_add_u32 v127, v5, 3, v4\n"
    "v_bfe_u32 v4, v1, 3, 2\n"
    "v_lshl_add_u32 v127, v4, 5, v127\n"
    "v_lshl_add_u32 v112, v3, 11, v127\n"
    "v_xor_b32 v113, 32, v112\n"
    "v_xor_b32 v114, 64, v112\n"
    "v_xor_b32 v115, 0x60, v112\n"
    "v_and_b32 v3, 15, v1\n"
    "v_lshlrev_b32 v3, 7, v3\n"
    "v_bfe_u32 v4, v1, 1, 3\n"
    "v_lshrrev_b32 v5, 4, v1\n"
    "v_xor_b32 v4, v4, v5\n"
    "v_lshl_add_u32 v3, v4, 4, v3\n"
    "v_add_u32 v3, s91, v3\n"
    "v_add_u32 v108, 0x10000, v3\n"
    "v_xor_b32 v109, 64, v108\n"
    "v_add_u32 v119, 0x10000, v108\n"
    "v_add_u32 v120, 0x10000, v109\n"
    "s_waitcnt vmcnt(14)\n"
    "s_barrier\n"
    "s_waitcnt vmcnt(6)\n"
    "ds_read_b128 a[0:3], v108 offset:32768\n"
    "ds_read_b128 a[4:7], v108 offset:34816\n"
    "ds_read_b128 a[8:11], v108 offset:36864\n"
    "ds_read_b128 a[12:15], v108 offset:38912\n"
    "ds_read_b64_tr_b16 v[40:41], v112\n"
    "ds_read_b64_tr_b16 v[42:43], v112 offset:512\n"
    "ds_read_b64_tr_b16 v[44:45], v113\n"
    "ds_read_b64_tr_b16 v[46:47], v113 offset:512\n"
    "ds_read_b64_tr_b16 v[48:49], v114\n"
    "ds_read_b64_tr_b16 v[50:51], v114 offset:512\n"
    "ds_read_b64_tr_b16 v[52:53], v115\n"
    "ds_read_b64_tr_b16 v[54:55], v115 offset:512\n"
    "ds_read_b64_tr_b16 v[56:57], v112 offset:1024\n"
    "ds_read_b64_tr_b16 v[58:59], v112 offset:1536\n"
    "ds_read_b64_tr_b16 v[60:61], v113 offset:1024\n"
    "ds_read_b64_tr_b16 v[62:63], v113 offset:1536\n"
    "ds_read_b64_tr_b16 v[64:65], v114 offset:1024\n"
    "ds_read_b64_tr_b16 v[66:67], v114 offset:1536\n"
    "ds_read_b64_tr_b16 v[68:69], v115 offset:1024\n"
    "ds_read_b64_tr_b16 v[70:71], v115 offset:1536\n"
    "s_waitcnt lgkmcnt(14)\n"
    "v_mfma_f32_16x16x32_bf16 v[128:131], a[0:3], v[40:43], 0\n"
    "s_add_u32 m0, s91, 0x20000\n"
    "v_mfma_f32_16x16x32_bf16 v[192:195], a[4:7], v[40:43], 0\n"
    "buffer_load_dwordx4 v110, s[12:15], s64 offen lds\n"
    "s_add_u32 m0, s91, 0x20400\n"
    "v_mfma_f32_16x16x32_bf16 v[160:163], a[8:11], v[40:43], 0\n"
    "s_waitcnt vmcnt(5)\n"
    "s_barrier\n"
    "v_mfma_f32_16x16x32_bf16 v[224:227], a[12:15], v[40:43], 0\n"
    "buffer_load_dwordx4 v111, s[12:15], s64 offen lds\n"
    "s_add_u32 m0, s91, 0x20800\n"
    "s_waitcnt lgkmcnt(12)\n"
    "v_mfma_f32_16x16x32_bf16 v[132:135], a[0:3], v[44:47], 0\n"
    "ds_read_b64_tr_b16 v[40:41], v112 offset:8192\n"
    "s_add_u32 s83, s64, 0x4000\n"
    "v_mfma_f32_16x16x32_bf16 v[196:199], a[4:7], v[44:47], 0\n"
    "buffer_load_dwordx4 v110, s[12:15], s83 offen lds\n"
    "s_add_u32 m0, s91, 0x20c00\n"
    "v_mfma_f32_16x16x32_bf16 v[164:167], a[8:11], v[44:47], 0\n"
    "ds_read_b64_tr_b16 v[42:43], v112 offset:8704\n"
    "buffer_load_dwordx4 v111, s[12:15], s83 offen lds\n"
    "v_mfma_f32_16x16x32_bf16 v[228:231], a[12:15], v[44:47], 0\n"
    "s_add_u32 m0, s91, 0x21000\n"
    "s_waitcnt lgkmcnt(12)\n"
    "v_mfma_f32_16x16x32_bf16 v[136:139], a[0:3], v[48:51], 0\n"
    "ds_read_b64_tr_b16 v[44:45], v113 offset:8192\n"
    "buffer_load_dwordx4 v125, s[12:15], s64 offen lds\n"
    "v_mfma_f32_16x16x32_bf16 v[200:203], a[4:7], v[48:51], 0\n"
    "ds_read_b64_tr_b16 v[46:47], v113 offset:8704\n"
    "s_add_u32 m0, s91, 0x21400\n"
    "v_mfma_f32_16x16x32_bf16 v[168:171], a[8:11], v[48:51], 0\n"
    "ds_read_b128 a[16:19], v109 offset:32768\n"
    "buffer_load_dwordx4 v126, s[12:15], s64 offen lds\n"
    "v_mfma_f32_16x16x32_bf16 v[232:235], a[12:15], v[48:51], 0\n"
    "s_add_u32 m0, s91, 0x21800\n"
    "s_add_u32 s83, s64, 0x4000\n"
    "s_waitcnt lgkmcnt(13)\n"
    "v_mfma_f32_16x16x32_bf16 v[140:143], a[0:3], v[52:55], 0\n"
    "ds_read_b64_tr_b16 v[48:49], v114 offset:8192\n"
    "buffer_load_dwordx4 v125, s[12:15], s83 offen lds\n"
    "v_mfma_f32_16x16x32_bf16 v[204:207], a[4:7], v[52:55], 0\n"
    "ds_read_b128 a[20:23], v109 offset:34816\n"
    "s_add_u32 m0, s91, 0x21c00\n"
    "v_mfma_f32_16x16x32_bf16 v[172:175], a[8:11], v[52:55], 0\n"
    "ds_read_b64_tr_b16 v[50:51], v114 offset:8704\n"
    "buffer_load_dwordx4 v126, s[12:15], s83 offen lds\n"
    "v_mfma_f32_16x16x32_bf16 v[236:239], a[12:15], v[52:55], 0\n"
    "s_add_u32 s64, s64, 0x80\n"
    "s_and_b32 s64, s64, 0x3ff\n"
    "s_waitcnt lgkmcnt(14)\n"
    "v_mfma_f32_16x16x32_bf16 v[144:147], a[0:3], v[56:59], 0\n"
    "ds_read_b64_tr_b16 v[52:53], v115 offset:8192\n"
    "ds_read_b128 a[24:27], v109 offset:36864\n"
    "v_mfma_f32_16x16x32_bf16 v[208:211], a[4:7], v[56:59], 0\n"
    "ds_read_b64_tr_b16 v[54:55], v115 offset:8704\n"
    "s_add_u32 m0, s65, 0x4000\n"
    "v_mfma_f32_16x16x32_bf16 v[176:179], a[8:11], v[56:59], 0\n"
    "buffer_load_dwordx4 v104, s[16:19], s63 offen lds\n"
    "s_add_u32 m0, s65, 0x4400\n"
    "v_mfma_f32_16x16x32_bf16 v[240:243], a[12:15], v[56:59], 0\n"
    "ds_read_b128 a[28:31], v109 offset:38912\n"
    "buffer_load_dwordx4 v105, s[16:19], s63 offen lds\n"
    "s_waitcnt lgkmcnt(15)\n"
    "v_mfma_f32_16x16x32_bf16 v[148:151], a[0:3], v[60:63], 0\n"
    "ds_read_b64_tr_b16 v[56:57], v112 offset:9216\n"
    "s_add_u32 s63, s63, 0x40000\n"
    "v_mfma_f32_16x16x32_bf16 v[212:215], a[4:7], v[60:63], 0\n"
    "s_and_b32 s63, s63, 0x3fffff\n"
    "s_add_u32 m0, s65, 0x6000\n"
    "v_mfma_f32_16x16x32_bf16 v[180:183], a[8:11], v[60:63], 0\n"
    "ds_read_b64_tr_b16 v[58:59], v112 offset:9728\n"
    "buffer_load_dwordx4 v104, s[16:19], s63 offen lds\n"
    "v_mfma_f32_16x16x32_bf16 v[244:247], a[12:15], v[60:63], 0\n"
    "s_add_u32 m0, s65, 0x6400\n"
    "s_waitcnt lgkmcnt(15)\n"
    "v_mfma_f32_16x16x32_bf16 v[152:155], a[0:3], v[64:67], 0\n"
    "ds_read_b64_tr_b16 v[60:61], v113 offset:9216\n"
    "buffer_load_dwordx4 v105, s[16:19], s63 offen lds\n"
    "v_mfma_f32_16x16x32_bf16 v[216:219], a[4:7], v[64:67], 0\n"
    "s_add_u32 s63, s63, 0x40000\n"
    "s_and_b32 s63, s63, 0x3fffff\n"
    "v_mfma_f32_16x16x32_bf16 v[184:187], a[8:11], v[64:67], 0\n"
    "ds_read_b64_tr_b16 v[62:63], v113 offset:9728\n"
    "s_add_u32 m0, s91, 0x10000\n"
    "v_mfma_f32_16x16x32_bf16 v[248:251], a[12:15], v[64:67], 0\n"
    "buffer_load_dwordx4 v110, s[12:15], s64 offen lds\n"
    "s_add_u32 m0, s91, 0x10400\n"
    "s_waitcnt lgkmcnt(15)\n"
    "v_mfma_f32_16x16x32_bf16 v[156:159], a[0:3], v[68:71], 0\n"
    "ds_read_b64_tr_b16 v[64:65], v114 offset:9216\n"
    "buffer_load_dwordx4 v111, s[12:15], s64 offen lds\n"
    "v_mfma_f32_16x16x32_bf16 v[220:223], a[4:7], v[68:71], 0\n"
    "s_add_u32 m0, s91, 0x10800\n"
    "s_add_u32 s83, s64, 0x4000\n"
    "v_mfma_f32_16x16x32_bf16 v[188:191], a[8:11], v[68:71], 0\n"
    "ds_read_b64_tr_b16 v[66:67], v114 offset:9728\n"
    "buffer_load_dwordx4 v110, s[12:15], s83 offen lds\n"
    "v_mfma_f32_16x16x32_bf16 v[252:255], a[12:15], v[68:71], 0\n"
    "s_add_u32 m0, s91, 0x10c00\n"
    "s_waitcnt lgkmcnt(13)\n"
    "v_mfma_f32_16x16x32_bf16 v[128:131], a[16:19], v[40:43], v[128:131]\n"
    "ds_read_b64_tr_b16 v[68:69], v115 offset:9216\n"
    "ds_read_b64_tr_b16 v[70:71], v115 offset:9728\n"
    "s_waitcnt lgkmcnt(13)\n"
    "v_mfma_f32_16x16x32_bf16 v[192:195], a[20:23], v[40:43], v[192:195]\n"
    "buffer_load_dwordx4 v111, s[12:15], s83 offen lds\n"
    "s_add_u32 m0, s91, 0x11000\n"
    "s_waitcnt lgkmcnt(10)\n"
    "v_mfma_f32_16x16x32_bf16 v[160:163], a[24:27], v[40:43], v[160:163]\n"
    "s_waitcnt vmcnt(6)\n"
    "s_barrier\n"
    "s_waitcnt lgkmcnt(8)\n"
    "v_mfma_f32_16x16x32_bf16 v[224:227], a[28:31], v[40:43], v[224:227]\n"
    "buffer_load_dwordx4 v125, s[12:15], s64 offen lds\n"
    "s_add_u32 m0, s91, 0x11400\n"
    "v_mfma_f32_16x16x32_bf16 v[132:135], a[16:19], v[44:47], v[132:135]\n"
    "ds_read_b64_tr_b16 v[40:41], v112 offset:16384\n"
    "buffer_load_dwordx4 v126, s[12:15], s64 offen lds\n"
    "v_mfma_f32_16x16x32_bf16 v[196:199], a[20:23], v[44:47], v[196:199]\n"
    "s_add_u32 m0, s91, 0x11800\n"
    "s_add_u32 s83, s64, 0x4000\n"
    "v_mfma_f32_16x16x32_bf16 v[164:167], a[24:27], v[44:47], v[164:167]\n"
    "ds_read_b64_tr_b16 v[42:43], v112 offset:16896\n"
    "buffer_load_dwordx4 v125, s[12:15], s83 offen lds\n"
    "v_mfma_f32_16x16x32_bf16 v[228:231], a[28:31], v[44:47], v[228:231]\n"
    "s_add_u32 m0, s91, 0x11c00\n"
    "v_mfma_f32_16x16x32_bf16 v[136:139], a[16:19], v[48:51], v[136:139]\n"
    "ds_read_b64_tr_b16 v[44:45], v113 offset:16384\n"
    "v_add_u32 v7, 1, v7\n"
    "v_mfma_f32_16x16x32_bf16 v[200:203], a[20:23], v[48:51], v[200:203]\n"
    "buffer_load_dwordx4 v126, s[12:15], s83 offen lds\n"
    "v_lshlrev_b32 v16, 16, v8\n"
    "v_mfma_f32_16x16x32_bf16 v[168:171], a[24:27], v[48:51], v[168:171]\n"
    "ds_read_b64_tr_b16 v[46:47], v113 offset:16896\n"
    "v_mfma_f32_16x16x32_bf16 v[232:235], a[28:31], v[48:51], v[232:235]\n"
    "v_and_b32 v17, 0xffff0000, v8\n"
    "s_add_u32 s64, s64, 0x80\n"
    "v_mfma_f32_16x16x32_bf16 v[140:143], a[16:19], v[52:55], v[140:143]\n"
    "ds_read_b64_tr_b16 v[48:49], v114 offset:16384\n"
    "v_lshlrev_b32 v18, 16, v9\n"
    "v_mfma_f32_16x16x32_bf16 v[204:207], a[20:23], v[52:55], v[204:207]\n"
    "ds_read_b128 a[32:35], v119\n"
    "s_and_b32 s64, s64, 0x3ff\n"
    "v_mfma_f32_16x16x32_bf16 v[172:175], a[24:27], v[52:55], v[172:175]\n"
    "ds_read_b64_tr_b16 v[50:51], v114 offset:16896\n"
    "v_and_b32 v19, 0xffff0000, v9\n"
    "v_mfma_f32_16x16x32_bf16 v[236:239], a[28:31], v[52:55], v[236:239]\n"
    "ds_read_b128 a[36:39], v119 offset:2048\n"
    "s_add_u32 m0, s65, 0x8000\n"
    "s_waitcnt lgkmcnt(14)\n"
    "v_mfma_f32_16x16x32_bf16 v[144:147], a[16:19], v[56:59], v[144:147]\n"
    "ds_read_b64_tr_b16 v[52:53], v115 offset:16384\n"
    "v_lshlrev_b32 v20, 16, v10\n"
    "v_mfma_f32_16x16x32_bf16 v[208:211], a[20:23], v[56:59], v[208:211]\n"
    "buffer_load_dwordx4 v104, s[16:19], s63 offen lds\n"
    "v_and_b32 v21, 0xffff0000, v10\n"
    "v_mfma_f32_16x16x32_bf16 v[176:179], a[24:27], v[56:59], v[176:179]\n"
    "ds_read_b64_tr_b16 v[54:55], v115 offset:16896\n"
    "ds_read_b128 a[40:43], v119 offset:4096\n"
    "v_mfma_f32_16x16x32_bf16 v[240:243], a[28:31], v[56:59], v[240:243]\n"
    "s_add_u32 m0, s65, 0x8400\n"
    "v_lshlrev_b32 v22, 16, v11\n"
    "s_waitcnt lgkmcnt(15)\n"
    "v_mfma_f32_16x16x32_bf16 v[148:151], a[16:19], v[60:63], v[148:151]\n"
    "ds_read_b64_tr_b16 v[56:57], v112 offset:17408\n"
    "buffer_load_dwordx4 v105, s[16:19], s63 offen lds\n"
    "v_mfma_f32_16x16x32_bf16 v[212:215], a[20:23], v[60:63], v[212:215]\n"
    "ds_read_b128 a[44:47], v119 offset:6144\n"
    "s_add_u32 s63, s63, 0x40000\n"
    "v_mfma_f32_16x16x32_bf16 v[180:183], a[24:27], v[60:63], v[180:183]\n"
    "v_and_b32 v23, 0xffff0000, v11\n"
    "v_lshlrev_b32 v24, 16, v12\n"
    "v_mfma_f32_16x16x32_bf16 v[244:247], a[28:31], v[60:63], v[244:247]\n"
    "ds_read_b64_tr_b16 v[58:59], v112 offset:17920\n"
    "s_and_b32 s63, s63, 0x3fffff\n"
    "s_waitcnt lgkmcnt(15)\n"
    "v_mfma_f32_16x16x32_bf16 v[152:155], a[16:19], v[64:67], v[152:155]\n"
    "ds_read_b64_tr_b16 v[60:61], v113 offset:17408\n"
    "s_add_u32 m0, s65, 0xa000\n"
    "v_mfma_f32_16x16x32_bf16 v[216:219], a[20:23], v[64:67], v[216:219]\n"
    "v_and_b32 v25, 0xffff0000, v12\n"
    "v_lshlrev_b32 v26, 16, v13\n"
    "v_mfma_f32_16x16x32_bf16 v[184:187], a[24:27], v[64:67], v[184:187]\n"
    "buffer_load_dwordx4 v104, s[16:19], s63 offen lds\n"
    "v_and_b32 v27, 0xffff0000, v13\n"
    "v_mfma_f32_16x16x32_bf16 v[248:251], a[28:31], v[64:67], v[248:251]\n"
    "ds_read_b64_tr_b16 v[62:63], v113 offset:17920\n"
    "s_add_u32 m0, s65, 0xa400\n"
    "s_waitcnt lgkmcnt(15)\n"
    "v_mfma_f32_16x16x32_bf16 v[156:159], a[16:19], v[68:71], v[156:159]\n"
    "ds_read_b64_tr_b16 v[64:65], v114 offset:17408\n"
    "v_lshlrev_b32 v28, 16, v14\n"
    "v_mfma_f32_16x16x32_bf16 v[220:223], a[20:23], v[68:71], v[220:223]\n"
    "buffer_load_dwordx4 v105, s[16:19], s63 offen lds\n"
    "v_and_b32 v29, 0xffff0000, v14\n"
    "v_mfma_f32_16x16x32_bf16 v[188:191], a[24:27], v[68:71], v[188:191]\n"
    "s_add_u32 s63, s63, 0x40000\n"
    "v_lshlrev_b32 v30, 16, v15\n"
    "v_mfma_f32_16x16x32_bf16 v[252:255], a[28:31], v[68:71], v[252:255]\n"
    "ds_read_b64_tr_b16 v[66:67], v114 offset:17920\n"
    "s_and_b32 s63, s63, 0x3fffff\n"
    "s_add_u32 m0, s65, 0xc000\n"
    "s_nop 0\n"
    "buffer_load_dwordx4 v104, s[16:19], s63 offen lds\n"
    "s_add_u32 m0, s65, 0xc400\n"
    "s_nop 0\n"
    "buffer_load_dwordx4 v105, s[16:19], s63 offen lds\n"
    "s_add_u32 s63, s63, 0x40000\n"
    "s_and_b32 s63, s63, 0x3fffff\n"
    "s_add_u32 m0, s65, 0xe000\n"
    "s_nop 0\n"
    "buffer_load_dwordx4 v104, s[16:19], s63 offen lds\n"
    "s_add_u32 m0, s65, 0xe400\n"
    "s_nop 0\n"
    "buffer_load_dwordx4 v105, s[16:19], s63 offen lds\n"
    "s_add_u32 s63, s63, 0x40000\n"
    "s_and_b32 s63, s63, 0x3fffff\n"
    "s_waitcnt lgkmcnt(12)\n"
    "v_mfma_f32_16x16x32_bf16 v[128:131], a[32:35], v[40:43], v[128:131]\n"
    "ds_read_b64_tr_b16 v[68:69], v115 offset:17408\n"
    "v_and_b32 v31, 0xffff0000, v15\n"
    "s_waitcnt lgkmcnt(11)\n"
    "v_mfma_f32_16x16x32_bf16 v[192:195], a[36:39], v[40:43], v[192:195]\n"
    "ds_read_b64_tr_b16 v[70:71], v115 offset:17920\n"
    "v_add_f32 v32, v16, v17\n"
    "s_waitcnt lgkmcnt(9)\n"
    "v_mfma_f32_16x16x32_bf16 v[160:163], a[40:43], v[40:43], v[160:163]\n"
    "s_waitcnt vmcnt(16)\n"
    "s_barrier\n"
    "s_waitcnt lgkmcnt(7)\n"
    "v_mfma_f32_16x16x32_bf16 v[224:227], a[44:47], v[40:43], v[224:227]\n"
    "v_add_f32 v33, v24, v25\n"
    "v_add_f32 v32, v32, v18\n"
    "v_mfma_f32_16x16x32_bf16 v[132:135], a[32:35], v[44:47], v[132:135]\n"
    "ds_read_b64_tr_b16 v[40:41], v112 offset:24576\n"
    "v_add_f32 v33, v33, v26\n"
    "v_mfma_f32_16x16x32_bf16 v[196:199], a[36:39], v[44:47], v[196:199]\n"
    "v_add_f32 v32, v32, v19\n"
    "v_add_f32 v33, v33, v27\n"
    "v_mfma_f32_16x16x32_bf16 v[164:167], a[40:43], v[44:47], v[164:167]\n"
    "s_add_u32 m0, s91, 0x18000\n"
    "v_add_f32 v32, v32, v20\n"
    "v_mfma_f32_16x16x32_bf16 v[228:231], a[44:47], v[44:47], v[228:231]\n"
    "ds_read_b64_tr_b16 v[42:43], v112 offset:25088\n"
    "v_add_f32 v33, v33, v28\n"
    "v_mfma_f32_16x16x32_bf16 v[136:139], a[32:35], v[48:51], v[136:139]\n"
    "buffer_load_dwordx4 v110, s[12:15], s64 offen lds\n"
    "v_add_f32 v32, v32, v21\n"
    "v_mfma_f32_16x16x32_bf16 v[200:203], a[36:39], v[48:51], v[200:203]\n"
    "ds_read_b64_tr_b16 v[44:45], v113 offset:24576\n"
    "s_add_u32 m0, s91, 0x18400\n"
    "v_mfma_f32_16x16x32_bf16 v[168:171], a[40:43], v[48:51], v[168:171]\n"
    "v_add_f32 v33, v33, v29\n"
    "v_add_f32 v32, v32, v22\n"
    "v_mfma_f32_16x16x32_bf16 v[232:235], a[44:47], v[48:51], v[232:235]\n"
    "ds_read_b64_tr_b16 v[46:47], v113 offset:25088\n"
    "buffer_load_dwordx4 v111, s[12:15], s64 offen lds\n"
    "v_mfma_f32_16x16x32_bf16 v[140:143], a[32:35], v[52:55], v[140:143]\n"
    "ds_read_b128 a[48:51], v120\n"
    "v_add_f32 v33, v33, v30\n"
    "v_mfma_f32_16x16x32_bf16 v[204:207], a[36:39], v[52:55], v[204:207]\n"
    "ds_read_b64_tr_b16 v[48:49], v114 offset:24576\n"
    "s_add_u32 m0, s91, 0x18800\n"
    "v_mfma_f32_16x16x32_bf16 v[172:175], a[40:43], v[52:55], v[172:175]\n"
    "v_add_f32 v32, v32, v23\n"
    "v_add_f32 v33, v33, v31\n"
    "v_mfma_f32_16x16x32_bf16 v[236:239], a[44:47], v[52:55], v[236:239]\n"
    "ds_read_b64_tr_b16 v[50:51], v114 offset:25088\n"
    "s_add_u32 s83, s64, 0x4000\n"
    "s_waitcnt lgkmcnt(13)\n"
    "v_mfma_f32_16x16x32_bf16 v[144:147], a[32:35], v[56:59], v[144:147]\n"
    "ds_read_b128 a[52:55], v120 offset:2048\n"
    "v_add_f32_dpp v32, v32, v32 quad_perm:[1,0,3,2] row_mask:0xf bank_mask:0xf\n"
    "v_mfma_f32_16x16x32_bf16 v[208:211], a[36:39], v[56:59], v[208:211]\n"
    "ds_read_b64_tr_b16 v[52:53], v115 offset:24576\n"
    "v_add_f32_dpp v33, v33, v33 quad_perm:[1,0,3,2] row_mask:0xf bank_mask:0xf\n"
    "v_mfma_f32_16x16x32_bf16 v[176:179], a[40:43], v[56:59], v[176:179]\n"
    "buffer_load_dwordx4 v110, s[12:15], s83 offen lds\n"
    "v_add_f32_dpp v32, v32, v32 quad_perm:[2,3,0,1] row_mask:0xf bank_mask:0xf\n"
    "v_mfma_f32_16x16x32_bf16 v[240:243], a[44:47], v[56:59], v[240:243]\n"
    "ds_read_b64_tr_b16 v[54:55], v115 offset:25088\n"
    "s_add_u32 m0, s91, 0x18c00\n"
    "s_waitcnt lgkmcnt(14)\n"
    "v_mfma_f32_16x16x32_bf16 v[148:151], a[32:35], v[60:63], v[148:151]\n"
    "ds_read_b128 a[56:59], v120 offset:4096\n"
    "v_add_f32_dpp v33, v33, v33 quad_perm:[2,3,0,1] row_mask:0xf bank_mask:0xf\n"
    "v_mfma_f32_16x16x32_bf16 v[212:215], a[36:39], v[60:63], v[212:215]\n"
    "ds_read_b64_tr_b16 v[56:57], v112 offset:25600\n"
    "v_add_f32_dpp v32, v32, v32 row_half_mirror row_mask:0xf bank_mask:0xf\n"
    "v_mfma_f32_16x16x32_bf16 v[180:183], a[40:43], v[60:63], v[180:183]\n"
    "buffer_load_dwordx4 v111, s[12:15], s83 offen lds\n"
    "v_add_f32_dpp v33, v33, v33 row_half_mirror row_mask:0xf bank_mask:0xf\n"
    "ds_read_b128 a[60:63], v120 offset:6144\n"
    "v_mfma_f32_16x16x32_bf16 v[244:247], a[44:47], v[60:63], v[244:247]\n"
    "ds_read_b64_tr_b16 v[58:59], v112 offset:26112\n"
    "s_add_u32 m0, s91, 0x19000\n"
    "s_waitcnt lgkmcnt(15)\n"
    "v_mfma_f32_16x16x32_bf16 v[152:155], a[32:35], v[64:67], v[152:155]\n"
    "v_add_f32_dpp v32, v32, v32 row_mirror row_mask:0xf bank_mask:0xf\n"
    "v_add_f32_dpp v33, v33, v33 row_mirror row_mask:0xf bank_mask:0xf\n"
    "v_mfma_f32_16x16x32_bf16 v[216:219], a[36:39], v[64:67], v[216:219]\n"
    "ds_read_b64_tr_b16 v[60:61], v113 offset:25600\n"
    "buffer_load_dwordx4 v125, s[12:15], s64 offen lds\n"
    "v_mfma_f32_16x16x32_bf16 v[184:187], a[40:43], v[64:67], v[184:187]\n"
    "v_readlane_b32 s75, v32, 0\n"
    "v_readlane_b32 s76, v32, 16\n"
    "v_mfma_f32_16x16x32_bf16 v[248:251], a[44:47], v[64:67], v[248:251]\n"
    "s_add_u32 m0, s91, 0x19400\n"
    "v_readlane_b32 s77, v32, 32\n"
    "s_waitcnt lgkmcnt(15)\n"
    "v_mfma_f32_16x16x32_bf16 v[156:159], a[32:35], v[68:71], v[156:159]\n"
    "ds_read_b64_tr_b16 v[62:63], v113 offset:26112\n"
    "v_readlane_b32 s78, v32, 48\n"
    "v_mfma_f32_16x16x32_bf16 v[220:223], a[36:39], v[68:71], v[220:223]\n"
    "ds_read_b64_tr_b16 v[64:65], v114 offset:25600\n"
    "buffer_load_dwordx4 v126, s[12:15], s64 offen lds\n"
    "v_mfma_f32_16x16x32_bf16 v[188:191], a[40:43], v[68:71], v[188:191]\n"
    "v_readlane_b32 s79, v33, 0\n"
    "v_readlane_b32 s80, v33, 16\n"
    "v_mfma_f32_16x16x32_bf16 v[252:255], a[44:47], v[68:71], v[252:255]\n"
    "s_add_u32 m0, s91, 0x19800\n"
    "v_readlane_b32 s81, v33, 32\n"
    "s_add_u32 s83, s64, 0x4000\n"
    "buffer_load_dwordx4 v125, s[12:15], s83 offen lds\n"
    "s_add_u32 m0, s91, 0x19c00\n"
    "s_nop 0\n"
    "buffer_load_dwordx4 v126, s[12:15], s83 offen lds\n"
    "s_add_u32 s64, s64, 0x80\n"
    "s_and_b32 s64, s64, 0x3ff\n"
    "s_waitcnt lgkmcnt(12)\n"
    "v_mfma_f32_16x16x32_bf16 v[128:131], a[48:51], v[40:43], v[128:131]\n"
    "ds_read_b64_tr_b16 v[68:69], v115 offset:25600\n"
    "ds_read_b64_tr_b16 v[66:67], v114 offset:26112\n"
    "s_waitcnt lgkmcnt(11)\n"
    "v_mfma_f32_16x16x32_bf16 v[192:195], a[52:55], v[40:43], v[192:195]\n"
    "ds_read_b64_tr_b16 v[70:71], v115 offset:26112\n"
    "v_readlane_b32 s82, v33, 48\n"
    "s_waitcnt lgkmcnt(9)\n"
    "v_mfma_f32_16x16x32_bf16 v[160:163], a[56:59], v[40:43], v[160:163]\n"
    "v_mov_b32 v32, s75\n"
    "v_mov_b32 v33, s79\n"
    "s_waitcnt lgkmcnt(7)\n"
    "v_mfma_f32_16x16x32_bf16 v[224:227], a[60:63], v[40:43], v[224:227]\n"
    "v_add_f32 v32, s76, v32\n"
    "v_add_f32 v33, s80, v33\n"
    "v_mfma_f32_16x16x32_bf16 v[132:135], a[48:51], v[44:47], v[132:135]\n"
    "v_add_f32 v32, s77, v32\n"
    "v_add_f32 v33, s81, v33\n"
    "v_mfma_f32_16x16x32_bf16 v[196:199], a[52:55], v[44:47], v[196:199]\n"
    "v_add_f32 v32, s78, v32\n"
    "v_add_f32 v33, s82, v33\n"
    "v_mfma_f32_16x16x32_bf16 v[164:167], a[56:59], v[44:47], v[164:167]\n"
    "s_waitcnt vmcnt(8)\n"
    "s_waitcnt lgkmcnt(0)\n"
    "s_barrier\n"
    "v_mfma_f32_16x16x32_bf16 v[228:231], a[60:63], v[44:47], v[228:231]\n"
    "v_mul_f32 v32, 0x3b000000, v32\n"
    "v_mul_f32 v33, 0x3b000000, v33\n"
    "v_mfma_f32_16x16x32_bf16 v[136:139], a[48:51], v[48:51], v[136:139]\n"
    "ds_read_b64_tr_b16 v[40:41], v112 offset:32768\n"
    "v_sub_f32 v16, v16, v32\n"
    "v_mfma_f32_16x16x32_bf16 v[200:203], a[52:55], v[48:51], v[200:203]\n"
    "ds_read_b64_tr_b16 v[44:45], v113 offset:32768\n"
    "v_sub_f32 v17, v17, v32\n"
    "v_mfma_f32_16x16x32_bf16 v[168:171], a[56:59], v[48:51], v[168:171]\n"
    "v_sub_f32 v18, v18, v32\n"
    "v_sub_f32 v19, v19, v32\n"
    "v_mfma_f32_16x16x32_bf16 v[232:235], a[60:63], v[48:51], v[232:235]\n"
    "ds_read_b64_tr_b16 v[42:43], v112 offset:33280\n"
    "v_sub_f32 v20, v20, v32\n"
    "v_mfma_f32_16x16x32_bf16 v[140:143], a[48:51], v[52:55], v[140:143]\n"
    "ds_read_b64_tr_b16 v[46:47], v113 offset:33280\n"
    "v_sub_f32 v21, v21, v32\n"
    "v_mfma_f32_16x16x32_bf16 v[204:207], a[52:55], v[52:55], v[204:207]\n"
    "ds_read_b64_tr_b16 v[48:49], v114 offset:32768\n"
    "v_sub_f32 v22, v22, v32\n"
    "v_mfma_f32_16x16x32_bf16 v[172:175], a[56:59], v[52:55], v[172:175]\n"
    "v_sub_f32 v23, v23, v32\n"
    "v_mfma_f32_16x16x32_bf16 v[236:239], a[60:63], v[52:55], v[236:239]\n"
    "v_sub_f32 v24, v24, v33\n"
    "v_sub_f32 v25, v25, v33\n"
    "v_mfma_f32_16x16x32_bf16 v[144:147], a[48:51], v[56:59], v[144:147]\n"
    "ds_read_b64_tr_b16 v[50:51], v114 offset:33280\n"
    "s_add_u32 m0, s65, 0\n"
    "v_mfma_f32_16x16x32_bf16 v[208:211], a[52:55], v[56:59], v[208:211]\n"
    "ds_read_b128 a[64:67], v108\n"
    "ds_read_b64_tr_b16 v[52:53], v115 offset:32768\n"
    "v_mfma_f32_16x16x32_bf16 v[176:179], a[56:59], v[56:59], v[176:179]\n"
    "buffer_load_dwordx4 v104, s[16:19], s63 offen lds\n"
    "v_sub_f32 v26, v26, v33\n"
    "v_mfma_f32_16x16x32_bf16 v[240:243], a[60:63], v[56:59], v[240:243]\n"
    "v_sub_f32 v27, v27, v33\n"
    "v_sub_f32 v28, v28, v33\n"
    "v_mfma_f32_16x16x32_bf16 v[148:151], a[48:51], v[60:63], v[148:151]\n"
    "ds_read_b64_tr_b16 v[54:55], v115 offset:33280\n"
    "ds_read_b128 a[68:71], v108 offset:2048\n"
    "v_mfma_f32_16x16x32_bf16 v[212:215], a[52:55], v[60:63], v[212:215]\n"
    "s_add_u32 m0, s65, 0x400\n"
    "v_sub_f32 v29, v29, v33\n"
    "v_mfma_f32_16x16x32_bf16 v[180:183], a[56:59], v[60:63], v[180:183]\n"
    "ds_read_b64_tr_b16 v[56:57], v112 offset:33792\n"
    "v_sub_f32 v30, v30, v33\n"
    "ds_read_b128 a[72:75], v108 offset:4096\n"
    "ds_read_b128 a[76:79], v108 offset:6144\n"
    "v_mfma_f32_16x16x32_bf16 v[244:247], a[60:63], v[60:63], v[244:247]\n"
    "buffer_load_dwordx4 v105, s[16:19], s63 offen lds\n"
    "v_sub_f32 v31, v31, v33\n"
    "v_mfma_f32_16x16x32_bf16 v[152:155], a[48:51], v[64:67], v[152:155]\n"
    "ds_read_b64_tr_b16 v[58:59], v112 offset:34304\n"
    "v_mul_f32 v32, v16, v16\n"
    "v_mfma_f32_16x16x32_bf16 v[216:219], a[52:55], v[64:67], v[216:219]\n"
    "v_mul_f32 v33, v24, v24\n"
    "v_fmac_f32 v32, v17, v17\n"
    "v_mfma_f32_16x16x32_bf16 v[184:187], a[56:59], v[64:67], v[184:187]\n"
    "ds_read_b64_tr_b16 v[60:61], v113 offset:33792\n"
    "s_add_u32 s63, s63, 0x40000\n"
    "v_mfma_f32_16x16x32_bf16 v[248:251], a[60:63], v[64:67], v[248:251]\n"
    "v_fmac_f32 v33, v25, v25\n"
    "v_fmac_f32 v32, v18, v18\n"
    "v_mfma_f32_16x16x32_bf16 v[156:159], a[48:51], v[68:71], v[156:159]\n"
    "ds_read_b64_tr_b16 v[62:63], v113 offset:34304\n"
    "s_and_b32 s63, s63, 0x3fffff\n"
    "v_mfma_f32_16x16x32_bf16 v[220:223], a[52:55], v[68:71], v[220:223]\n"
    "ds_read_b64_tr_b16 v[64:65], v114 offset:33792\n"
    "v_fmac_f32 v33, v26, v26\n"
    "v_mfma_f32_16x16x32_bf16 v[188:191], a[56:59], v[68:71], v[188:191]\n"
    "s_add_u32 m0, s65, 0x2000\n"
    "v_fmac_f32 v32, v19, v19\n"
    "v_mfma_f32_16x16x32_bf16 v[252:255], a[60:63], v[68:71], v[252:255]\n"
    "ds_read_b64_tr_b16 v[68:69], v115 offset:33792\n"
    "ds_read_b64_tr_b16 v[70:71], v115 offset:34304\n"
    "ds_read_b64_tr_b16 v[66:67], v114 offset:34304\n"
    "buffer_load_dwordx4 v104, s[16:19], s63 offen lds\n"
    "s_add_u32 m0, s65, 0x2400\n"
    "s_nop 0\n"
    "buffer_load_dwordx4 v105, s[16:19], s63 offen lds\n"
    "s_add_u32 s63, s63, 0x40000\n"
    "s_and_b32 s63, s63, 0x3fffff\n"
    "v_fmac_f32 v33, v27, v27\n"
    "v_fmac_f32 v32, v20, v20\n"
    "v_fmac_f32 v33, v28, v28\n"
    "v_fmac_f32 v32, v21, v21\n"
    "v_fmac_f32 v33, v29, v29\n"
    "v_fmac_f32 v32, v22, v22\n"
    "v_fmac_f32 v33, v30, v30\n"
    "v_fmac_f32 v32, v23, v23\n"
    "v_fmac_f32 v33, v31, v31\n"
    "s_nop 0\n"
    "v_add_f32_dpp v32, v32, v32 quad_perm:[1,0,3,2] row_mask:0xf bank_mask:0xf\n"
    "v_add_f32_dpp v33, v33, v33 quad_perm:[1,0,3,2] row_mask:0xf bank_mask:0xf\n"
    "s_nop 0\n"
    "v_add_f32_dpp v32, v32, v32 quad_perm:[2,3,0,1] row_mask:0xf bank_mask:0xf\n"
    "v_add_f32_dpp v33, v33, v33 quad_perm:[2,3,0,1] row_mask:0xf bank_mask:0xf\n"
    "s_nop 0\n"
    "v_add_f32_dpp v32, v32, v32 row_half_mirror row_mask:0xf bank_mask:0xf\n"
    "v_add_f32_dpp v33, v33, v33 row_half_mirror row_mask:0xf bank_mask:0xf\n"
    "s_nop 0\n"
    "v_add_f32_dpp v32, v32, v32 row_mirror row_mask:0xf bank_mask:0xf\n"
    "v_add_f32_dpp v33, v33, v33 row_mirror row_mask:0xf bank_mask:0xf\n"
    "v_readlane_b32 s75, v32, 0\n"
    "v_readlane_b32 s76, v32, 16\n"
    "v_readlane_b32 s77, v32, 32\n"
    "v_readlane_b32 s78, v32, 48\n"
    "v_readlane_b32 s79, v33, 0\n"
    "v_readlane_b32 s80, v33, 16\n"
    "v_readlane_b32 s81, v33, 32\n"
    "v_readlane_b32 s82, v33, 48\n"
    "v_mov_b32 v32, s75\n"
    "v_mov_b32 v33, s79\n"
    "v_add_f32 v32, s76, v32\n"
    "v_add_f32 v33, s80, v33\n"
    "v_add_f32 v32, s77, v32\n"
    "v_add_f32 v33, s81, v33\n"
    "v_add_f32 v32, s78, v32\n"
    "v_add_f32 v33, s82, v33\n"
    "v_mov_b32 v34, 0x3727c5ac\n"
    "v_mul_f32 v32, 0x3b000000, v32\n"
    "v_mul_f32 v33, 0x3b000000, v33\n"
    "v_add_f32 v32, v32, v34\n"
    "v_add_f32 v33, v33, v34\n"
    "v_rsq_f32 v32, v32\n"
    "v_rsq_f32 v33, v33\n"
    "v_mul_f32 v16, v16, v32\n"
    "v_mul_f32 v17, v17, v32\n"
    "v_mul_f32 v18, v18, v32\n"
    "v_mul_f32 v19, v19, v32\n"
    "v_mul_f32 v20, v20, v32\n"
    "v_mul_f32 v21, v21, v32\n"
    "v_mul_f32 v22, v22, v32\n"
    "v_mul_f32 v23, v23, v32\n"
    "v_mul_f32 v24, v24, v33\n"
    "v_mul_f32 v25, v25, v33\n"
    "v_mul_f32 v26, v26, v33\n"
    "v_mul_f32 v27, v27, v33\n"
    "v_mul_f32 v28, v28, v33\n"
    "v_mul_f32 v29, v29, v33\n"
    "v_mul_f32 v30, v30, v33\n"
    "v_mul_f32 v31, v31, v33\n"
    "v_cvt_pk_bf16_f32 v8, v16, v17\n"
    "v_cvt_pk_bf16_f32 v9, v18, v19\n"
    "v_cvt_pk_bf16_f32 v10, v20, v21\n"
    "v_cvt_pk_bf16_f32 v11, v22, v23\n"
    "v_cvt_pk_bf16_f32 v12, v24, v25\n"
    "v_cvt_pk_bf16_f32 v13, v26, v27\n"
    "v_cvt_pk_bf16_f32 v14, v28, v29\n"
    "v_cvt_pk_bf16_f32 v15, v30, v31\n"
    "s_add_u32 s68, s68, 0x20000\n"
    "buffer_store_dwordx4 v[8:11], v2, s[8:11], s68 offen sc1\n"
    "buffer_store_dwordx4 v[12:15], v2, s[8:11], s68 offen offset:1024 sc1\n"
    "s_waitcnt lgkmcnt(13)\n"
    "v_mfma_f32_16x16x32_bf16 v[128:131], a[64:67], v[40:43], v[128:131]\n"
    "s_add_u32 m0, s91, 0x20000\n"
    "s_waitcnt lgkmcnt(10)\n"
    "v_mfma_f32_16x16x32_bf16 v[192:195], a[68:71], v[40:43], v[192:195]\n"
    "buffer_load_dwordx4 v110, s[12:15], s64 offen lds\n"
    "s_waitcnt lgkmcnt(8)\n"
    "v_mfma_f32_16x16x32_bf16 v[160:163], a[72:75], v[40:43], v[160:163]\n"
    "s_waitcnt lgkmcnt(7)\n"
    "v_mfma_f32_16x16x32_bf16 v[224:227], a[76:79], v[40:43], v[224:227]\n"
    "s_add_u32 m0, s91, 0x20400\n"
    "v_mfma_f32_16x16x32_bf16 v[132:135], a[64:67], v[44:47], v[132:135]\n"
    "ds_read_b64_tr_b16 v[40:41], v112 offset:40960\n"
    "buffer_load_dwordx4 v111, s[12:15], s64 offen lds\n"
    "v_mfma_f32_16x16x32_bf16 v[196:199], a[68:71], v[44:47], v[196:199]\n"
    "ds_read_b64_tr_b16 v[42:43], v112 offset:41472\n"
    "v_mfma_f32_16x16x32_bf16 v[164:167], a[72:75], v[44:47], v[164:167]\n"
    "s_add_u32 m0, s91, 0x20800\n"
    "v_mfma_f32_16x16x32_bf16 v[228:231], a[76:79], v[44:47], v[228:231]\n"
    "v_mfma_f32_16x16x32_bf16 v[136:139], a[64:67], v[48:51], v[136:139]\n"
    "ds_read_b64_tr_b16 v[44:45], v113 offset:40960\n"
    "s_add_u32 s83, s64, 0x4000\n"
    "v_mfma_f32_16x16x32_bf16 v[200:203], a[68:71], v[48:51], v[200:203]\n"
    "buffer_load_dwordx4 v110, s[12:15], s83 offen lds\n"
    "ds_read_b64_tr_b16 v[46:47], v113 offset:41472\n"
    "v_mfma_f32_16x16x32_bf16 v[168:171], a[72:75], v[48:51], v[168:171]\n"
    "ds_read_b128 a[80:83], v109\n"
    "ds_read_b128 a[84:87], v109 offset:2048\n"
    "v_mfma_f32_16x16x32_bf16 v[232:235], a[76:79], v[48:51], v[232:235]\n"
    "ds_read_b128 a[88:91], v109 offset:4096\n"
    "s_add_u32 m0, s91, 0x20c00\n"
    "v_mfma_f32_16x16x32_bf16 v[140:143], a[64:67], v[52:55], v[140:143]\n"
    "ds_read_b64_tr_b16 v[48:49], v114 offset:40960\n"
    "buffer_load_dwordx4 v111, s[12:15], s83 offen lds\n"
    "v_mfma_f32_16x16x32_bf16 v[204:207], a[68:71], v[52:55], v[204:207]\n"
    "ds_read_b64_tr_b16 v[50:51], v114 offset:41472\n"
    "ds_read_b128 a[92:95], v109 offset:6144\n"
    "v_mfma_f32_16x16x32_bf16 v[172:175], a[72:75], v[52:55], v[172:175]\n"
    "s_add_u32 m0, s91, 0x21000\n"
    "v_mfma_f32_16x16x32_bf16 v[236:239], a[76:79], v[52:55], v[236:239]\n"
    "s_waitcnt lgkmcnt(15)\n"
    "v_mfma_f32_16x16x32_bf16 v[144:147], a[64:67], v[56:59], v[144:147]\n"
    "ds_read_b64_tr_b16 v[52:53], v115 offset:40960\n"
    "buffer_load_dwordx4 v125, s[12:15], s64 offen lds\n"
    "v_mfma_f32_16x16x32_bf16 v[208:211], a[68:71], v[56:59], v[208:211]\n"
    "ds_read_b64_tr_b16 v[54:55], v115 offset:41472\n"
    "s_add_u32 m0, s91, 0x21400\n"
    "v_mfma_f32_16x16x32_bf16 v[176:179], a[72:75], v[56:59], v[176:179]\n"
    "v_mfma_f32_16x16x32_bf16 v[240:243], a[76:79], v[56:59], v[240:243]\n"
    "buffer_load_dwordx4 v126, s[12:15], s64 offen lds\n"
    "s_waitcnt lgkmcnt(15)\n"
    "v_mfma_f32_16x16x32_bf16 v[148:151], a[64:67], v[60:63], v[148:151]\n"
    "ds_read_b64_tr_b16 v[56:57], v112 offset:41984\n"
    "s_add_u32 m0, s91, 0x21800\n"
    "v_mfma_f32_16x16x32_bf16 v[212:215], a[68:71], v[60:63], v[212:215]\n"
    "ds_read_b64_tr_b16 v[58:59], v112 offset:42496\n"
    "v_mfma_f32_16x16x32_bf16 v[180:183], a[72:75], v[60:63], v[180:183]\n"
    "s_add_u32 s83, s64, 0x4000\n"
    "v_mfma_f32_16x16x32_bf16 v[244:247], a[76:79], v[60:63], v[244:247]\n"
    "s_waitcnt lgkmcnt(14)\n"
    "v_mfma_f32_16x16x32_bf16 v[152:155], a[64:67], v[64:67], v[152:155]\n"
    "ds_read_b64_tr_b16 v[60:61], v113 offset:41984\n"
    "buffer_load_dwordx4 v125, s[12:15], s83 offen lds\n"
    "v_mfma_f32_16x16x32_bf16 v[216:219], a[68:71], v[64:67], v[216:219]\n"
    "ds_read_b64_tr_b16 v[62:63], v113 offset:42496\n"
    "s_add_u32 m0, s91, 0x21c00\n"
    "v_mfma_f32_16x16x32_bf16 v[184:187], a[72:75], v[64:67], v[184:187]\n"
    "v_mfma_f32_16x16x32_bf16 v[248:251], a[76:79], v[64:67], v[248:251]\n"
    "buffer_load_dwordx4 v126, s[12:15], s83 offen lds\n"
    "v_mfma_f32_16x16x32_bf16 v[156:159], a[64:67], v[68:71], v[156:159]\n"
    "ds_read_b64_tr_b16 v[64:65], v114 offset:41984\n"
    "s_add_u32 s64, s64, 0x80\n"
    "v_mfma_f32_16x16x32_bf16 v[220:223], a[68:71], v[68:71], v[220:223]\n"
    "ds_read_b64_tr_b16 v[66:67], v114 offset:42496\n"
    "v_mfma_f32_16x16x32_bf16 v[188:191], a[72:75], v[68:71], v[188:191]\n"
    "s_and_b32 s64, s64, 0x3ff\n"
    "v_mfma_f32_16x16x32_bf16 v[252:255], a[76:79], v[68:71], v[252:255]\n"
    "s_waitcnt lgkmcnt(13)\n"
    "v_mfma_f32_16x16x32_bf16 v[128:131], a[80:83], v[40:43], v[128:131]\n"
    "ds_read_b64_tr_b16 v[68:69], v115 offset:41984\n"
    "s_add_u32 m0, s65, 0x4000\n"
    "s_waitcnt lgkmcnt(13)\n"
    "v_mfma_f32_16x16x32_bf16 v[192:195], a[84:87], v[40:43], v[192:195]\n"
    "ds_read_b64_tr_b16 v[70:71], v115 offset:42496\n"
    "buffer_load_dwordx4 v104, s[16:19], s63 offen lds\n"
    "s_waitcnt lgkmcnt(13)\n"
    "v_mfma_f32_16x16x32_bf16 v[160:163], a[88:91], v[40:43], v[160:163]\n"
    "s_add_u32 m0, s65, 0x4400\n"
    "s_waitcnt lgkmcnt(10)\n"
    "v_mfma_f32_16x16x32_bf16 v[224:227], a[92:95], v[40:43], v[224:227]\n"
    "buffer_load_dwordx4 v105, s[16:19], s63 offen lds\n"
    "s_add_u32 s63, s63, 0x40000\n"
    "v_mfma_f32_16x16x32_bf16 v[132:135], a[80:83], v[44:47], v[132:135]\n"
    "ds_read_b64_tr_b16 v[40:41], v112 offset:49152\n"
    "s_and_b32 s63, s63, 0x3fffff\n"
    "v_mfma_f32_16x16x32_bf16 v[196:199], a[84:87], v[44:47], v[196:199]\n"
    "ds_read_b64_tr_b16 v[42:43], v112 offset:49664\n"
    "s_add_u32 m0, s65, 0x6000\n"
    "v_mfma_f32_16x16x32_bf16 v[164:167], a[88:91], v[44:47], v[164:167]\n"
    "buffer_load_dwordx4 v104, s[16:19], s63 offen lds\n"
    "s_add_u32 m0, s65, 0x6400\n"
    "v_mfma_f32_16x16x32_bf16 v[228:231], a[92:95], v[44:47], v[228:231]\n"
    "buffer_load_dwordx4 v105, s[16:19], s63 offen lds\n"
    "s_add_u32 s63, s63, 0x40000\n"
    "v_mfma_f32_16x16x32_bf16 v[136:139], a[80:83], v[48:51], v[136:139]\n"
    "ds_read_b64_tr_b16 v[44:45], v113 offset:49152\n"
    "s_and_b32 s63, s63, 0x3fffff\n"
    "v_mfma_f32_16x16x32_bf16 v[200:203], a[84:87], v[48:51], v[200:203]\n"
    "s_cmp_eq_u32 s58, 2\n"
    "s_cselect_b32 s70, s46, s48\n"
    "v_mfma_f32_16x16x32_bf16 v[168:171], a[88:91], v[48:51], v[168:171]\n"
    "s_cselect_b32 s71, s47, s49\n"
    "s_mul_i32 s6, s56, 0x60\n"
    "v_mfma_f32_16x16x32_bf16 v[232:235], a[92:95], v[48:51], v[232:235]\n"
    "ds_read_b64_tr_b16 v[46:47], v113 offset:49664\n"
    "s_waitcnt vmcnt(18)\n"
    "s_waitcnt lgkmcnt(12)\n"
    "v_mfma_f32_16x16x32_bf16 v[140:143], a[80:83], v[52:55], v[140:143]\n"
    "ds_read_b64_tr_b16 v[48:49], v114 offset:49152\n"
    "s_lshl_b32 s5, s56, 8\n"
    "v_mfma_f32_16x16x32_bf16 v[204:207], a[84:87], v[52:55], v[204:207]\n"
    "s_mov_b32 s7, 0x158000\n"
    "s_mov_b32 s69, 0x15800\n"
    "v_mfma_f32_16x16x32_bf16 v[172:175], a[88:91], v[52:55], v[172:175]\n"
    "ds_read_b128 a[96:99], v108 offset:32768\n"
    "s_cmp_lt_u32 s58, 2\n"
    "v_mfma_f32_16x16x32_bf16 v[236:239], a[92:95], v[52:55], v[236:239]\n"
    "ds_read_b64_tr_b16 v[50:51], v114 offset:49664\n"
    "s_cselect_b32 s70, s36, s70\n"
    "s_waitcnt lgkmcnt(13)\n"
    "v_mfma_f32_16x16x32_bf16 v[144:147], a[80:83], v[56:59], v[144:147]\n"
    "s_cselect_b32 s71, s37, s71\n"
    "s_cselect_b32 s6, s5, s6\n"
    "v_mfma_f32_16x16x32_bf16 v[208:211], a[84:87], v[56:59], v[208:211]\n"
    "ds_read_b64_tr_b16 v[52:53], v115 offset:49152\n"
    "s_cselect_b32 s7, 0x400000, s7\n"
    "v_mfma_f32_16x16x32_bf16 v[176:179], a[88:91], v[56:59], v[176:179]\n"
    "ds_read_b128 a[100:103], v108 offset:34816\n"
    "s_cselect_b32 s69, 0x40000, s69\n"
    "v_mfma_f32_16x16x32_bf16 v[240:243], a[92:95], v[56:59], v[240:243]\n"
    "ds_read_b64_tr_b16 v[54:55], v115 offset:49664\n"
    "s_cselect_b64 s[84:85], -1, 0\n"
    "s_waitcnt lgkmcnt(14)\n"
    "v_mfma_f32_16x16x32_bf16 v[148:151], a[80:83], v[60:63], v[148:151]\n"
    "s_sub_u32 s7, s7, s6\n"
    "s_add_u32 s24, s70, s6\n"
    "v_mfma_f32_16x16x32_bf16 v[212:215], a[84:87], v[60:63], v[212:215]\n"
    "ds_read_b64_tr_b16 v[56:57], v112 offset:50176\n"
    "ds_read_b128 a[104:107], v108 offset:36864\n"
    "v_mfma_f32_16x16x32_bf16 v[180:183], a[88:91], v[60:63], v[180:183]\n"
    "s_addc_u32 s25, s71, 0\n"
    "s_and_b32 s25, s25, 0xffff\n"
    "ds_read_b128 a[108:111], v108 offset:38912\n"
    "v_mfma_f32_16x16x32_bf16 v[244:247], a[92:95], v[60:63], v[244:247]\n"
    "s_mov_b32 s26, s7\n"
    "s_mov_b32 s27, 0x20000\n"
    "s_waitcnt lgkmcnt(15)\n"
    "v_mfma_f32_16x16x32_bf16 v[152:155], a[80:83], v[64:67], v[152:155]\n"
    "ds_read_b64_tr_b16 v[58:59], v112 offset:50688\n"
    "s_lshl_b32 s67, s69, 4\n"
    "v_mfma_f32_16x16x32_bf16 v[216:219], a[84:87], v[64:67], v[216:219]\n"
    "ds_read_b64_tr_b16 v[60:61], v113 offset:50176\n"
    "s_mul_i32 s90, s62, s69\n"
    "v_mfma_f32_16x16x32_bf16 v[184:187], a[88:91], v[64:67], v[184:187]\n"
    "s_mov_b32 s74, s90\n"
    "s_movk_i32 s4, 0xac0\n"
    "v_mfma_f32_16x16x32_bf16 v[248:251], a[92:95], v[64:67], v[248:251]\n"
    "s_and_b32 s5, s58, 1\n"
    "s_lshl_b32 s5, s5, 4\n"
    "s_waitcnt lgkmcnt(15)\n"
    "v_mfma_f32_16x16x32_bf16 v[156:159], a[80:83], v[68:71], v[156:159]\n"
    "ds_read_b64_tr_b16 v[62:63], v113 offset:50688\n"
    "v_mul_lo_u32 v3, s4, v116\n"
    "v_mfma_f32_16x16x32_bf16 v[220:223], a[84:87], v[68:71], v[220:223]\n"
    "ds_read_b64_tr_b16 v[64:65], v114 offset:50176\n"
    "v_add_u32 v4, s5, v116\n"
    "v_mfma_f32_16x16x32_bf16 v[188:191], a[88:91], v[68:71], v[188:191]\n"
    "v_lshlrev_b32 v4, 13, v4\n"
    "v_add_u32 v5, 0, v3\n"
    "v_mfma_f32_16x16x32_bf16 v[252:255], a[92:95], v[68:71], v[252:255]\n"
    "v_add_u32 v6, 0, v4\n"
    "v_cndmask_b32 v5, v5, v6, s[84:85]\n"
    "v_mov_b32 v6, 0\n"
    "v_cndmask_b32 v6, 0, v6, s[84:85]\n"
    "v_xor_b32 v6, v6, v117\n"
    "v_add_u32 v106, v5, v6\n"
    "v_add_u32 v5, 0x5600, v3\n"
    "v_add_u32 v6, 0x80, v4\n"
    "v_cndmask_b32 v5, v5, v6, s[84:85]\n"
    "v_mov_b32 v6, 0\n"
    "v_cndmask_b32 v6, 64, v6, s[84:85]\n"
    "v_xor_b32 v6, v6, v117\n"
    "v_add_u32 v107, v5, v6\n"
    "v_add_u32 v5, 0xac00, v3\n"
    "v_add_u32 v6, 0x10000, v4\n"
    "v_cndmask_b32 v5, v5, v6, s[84:85]\n"
    "v_mov_b32 v6, 64\n"
    "v_cndmask_b32 v6, 0, v6, s[84:85]\n"
    "v_xor_b32 v6, v6, v117\n"
    "v_add_u32 v121, v5, v6\n"
    "v_add_u32 v5, 0x10200, v3\n"
    "v_add_u32 v6, 0x10080, v4\n"
    "v_cndmask_b32 v5, v5, v6, s[84:85]\n"
    "v_mov_b32 v6, 64\n"
    "v_cndmask_b32 v6, 64, v6, s[84:85]\n"
    "v_xor_b32 v6, v6, v117\n"
    "v_add_u32 v123, v5, v6\n"
    "s_waitcnt lgkmcnt(11)\n"
    "v_mfma_f32_16x16x32_bf16 v[128:131], a[96:99], v[40:43], v[128:131]\n"
    "ds_read_b64_tr_b16 v[68:69], v115 offset:50176\n"
    "ds_read_b64_tr_b16 v[66:67], v114 offset:50688\n"
    "s_waitcnt lgkmcnt(10)\n"
    "v_mfma_f32_16x16x32_bf16 v[192:195], a[100:103], v[40:43], v[192:195]\n"
    "ds_read_b64_tr_b16 v[70:71], v115 offset:50688\n"
    "s_add_u32 m0, s91, 0x10000\n"
    "s_waitcnt lgkmcnt(8)\n"
    "v_mfma_f32_16x16x32_bf16 v[160:163], a[104:107], v[40:43], v[160:163]\n"
    "buffer_load_dwordx4 v110, s[12:15], s64 offen lds\n"
    "s_waitcnt lgkmcnt(7)\n"
    "v_mfma_f32_16x16x32_bf16 v[224:227], a[108:111], v[40:43], v[224:227]\n"
    "s_add_u32 m0, s91, 0x10400\n"
    "v_mfma_f32_16x16x32_bf16 v[132:135], a[96:99], v[44:47], v[132:135]\n"
    "ds_read_b64_tr_b16 v[40:41], v112 offset:57344\n"
    "buffer_load_dwordx4 v111, s[12:15], s64 offen lds\n"
    "v_mfma_f32_16x16x32_bf16 v[196:199], a[100:103], v[44:47], v[196:199]\n"
    "ds_read_b64_tr_b16 v[42:43], v112 offset:57856\n"
    "v_mfma_f32_16x16x32_bf16 v[164:167], a[104:107], v[44:47], v[164:167]\n"
    "s_add_u32 m0, s91, 0x10800\n"
    "v_mfma_f32_16x16x32_bf16 v[228:231], a[108:111], v[44:47], v[228:231]\n"
    "v_mfma_f32_16x16x32_bf16 v[136:139], a[96:99], v[48:51], v[136:139]\n"
    "ds_read_b64_tr_b16 v[44:45], v113 offset:57344\n"
    "s_add_u32 s83, s64, 0x4000\n"
    "v_mfma_f32_16x16x32_bf16 v[200:203], a[100:103], v[48:51], v[200:203]\n"
    "buffer_load_dwordx4 v110, s[12:15], s83 offen lds\n"
    "ds_read_b64_tr_b16 v[46:47], v113 offset:57856\n"
    "v_mfma_f32_16x16x32_bf16 v[168:171], a[104:107], v[48:51], v[168:171]\n"
    "ds_read_b128 a[112:115], v109 offset:32768\n"
    "ds_read_b128 a[116:119], v109 offset:34816\n"
    "v_mfma_f32_16x16x32_bf16 v[232:235], a[108:111], v[48:51], v[232:235]\n"
    "ds_read_b128 a[120:123], v109 offset:36864\n"
    "s_add_u32 m0, s91, 0x10c00\n"
    "v_mfma_f32_16x16x32_bf16 v[140:143], a[96:99], v[52:55], v[140:143]\n"
    "ds_read_b64_tr_b16 v[48:49], v114 offset:57344\n"
    "buffer_load_dwordx4 v111, s[12:15], s83 offen lds\n"
    "v_mfma_f32_16x16x32_bf16 v[204:207], a[100:103], v[52:55], v[204:207]\n"
    "ds_read_b64_tr_b16 v[50:51], v114 offset:57856\n"
    "ds_read_b128 a[124:127], v109 offset:38912\n"
    "v_mfma_f32_16x16x32_bf16 v[172:175], a[104:107], v[52:55], v[172:175]\n"
    "s_add_u32 m0, s91, 0x11000\n"
    "v_mfma_f32_16x16x32_bf16 v[236:239], a[108:111], v[52:55], v[236:239]\n"
    "s_waitcnt lgkmcnt(15)\n"
    "v_mfma_f32_16x16x32_bf16 v[144:147], a[96:99], v[56:59], v[144:147]\n"
    "ds_read_b64_tr_b16 v[52:53], v115 offset:57344\n"
    "buffer_load_dwordx4 v125, s[12:15], s64 offen lds\n"
    "v_mfma_f32_16x16x32_bf16 v[208:211], a[100:103], v[56:59], v[208:211]\n"
    "ds_read_b64_tr_b16 v[54:55], v115 offset:57856\n"
    "s_add_u32 m0, s91, 0x11400\n"
    "v_mfma_f32_16x16x32_bf16 v[176:179], a[104:107], v[56:59], v[176:179]\n"
    "v_mfma_f32_16x16x32_bf16 v[240:243], a[108:111], v[56:59], v[240:243]\n"
    "buffer_load_dwordx4 v126, s[12:15], s64 offen lds\n"
    "s_waitcnt lgkmcnt(15)\n"
    "v_mfma_f32_16x16x32_bf16 v[148:151], a[96:99], v[60:63], v[148:151]\n"
    "ds_read_b64_tr_b16 v[56:57], v112 offset:58368\n"
    "s_add_u32 m0, s91, 0x11800\n"
    "v_mfma_f32_16x16x32_bf16 v[212:215], a[100:103], v[60:63], v[212:215]\n"
    "ds_read_b64_tr_b16 v[58:59], v112 offset:58880\n"
    "v_mfma_f32_16x16x32_bf16 v[180:183], a[104:107], v[60:63], v[180:183]\n"
    "s_add_u32 s83, s64, 0x4000\n"
    "v_mfma_f32_16x16x32_bf16 v[244:247], a[108:111], v[60:63], v[244:247]\n"
    "s_waitcnt lgkmcnt(15)\n"
    "v_mfma_f32_16x16x32_bf16 v[152:155], a[96:99], v[64:67], v[152:155]\n"
    "ds_read_b64_tr_b16 v[60:61], v113 offset:58368\n"
    "buffer_load_dwordx4 v125, s[12:15], s83 offen lds\n"
    "v_mfma_f32_16x16x32_bf16 v[216:219], a[100:103], v[64:67], v[216:219]\n"
    "ds_read_b64_tr_b16 v[62:63], v113 offset:58880\n"
    "s_add_u32 m0, s91, 0x11c00\n"
    "v_mfma_f32_16x16x32_bf16 v[184:187], a[104:107], v[64:67], v[184:187]\n"
    "v_mfma_f32_16x16x32_bf16 v[248:251], a[108:111], v[64:67], v[248:251]\n"
    "buffer_load_dwordx4 v126, s[12:15], s83 offen lds\n"
    "s_waitcnt lgkmcnt(15)\n"
    "v_mfma_f32_16x16x32_bf16 v[156:159], a[96:99], v[68:71], v[156:159]\n"
    "ds_read_b64_tr_b16 v[64:65], v114 offset:58368\n"
    "s_add_u32 s64, s64, 0x80\n"
    "v_mfma_f32_16x16x32_bf16 v[220:223], a[100:103], v[68:71], v[220:223]\n"
    "ds_read_b64_tr_b16 v[66:67], v114 offset:58880\n"
    "v_mfma_f32_16x16x32_bf16 v[188:191], a[104:107], v[68:71], v[188:191]\n"
    "s_and_b32 s64, s64, 0x3ff\n"
    "v_mfma_f32_16x16x32_bf16 v[252:255], a[108:111], v[68:71], v[252:255]\n"
    "s_waitcnt lgkmcnt(13)\n"
    "v_mfma_f32_16x16x32_bf16 v[128:131], a[112:115], v[40:43], v[128:131]\n"
    "ds_read_b64_tr_b16 v[68:69], v115 offset:58368\n"
    "ds_read_b64_tr_b16 v[70:71], v115 offset:58880\n"
    "s_waitcnt lgkmcnt(14)\n"
    "v_mfma_f32_16x16x32_bf16 v[192:195], a[116:119], v[40:43], v[192:195]\n"
    "s_waitcnt lgkmcnt(13)\n"
    "v_mfma_f32_16x16x32_bf16 v[160:163], a[120:123], v[40:43], v[160:163]\n"
    "s_waitcnt lgkmcnt(10)\n"
    "v_mfma_f32_16x16x32_bf16 v[224:227], a[124:127], v[40:43], v[224:227]\n"
    "v_mfma_f32_16x16x32_bf16 v[132:135], a[112:115], v[44:47], v[132:135]\n"
    "v_mfma_f32_16x16x32_bf16 v[196:199], a[116:119], v[44:47], v[196:199]\n"
    "v_mfma_f32_16x16x32_bf16 v[164:167], a[120:123], v[44:47], v[164:167]\n"
    "s_waitcnt vmcnt(20)\n"
    "s_waitcnt vmcnt(8)\n"
    "s_waitcnt lgkmcnt(0)\n"
    "s_barrier\n"
    "v_mfma_f32_16x16x32_bf16 v[228:231], a[124:127], v[44:47], v[228:231]\n"
    "ds_read_b64_tr_b16 v[40:41], v112\n"
    "s_add_u32 m0, s65, 0x8000\n"
    "v_mfma_f32_16x16x32_bf16 v[136:139], a[112:115], v[48:51], v[136:139]\n"
    "ds_read_b64_tr_b16 v[44:45], v113\n"
    "buffer_load_dwordx4 v104, s[16:19], s63 offen lds\n"
    "v_mfma_f32_16x16x32_bf16 v[200:203], a[116:119], v[48:51], v[200:203]\n"
    "ds_read_b64_tr_b16 v[42:43], v112 offset:512\n"
    "s_add_u32 m0, s65, 0x8400\n"
    "v_mfma_f32_16x16x32_bf16 v[168:171], a[120:123], v[48:51], v[168:171]\n"
    "ds_read_b64_tr_b16 v[46:47], v113 offset:512\n"
    "v_mfma_f32_16x16x32_bf16 v[232:235], a[124:127], v[48:51], v[232:235]\n"
    "buffer_load_dwordx4 v105, s[16:19], s63 offen lds\n"
    "s_add_u32 s63, s63, 0x40000\n"
    "v_mfma_f32_16x16x32_bf16 v[140:143], a[112:115], v[52:55], v[140:143]\n"
    "ds_read_b64_tr_b16 v[48:49], v114\n"
    "ds_read_b128 a[128:131], v119\n"
    "v_mfma_f32_16x16x32_bf16 v[204:207], a[116:119], v[52:55], v[204:207]\n"
    "s_and_b32 s63, s63, 0x3fffff\n"
    "s_add_u32 m0, s65, 0xa000\n"
    "v_mfma_f32_16x16x32_bf16 v[172:175], a[120:123], v[52:55], v[172:175]\n"
    "ds_read_b64_tr_b16 v[50:51], v114 offset:512\n"
    "buffer_load_dwordx4 v104, s[16:19], s63 offen lds\n"
    "v_mfma_f32_16x16x32_bf16 v[236:239], a[124:127], v[52:55], v[236:239]\n"
    "ds_read_b128 a[132:135], v119 offset:2048\n"
    "ds_read_b128 a[136:139], v119 offset:4096\n"
    "v_mfma_f32_16x16x32_bf16 v[144:147], a[112:115], v[56:59], v[144:147]\n"
    "ds_read_b64_tr_b16 v[52:53], v115\n"
    "s_add_u32 m0, s65, 0xa400\n"
    "v_mfma_f32_16x16x32_bf16 v[208:211], a[116:119], v[56:59], v[208:211]\n"
    "buffer_load_dwordx4 v105, s[16:19], s63 offen lds\n"
    "ds_read_b64_tr_b16 v[54:55], v115 offset:512\n"
    "v_mfma_f32_16x16x32_bf16 v[176:179], a[120:123], v[56:59], v[176:179]\n"
    "ds_read_b128 a[140:143], v119 offset:6144\n"
    "s_add_u32 s63, s63, 0x40000\n"
    "v_mfma_f32_16x16x32_bf16 v[240:243], a[124:127], v[56:59], v[240:243]\n"
    "s_and_b32 s63, s63, 0x3fffff\n"
    "v_mfma_f32_16x16x32_bf16 v[148:151], a[112:115], v[60:63], v[148:151]\n"
    "s_cmp_lg_u32 s58, 0\n"
    "s_cbranch_scc1 Lnoln\n"
    "s_lshl_b32 s4, s57, 5\n"
    "s_add_u32 s4, s4, s56\n"
    "s_lshl_b32 s4, s4, 2\n"
    "s_add_u32 s4, s4, 0x1000\n"
    "v_mov_b32 v6, 0\n"
    "s_mov_b64 exec, 1\n"
    "buffer_store_dword v7, v6, s[8:11], s4 offen sc1\n"
    "s_mov_b64 exec, -1\n"
    "Lnoln:\n"
    "v_mfma_f32_16x16x32_bf16 v[212:215], a[116:119], v[60:63], v[212:215]\n"
    "ds_read_b64_tr_b16 v[56:57], v112 offset:1024\n"
    "ds_read_b64_tr_b16 v[58:59], v112 offset:1536\n"
    "v_mfma_f32_16x16x32_bf16 v[180:183], a[120:123], v[60:63], v[180:183]\n"
    "v_mfma_f32_16x16x32_bf16 v[244:247], a[124:127], v[60:63], v[244:247]\n"
    "v_mfma_f32_16x16x32_bf16 v[152:155], a[112:115], v[64:67], v[152:155]\n"
    "ds_read_b64_tr_b16 v[60:61], v113 offset:1024\n"
    "ds_read_b64_tr_b16 v[62:63], v113 offset:1536\n"
    "v_mfma_f32_16x16x32_bf16 v[216:219], a[116:119], v[64:67], v[216:219]\n"
    "v_mfma_f32_16x16x32_bf16 v[184:187], a[120:123], v[64:67], v[184:187]\n"
    "v_mfma_f32_16x16x32_bf16 v[248:251], a[124:127], v[64:67], v[248:251]\n"
    "v_mfma_f32_16x16x32_bf16 v[156:159], a[112:115], v[68:71], v[156:159]\n"
    "ds_read_b64_tr_b16 v[64:65], v114 offset:1024\n"
    "ds_read_b64_tr_b16 v[66:67], v114 offset:1536\n"
    "v_mfma_f32_16x16x32_bf16 v[220:223], a[116:119], v[68:71], v[220:223]\n"
    "v_mfma_f32_16x16x32_bf16 v[188:191], a[120:123], v[68:71], v[188:191]\n"
    "v_mfma_f32_16x16x32_bf16 v[252:255], a[124:127], v[68:71], v[252:255]\n"
    "ds_read_b64_tr_b16 v[68:69], v115 offset:1024\n"
    "ds_read_b64_tr_b16 v[70:71], v115 offset:1536\n"
    "s_waitcnt lgkmcnt(14)\n"
    "v_mfma_f32_16x16x32_bf16 v[128:131], a[128:131], v[40:43], v[128:131]\n"
    "s_add_u32 m0, s91, 0x18000\n"
    "s_waitcnt lgkmcnt(12)\n"
    "v_mfma_f32_16x16x32_bf16 v[192:195], a[132:135], v[40:43], v[192:195]\n"
    "buffer_load_dwordx4 v110, s[12:15], s64 offen lds\n"
    "s_waitcnt lgkmcnt(11)\n"
    "v_mfma_f32_16x16x32_bf16 v[160:163], a[136:139], v[40:43], v[160:163]\n"
    "s_waitcnt lgkmcnt(8)\n"
    "v_mfma_f32_16x16x32_bf16 v[224:227], a[140:143], v[40:43], v[224:227]\n"
    "s_add_u32 m0, s91, 0x18400\n"
    "v_mfma_f32_16x16x32_bf16 v[132:135], a[128:131], v[44:47], v[132:135]\n"
    "ds_read_b64_tr_b16 v[40:41], v112 offset:8192\n"
    "buffer_load_dwordx4 v111, s[12:15], s64 offen lds\n"
    "v_mfma_f32_16x16x32_bf16 v[196:199], a[132:135], v[44:47], v[196:199]\n"
    "ds_read_b64_tr_b16 v[42:43], v112 offset:8704\n"
    "v_mfma_f32_16x16x32_bf16 v[164:167], a[136:139], v[44:47], v[164:167]\n"
    "s_add_u32 m0, s91, 0x18800\n"
    "v_mfma_f32_16x16x32_bf16 v[228:231], a[140:143], v[44:47], v[228:231]\n"
    "v_mfma_f32_16x16x32_bf16 v[136:139], a[128:131], v[48:51], v[136:139]\n"
    "ds_read_b64_tr_b16 v[44:45], v113 offset:8192\n"
    "s_add_u32 s83, s64, 0x4000\n"
    "v_mfma_f32_16x16x32_bf16 v[200:203], a[132:135], v[48:51], v[200:203]\n"
    "buffer_load_dwordx4 v110, s[12:15], s83 offen lds\n"
    "ds_read_b64_tr_b16 v[46:47], v113 offset:8704\n"
    "v_mfma_f32_16x16x32_bf16 v[168:171], a[136:139], v[48:51], v[168:171]\n"
    "ds_read_b128 a[144:147], v120\n"
    "ds_read_b128 a[148:151], v120 offset:2048\n"
    "v_mfma_f32_16x16x32_bf16 v[232:235], a[140:143], v[48:51], v[232:235]\n"
    "ds_read_b128 a[152:155], v120 offset:4096\n"
    "s_add_u32 m0, s91, 0x18c00\n"
    "v_mfma_f32_16x16x32_bf16 v[140:143], a[128:131], v[52:55], v[140:143]\n"
    "ds_read_b64_tr_b16 v[48:49], v114 offset:8192\n"
    "buffer_load_dwordx4 v111, s[12:15], s83 offen lds\n"
    "v_mfma_f32_16x16x32_bf16 v[204:207], a[132:135], v[52:55], v[204:207]\n"
    "ds_read_b64_tr_b16 v[50:51], v114 offset:8704\n"
    "ds_read_b128 a[156:159], v120 offset:6144\n"
    "v_mfma_f32_16x16x32_bf16 v[172:175], a[136:139], v[52:55], v[172:175]\n"
    "s_add_u32 m0, s91, 0x19000\n"
    "v_mfma_f32_16x16x32_bf16 v[236:239], a[140:143], v[52:55], v[236:239]\n"
    "s_waitcnt lgkmcnt(15)\n"
    "v_mfma_f32_16x16x32_bf16 v[144:147], a[128:131], v[56:59], v[144:147]\n"
    "ds_read_b64_tr_b16 v[52:53], v115 offset:8192\n"
    "buffer_load_dwordx4 v125, s[12:15], s64 offen lds\n"
    "v_mfma_f32_16x16x32_bf16 v[208:211], a[132:135], v[56:59], v[208:211]\n"
    "ds_read_b64_tr_b16 v[54:55], v115 offset:8704\n"
    "s_add_u32 m0, s91, 0x19400\n"
    "v_mfma_f32_16x16x32_bf16 v[176:179], a[136:139], v[56:59], v[176:179]\n"
    "v_mfma_f32_16x16x32_bf16 v[240:243], a[140:143], v[56:59], v[240:243]\n"
    "buffer_load_dwordx4 v126, s[12:15], s64 offen lds\n"
    "s_waitcnt lgkmcnt(15)\n"
    "v_mfma_f32_16x16x32_bf16 v[148:151], a[128:131], v[60:63], v[148:151]\n"
    "ds_read_b64_tr_b16 v[56:57], v112 offset:9216\n"
    "s_add_u32 m0, s91, 0x19800\n"
    "v_mfma_f32_16x16x32_bf16 v[212:215], a[132:135], v[60:63], v[212:215]\n"
    "ds_read_b64_tr_b16 v[58:59], v112 offset:9728\n"
    "v_mfma_f32_16x16x32_bf16 v[180:183], a[136:139], v[60:63], v[180:183]\n"
    "s_add_u32 s83, s64, 0x4000\n"
    "v_mfma_f32_16x16x32_bf16 v[244:247], a[140:143], v[60:63], v[244:247]\n"
    "s_waitcnt lgkmcnt(15)\n"
    "v_mfma_f32_16x16x32_bf16 v[152:155], a[128:131], v[64:67], v[152:155]\n"
    "ds_read_b64_tr_b16 v[60:61], v113 offset:9216\n"
    "buffer_load_dwordx4 v125, s[12:15], s83 offen lds\n"
    "v_mfma_f32_16x16x32_bf16 v[216:219], a[132:135], v[64:67], v[216:219]\n"
    "ds_read_b64_tr_b16 v[62:63], v113 offset:9728\n"
    "s_add_u32 m0, s91, 0x19c00\n"
    "v_mfma_f32_16x16x32_bf16 v[184:187], a[136:139], v[64:67], v[184:187]\n"
    "v_mfma_f32_16x16x32_bf16 v[248:251], a[140:143], v[64:67], v[248:251]\n"
    "buffer_load_dwordx4 v126, s[12:15], s83 offen lds\n"
    "s_waitcnt lgkmcnt(15)\n"
    "v_mfma_f32_16x16x32_bf16 v[156:159], a[128:131], v[68:71], v[156:159]\n"
    "ds_read_b64_tr_b16 v[64:65], v114 offset:9216\n"
    "s_add_u32 s64, s64, 0x80\n"
    "v_mfma_f32_16x16x32_bf16 v[220:223], a[132:135], v[68:71], v[220:223]\n"
    "ds_read_b64_tr_b16 v[66:67], v114 offset:9728\n"
    "v_mfma_f32_16x16x32_bf16 v[188:191], a[136:139], v[68:71], v[188:191]\n"
    "s_and_b32 s64, s64, 0x3ff\n"
    "v_mfma_f32_16x16x32_bf16 v[252:255], a[140:143], v[68:71], v[252:255]\n"
    "s_waitcnt lgkmcnt(13)\n"
    "v_mfma_f32_16x16x32_bf16 v[128:131], a[144:147], v[40:43], v[128:131]\n"
    "ds_read_b64_tr_b16 v[68:69], v115 offset:9216\n"
    "s_add_u32 m0, s65, 0xc000\n"
    "s_waitcnt lgkmcnt(13)\n"
    "v_mfma_f32_16x16x32_bf16 v[192:195], a[148:151], v[40:43], v[192:195]\n"
    "ds_read_b64_tr_b16 v[70:71], v115 offset:9728\n"
    "s_waitcnt lgkmcnt(13)\n"
    "v_mfma_f32_16x16x32_bf16 v[160:163], a[152:155], v[40:43], v[160:163]\n"
    "buffer_load_dwordx4 v104, s[16:19], s63 offen lds\n"
    "s_waitcnt lgkmcnt(10)\n"
    "v_mfma_f32_16x16x32_bf16 v[224:227], a[156:159], v[40:43], v[224:227]\n"
    "v_mfma_f32_16x16x32_bf16 v[132:135], a[144:147], v[44:47], v[132:135]\n"
    "ds_read_b64_tr_b16 v[40:41], v112 offset:16384\n"
    "ds_read_b64_tr_b16 v[42:43], v112 offset:16896\n"
    "v_mfma_f32_16x16x32_bf16 v[196:199], a[148:151], v[44:47], v[196:199]\n"
    "s_add_u32 m0, s65, 0xc400\n"
    "v_mfma_f32_16x16x32_bf16 v[164:167], a[152:155], v[44:47], v[164:167]\n"
    "v_mfma_f32_16x16x32_bf16 v[228:231], a[156:159], v[44:47], v[228:231]\n"
    "v_mfma_f32_16x16x32_bf16 v[136:139], a[144:147], v[48:51], v[136:139]\n"
    "ds_read_b64_tr_b16 v[44:45], v113 offset:16384\n"
    "buffer_load_dwordx4 v105, s[16:19], s63 offen lds\n"
    "v_mfma_f32_16x16x32_bf16 v[200:203], a[148:151], v[48:51], v[200:203]\n"
    "ds_read_b64_tr_b16 v[46:47], v113 offset:16896\n"
    "v_mfma_f32_16x16x32_bf16 v[168:171], a[152:155], v[48:51], v[168:171]\n"
    "s_waitcnt vmcnt(14)\n"
    "s_add_u32 s63, s63, 0x40000\n"
    "v_mfma_f32_16x16x32_bf16 v[232:235], a[156:159], v[48:51], v[232:235]\n"
    "ds_read_b128 a[160:163], v108\n"
    "ds_read_b128 a[164:167], v108 offset:2048\n"
    "s_waitcnt lgkmcnt(14)\n"
    "v_mfma_f32_16x16x32_bf16 v[140:143], a[144:147], v[52:55], v[140:143]\n"
    "ds_read_b64_tr_b16 v[48:49], v114 offset:16384\n"
    "ds_read_b64_tr_b16 v[50:51], v114 offset:16896\n"
    "v_mfma_f32_16x16x32_bf16 v[204:207], a[148:151], v[52:55], v[204:207]\n"
    "ds_read_b128 a[168:171], v108 offset:4096\n"
    "s_and_b32 s63, s63, 0x3fffff\n"
    "v_mfma_f32_16x16x32_bf16 v[172:175], a[152:155], v[52:55], v[172:175]\n"
    "ds_read_b128 a[172:175], v108 offset:6144\n"
    "v_mfma_f32_16x16x32_bf16 v[236:239], a[156:159], v[52:55], v[236:239]\n"
    "s_waitcnt lgkmcnt(15)\n"
    "v_mfma_f32_16x16x32_bf16 v[144:147], a[144:147], v[56:59], v[144:147]\n"
    "ds_read_b64_tr_b16 v[52:53], v115 offset:16384\n"
    "s_add_u32 m0, s65, 0xe000\n"
    "v_mfma_f32_16x16x32_bf16 v[208:211], a[148:151], v[56:59], v[208:211]\n"
    "ds_read_b64_tr_b16 v[54:55], v115 offset:16896\n"
    "v_mfma_f32_16x16x32_bf16 v[176:179], a[152:155], v[56:59], v[176:179]\n"
    "buffer_load_dwordx4 v104, s[16:19], s63 offen lds\n"
    "v_mfma_f32_16x16x32_bf16 v[240:243], a[156:159], v[56:59], v[240:243]\n"
    "s_waitcnt lgkmcnt(15)\n"
    "v_mfma_f32_16x16x32_bf16 v[148:151], a[144:147], v[60:63], v[148:151]\n"
    "ds_read_b64_tr_b16 v[56:57], v112 offset:17408\n"
    "ds_read_b64_tr_b16 v[58:59], v112 offset:17920\n"
    "v_mfma_f32_16x16x32_bf16 v[212:215], a[148:151], v[60:63], v[212:215]\n"
    "s_add_u32 m0, s65, 0xe400\n"
    "v_mfma_f32_16x16x32_bf16 v[180:183], a[152:155], v[60:63], v[180:183]\n"
    "v_mfma_f32_16x16x32_bf16 v[244:247], a[156:159], v[60:63], v[244:247]\n"
    "s_waitcnt lgkmcnt(15)\n"
    "v_mfma_f32_16x16x32_bf16 v[152:155], a[144:147], v[64:67], v[152:155]\n"
    "ds_read_b64_tr_b16 v[60:61], v113 offset:17408\n"
    "buffer_load_dwordx4 v105, s[16:19], s63 offen lds\n"
    "v_mfma_f32_16x16x32_bf16 v[216:219], a[148:151], v[64:67], v[216:219]\n"
    "ds_read_b64_tr_b16 v[62:63], v113 offset:17920\n"
    "v_mfma_f32_16x16x32_bf16 v[184:187], a[152:155], v[64:67], v[184:187]\n"
    "s_add_u32 s63, s63, 0x40000\n"
    "v_mfma_f32_16x16x32_bf16 v[248:251], a[156:159], v[64:67], v[248:251]\n"
    "s_waitcnt lgkmcnt(15)\n"
    "v_mfma_f32_16x16x32_bf16 v[156:159], a[144:147], v[68:71], v[156:159]\n"
    "ds_read_b64_tr_b16 v[64:65], v114 offset:17408\n"
    "ds_read_b64_tr_b16 v[66:67], v114 offset:17920\n"
    "v_mfma_f32_16x16x32_bf16 v[220:223], a[148:151], v[68:71], v[220:223]\n"
    "s_and_b32 s63, s63, 0x3fffff\n"
    "v_mfma_f32_16x16x32_bf16 v[188:191], a[152:155], v[68:71], v[188:191]\n"
    "v_mfma_f32_16x16x32_bf16 v[252:255], a[156:159], v[68:71], v[252:255]\n"
    "s_waitcnt lgkmcnt(13)\n"
    "v_mfma_f32_16x16x32_bf16 v[128:131], a[160:163], v[40:43], v[128:131]\n"
    "ds_read_b64_tr_b16 v[68:69], v115 offset:17408\n"
    "s_add_u32 m0, s91, 0x20000\n"
    "s_waitcnt lgkmcnt(13)\n"
    "v_mfma_f32_16x16x32_bf16 v[192:195], a[164:167], v[40:43], v[192:195]\n"
    "ds_read_b64_tr_b16 v[70:71], v115 offset:17920\n"
    "buffer_load_dwordx4 v110, s[12:15], s64 offen lds\n"
    "s_waitcnt lgkmcnt(11)\n"
    "v_mfma_f32_16x16x32_bf16 v[160:163], a[168:171], v[40:43], v[160:163]\n"
    "s_waitcnt lgkmcnt(10)\n"
    "v_mfma_f32_16x16x32_bf16 v[224:227], a[172:175], v[40:43], v[224:227]\n"
    "s_add_u32 m0, s91, 0x20400\n"
    "v_mfma_f32_16x16x32_bf16 v[132:135], a[160:163], v[44:47], v[132:135]\n"
    "ds_read_b64_tr_b16 v[40:41], v112 offset:24576\n"
    "buffer_load_dwordx4 v111, s[12:15], s64 offen lds\n"
    "v_mfma_f32_16x16x32_bf16 v[196:199], a[164:167], v[44:47], v[196:199]\n"
    "ds_read_b64_tr_b16 v[42:43], v112 offset:25088\n"
    "v_mfma_f32_16x16x32_bf16 v[164:167], a[168:171], v[44:47], v[164:167]\n"
    "s_add_u32 m0, s91, 0x20800\n"
    "v_mfma_f32_16x16x32_bf16 v[228:231], a[172:175], v[44:47], v[228:231]\n"
    "v_mfma_f32_16x16x32_bf16 v[136:139], a[160:163], v[48:51], v[136:139]\n"
    "ds_read_b64_tr_b16 v[44:45], v113 offset:24576\n"
    "s_add_u32 s83, s64, 0x4000\n"
    "v_mfma_f32_16x16x32_bf16 v[200:203], a[164:167], v[48:51], v[200:203]\n"
    "buffer_load_dwordx4 v110, s[12:15], s83 offen lds\n"
    "ds_read_b64_tr_b16 v[46:47], v113 offset:25088\n"
    "v_mfma_f32_16x16x32_bf16 v[168:171], a[168:171], v[48:51], v[168:171]\n"
    "ds_read_b128 a[176:179], v109\n"
    "ds_read_b128 a[180:183], v109 offset:2048\n"
    "v_mfma_f32_16x16x32_bf16 v[232:235], a[172:175], v[48:51], v[232:235]\n"
    "ds_read_b128 a[184:187], v109 offset:4096\n"
    "s_add_u32 m0, s91, 0x20c00\n"
    "s_waitcnt lgkmcnt(15)\n"
    "v_mfma_f32_16x16x32_bf16 v[140:143], a[160:163], v[52:55], v[140:143]\n"
    "ds_read_b64_tr_b16 v[48:49], v114 offset:24576\n"
    "buffer_load_dwordx4 v111, s[12:15], s83 offen lds\n"
    "v_mfma_f32_16x16x32_bf16 v[204:207], a[164:167], v[52:55], v[204:207]\n"
    "ds_read_b64_tr_b16 v[50:51], v114 offset:25088\n"
    "ds_read_b128 a[188:191], v109 offset:6144\n"
    "v_mfma_f32_16x16x32_bf16 v[172:175], a[168:171], v[52:55], v[172:175]\n"
    "s_add_u32 m0, s91, 0x21000\n"
    "v_mfma_f32_16x16x32_bf16 v[236:239], a[172:175], v[52:55], v[236:239]\n"
    "s_waitcnt lgkmcnt(15)\n"
    "v_mfma_f32_16x16x32_bf16 v[144:147], a[160:163], v[56:59], v[144:147]\n"
    "ds_read_b64_tr_b16 v[52:53], v115 offset:24576\n"
    "buffer_load_dwordx4 v125, s[12:15], s64 offen lds\n"
    "v_mfma_f32_16x16x32_bf16 v[208:211], a[164:167], v[56:59], v[208:211]\n"
    "ds_read_b64_tr_b16 v[54:55], v115 offset:25088\n"
    "s_add_u32 m0, s91, 0x21400\n"
    "v_mfma_f32_16x16x32_bf16 v[176:179], a[168:171], v[56:59], v[176:179]\n"
    "v_mfma_f32_16x16x32_bf16 v[240:243], a[172:175], v[56:59], v[240:243]\n"
    "buffer_load_dwordx4 v126, s[12:15], s64 offen lds\n"
    "s_waitcnt lgkmcnt(15)\n"
    "v_mfma_f32_16x16x32_bf16 v[148:151], a[160:163], v[60:63], v[148:151]\n"
    "ds_read_b64_tr_b16 v[56:57], v112 offset:25600\n"
    "s_add_u32 m0, s91, 0x21800\n"
    "v_mfma_f32_16x16x32_bf16 v[212:215], a[164:167], v[60:63], v[212:215]\n"
    "ds_read_b64_tr_b16 v[58:59], v112 offset:26112\n"
    "v_mfma_f32_16x16x32_bf16 v[180:183], a[168:171], v[60:63], v[180:183]\n"
    "s_add_u32 s83, s64, 0x4000\n"
    "v_mfma_f32_16x16x32_bf16 v[244:247], a[172:175], v[60:63], v[244:247]\n"
    "s_waitcnt lgkmcnt(15)\n"
    "v_mfma_f32_16x16x32_bf16 v[152:155], a[160:163], v[64:67], v[152:155]\n"
    "ds_read_b64_tr_b16 v[60:61], v113 offset:25600\n"
    "buffer_load_dwordx4 v125, s[12:15], s83 offen lds\n"
    "v_mfma_f32_16x16x32_bf16 v[216:219], a[164:167], v[64:67], v[216:219]\n"
    "ds_read_b64_tr_b16 v[62:63], v113 offset:26112\n"
    "s_add_u32 m0, s91, 0x21c00\n"
    "v_mfma_f32_16x16x32_bf16 v[184:187], a[168:171], v[64:67], v[184:187]\n"
    "v_mfma_f32_16x16x32_bf16 v[248:251], a[172:175], v[64:67], v[248:251]\n"
    "buffer_load_dwordx4 v126, s[12:15], s83 offen lds\n"
    "s_waitcnt lgkmcnt(15)\n"
    "v_mfma_f32_16x16x32_bf16 v[156:159], a[160:163], v[68:71], v[156:159]\n"
    "ds_read_b64_tr_b16 v[64:65], v114 offset:25600\n"
    "s_add_u32 s64, s64, 0x80\n"
    "v_mfma_f32_16x16x32_bf16 v[220:223], a[164:167], v[68:71], v[220:223]\n"
    "ds_read_b64_tr_b16 v[66:67], v114 offset:26112\n"
    "v_mfma_f32_16x16x32_bf16 v[188:191], a[168:171], v[68:71], v[188:191]\n"
    "s_and_b32 s64, s64, 0x3ff\n"
    "v_mfma_f32_16x16x32_bf16 v[252:255], a[172:175], v[68:71], v[252:255]\n"
    "s_waitcnt lgkmcnt(13)\n"
    "v_mfma_f32_16x16x32_bf16 v[128:131], a[176:179], v[40:43], v[128:131]\n"
    "ds_read_b64_tr_b16 v[68:69], v115 offset:25600\n"
    "ds_read_b64_tr_b16 v[70:71], v115 offset:26112\n"
    "s_waitcnt lgkmcnt(14)\n"
    "v_mfma_f32_16x16x32_bf16 v[192:195], a[180:183], v[40:43], v[192:195]\n"
    "s_waitcnt lgkmcnt(13)\n"
    "v_mfma_f32_16x16x32_bf16 v[160:163], a[184:187], v[40:43], v[160:163]\n"
    "s_waitcnt lgkmcnt(10)\n"
    "v_mfma_f32_16x16x32_bf16 v[224:227], a[188:191], v[40:43], v[224:227]\n"
    "v_mfma_f32_16x16x32_bf16 v[132:135], a[176:179], v[44:47], v[132:135]\n"
    "v_mfma_f32_16x16x32_bf16 v[196:199], a[180:183], v[44:47], v[196:199]\n"
    "v_mfma_f32_16x16x32_bf16 v[164:167], a[184:187], v[44:47], v[164:167]\n"
    "s_waitcnt vmcnt(8)\n"
    "s_waitcnt lgkmcnt(0)\n"
    "s_barrier\n"
    "v_mfma_f32_16x16x32_bf16 v[228:231], a[188:191], v[44:47], v[228:231]\n"
    "ds_read_b64_tr_b16 v[40:41], v112 offset:32768\n"
    "s_add_u32 m0, s65, 0\n"
    "v_mfma_f32_16x16x32_bf16 v[136:139], a[176:179], v[48:51], v[136:139]\n"
    "ds_read_b64_tr_b16 v[44:45], v113 offset:32768\n"
    "buffer_load_dwordx4 v104, s[28:31], s63 offen lds\n"
    "v_mfma_f32_16x16x32_bf16 v[200:203], a[180:183], v[48:51], v[200:203]\n"
    "ds_read_b64_tr_b16 v[42:43], v112 offset:33280\n"
    "s_add_u32 m0, s65, 0x400\n"
    "v_mfma_f32_16x16x32_bf16 v[168:171], a[184:187], v[48:51], v[168:171]\n"
    "ds_read_b64_tr_b16 v[46:47], v113 offset:33280\n"
    "v_mfma_f32_16x16x32_bf16 v[232:235], a[188:191], v[48:51], v[232:235]\n"
    "buffer_load_dwordx4 v105, s[28:31], s63 offen lds\n"
    "s_add_u32 s63, s63, 0x40000\n"
    "v_mfma_f32_16x16x32_bf16 v[140:143], a[176:179], v[52:55], v[140:143]\n"
    "ds_read_b64_tr_b16 v[48:49], v114 offset:32768\n"
    "s_and_b32 s63, s63, 0x3fffff\n"
    "v_mfma_f32_16x16x32_bf16 v[204:207], a[180:183], v[52:55], v[204:207]\n"
    "ds_read_b128 a[192:195], v108 offset:32768\n"
    "s_add_u32 m0, s65, 0x2000\n"
    "v_mfma_f32_16x16x32_bf16 v[172:175], a[184:187], v[52:55], v[172:175]\n"
    "ds_read_b64_tr_b16 v[50:51], v114 offset:33280\n"
    "buffer_load_dwordx4 v104, s[28:31], s63 offen lds\n"
    "v_mfma_f32_16x16x32_bf16 v[236:239], a[188:191], v[52:55], v[236:239]\n"
    "ds_read_b128 a[196:199], v108 offset:34816\n"
    "s_add_u32 m0, s65, 0x2400\n"
    "v_mfma_f32_16x16x32_bf16 v[144:147], a[176:179], v[56:59], v[144:147]\n"
    "ds_read_b64_tr_b16 v[52:53], v115 offset:32768\n"
    "buffer_load_dwordx4 v105, s[28:31], s63 offen lds\n"
    "v_mfma_f32_16x16x32_bf16 v[208:211], a[180:183], v[56:59], v[208:211]\n"
    "ds_read_b128 a[200:203], v108 offset:36864\n"
    "s_add_u32 s63, s63, 0x40000\n"
    "v_mfma_f32_16x16x32_bf16 v[176:179], a[184:187], v[56:59], v[176:179]\n"
    "ds_read_b64_tr_b16 v[54:55], v115 offset:33280\n"
    "s_and_b32 s63, s63, 0x3fffff\n"
    "v_mfma_f32_16x16x32_bf16 v[240:243], a[188:191], v[56:59], v[240:243]\n"
    "ds_read_b128 a[204:207], v108 offset:38912\n"
    "s_add_u32 m0, s65, 0x4000\n"
    "v_mfma_f32_16x16x32_bf16 v[148:151], a[176:179], v[60:63], v[148:151]\n"
    "ds_read_b64_tr_b16 v[56:57], v112 offset:33792\n"
    "buffer_load_dwordx4 v104, s[28:31], s63 offen lds\n"
    "v_mfma_f32_16x16x32_bf16 v[212:215], a[180:183], v[60:63], v[212:215]\n"
    "ds_read_b64_tr_b16 v[58:59], v112 offset:34304\n"
    "s_add_u32 m0, s65, 0x4400\n"
    "v_mfma_f32_16x16x32_bf16 v[180:183], a[184:187], v[60:63], v[180:183]\n"
    "buffer_load_dwordx4 v105, s[28:31], s63 offen lds\n"
    "v_mfma_f32_16x16x32_bf16 v[244:247], a[188:191], v[60:63], v[244:247]\n"
    "s_add_u32 s63, s63, 0x40000\n"
    "v_mfma_f32_16x16x32_bf16 v[152:155], a[176:179], v[64:67], v[152:155]\n"
    "ds_read_b64_tr_b16 v[60:61], v113 offset:33792\n"
    "s_and_b32 s63, s63, 0x3fffff\n"
    "v_mfma_f32_16x16x32_bf16 v[216:219], a[180:183], v[64:67], v[216:219]\n"
    "ds_read_b64_tr_b16 v[62:63], v113 offset:34304\n"
    "s_add_u32 m0, s65, 0x6000\n"
    "v_mfma_f32_16x16x32_bf16 v[184:187], a[184:187], v[64:67], v[184:187]\n"
    "buffer_load_dwordx4 v104, s[28:31], s63 offen lds\n"
    "v_mfma_f32_16x16x32_bf16 v[248:251], a[188:191], v[64:67], v[248:251]\n"
    "s_add_u32 m0, s65, 0x6400\n"
    "v_mfma_f32_16x16x32_bf16 v[156:159], a[176:179], v[68:71], v[156:159]\n"
    "ds_read_b64_tr_b16 v[64:65], v114 offset:33792\n"
    "buffer_load_dwordx4 v105, s[28:31], s63 offen lds\n"
    "v_mfma_f32_16x16x32_bf16 v[220:223], a[180:183], v[68:71], v[220:223]\n"
    "ds_read_b64_tr_b16 v[66:67], v114 offset:34304\n"
    "s_add_u32 s63, s63, 0x40000\n"
    "v_mfma_f32_16x16x32_bf16 v[188:191], a[184:187], v[68:71], v[188:191]\n"
    "s_and_b32 s63, s63, 0x3fffff\n"
    "v_mfma_f32_16x16x32_bf16 v[252:255], a[188:191], v[68:71], v[252:255]\n"
    "ds_read_b64_tr_b16 v[68:69], v115 offset:33792\n"
    "ds_read_b64_tr_b16 v[70:71], v115 offset:34304\n"
    "s_waitcnt lgkmcnt(14)\n"
    "v_mfma_f32_16x16x32_bf16 v[128:131], a[192:195], v[40:43], v[128:131]\n"
    "s_or_b32 s4, s2, s58\n"
    "s_cmp_lg_u32 s4, 0\n"
    "s_cbranch_scc1 Lep1\n"
    "s_mov_b32 s61, 0x1000\n"
    "buffer_load_dwordx4 v[96:99], v2, s[8:11], s61 offen sc1\n"
    "Lep1:\n"
    "s_waitcnt lgkmcnt(12)\n"
    "v_mfma_f32_16x16x32_bf16 v[192:195], a[196:199], v[40:43], v[192:195]\n"
    "s_waitcnt lgkmcnt(10)\n"
    "v_mfma_f32_16x16x32_bf16 v[160:163], a[200:203], v[40:43], v[160:163]\n"
    "s_waitcnt lgkmcnt(8)\n"
    "v_mfma_f32_16x16x32_bf16 v[224:227], a[204:207], v[40:43], v[224:227]\n"
    "v_mfma_f32_16x16x32_bf16 v[132:135], a[192:195], v[44:47], v[132:135]\n"
    "ds_read_b64_tr_b16 v[40:41], v112 offset:40960\n"
    "ds_read_b64_tr_b16 v[42:43], v112 offset:41472\n"
    "v_mfma_f32_16x16x32_bf16 v[196:199], a[196:199], v[44:47], v[196:199]\n"
    "v_mfma_f32_16x16x32_bf16 v[164:167], a[200:203], v[44:47], v[164:167]\n"
    "v_mfma_f32_16x16x32_bf16 v[228:231], a[204:207], v[44:47], v[228:231]\n"
    "v_mfma_f32_16x16x32_bf16 v[136:139], a[192:195], v[48:51], v[136:139]\n"
    "ds_read_b64_tr_b16 v[44:45], v113 offset:40960\n"
    "ds_read_b64_tr_b16 v[46:47], v113 offset:41472\n"
    "v_mfma_f32_16x16x32_bf16 v[200:203], a[196:199], v[48:51], v[200:203]\n"
    "v_mfma_f32_16x16x32_bf16 v[168:171], a[200:203], v[48:51], v[168:171]\n"
    "ds_read_b128 a[208:211], v109 offset:32768\n"
    "s_lshl_b32 s4, s57, 1\n"
    "v_mfma_f32_16x16x32_bf16 v[232:235], a[204:207], v[48:51], v[232:235]\n"
    "ds_read_b128 a[212:215], v109 offset:34816\n"
    "ds_read_b128 a[216:219], v109 offset:36864\n"
    "v_mfma_f32_16x16x32_bf16 v[140:143], a[192:195], v[52:55], v[140:143]\n"
    "ds_read_b64_tr_b16 v[48:49], v114 offset:40960\n"
    "s_sub_u32 s4, 15, s4\n"
    "v_mfma_f32_16x16x32_bf16 v[204:207], a[196:199], v[52:55], v[204:207]\n"
    "ds_read_b64_tr_b16 v[50:51], v114 offset:41472\n"
    "ds_read_b128 a[220:223], v109 offset:38912\n"
    "v_mfma_f32_16x16x32_bf16 v[172:175], a[200:203], v[52:55], v[172:175]\n"
    "s_lshl_b32 s4, s4, 6\n"
    "v_mfma_f32_16x16x32_bf16 v[236:239], a[204:207], v[52:55], v[236:239]\n"
    "s_waitcnt lgkmcnt(15)\n"
    "v_mfma_f32_16x16x32_bf16 v[144:147], a[192:195], v[56:59], v[144:147]\n"
    "ds_read_b64_tr_b16 v[52:53], v115 offset:40960\n"
    "ds_read_b64_tr_b16 v[54:55], v115 offset:41472\n"
    "v_mfma_f32_16x16x32_bf16 v[208:211], a[196:199], v[56:59], v[208:211]\n"
    "v_bfe_u32 v3, v1, 4, 1\n"
    "v_mfma_f32_16x16x32_bf16 v[176:179], a[200:203], v[56:59], v[176:179]\n"
    "v_mfma_f32_16x16x32_bf16 v[240:243], a[204:207], v[56:59], v[240:243]\n"
    "v_mul_lo_u32 v3, s4, v3\n"
    "s_waitcnt lgkmcnt(15)\n"
    "v_mfma_f32_16x16x32_bf16 v[148:151], a[192:195], v[60:63], v[148:151]\n"
    "ds_read_b64_tr_b16 v[56:57], v112 offset:41984\n"
    "ds_read_b64_tr_b16 v[58:59], v112 offset:42496\n"
    "v_mfma_f32_16x16x32_bf16 v[212:215], a[196:199], v[60:63], v[212:215]\n"
    "v_and_b32 v4, 15, v1\n"
    "v_mfma_f32_16x16x32_bf16 v[180:183], a[200:203], v[60:63], v[180:183]\n"
    "v_mfma_f32_16x16x32_bf16 v[244:247], a[204:207], v[60:63], v[244:247]\n"
    "v_lshl_add_u32 v5, v4, 2, v3\n"
    "s_waitcnt lgkmcnt(15)\n"
    "v_mfma_f32_16x16x32_bf16 v[152:155], a[192:195], v[64:67], v[152:155]\n"
    "ds_read_b64_tr_b16 v[60:61], v113 offset:41984\n"
    "ds_read_b64_tr_b16 v[62:63], v113 offset:42496\n"
    "v_mfma_f32_16x16x32_bf16 v[216:219], a[196:199], v[64:67], v[216:219]\n"
    "s_lshl_b32 s7, s57, 6\n"
    "v_mfma_f32_16x16x32_bf16 v[184:187], a[200:203], v[64:67], v[184:187]\n"
    "v_mfma_f32_16x16x32_bf16 v[248:251], a[204:207], v[64:67], v[248:251]\n"
    "s_add_u32 s7, s7, 0x1000\n"
    "s_waitcnt lgkmcnt(15)\n"
    "v_mfma_f32_16x16x32_bf16 v[156:159], a[192:195], v[68:71], v[156:159]\n"
    "ds_read_b64_tr_b16 v[64:65], v114 offset:41984\n"
    "ds_read_b64_tr_b16 v[66:67], v114 offset:42496\n"
    "v_mfma_f32_16x16x32_bf16 v[220:223], a[196:199], v[68:71], v[220:223]\n"
    "buffer_load_dword v122, v5, s[8:11], s7 offen sc1\n"
    "v_mfma_f32_16x16x32_bf16 v[188:191], a[200:203], v[68:71], v[188:191]\n"
    "v_mfma_f32_16x16x32_bf16 v[252:255], a[204:207], v[68:71], v[252:255]\n"
    "s_waitcnt lgkmcnt(13)\n"
    "v_mfma_f32_16x16x32_bf16 v[128:131], a[208:211], v[40:43], v[128:131]\n"
    "ds_read_b64_tr_b16 v[68:69], v115 offset:41984\n"
    "s_lshl_b32 s66, s94, 5\n"
    "s_waitcnt lgkmcnt(13)\n"
    "v_mfma_f32_16x16x32_bf16 v[192:195], a[212:215], v[40:43], v[192:195]\n"
    "ds_read_b64_tr_b16 v[70:71], v115 offset:42496\n"
    "s_lshl_b32 s4, s95, 2\n"
    "s_waitcnt lgkmcnt(13)\n"
    "v_mfma_f32_16x16x32_bf16 v[160:163], a[216:219], v[40:43], v[160:163]\n"
    "s_add_u32 s66, s66, s4\n"
    "s_add_u32 s66, s66, s58\n"
    "s_waitcnt lgkmcnt(10)\n"
    "v_mfma_f32_16x16x32_bf16 v[224:227], a[220:223], v[40:43], v[224:227]\n"
    "s_lshl_b32 s66, s66, 15\n"
    "s_add_u32 s66, s66, 0x224000\n"
    "v_mfma_f32_16x16x32_bf16 v[132:135], a[208:211], v[44:47], v[132:135]\n"
    "ds_read_b64_tr_b16 v[40:41], v112 offset:49152\n"
    "ds_read_b64_tr_b16 v[42:43], v112 offset:49664\n"
    "v_mfma_f32_16x16x32_bf16 v[196:199], a[212:215], v[44:47], v[196:199]\n"
    "v_mfma_f32_16x16x32_bf16 v[164:167], a[216:219], v[44:47], v[164:167]\n"
    "v_mfma_f32_16x16x32_bf16 v[228:231], a[220:223], v[44:47], v[228:231]\n"
    "v_mfma_f32_16x16x32_bf16 v[136:139], a[208:211], v[48:51], v[136:139]\n"
    "ds_read_b64_tr_b16 v[44:45], v113 offset:49152\n"
    "ds_read_b64_tr_b16 v[46:47], v113 offset:49664\n"
    "v_mfma_f32_16x16x32_bf16 v[200:203], a[212:215], v[48:51], v[200:203]\n"
    "v_mfma_f32_16x16x32_bf16 v[168:171], a[216:219], v[48:51], v[168:171]\n"
    "s_waitcnt vmcnt(9)\n"
    "v_mfma_f32_16x16x32_bf16 v[232:235], a[220:223], v[48:51], v[232:235]\n"
    "ds_read_b128 a[224:227], v119\n"
    "ds_read_b128 a[228:231], v119 offset:2048\n"
    "s_waitcnt lgkmcnt(14)\n"
    "v_mfma_f32_16x16x32_bf16 v[140:143], a[208:211], v[52:55], v[140:143]\n"
    "ds_read_b64_tr_b16 v[48:49], v114 offset:49152\n"
    "ds_read_b64_tr_b16 v[50:51], v114 offset:49664\n"
    "v_mfma_f32_16x16x32_bf16 v[204:207], a[212:215], v[52:55], v[204:207]\n"
    "ds_read_b128 a[232:235], v119 offset:4096\n"
    "ds_read_b128 a[236:239], v119 offset:6144\n"
    "v_mfma_f32_16x16x32_bf16 v[172:175], a[216:219], v[52:55], v[172:175]\n"
    "v_mfma_f32_16x16x32_bf16 v[236:239], a[220:223], v[52:55], v[236:239]\n"
    "s_waitcnt lgkmcnt(15)\n"
    "v_mfma_f32_16x16x32_bf16 v[144:147], a[208:211], v[56:59], v[144:147]\n"
    "ds_read_b64_tr_b16 v[52:53], v115 offset:49152\n"
    "ds_read_b64_tr_b16 v[54:55], v115 offset:49664\n"
    "v_mfma_f32_16x16x32_bf16 v[208:211], a[212:215], v[56:59], v[208:211]\n"
    "v_mfma_f32_16x16x32_bf16 v[176:179], a[216:219], v[56:59], v[176:179]\n"
    "v_mfma_f32_16x16x32_bf16 v[240:243], a[220:223], v[56:59], v[240:243]\n"
    "s_waitcnt lgkmcnt(15)\n"
    "v_mfma_f32_16x16x32_bf16 v[148:151], a[208:211], v[60:63], v[148:151]\n"
    "ds_read_b64_tr_b16 v[56:57], v112 offset:50176\n"
    "ds_read_b64_tr_b16 v[58:59], v112 offset:50688\n"
    "v_mfma_f32_16x16x32_bf16 v[212:215], a[212:215], v[60:63], v[212:215]\n"
    "v_mfma_f32_16x16x32_bf16 v[180:183], a[216:219], v[60:63], v[180:183]\n"
    "v_mfma_f32_16x16x32_bf16 v[244:247], a[220:223], v[60:63], v[244:247]\n"
    "s_waitcnt lgkmcnt(15)\n"
    "v_mfma_f32_16x16x32_bf16 v[152:155], a[208:211], v[64:67], v[152:155]\n"
    "ds_read_b64_tr_b16 v[60:61], v113 offset:50176\n"
    "ds_read_b64_tr_b16 v[62:63], v113 offset:50688\n"
    "v_mfma_f32_16x16x32_bf16 v[216:219], a[212:215], v[64:67], v[216:219]\n"
    "v_mfma_f32_16x16x32_bf16 v[184:187], a[216:219], v[64:67], v[184:187]\n"
    "v_mfma_f32_16x16x32_bf16 v[248:251], a[220:223], v[64:67], v[248:251]\n"
    "s_waitcnt lgkmcnt(15)\n"
    "v_mfma_f32_16x16x32_bf16 v[156:159], a[208:211], v[68:71], v[156:159]\n"
    "ds_read_b64_tr_b16 v[64:65], v114 offset:50176\n"
    "ds_read_b64_tr_b16 v[66:67], v114 offset:50688\n"
    "v_mfma_f32_16x16x32_bf16 v[220:223], a[212:215], v[68:71], v[220:223]\n"
    "v_mfma_f32_16x16x32_bf16 v[188:191], a[216:219], v[68:71], v[188:191]\n"
    "v_mfma_f32_16x16x32_bf16 v[252:255], a[220:223], v[68:71], v[252:255]\n"
    "s_waitcnt lgkmcnt(13)\n"
    "v_mfma_f32_16x16x32_bf16 v[128:131], a[224:227], v[40:43], v[128:131]\n"
    "ds_read_b64_tr_b16 v[68:69], v115 offset:50176\n"
    "ds_read_b64_tr_b16 v[70:71], v115 offset:50688\n"
    "s_waitcnt lgkmcnt(14)\n"
    "v_mfma_f32_16x16x32_bf16 v[192:195], a[228:231], v[40:43], v[192:195]\n"
    "s_or_b32 s4, s2, s58\n"
    "s_cmp_lg_u32 s4, 0\n"
    "s_cbranch_scc1 Lep2\n"
    "s_mov_b32 s5, 0x1000\n"
    "s_waitcnt vmcnt(1)\n"
    "v_xor_b32 v3, v96, v7\n"
    "v_xor_b32 v4, v97, v7\n"
    "v_or_b32 v3, v3, v4\n"
    "v_xor_b32 v4, v98, v7\n"
    "v_or_b32 v3, v3, v4\n"
    "v_xor_b32 v4, v99, v7\n"
    "v_or_b32 v3, v3, v4\n"
    "v_cmp_ne_u32 vcc, 0, v3\n"
    "s_nop 0\n"
    "s_cbranch_vccnz Lspin0\n"
    "Lspinret0:\n"
    "s_mov_b32 s4, 0\n"
    "v_mov_b32 v6, 0\n"
    "s_mov_b64 exec, 1\n"
    "buffer_store_dword v7, v6, s[8:11], s4 offen sc1\n"
    "s_mov_b64 exec, -1\n"
    "Lep2:\n"
    "s_waitcnt lgkmcnt(11)\n"
    "v_mfma_f32_16x16x32_bf16 v[160:163], a[232:235], v[40:43], v[160:163]\n"
    "s_waitcnt lgkmcnt(10)\n"
    "v_mfma_f32_16x16x32_bf16 v[224:227], a[236:239], v[40:43], v[224:227]\n"
    "v_mfma_f32_16x16x32_bf16 v[132:135], a[224:227], v[44:47], v[132:135]\n"
    "ds_read_b64_tr_b16 v[40:41], v112 offset:57344\n"
    "ds_read_b64_tr_b16 v[42:43], v112 offset:57856\n"
    "v_mfma_f32_16x16x32_bf16 v[196:199], a[228:231], v[44:47], v[196:199]\n"
    "v_mfma_f32_16x16x32_bf16 v[164:167], a[232:235], v[44:47], v[164:167]\n"
    "v_mfma_f32_16x16x32_bf16 v[228:231], a[236:239], v[44:47], v[228:231]\n"
    "v_mfma_f32_16x16x32_bf16 v[136:139], a[224:227], v[48:51], v[136:139]\n"
    "ds_read_b64_tr_b16 v[44:45], v113 offset:57344\n"
    "ds_read_b64_tr_b16 v[46:47], v113 offset:57856\n"
    "v_mfma_f32_16x16x32_bf16 v[200:203], a[228:231], v[48:51], v[200:203]\n"
    "v_mfma_f32_16x16x32_bf16 v[168:171], a[232:235], v[48:51], v[168:171]\n"
    "ds_read_b128 a[240:243], v120\n"
    "ds_read_b128 a[244:247], v120 offset:2048\n"
    "v_mfma_f32_16x16x32_bf16 v[232:235], a[236:239], v[48:51], v[232:235]\n"
    "ds_read_b128 a[248:251], v120 offset:4096\n"
    "ds_read_b128 a[252:255], v120 offset:6144\n"
    "s_waitcnt lgkmcnt(15)\n"
    "v_mfma_f32_16x16x32_bf16 v[140:143], a[224:227], v[52:55], v[140:143]\n"
    "ds_read_b64_tr_b16 v[48:49], v114 offset:57344\n"
    "ds_read_b64_tr_b16 v[50:51], v114 offset:57856\n"
    "v_mfma_f32_16x16x32_bf16 v[204:207], a[228:231], v[52:55], v[204:207]\n"
    "v_mfma_f32_16x16x32_bf16 v[172:175], a[232:235], v[52:55], v[172:175]\n"
    "v_mfma_f32_16x16x32_bf16 v[236:239], a[236:239], v[52:55], v[236:239]\n"
    "s_waitcnt lgkmcnt(15)\n"
    "v_mfma_f32_16x16x32_bf16 v[144:147], a[224:227], v[56:59], v[144:147]\n"
    "ds_read_b64_tr_b16 v[52:53], v115 offset:57344\n"
    "s_mov_b64 s[16:17], s[24:25]\n"
    "v_mfma_f32_16x16x32_bf16 v[208:211], a[228:231], v[56:59], v[208:211]\n"
    "ds_read_b64_tr_b16 v[54:55], v115 offset:57856\n"
    "s_mov_b64 s[18:19], s[26:27]\n"
    "v_mfma_f32_16x16x32_bf16 v[176:179], a[232:235], v[56:59], v[176:179]\n"
    "s_lshl_b32 s64, s62, 6\n"
    "v_mfma_f32_16x16x32_bf16 v[240:243], a[236:239], v[56:59], v[240:243]\n"
    "s_lshl_b32 s62, s58, 1\n"
    "s_sub_u32 s62, 7, s62\n"
    "s_waitcnt lgkmcnt(15)\n"
    "v_mfma_f32_16x16x32_bf16 v[148:151], a[224:227], v[60:63], v[148:151]\n"
    "ds_read_b64_tr_b16 v[56:57], v112 offset:58368\n"
    "s_lshl_b32 s62, s62, 14\n"
    "v_mfma_f32_16x16x32_bf16 v[212:215], a[228:231], v[60:63], v[212:215]\n"
    "ds_read_b64_tr_b16 v[58:59], v112 offset:58880\n"
    "s_lshl_b32 s4, s57, 17\n"
    "v_mfma_f32_16x16x32_bf16 v[180:183], a[232:235], v[60:63], v[180:183]\n"
    "s_lshl_b32 s5, s58, 14\n"
    "v_mfma_f32_16x16x32_bf16 v[244:247], a[236:239], v[60:63], v[244:247]\n"
    "s_add_u32 s4, s4, s5\n"
    "s_waitcnt lgkmcnt(15)\n"
    "v_mfma_f32_16x16x32_bf16 v[152:155], a[224:227], v[64:67], v[152:155]\n"
    "ds_read_b64_tr_b16 v[60:61], v113 offset:58368\n"
    "s_add_u32 s4, s4, 0x20000\n"
    "v_mfma_f32_16x16x32_bf16 v[216:219], a[228:231], v[64:67], v[216:219]\n"
    "v_add_u32 v110, s4, v118\n"
    "v_xor_b32 v111, 64, v110\n"
    "v_mfma_f32_16x16x32_bf16 v[184:187], a[232:235], v[64:67], v[184:187]\n"
    "ds_read_b64_tr_b16 v[62:63], v113 offset:58880\n"
    "v_add_u32 v111, 0x2000, v111\n"
    "v_mfma_f32_16x16x32_bf16 v[248:251], a[236:239], v[64:67], v[248:251]\n"
    "s_lshl_b32 s4, s57, 1\n"
    "s_waitcnt lgkmcnt(15)\n"
    "v_mfma_f32_16x16x32_bf16 v[156:159], a[224:227], v[68:71], v[156:159]\n"
    "ds_read_b64_tr_b16 v[64:65], v114 offset:58368\n"
    "s_sub_u32 s4, 15, s4\n"
    "v_mfma_f32_16x16x32_bf16 v[220:223], a[228:231], v[68:71], v[220:223]\n"
    "ds_read_b64_tr_b16 v[66:67], v114 offset:58880\n"
    "s_lshl_b32 s4, s4, 17\n"
    "v_mfma_f32_16x16x32_bf16 v[188:191], a[232:235], v[68:71], v[188:191]\n"
    "v_add_u32 v125, s4, v110\n"
    "v_mfma_f32_16x16x32_bf16 v[252:255], a[236:239], v[68:71], v[252:255]\n"
    "v_add_u32 v126, s4, v111\n"
    "s_waitcnt lgkmcnt(13)\n"
    "v_mfma_f32_16x16x32_bf16 v[128:131], a[240:243], v[40:43], v[128:131]\n"
    "ds_read_b64_tr_b16 v[68:69], v115 offset:58368\n"
    "ds_read_b64_tr_b16 v[70:71], v115 offset:58880\n"
    "s_waitcnt lgkmcnt(14)\n"
    "v_mfma_f32_16x16x32_bf16 v[192:195], a[244:247], v[40:43], v[192:195]\n"
    "s_waitcnt lgkmcnt(13)\n"
    "v_mfma_f32_16x16x32_bf16 v[160:163], a[248:251], v[40:43], v[160:163]\n"
    "s_waitcnt lgkmcnt(12)\n"
    "v_mfma_f32_16x16x32_bf16 v[224:227], a[252:255], v[40:43], v[224:227]\n"
    "v_mfma_f32_16x16x32_bf16 v[132:135], a[240:243], v[44:47], v[132:135]\n"
    "v_mfma_f32_16x16x32_bf16 v[196:199], a[244:247], v[44:47], v[196:199]\n"
    "v_mfma_f32_16x16x32_bf16 v[164:167], a[248:251], v[44:47], v[164:167]\n"
    "s_waitcnt vmcnt(1)\n"
    "s_waitcnt lgkmcnt(0)\n"
    "s_barrier\n"
    "v_mfma_f32_16x16x32_bf16 v[228:231], a[252:255], v[44:47], v[228:231]\n"
    "ds_read_b64_tr_b16 v[40:41], v112\n"
    "s_add_u32 m0, s65, 0x8000\n"
    "v_mfma_f32_16x16x32_bf16 v[136:139], a[240:243], v[48:51], v[136:139]\n"
    "ds_read_b64_tr_b16 v[44:45], v113\n"
    "buffer_load_dwordx4 v104, s[28:31], s63 offen lds\n"
    "v_mfma_f32_16x16x32_bf16 v[200:203], a[244:247], v[48:51], v[200:203]\n"
    "ds_read_b64_tr_b16 v[42:43], v112 offset:512\n"
    "v_cvt_pk_bf16_f32 v128, v128, v129\n"
    "v_mfma_f32_16x16x32_bf16 v[168:171], a[248:251], v[48:51], v[168:171]\n"
    "ds_read_b64_tr_b16 v[46:47], v113 offset:512\n"
    "v_cvt_pk_bf16_f32 v129, v130, v131\n"
    "v_mfma_f32_16x16x32_bf16 v[232:235], a[252:255], v[48:51], v[232:235]\n"
    "v_cvt_pk_bf16_f32 v130, v192, v193\n"
    "v_cvt_pk_bf16_f32 v131, v194, v195\n"
    "v_mfma_f32_16x16x32_bf16 v[140:143], a[240:243], v[52:55], v[140:143]\n"
    "ds_read_b64_tr_b16 v[48:49], v114\n"
    "v_cvt_pk_bf16_f32 v160, v160, v161\n"
    "v_mfma_f32_16x16x32_bf16 v[204:207], a[244:247], v[52:55], v[204:207]\n"
    "v_cvt_pk_bf16_f32 v161, v162, v163\n"
    "s_add_u32 m0, s65, 0x8400\n"
    "v_mfma_f32_16x16x32_bf16 v[172:175], a[248:251], v[52:55], v[172:175]\n"
    "v_cvt_pk_bf16_f32 v132, v132, v133\n"
    "v_cvt_pk_bf16_f32 v162, v224, v225\n"
    "v_cvt_pk_bf16_f32 v163, v226, v227\n"
    "v_mfma_f32_16x16x32_bf16 v[236:239], a[252:255], v[52:55], v[236:239]\n"
    "ds_read_b64_tr_b16 v[50:51], v114 offset:512\n"
    "v_cvt_pk_bf16_f32 v133, v134, v135\n"
    "v_mfma_f32_16x16x32_bf16 v[144:147], a[240:243], v[56:59], v[144:147]\n"
    "v_cvt_pk_bf16_f32 v134, v196, v197\n"
    "v_cvt_pk_bf16_f32 v135, v198, v199\n"
    "v_mfma_f32_16x16x32_bf16 v[208:211], a[244:247], v[56:59], v[208:211]\n"
    "ds_read_b64_tr_b16 v[52:53], v115\n"
    "v_cvt_pk_bf16_f32 v164, v164, v165\n"
    "v_mfma_f32_16x16x32_bf16 v[176:179], a[248:251], v[56:59], v[176:179]\n"
    "buffer_load_dwordx4 v105, s[28:31], s63 offen lds\n"
    "v_cvt_pk_bf16_f32 v165, v166, v167\n"
    "v_cvt_pk_bf16_f32 v166, v228, v229\n"
    "v_cvt_pk_bf16_f32 v167, v230, v231\n"
    "v_mfma_f32_16x16x32_bf16 v[240:243], a[252:255], v[56:59], v[240:243]\n"
    "v_cvt_pk_bf16_f32 v136, v136, v137\n"
    "v_cvt_pk_bf16_f32 v137, v138, v139\n"
    "v_mfma_f32_16x16x32_bf16 v[148:151], a[240:243], v[60:63], v[148:151]\n"
    "ds_read_b64_tr_b16 v[54:55], v115 offset:512\n"
    "v_cvt_pk_bf16_f32 v138, v200, v201\n"
    "v_mfma_f32_16x16x32_bf16 v[212:215], a[244:247], v[60:63], v[212:215]\n"
    "v_cvt_pk_bf16_f32 v139, v202, v203\n"
    "s_add_u32 s63, s63, 0x40000\n"
    "v_mfma_f32_16x16x32_bf16 v[180:183], a[248:251], v[60:63], v[180:183]\n"
    "ds_read_b64_tr_b16 v[56:57], v112 offset:1024\n"
    "v_cvt_pk_bf16_f32 v168, v168, v169\n"
    "v_cvt_pk_bf16_f32 v169, v170, v171\n"
    "v_cvt_pk_bf16_f32 v170, v232, v233\n"
    "v_cvt_pk_bf16_f32 v171, v234, v235\n"
    "v_mfma_f32_16x16x32_bf16 v[244:247], a[252:255], v[60:63], v[244:247]\n"
    "v_cvt_pk_bf16_f32 v140, v140, v141\n"
    "s_and_b32 s63, s63, 0x3fffff\n"
    "v_mfma_f32_16x16x32_bf16 v[152:155], a[240:243], v[64:67], v[152:155]\n"
    "ds_read_b64_tr_b16 v[58:59], v112 offset:1536\n"
    "v_cvt_pk_bf16_f32 v141, v142, v143\n"
    "v_mfma_f32_16x16x32_bf16 v[216:219], a[244:247], v[64:67], v[216:219]\n"
    "v_cvt_pk_bf16_f32 v142, v204, v205\n"
    "s_add_u32 m0, s65, 0xa000\n"
    "v_mfma_f32_16x16x32_bf16 v[184:187], a[248:251], v[64:67], v[184:187]\n"
    "ds_read_b64_tr_b16 v[60:61], v113 offset:1024\n"
    "v_cvt_pk_bf16_f32 v143, v206, v207\n"
    "v_cvt_pk_bf16_f32 v172, v172, v173\n"
    "v_cvt_pk_bf16_f32 v173, v174, v175\n"
    "v_cvt_pk_bf16_f32 v174, v236, v237\n"
    "v_cvt_pk_bf16_f32 v175, v238, v239\n"
    "v_mfma_f32_16x16x32_bf16 v[248:251], a[252:255], v[64:67], v[248:251]\n"
    "buffer_load_dwordx4 v104, s[28:31], s63 offen lds\n"
    "s_add_u32 m0, s65, 0xa400\n"
    "v_mfma_f32_16x16x32_bf16 v[156:159], a[240:243], v[68:71], v[156:159]\n"
    "v_cvt_pk_bf16_f32 v144, v144, v145\n"
    "v_cvt_pk_bf16_f32 v145, v146, v147\n"
    "ds_read_b64_tr_b16 v[62:63], v113 offset:1536\n"
    "v_mfma_f32_16x16x32_bf16 v[220:223], a[244:247], v[68:71], v[220:223]\n"
    "buffer_load_dwordx4 v105, s[28:31], s63 offen lds\n"
    "v_cvt_pk_bf16_f32 v146, v208, v209\n"
    "v_mfma_f32_16x16x32_bf16 v[188:191], a[248:251], v[68:71], v[188:191]\n"
    "ds_read_b64_tr_b16 v[64:65], v114 offset:1024\n"
    "v_cvt_pk_bf16_f32 v147, v210, v211\n"
    "v_cvt_pk_bf16_f32 v176, v176, v177\n"
    "v_cvt_pk_bf16_f32 v177, v178, v179\n"
    "v_cvt_pk_bf16_f32 v178, v240, v241\n"
    "v_cvt_pk_bf16_f32 v179, v242, v243\n"
    "v_mfma_f32_16x16x32_bf16 v[252:255], a[252:255], v[68:71], v[252:255]\n"
    "ds_read_b64_tr_b16 v[68:69], v115 offset:1024\n"
    "s_add_u32 s63, s63, 0x40000\n"
    "ds_read_b64_tr_b16 v[66:67], v114 offset:1536\n"
    "ds_read_b64_tr_b16 v[70:71], v115 offset:1536\n"
    "s_and_b32 s63, s63, 0x3fffff\n"
    "s_add_u32 m0, s65, 0xc000\n"
    "s_nop 0\n"
    "buffer_load_dwordx4 v104, s[28:31], s63 offen lds\n"
    "s_add_u32 m0, s65, 0xc400\n"
    "s_nop 0\n"
    "buffer_load_dwordx4 v105, s[28:31], s63 offen lds\n"
    "s_add_u32 s63, s63, 0x40000\n"
    "s_and_b32 s63, s63, 0x3fffff\n"
    "s_add_u32 m0, s65, 0xe000\n"
    "s_nop 0\n"
    "buffer_load_dwordx4 v104, s[28:31], s63 offen lds\n"
    "s_add_u32 m0, s65, 0xe400\n"
    "s_nop 0\n"
    "buffer_load_dwordx4 v105, s[28:31], s63 offen lds\n"
    "s_add_u32 s63, s63, 0x40000\n"
    "s_and_b32 s63, s63, 0x3fffff\n"
    "s_waitcnt lgkmcnt(13)\n"
    "v_mfma_f32_16x16x32_bf16 v[192:195], v[40:43], a[0:3], 0\n"
    "buffer_load_dword v124, v106, s[24:27], s74 offen\n"
    "v_mfma_f32_16x16x32_bf16 v[208:211], v[40:43], a[4:7], 0\n"
    "v_cvt_pk_bf16_f32 v148, v148, v149\n"
    "v_cvt_pk_bf16_f32 v149, v150, v151\n"
    "v_mfma_f32_16x16x32_bf16 v[224:227], v[40:43], a[8:11], 0\n"
    "v_cvt_pk_bf16_f32 v150, v212, v213\n"
    "v_cvt_pk_bf16_f32 v151, v214, v215\n"
    "v_mfma_f32_16x16x32_bf16 v[240:243], v[40:43], a[12:15], 0\n"
    "buffer_load_dword v124, v107, s[24:27], s74 offen\n"
    "v_cvt_pk_bf16_f32 v180, v180, v181\n"
    "s_waitcnt lgkmcnt(12)\n"
    "v_mfma_f32_16x16x32_bf16 v[8:11], v[44:47], a[0:3], 0\n"
    "ds_read_b64_tr_b16 v[40:41], v112 offset:8192\n"
    "v_cvt_pk_bf16_f32 v181, v182, v183\n"
    "v_mfma_f32_16x16x32_bf16 v[24:27], v[44:47], a[4:7], 0\n"
    "v_cvt_pk_bf16_f32 v152, v152, v153\n"
    "v_cvt_pk_bf16_f32 v182, v244, v245\n"
    "v_mfma_f32_16x16x32_bf16 v[72:75], v[44:47], a[8:11], 0\n"
    "ds_read_b64_tr_b16 v[42:43], v112 offset:8704\n"
    "v_cvt_pk_bf16_f32 v183, v246, v247\n"
    "v_mfma_f32_16x16x32_bf16 v[88:91], v[44:47], a[12:15], 0\n"
    "v_cvt_pk_bf16_f32 v153, v154, v155\n"
    "v_cvt_pk_bf16_f32 v154, v216, v217\n"
    "s_waitcnt lgkmcnt(12)\n"
    "v_mfma_f32_16x16x32_bf16 v[196:199], v[48:51], a[0:3], 0\n"
    "ds_read_b64_tr_b16 v[44:45], v113 offset:8192\n"
    "buffer_load_dword v124, v121, s[24:27], s74 offen\n"
    "v_mfma_f32_16x16x32_bf16 v[212:215], v[48:51], a[4:7], 0\n"
    "v_cvt_pk_bf16_f32 v155, v218, v219\n"
    "v_cvt_pk_bf16_f32 v156, v156, v157\n"
    "v_mfma_f32_16x16x32_bf16 v[228:231], v[48:51], a[8:11], 0\n"
    "ds_read_b64_tr_b16 v[46:47], v113 offset:8704\n"
    "v_cvt_pk_bf16_f32 v184, v184, v185\n"
    "v_mfma_f32_16x16x32_bf16 v[244:247], v[48:51], a[12:15], 0\n"
    "v_cvt_pk_bf16_f32 v185, v186, v187\n"
    "v_cvt_pk_bf16_f32 v157, v158, v159\n"
    "s_waitcnt lgkmcnt(12)\n"
    "v_mfma_f32_16x16x32_bf16 v[12:15], v[52:55], a[0:3], 0\n"
    "ds_read_b64_tr_b16 v[48:49], v114 offset:8192\n"
    "v_cvt_pk_bf16_f32 v186, v248, v249\n"
    "v_mfma_f32_16x16x32_bf16 v[28:31], v[52:55], a[4:7], 0\n"
    "buffer_load_dword v124, v123, s[24:27], s74 offen\n"
    "v_cvt_pk_bf16_f32 v158, v220, v221\n"
    "v_cvt_pk_bf16_f32 v187, v250, v251\n"
    "v_mfma_f32_16x16x32_bf16 v[76:79], v[52:55], a[8:11], 0\n"
    "ds_read_b64_tr_b16 v[50:51], v114 offset:8704\n"
    "v_cvt_pk_bf16_f32 v159, v222, v223\n"
    "v_mfma_f32_16x16x32_bf16 v[92:95], v[52:55], a[12:15], 0\n"
    "s_add_u32 s74, s74, s69\n"
    "v_cvt_pk_bf16_f32 v188, v188, v189\n"
    "s_waitcnt lgkmcnt(12)\n"
    "v_mfma_f32_16x16x32_bf16 v[200:203], v[56:59], a[0:3], 0\n"
    "ds_read_b64_tr_b16 v[52:53], v115 offset:8192\n"
    "v_cvt_pk_bf16_f32 v189, v190, v191\n"
    "v_mfma_f32_16x16x32_bf16 v[216:219], v[56:59], a[4:7], 0\n"
    "buffer_load_dword v124, v106, s[24:27], s74 offen\n"
    "v_cvt_pk_bf16_f32 v190, v252, v253\n"
    "v_mfma_f32_16x16x32_bf16 v[232:235], v[56:59], a[8:11], 0\n"
    "ds_read_b64_tr_b16 v[54:55], v115 offset:8704\n"
    "v_cvt_pk_bf16_f32 v191, v254, v255\n"
    "v_mfma_f32_16x16x32_bf16 v[248:251], v[56:59], a[12:15], 0\n"
    "buffer_load_dword v124, v107, s[24:27], s74 offen\n"
    "s_waitcnt lgkmcnt(12)\n"
    "v_mfma_f32_16x16x32_bf16 v[16:19], v[60:63], a[0:3], 0\n"
    "ds_read_b64_tr_b16 v[56:57], v112 offset:9216\n"
    "ds_read_b64_tr_b16 v[58:59], v112 offset:9728\n"
    "v_mfma_f32_16x16x32_bf16 v[32:35], v[60:63], a[4:7], 0\n"
    "v_mfma_f32_16x16x32_bf16 v[80:83], v[60:63], a[8:11], 0\n"
    "buffer_load_dword v124, v121, s[24:27], s74 offen\n"
    "v_mfma_f32_16x16x32_bf16 v[96:99], v[60:63], a[12:15], 0\n"
    "s_waitcnt lgkmcnt(11)\n"
    "v_mfma_f32_16x16x32_bf16 v[204:207], v[64:67], a[0:3], 0\n"
    "ds_read_b64_tr_b16 v[60:61], v113 offset:9216\n"
    "ds_read_b64_tr_b16 v[62:63], v113 offset:9728\n"
    "v_mfma_f32_16x16x32_bf16 v[220:223], v[64:67], a[4:7], 0\n"
    "buffer_load_dword v124, v123, s[24:27], s74 offen\n"
    "v_mfma_f32_16x16x32_bf16 v[236:239], v[64:67], a[8:11], 0\n"
    "v_mfma_f32_16x16x32_bf16 v[252:255], v[64:67], a[12:15], 0\n"
    "s_waitcnt lgkmcnt(12)\n"
    "v_mfma_f32_16x16x32_bf16 v[20:23], v[68:71], a[0:3], 0\n"
    "ds_read_b64_tr_b16 v[64:65], v114 offset:9216\n"
    "s_add_u32 s74, s74, s69\n"
    "v_mfma_f32_16x16x32_bf16 v[36:39], v[68:71], a[4:7], 0\n"
    "ds_read_b64_tr_b16 v[66:67], v114 offset:9728\n"
    "v_mfma_f32_16x16x32_bf16 v[84:87], v[68:71], a[8:11], 0\n"
    "v_mfma_f32_16x16x32_bf16 v[100:103], v[68:71], a[12:15], 0\n"
    "s_waitcnt lgkmcnt(12)\n"
    "v_mfma_f32_16x16x32_bf16 v[192:195], v[40:43], a[16:19], v[192:195]\n"
    "ds_read_b64_tr_b16 v[68:69], v115 offset:9216\n"
    "ds_read_b64_tr_b16 v[70:71], v115 offset:9728\n"
    "v_mfma_f32_16x16x32_bf16 v[208:211], v[40:43], a[20:23], v[208:211]\n"
    "v_mfma_f32_16x16x32_bf16 v[224:227], v[40:43], a[24:27], v[224:227]\n"
    "v_mfma_f32_16x16x32_bf16 v[240:243], v[40:43], a[28:31], v[240:243]\n"
    "s_waitcnt lgkmcnt(12)\n"
    "v_mfma_f32_16x16x32_bf16 v[8:11], v[44:47], a[16:19], v[8:11]\n"
    "ds_read_b64_tr_b16 v[40:41], v112 offset:16384\n"
    "ds_read_b64_tr_b16 v[42:43], v112 offset:16896\n"
    "v_mfma_f32_16x16x32_bf16 v[24:27], v[44:47], a[20:23], v[24:27]\n"
    "v_mfma_f32_16x16x32_bf16 v[72:75], v[44:47], a[24:27], v[72:75]\n"
    "v_mfma_f32_16x16x32_bf16 v[88:91], v[44:47], a[28:31], v[88:91]\n"
    "s_waitcnt lgkmcnt(12)\n"
    "v_mfma_f32_16x16x32_bf16 v[196:199], v[48:51], a[16:19], v[196:199]\n"
    "ds_read_b64_tr_b16 v[44:45], v113 offset:16384\n"
    "ds_read_b64_tr_b16 v[46:47], v113 offset:16896\n"
    "v_mfma_f32_16x16x32_bf16 v[212:215], v[48:51], a[20:23], v[212:215]\n"
    "v_mfma_f32_16x16x32_bf16 v[228:231], v[48:51], a[24:27], v[228:231]\n"
    "v_mfma_f32_16x16x32_bf16 v[244:247], v[48:51], a[28:31], v[244:247]\n"
    "s_waitcnt lgkmcnt(12)\n"
    "v_mfma_f32_16x16x32_bf16 v[12:15], v[52:55], a[16:19], v[12:15]\n"
    "ds_read_b64_tr_b16 v[48:49], v114 offset:16384\n"
    "ds_read_b64_tr_b16 v[50:51], v114 offset:16896\n"
    "v_mfma_f32_16x16x32_bf16 v[28:31], v[52:55], a[20:23], v[28:31]\n"
    "v_mfma_f32_16x16x32_bf16 v[76:79], v[52:55], a[24:27], v[76:79]\n"
    "v_mfma_f32_16x16x32_bf16 v[92:95], v[52:55], a[28:31], v[92:95]\n"
    "s_waitcnt lgkmcnt(12)\n"
    "v_mfma_f32_16x16x32_bf16 v[200:203], v[56:59], a[16:19], v[200:203]\n"
    "ds_read_b64_tr_b16 v[52:53], v115 offset:16384\n"
    "ds_read_b64_tr_b16 v[54:55], v115 offset:16896\n"
    "v_mfma_f32_16x16x32_bf16 v[216:219], v[56:59], a[20:23], v[216:219]\n"
    "v_mfma_f32_16x16x32_bf16 v[232:235], v[56:59], a[24:27], v[232:235]\n"
    "v_mfma_f32_16x16x32_bf16 v[248:251], v[56:59], a[28:31], v[248:251]\n"
    "s_waitcnt lgkmcnt(12)\n"
    "v_mfma_f32_16x16x32_bf16 v[16:19], v[60:63], a[16:19], v[16:19]\n"
    "ds_read_b64_tr_b16 v[56:57], v112 offset:17408\n"
    "ds_read_b64_tr_b16 v[58:59], v112 offset:17920\n"
    "v_mfma_f32_16x16x32_bf16 v[32:35], v[60:63], a[20:23], v[32:35]\n"
    "v_mfma_f32_16x16x32_bf16 v[80:83], v[60:63], a[24:27], v[80:83]\n"
    "v_mfma_f32_16x16x32_bf16 v[96:99], v[60:63], a[28:31], v[96:99]\n"
    "s_waitcnt lgkmcnt(12)\n"
    "v_mfma_f32_16x16x32_bf16 v[204:207], v[64:67], a[16:19], v[204:207]\n"
    "ds_read_b64_tr_b16 v[60:61], v113 offset:17408\n"
    "ds_read_b64_tr_b16 v[62:63], v113 offset:17920\n"
    "v_mfma_f32_16x16x32_bf16 v[220:223], v[64:67], a[20:23], v[220:223]\n"
    "v_mfma_f32_16x16x32_bf16 v[236:239], v[64:67], a[24:27], v[236:239]\n"
    "v_mfma_f32_16x16x32_bf16 v[252:255], v[64:67], a[28:31], v[252:255]\n"
    "s_waitcnt lgkmcnt(12)\n"
    "v_mfma_f32_16x16x32_bf16 v[20:23], v[68:71], a[16:19], v[20:23]\n"
    "ds_read_b64_tr_b16 v[64:65], v114 offset:17408\n"
    "ds_read_b64_tr_b16 v[66:67], v114 offset:17920\n"
    "v_mfma_f32_16x16x32_bf16 v[36:39], v[68:71], a[20:23], v[36:39]\n"
    "v_mfma_f32_16x16x32_bf16 v[84:87], v[68:71], a[24:27], v[84:87]\n"
    "v_mfma_f32_16x16x32_bf16 v[100:103], v[68:71], a[28:31], v[100:103]\n"
    "s_waitcnt lgkmcnt(12)\n"
    "v_mfma_f32_16x16x32_bf16 v[192:195], v[40:43], a[32:35], v[192:195]\n"
    "ds_read_b64_tr_b16 v[68:69], v115 offset:17408\n"
    "ds_read_b64_tr_b16 v[70:71], v115 offset:17920\n"
    "v_mfma_f32_16x16x32_bf16 v[208:211], v[40:43], a[36:39], v[208:211]\n"
    "s_waitcnt vmcnt(16)\n"
    "v_cmp_ne_u32 vcc, v122, v7\n"
    "s_nop 0\n"
    "s_cbranch_vccnz Lspin1\n"
    "Lspinret1:\n"
    "v_mfma_f32_16x16x32_bf16 v[224:227], v[40:43], a[40:43], v[224:227]\n"
    "v_mfma_f32_16x16x32_bf16 v[240:243], v[40:43], a[44:47], v[240:243]\n"
    "s_waitcnt lgkmcnt(12)\n"
    "v_mfma_f32_16x16x32_bf16 v[8:11], v[44:47], a[32:35], v[8:11]\n"
    "ds_read_b64_tr_b16 v[40:41], v112 offset:24576\n"
    "ds_read_b64_tr_b16 v[42:43], v112 offset:25088\n"
    "v_mfma_f32_16x16x32_bf16 v[24:27], v[44:47], a[36:39], v[24:27]\n"
    "v_mfma_f32_16x16x32_bf16 v[72:75], v[44:47], a[40:43], v[72:75]\n"
    "v_mfma_f32_16x16x32_bf16 v[88:91], v[44:47], a[44:47], v[88:91]\n"
    "s_waitcnt lgkmcnt(12)\n"
    "v_mfma_f32_16x16x32_bf16 v[196:199], v[48:51], a[32:35], v[196:199]\n"
    "ds_read_b64_tr_b16 v[44:45], v113 offset:24576\n"
    "ds_read_b64_tr_b16 v[46:47], v113 offset:25088\n"
    "v_mfma_f32_16x16x32_bf16 v[212:215], v[48:51], a[36:39], v[212:215]\n"
    "v_mfma_f32_16x16x32_bf16 v[228:231], v[48:51], a[40:43], v[228:231]\n"
    "v_mfma_f32_16x16x32_bf16 v[244:247], v[48:51], a[44:47], v[244:247]\n"
    "s_waitcnt lgkmcnt(12)\n"
    "v_mfma_f32_16x16x32_bf16 v[12:15], v[52:55], a[32:35], v[12:15]\n"
    "ds_read_b64_tr_b16 v[48:49], v114 offset:24576\n"
    "ds_read_b64_tr_b16 v[50:51], v114 offset:25088\n"
    "v_mfma_f32_16x16x32_bf16 v[28:31], v[52:55], a[36:39], v[28:31]\n"
    "v_mfma_f32_16x16x32_bf16 v[76:79], v[52:55], a[40:43], v[76:79]\n"
    "v_mfma_f32_16x16x32_bf16 v[92:95], v[52:55], a[44:47], v[92:95]\n"
    "s_waitcnt lgkmcnt(12)\n"
    "v_mfma_f32_16x16x32_bf16 v[200:203], v[56:59], a[32:35], v[200:203]\n"
    "ds_read_b64_tr_b16 v[52:53], v115 offset:24576\n"
    "ds_read_b64_tr_b16 v[54:55], v115 offset:25088\n"
    "v_mfma_f32_16x16x32_bf16 v[216:219], v[56:59], a[36:39], v[216:219]\n"
    "v_mfma_f32_16x16x32_bf16 v[232:235], v[56:59], a[40:43], v[232:235]\n"
    "v_mfma_f32_16x16x32_bf16 v[248:251], v[56:59], a[44:47], v[248:251]\n"
    "s_waitcnt lgkmcnt(12)\n"
    "v_mfma_f32_16x16x32_bf16 v[16:19], v[60:63], a[32:35], v[16:19]\n"
    "ds_read_b64_tr_b16 v[56:57], v112 offset:25600\n"
    "ds_read_b64_tr_b16 v[58:59], v112 offset:26112\n"
    "v_mfma_f32_16x16x32_bf16 v[32:35], v[60:63], a[36:39], v[32:35]\n"
    "v_mfma_f32_16x16x32_bf16 v[80:83], v[60:63], a[40:43], v[80:83]\n"
    "v_mfma_f32_16x16x32_bf16 v[96:99], v[60:63], a[44:47], v[96:99]\n"
    "s_waitcnt lgkmcnt(12)\n"
    "v_mfma_f32_16x16x32_bf16 v[204:207], v[64:67], a[32:35], v[204:207]\n"
    "ds_read_b64_tr_b16 v[60:61], v113 offset:25600\n"
    "ds_read_b64_tr_b16 v[62:63], v113 offset:26112\n"
    "v_mfma_f32_16x16x32_bf16 v[220:223], v[64:67], a[36:39], v[220:223]\n"
    "v_mfma_f32_16x16x32_bf16 v[236:239], v[64:67], a[40:43], v[236:239]\n"
    "v_mfma_f32_16x16x32_bf16 v[252:255], v[64:67], a[44:47], v[252:255]\n"
    "s_waitcnt lgkmcnt(12)\n"
    "v_mfma_f32_16x16x32_bf16 v[20:23], v[68:71], a[32:35], v[20:23]\n"
    "ds_read_b64_tr_b16 v[64:65], v114 offset:25600\n"
    "ds_read_b64_tr_b16 v[66:67], v114 offset:26112\n"
    "v_mfma_f32_16x16x32_bf16 v[36:39], v[68:71], a[36:39], v[36:39]\n"
    "v_mfma_f32_16x16x32_bf16 v[84:87], v[68:71], a[40:43], v[84:87]\n"
    "v_mfma_f32_16x16x32_bf16 v[100:103], v[68:71], a[44:47], v[100:103]\n"
    "s_waitcnt lgkmcnt(12)\n"
    "v_mfma_f32_16x16x32_bf16 v[192:195], v[40:43], a[48:51], v[192:195]\n"
    "ds_read_b64_tr_b16 v[68:69], v115 offset:25600\n"
    "ds_read_b64_tr_b16 v[70:71], v115 offset:26112\n"
    "v_mfma_f32_16x16x32_bf16 v[208:211], v[40:43], a[52:55], v[208:211]\n"
    "v_mfma_f32_16x16x32_bf16 v[224:227], v[40:43], a[56:59], v[224:227]\n"
    "v_mfma_f32_16x16x32_bf16 v[240:243], v[40:43], a[60:63], v[240:243]\n"
    "s_waitcnt lgkmcnt(12)\n"
    "v_mfma_f32_16x16x32_bf16 v[8:11], v[44:47], a[48:51], v[8:11]\n"
    "v_mfma_f32_16x16x32_bf16 v[24:27], v[44:47], a[52:55], v[24:27]\n"
    "v_mfma_f32_16x16x32_bf16 v[72:75], v[44:47], a[56:59], v[72:75]\n"
    "s_waitcnt vmcnt(8)\n"
    "s_waitcnt lgkmcnt(0)\n"
    "s_barrier\n"
    "v_mfma_f32_16x16x32_bf16 v[88:91], v[44:47], a[60:63], v[88:91]\n"
    "ds_read_b64_tr_b16 v[40:41], v112 offset:32768\n"
    "s_add_u32 m0, s65, 0\n"
    "v_mfma_f32_16x16x32_bf16 v[196:199], v[48:51], a[48:51], v[196:199]\n"
    "ds_read_b64_tr_b16 v[44:45], v113 offset:32768\n"
    "buffer_load_dwordx4 v104, s[28:31], s63 offen lds\n"
    "v_mfma_f32_16x16x32_bf16 v[212:215], v[48:51], a[52:55], v[212:215]\n"
    "ds_read_b64_tr_b16 v[42:43], v112 offset:33280\n"
    "s_add_u32 m0, s65, 0x400\n"
    "v_mfma_f32_16x16x32_bf16 v[228:231], v[48:51], a[56:59], v[228:231]\n"
    "ds_read_b64_tr_b16 v[46:47], v113 offset:33280\n"
    "buffer_load_dwordx4 v105, s[28:31], s63 offen lds\n"
    "v_mfma_f32_16x16x32_bf16 v[244:247], v[48:51], a[60:63], v[244:247]\n"
    "s_add_u32 s63, s63, 0x40000\n"
    "v_mfma_f32_16x16x32_bf16 v[12:15], v[52:55], a[48:51], v[12:15]\n"
    "ds_read_b64_tr_b16 v[48:49], v114 offset:32768\n"
    "s_and_b32 s63, s63, 0x3fffff\n"
    "v_mfma_f32_16x16x32_bf16 v[28:31], v[52:55], a[52:55], v[28:31]\n"
    "ds_read_b64_tr_b16 v[50:51], v114 offset:33280\n"
    "s_add_u32 m0, s65, 0x2000\n"
    "v_mfma_f32_16x16x32_bf16 v[76:79], v[52:55], a[56:59], v[76:79]\n"
    "buffer_load_dwordx4 v104, s[28:31], s63 offen lds\n"
    "v_mfma_f32_16x16x32_bf16 v[92:95], v[52:55], a[60:63], v[92:95]\n"
    "s_add_u32 m0, s65, 0x2400\n"
    "v_mfma_f32_16x16x32_bf16 v[200:203], v[56:59], a[48:51], v[200:203]\n"
    "ds_read_b64_tr_b16 v[52:53], v115 offset:32768\n"
    "buffer_load_dwordx4 v105, s[28:31], s63 offen lds\n"
    "v_mfma_f32_16x16x32_bf16 v[216:219], v[56:59], a[52:55], v[216:219]\n"
    "ds_read_b64_tr_b16 v[54:55], v115 offset:33280\n"
    "s_add_u32 s63, s63, 0x40000\n"
    "v_mfma_f32_16x16x32_bf16 v[232:235], v[56:59], a[56:59], v[232:235]\n"
    "s_and_b32 s63, s63, 0x3fffff\n"
    "v_mfma_f32_16x16x32_bf16 v[248:251], v[56:59], a[60:63], v[248:251]\n"
    "s_add_u32 m0, s65, 0x4000\n"
    "v_mfma_f32_16x16x32_bf16 v[16:19], v[60:63], a[48:51], v[16:19]\n"
    "ds_read_b64_tr_b16 v[56:57], v112 offset:33792\n"
    "buffer_load_dwordx4 v104, s[28:31], s63 offen lds\n"
    "v_mfma_f32_16x16x32_bf16 v[32:35], v[60:63], a[52:55], v[32:35]\n"
    "ds_read_b64_tr_b16 v[58:59], v112 offset:34304\n"
    "s_add_u32 m0, s65, 0x4400\n"
    "v_mfma_f32_16x16x32_bf16 v[80:83], v[60:63], a[56:59], v[80:83]\n"
    "buffer_load_dwordx4 v105, s[28:31], s63 offen lds\n"
    "v_mfma_f32_16x16x32_bf16 v[96:99], v[60:63], a[60:63], v[96:99]\n"
    "s_add_u32 s63, s63, 0x40000\n"
    "v_mfma_f32_16x16x32_bf16 v[204:207], v[64:67], a[48:51], v[204:207]\n"
    "ds_read_b64_tr_b16 v[60:61], v113 offset:33792\n"
    "s_and_b32 s63, s63, 0x3fffff\n"
    "v_mfma_f32_16x16x32_bf16 v[220:223], v[64:67], a[52:55], v[220:223]\n"
    "ds_read_b64_tr_b16 v[62:63], v113 offset:34304\n"
    "s_add_u32 m0, s65, 0x6000\n"
    "v_mfma_f32_16x16x32_bf16 v[236:239], v[64:67], a[56:59], v[236:239]\n"
    "buffer_load_dwordx4 v104, s[28:31], s63 offen lds\n"
    "v_mfma_f32_16x16x32_bf16 v[252:255], v[64:67], a[60:63], v[252:255]\n"
    "s_add_u32 m0, s65, 0x6400\n"
    "v_mfma_f32_16x16x32_bf16 v[20:23], v[68:71], a[48:51], v[20:23]\n"
    "ds_read_b64_tr_b16 v[64:65], v114 offset:33792\n"
    "buffer_load_dwordx4 v105, s[28:31], s63 offen lds\n"
    "v_mfma_f32_16x16x32_bf16 v[36:39], v[68:71], a[52:55], v[36:39]\n"
    "ds_read_b64_tr_b16 v[66:67], v114 offset:34304\n"
    "s_add_u32 s63, s63, 0x40000\n"
    "v_mfma_f32_16x16x32_bf16 v[84:87], v[68:71], a[56:59], v[84:87]\n"
    "s_and_b32 s63, s63, 0x3fffff\n"
    "v_mfma_f32_16x16x32_bf16 v[100:103], v[68:71], a[60:63], v[100:103]\n"
    "ds_read_b64_tr_b16 v[68:69], v115 offset:33792\n"
    "ds_read_b64_tr_b16 v[70:71], v115 offset:34304\n"
    "s_waitcnt lgkmcnt(13)\n"
    "v_mfma_f32_16x16x32_bf16 v[192:195], v[40:43], a[64:67], v[192:195]\n"
    "buffer_store_dwordx4 v[128:131], v2, s[8:11], s66 offen sc1\n"
    "v_mfma_f32_16x16x32_bf16 v[208:211], v[40:43], a[68:71], v[208:211]\n"
    "v_mfma_f32_16x16x32_bf16 v[224:227], v[40:43], a[72:75], v[224:227]\n"
    "v_mfma_f32_16x16x32_bf16 v[240:243], v[40:43], a[76:79], v[240:243]\n"
    "s_waitcnt lgkmcnt(12)\n"
    "v_mfma_f32_16x16x32_bf16 v[8:11], v[44:47], a[64:67], v[8:11]\n"
    "ds_read_b64_tr_b16 v[40:41], v112 offset:40960\n"
    "ds_read_b64_tr_b16 v[42:43], v112 offset:41472\n"
    "v_mfma_f32_16x16x32_bf16 v[24:27], v[44:47], a[68:71], v[24:27]\n"
    "v_mfma_f32_16x16x32_bf16 v[72:75], v[44:47], a[72:75], v[72:75]\n"
    "v_mfma_f32_16x16x32_bf16 v[88:91], v[44:47], a[76:79], v[88:91]\n"
    "s_waitcnt lgkmcnt(12)\n"
    "v_mfma_f32_16x16x32_bf16 v[196:199], v[48:51], a[64:67], v[196:199]\n"
    "ds_read_b64_tr_b16 v[44:45], v113 offset:40960\n"
    "ds_read_b64_tr_b16 v[46:47], v113 offset:41472\n"
    "v_mfma_f32_16x16x32_bf16 v[212:215], v[48:51], a[68:71], v[212:215]\n"
    "v_mfma_f32_16x16x32_bf16 v[228:231], v[48:51], a[72:75], v[228:231]\n"
    "v_mfma_f32_16x16x32_bf16 v[244:247], v[48:51], a[76:79], v[244:247]\n"
    "s_waitcnt lgkmcnt(12)\n"
    "v_mfma_f32_16x16x32_bf16 v[12:15], v[52:55], a[64:67], v[12:15]\n"
    "ds_read_b64_tr_b16 v[48:49], v114 offset:40960\n"
    "ds_read_b64_tr_b16 v[50:51], v114 offset:41472\n"
    "v_mfma_f32_16x16x32_bf16 v[28:31], v[52:55], a[68:71], v[28:31]\n"
    "v_mfma_f32_16x16x32_bf16 v[76:79], v[52:55], a[72:75], v[76:79]\n"
    "v_mfma_f32_16x16x32_bf16 v[92:95], v[52:55], a[76:79], v[92:95]\n"
    "s_waitcnt lgkmcnt(12)\n"
    "v_mfma_f32_16x16x32_bf16 v[200:203], v[56:59], a[64:67], v[200:203]\n"
    "ds_read_b64_tr_b16 v[52:53], v115 offset:40960\n"
    "buffer_store_dwordx4 v[132:135], v2, s[8:11], s66 offen offset:1024 sc1\n"
    "v_mfma_f32_16x16x32_bf16 v[216:219], v[56:59], a[68:71], v[216:219]\n"
    "ds_read_b64_tr_b16 v[54:55], v115 offset:41472\n"
    "v_mfma_f32_16x16x32_bf16 v[232:235], v[56:59], a[72:75], v[232:235]\n"
    "v_mfma_f32_16x16x32_bf16 v[248:251], v[56:59], a[76:79], v[248:251]\n"
    "s_waitcnt lgkmcnt(12)\n"
    "v_mfma_f32_16x16x32_bf16 v[16:19], v[60:63], a[64:67], v[16:19]\n"
    "ds_read_b64_tr_b16 v[56:57], v112 offset:41984\n"
    "ds_read_b64_tr_b16 v[58:59], v112 offset:42496\n"
    "v_mfma_f32_16x16x32_bf16 v[32:35], v[60:63], a[68:71], v[32:35]\n"
    "v_mfma_f32_16x16x32_bf16 v[80:83], v[60:63], a[72:75], v[80:83]\n"
    "v_mfma_f32_16x16x32_bf16 v[96:99], v[60:63], a[76:79], v[96:99]\n"
    "s_waitcnt lgkmcnt(12)\n"
    "v_mfma_f32_16x16x32_bf16 v[204:207], v[64:67], a[64:67], v[204:207]\n"
    "ds_read_b64_tr_b16 v[60:61], v113 offset:41984\n"
    "ds_read_b64_tr_b16 v[62:63], v113 offset:42496\n"
    "v_mfma_f32_16x16x32_bf16 v[220:223], v[64:67], a[68:71], v[220:223]\n"
    "v_mfma_f32_16x16x32_bf16 v[236:239], v[64:67], a[72:75], v[236:239]\n"
    "v_mfma_f32_16x16x32_bf16 v[252:255], v[64:67], a[76:79], v[252:255]\n"
    "s_waitcnt lgkmcnt(12)\n"
    "v_mfma_f32_16x16x32_bf16 v[20:23], v[68:71], a[64:67], v[20:23]\n"
    "ds_read_b64_tr_b16 v[64:65], v114 offset:41984\n"
    "ds_read_b64_tr_b16 v[66:67], v114 offset:42496\n"
    "v_mfma_f32_16x16x32_bf16 v[36:39], v[68:71], a[68:71], v[36:39]\n"
    "v_mfma_f32_16x16x32_bf16 v[84:87], v[68:71], a[72:75], v[84:87]\n"
    "v_mfma_f32_16x16x32_bf16 v[100:103], v[68:71], a[76:79], v[100:103]\n"
    "s_waitcnt lgkmcnt(12)\n"
    "v_mfma_f32_16x16x32_bf16 v[192:195], v[40:43], a[80:83], v[192:195]\n"
    "ds_read_b64_tr_b16 v[68:69], v115 offset:41984\n"
    "s_add_u32 m0, s91, 0x18000\n"
    "v_mfma_f32_16x16x32_bf16 v[208:211], v[40:43], a[84:87], v[208:211]\n"
    "ds_read_b64_tr_b16 v[70:71], v115 offset:42496\n"
    "v_mfma_f32_16x16x32_bf16 v[224:227], v[40:43], a[88:91], v[224:227]\n"
    "buffer_load_dwordx4 v110, s[8:11], s64 offen sc1 lds\n"
    "v_mfma_f32_16x16x32_bf16 v[240:243], v[40:43], a[92:95], v[240:243]\n"
    "s_waitcnt lgkmcnt(12)\n"
    "v_mfma_f32_16x16x32_bf16 v[8:11], v[44:47], a[80:83], v[8:11]\n"
    "ds_read_b64_tr_b16 v[40:41], v112 offset:49152\n"
    "ds_read_b64_tr_b16 v[42:43], v112 offset:49664\n"
    "v_mfma_f32_16x16x32_bf16 v[24:27], v[44:47], a[84:87], v[24:27]\n"
    "s_add_u32 m0, s91, 0x18400\n"
    "v_mfma_f32_16x16x32_bf16 v[72:75], v[44:47], a[88:91], v[72:75]\n"
    "v_mfma_f32_16x16x32_bf16 v[88:91], v[44:47], a[92:95], v[88:91]\n"
    "s_waitcnt lgkmcnt(12)\n"
    "v_mfma_f32_16x16x32_bf16 v[196:199], v[48:51], a[80:83], v[196:199]\n"
    "ds_read_b64_tr_b16 v[44:45], v113 offset:49152\n"
    "buffer_load_dwordx4 v111, s[8:11], s64 offen sc1 lds\n"
    "v_mfma_f32_16x16x32_bf16 v[212:215], v[48:51], a[84:87], v[212:215]\n"
    "ds_read_b64_tr_b16 v[46:47], v113 offset:49664\n"
    "v_mfma_f32_16x16x32_bf16 v[228:231], v[48:51], a[88:91], v[228:231]\n"
    "s_add_u32 m0, s91, 0x18800\n"
    "v_mfma_f32_16x16x32_bf16 v[244:247], v[48:51], a[92:95], v[244:247]\n"
    "s_waitcnt lgkmcnt(12)\n"
    "v_mfma_f32_16x16x32_bf16 v[12:15], v[52:55], a[80:83], v[12:15]\n"
    "ds_read_b64_tr_b16 v[48:49], v114 offset:49152\n"
    "ds_read_b64_tr_b16 v[50:51], v114 offset:49664\n"
    "v_mfma_f32_16x16x32_bf16 v[28:31], v[52:55], a[84:87], v[28:31]\n"
    "s_add_u32 s83, s64, s62\n"
    "v_mfma_f32_16x16x32_bf16 v[76:79], v[52:55], a[88:91], v[76:79]\n"
    "v_mfma_f32_16x16x32_bf16 v[92:95], v[52:55], a[92:95], v[92:95]\n"
    "s_waitcnt lgkmcnt(12)\n"
    "v_mfma_f32_16x16x32_bf16 v[200:203], v[56:59], a[80:83], v[200:203]\n"
    "ds_read_b64_tr_b16 v[52:53], v115 offset:49152\n"
    "buffer_load_dwordx4 v110, s[8:11], s83 offen sc1 lds\n"
    "v_mfma_f32_16x16x32_bf16 v[216:219], v[56:59], a[84:87], v[216:219]\n"
    "ds_read_b64_tr_b16 v[54:55], v115 offset:49664\n"
    "v_mfma_f32_16x16x32_bf16 v[232:235], v[56:59], a[88:91], v[232:235]\n"
    "s_add_u32 m0, s91, 0x18c00\n"
    "v_mfma_f32_16x16x32_bf16 v[248:251], v[56:59], a[92:95], v[248:251]\n"
    "s_waitcnt lgkmcnt(12)\n"
    "v_mfma_f32_16x16x32_bf16 v[16:19], v[60:63], a[80:83], v[16:19]\n"
    "ds_read_b64_tr_b16 v[56:57], v112 offset:50176\n"
    "ds_read_b64_tr_b16 v[58:59], v112 offset:50688\n"
    "v_mfma_f32_16x16x32_bf16 v[32:35], v[60:63], a[84:87], v[32:35]\n"
    "buffer_load_dwordx4 v111, s[8:11], s83 offen sc1 lds\n"
    "v_mfma_f32_16x16x32_bf16 v[80:83], v[60:63], a[88:91], v[80:83]\n"
    "v_mfma_f32_16x16x32_bf16 v[96:99], v[60:63], a[92:95], v[96:99]\n"
    "s_waitcnt lgkmcnt(12)\n"
    "v_mfma_f32_16x16x32_bf16 v[204:207], v[64:67], a[80:83], v[204:207]\n"
    "ds_read_b64_tr_b16 v[60:61], v113 offset:50176\n"
    "buffer_store_dwordx4 v[136:139], v2, s[8:11], s66 offen offset:2048 sc1\n"
    "v_mfma_f32_16x16x32_bf16 v[220:223], v[64:67], a[84:87], v[220:223]\n"
    "ds_read_b64_tr_b16 v[62:63], v113 offset:50688\n"
    "v_mfma_f32_16x16x32_bf16 v[236:239], v[64:67], a[88:91], v[236:239]\n"
    "buffer_store_dwordx4 v[140:143], v2, s[8:11], s66 offen offset:3072 sc1\n"
    "v_mfma_f32_16x16x32_bf16 v[252:255], v[64:67], a[92:95], v[252:255]\n"
    "s_waitcnt lgkmcnt(12)\n"
    "v_mfma_f32_16x16x32_bf16 v[20:23], v[68:71], a[80:83], v[20:23]\n"
    "ds_read_b64_tr_b16 v[64:65], v114 offset:50176\n"
    "ds_read_b64_tr_b16 v[66:67], v114 offset:50688\n"
    "v_mfma_f32_16x16x32_bf16 v[36:39], v[68:71], a[84:87], v[36:39]\n"
    "s_add_u32 s66, s66, 0x1000\n"
    "v_mfma_f32_16x16x32_bf16 v[84:87], v[68:71], a[88:91], v[84:87]\n"
    "v_mfma_f32_16x16x32_bf16 v[100:103], v[68:71], a[92:95], v[100:103]\n"
    "s_waitcnt lgkmcnt(12)\n"
    "v_mfma_f32_16x16x32_bf16 v[192:195], v[40:43], a[96:99], v[192:195]\n"
    "ds_read_b64_tr_b16 v[68:69], v115 offset:50176\n"
    "s_add_u32 m0, s91, 0x19000\n"
    "v_mfma_f32_16x16x32_bf16 v[208:211], v[40:43], a[100:103], v[208:211]\n"
    "ds_read_b64_tr_b16 v[70:71], v115 offset:50688\n"
    "v_mfma_f32_16x16x32_bf16 v[224:227], v[40:43], a[104:107], v[224:227]\n"
    "buffer_load_dwordx4 v125, s[8:11], s64 offen sc1 lds\n"
    "v_mfma_f32_16x16x32_bf16 v[240:243], v[40:43], a[108:111], v[240:243]\n"
    "s_waitcnt lgkmcnt(12)\n"
    "v_mfma_f32_16x16x32_bf16 v[8:11], v[44:47], a[96:99], v[8:11]\n"
    "ds_read_b64_tr_b16 v[40:41], v112 offset:57344\n"
    "ds_read_b64_tr_b16 v[42:43], v112 offset:57856\n"
    "v_mfma_f32_16x16x32_bf16 v[24:27], v[44:47], a[100:103], v[24:27]\n"
    "s_add_u32 m0, s91, 0x19400\n"
    "v_mfma_f32_16x16x32_bf16 v[72:75], v[44:47], a[104:107], v[72:75]\n"
    "v_mfma_f32_16x16x32_bf16 v[88:91], v[44:47], a[108:111], v[88:91]\n"
    "s_waitcnt lgkmcnt(12)\n"
    "v_mfma_f32_16x16x32_bf16 v[196:199], v[48:51], a[96:99], v[196:199]\n"
    "ds_read_b64_tr_b16 v[44:45], v113 offset:57344\n"
    "buffer_load_dwordx4 v126, s[8:11], s64 offen sc1 lds\n"
    "v_mfma_f32_16x16x32_bf16 v[212:215], v[48:51], a[100:103], v[212:215]\n"
    "ds_read_b64_tr_b16 v[46:47], v113 offset:57856\n"
    "v_mfma_f32_16x16x32_bf16 v[228:231], v[48:51], a[104:107], v[228:231]\n"
    "s_add_u32 m0, s91, 0x19800\n"
    "v_mfma_f32_16x16x32_bf16 v[244:247], v[48:51], a[108:111], v[244:247]\n"
    "s_waitcnt lgkmcnt(12)\n"
    "v_mfma_f32_16x16x32_bf16 v[12:15], v[52:55], a[96:99], v[12:15]\n"
    "ds_read_b64_tr_b16 v[48:49], v114 offset:57344\n"
    "ds_read_b64_tr_b16 v[50:51], v114 offset:57856\n"
    "v_mfma_f32_16x16x32_bf16 v[28:31], v[52:55], a[100:103], v[28:31]\n"
    "s_add_u32 s83, s64, s62\n"
    "v_mfma_f32_16x16x32_bf16 v[76:79], v[52:55], a[104:107], v[76:79]\n"
    "v_mfma_f32_16x16x32_bf16 v[92:95], v[52:55], a[108:111], v[92:95]\n"
    "s_waitcnt lgkmcnt(12)\n"
    "v_mfma_f32_16x16x32_bf16 v[200:203], v[56:59], a[96:99], v[200:203]\n"
    "ds_read_b64_tr_b16 v[52:53], v115 offset:57344\n"
    "buffer_load_dwordx4 v125, s[8:11], s83 offen sc1 lds\n"
    "v_mfma_f32_16x16x32_bf16 v[216:219], v[56:59], a[100:103], v[216:219]\n"
    "ds_read_b64_tr_b16 v[54:55], v115 offset:57856\n"
    "v_mfma_f32_16x16x32_bf16 v[232:235], v[56:59], a[104:107], v[232:235]\n"
    "s_add_u32 m0, s91, 0x19c00\n"
    "v_mfma_f32_16x16x32_bf16 v[248:251], v[56:59], a[108:111], v[248:251]\n"
    "s_waitcnt lgkmcnt(12)\n"
    "v_mfma_f32_16x16x32_bf16 v[16:19], v[60:63], a[96:99], v[16:19]\n"
    "ds_read_b64_tr_b16 v[56:57], v112 offset:58368\n"
    "ds_read_b64_tr_b16 v[58:59], v112 offset:58880\n"
    "v_mfma_f32_16x16x32_bf16 v[32:35], v[60:63], a[100:103], v[32:35]\n"
    "buffer_load_dwordx4 v126, s[8:11], s83 offen sc1 lds\n"
    "v_mfma_f32_16x16x32_bf16 v[80:83], v[60:63], a[104:107], v[80:83]\n"
    "v_mfma_f32_16x16x32_bf16 v[96:99], v[60:63], a[108:111], v[96:99]\n"
    "s_waitcnt lgkmcnt(12)\n"
    "v_mfma_f32_16x16x32_bf16 v[204:207], v[64:67], a[96:99], v[204:207]\n"
    "ds_read_b64_tr_b16 v[60:61], v113 offset:58368\n"
    "s_add_u32 s64, s64, 0x80\n"
    "v_mfma_f32_16x16x32_bf16 v[220:223], v[64:67], a[100:103], v[220:223]\n"
    "ds_read_b64_tr_b16 v[62:63], v113 offset:58880\n"
    "v_mfma_f32_16x16x32_bf16 v[236:239], v[64:67], a[104:107], v[236:239]\n"
    "s_and_b32 s64, s64, 0x3ff\n"
    "v_mfma_f32_16x16x32_bf16 v[252:255], v[64:67], a[108:111], v[252:255]\n"
    "s_waitcnt lgkmcnt(12)\n"
    "v_mfma_f32_16x16x32_bf16 v[20:23], v[68:71], a[96:99], v[20:23]\n"
    "ds_read_b64_tr_b16 v[64:65], v114 offset:58368\n"
    "ds_read_b64_tr_b16 v[66:67], v114 offset:58880\n"
    "v_mfma_f32_16x16x32_bf16 v[36:39], v[68:71], a[100:103], v[36:39]\n"
    "buffer_store_dwordx4 v[144:147], v2, s[8:11], s66 offen sc1\n"
    "v_mfma_f32_16x16x32_bf16 v[84:87], v[68:71], a[104:107], v[84:87]\n"
    "v_mfma_f32_16x16x32_bf16 v[100:103], v[68:71], a[108:111], v[100:103]\n"
    "s_waitcnt lgkmcnt(12)\n"
    "v_mfma_f32_16x16x32_bf16 v[192:195], v[40:43], a[112:115], v[192:195]\n"
    "ds_read_b64_tr_b16 v[68:69], v115 offset:58368\n"
    "ds_read_b64_tr_b16 v[70:71], v115 offset:58880\n"
    "v_mfma_f32_16x16x32_bf16 v[208:211], v[40:43], a[116:119], v[208:211]\n"
    "v_mfma_f32_16x16x32_bf16 v[224:227], v[40:43], a[120:123], v[224:227]\n"
    "v_mfma_f32_16x16x32_bf16 v[240:243], v[40:43], a[124:127], v[240:243]\n"
    "s_waitcnt lgkmcnt(12)\n"
    "v_mfma_f32_16x16x32_bf16 v[8:11], v[44:47], a[112:115], v[8:11]\n"
    "v_mfma_f32_16x16x32_bf16 v[24:27], v[44:47], a[116:119], v[24:27]\n"
    "v_mfma_f32_16x16x32_bf16 v[72:75], v[44:47], a[120:123], v[72:75]\n"
    "s_waitcnt vmcnt(13)\n"
    "s_waitcnt lgkmcnt(0)\n"
    "s_barrier\n"
    "v_mfma_f32_16x16x32_bf16 v[88:91], v[44:47], a[124:127], v[88:91]\n"
    "ds_read_b64_tr_b16 v[40:41], v112\n"
    "s_add_u32 m0, s65, 0x8000\n"
    "v_mfma_f32_16x16x32_bf16 v[196:199], v[48:51], a[112:115], v[196:199]\n"
    "ds_read_b64_tr_b16 v[44:45], v113\n"
    "buffer_load_dwordx4 v104, s[28:31], s63 offen lds\n"
    "v_mfma_f32_16x16x32_bf16 v[212:215], v[48:51], a[116:119], v[212:215]\n"
    "ds_read_b64_tr_b16 v[42:43], v112 offset:512\n"
    "s_add_u32 m0, s65, 0x8400\n"
    "v_mfma_f32_16x16x32_bf16 v[228:231], v[48:51], a[120:123], v[228:231]\n"
    "ds_read_b64_tr_b16 v[46:47], v113 offset:512\n"
    "buffer_load_dwordx4 v105, s[28:31], s63 offen lds\n"
    "v_mfma_f32_16x16x32_bf16 v[244:247], v[48:51], a[124:127], v[244:247]\n"
    "s_add_u32 s63, s63, 0x40000\n"
    "v_mfma_f32_16x16x32_bf16 v[12:15], v[52:55], a[112:115], v[12:15]\n"
    "ds_read_b64_tr_b16 v[48:49], v114\n"
    "s_and_b32 s63, s63, 0x3fffff\n"
    "v_mfma_f32_16x16x32_bf16 v[28:31], v[52:55], a[116:119], v[28:31]\n"
    "ds_read_b64_tr_b16 v[50:51], v114 offset:512\n"
    "s_add_u32 m0, s65, 0xa000\n"
    "v_mfma_f32_16x16x32_bf16 v[76:79], v[52:55], a[120:123], v[76:79]\n"
    "buffer_load_dwordx4 v104, s[28:31], s63 offen lds\n"
    "v_mfma_f32_16x16x32_bf16 v[92:95], v[52:55], a[124:127], v[92:95]\n"
    "s_add_u32 m0, s65, 0xa400\n"
    "v_mfma_f32_16x16x32_bf16 v[200:203], v[56:59], a[112:115], v[200:203]\n"
    "ds_read_b64_tr_b16 v[52:53], v115\n"
    "buffer_load_dwordx4 v105, s[28:31], s63 offen lds\n"
    "v_mfma_f32_16x16x32_bf16 v[216:219], v[56:59], a[116:119], v[216:219]\n"
    "ds_read_b64_tr_b16 v[54:55], v115 offset:512\n"
    "s_add_u32 s63, s63, 0x40000\n"
    "v_mfma_f32_16x16x32_bf16 v[232:235], v[56:59], a[120:123], v[232:235]\n"
    "s_and_b32 s63, s63, 0x3fffff\n"
    "v_mfma_f32_16x16x32_bf16 v[248:251], v[56:59], a[124:127], v[248:251]\n"
    "s_add_u32 m0, s65, 0xc000\n"
    "v_mfma_f32_16x16x32_bf16 v[16:19], v[60:63], a[112:115], v[16:19]\n"
    "ds_read_b64_tr_b16 v[56:57], v112 offset:1024\n"
    "buffer_load_dwordx4 v104, s[28:31], s63 offen lds\n"
    "v_mfma_f32_16x16x32_bf16 v[32:35], v[60:63], a[116:119], v[32:35]\n"
    "ds_read_b64_tr_b16 v[58:59], v112 offset:1536\n"
    "s_add_u32 m0, s65, 0xc400\n"
    "v_mfma_f32_16x16x32_bf16 v[80:83], v[60:63], a[120:123], v[80:83]\n"
    "buffer_load_dwordx4 v105, s[28:31], s63 offen lds\n"
    "v_mfma_f32_16x16x32_bf16 v[96:99], v[60:63], a[124:127], v[96:99]\n"
    "s_add_u32 s63, s63, 0x40000\n"
    "v_mfma_f32_16x16x32_bf16 v[204:207], v[64:67], a[112:115], v[204:207]\n"
    "ds_read_b64_tr_b16 v[60:61], v113 offset:1024\n"
    "s_and_b32 s63, s63, 0x3fffff\n"
    "v_mfma_f32_16x16x32_bf16 v[220:223], v[64:67], a[116:119], v[220:223]\n"
    "ds_read_b64_tr_b16 v[62:63], v113 offset:1536\n"
    "s_add_u32 m0, s65, 0xe000\n"
    "v_mfma_f32_16x16x32_bf16 v[236:239], v[64:67], a[120:123], v[236:239]\n"
    "buffer_load_dwordx4 v104, s[28:31], s63 offen lds\n"
    "v_mfma_f32_16x16x32_bf16 v[252:255], v[64:67], a[124:127], v[252:255]\n"
    "s_add_u32 m0, s65, 0xe400\n"
    "v_mfma_f32_16x16x32_bf16 v[20:23], v[68:71], a[112:115], v[20:23]\n"
    "ds_read_b64_tr_b16 v[64:65], v114 offset:1024\n"
    "buffer_load_dwordx4 v105, s[28:31], s63 offen lds\n"
    "v_mfma_f32_16x16x32_bf16 v[36:39], v[68:71], a[116:119], v[36:39]\n"
    "ds_read_b64_tr_b16 v[66:67], v114 offset:1536\n"
    "s_add_u32 s63, s63, 0x40000\n"
    "v_mfma_f32_16x16x32_bf16 v[84:87], v[68:71], a[120:123], v[84:87]\n"
    "s_and_b32 s63, s63, 0x3fffff\n"
    "v_mfma_f32_16x16x32_bf16 v[100:103], v[68:71], a[124:127], v[100:103]\n"
    "ds_read_b64_tr_b16 v[68:69], v115 offset:1024\n"
    "ds_read_b64_tr_b16 v[70:71], v115 offset:1536\n"
    "s_waitcnt lgkmcnt(13)\n"
    "v_mfma_f32_16x16x32_bf16 v[192:195], v[40:43], a[128:131], v[192:195]\n"
    "buffer_store_dwordx4 v[148:151], v2, s[8:11], s66 offen offset:1024 sc1\n"
    "v_mfma_f32_16x16x32_bf16 v[208:211], v[40:43], a[132:135], v[208:211]\n"
    "v_mfma_f32_16x16x32_bf16 v[224:227], v[40:43], a[136:139], v[224:227]\n"
    "v_mfma_f32_16x16x32_bf16 v[240:243], v[40:43], a[140:143], v[240:243]\n"
    "s_waitcnt lgkmcnt(12)\n"
    "v_mfma_f32_16x16x32_bf16 v[8:11], v[44:47], a[128:131], v[8:11]\n"
    "ds_read_b64_tr_b16 v[40:41], v112 offset:8192\n"
    "ds_read_b64_tr_b16 v[42:43], v112 offset:8704\n"
    "v_mfma_f32_16x16x32_bf16 v[24:27], v[44:47], a[132:135], v[24:27]\n"
    "v_mfma_f32_16x16x32_bf16 v[72:75], v[44:47], a[136:139], v[72:75]\n"
    "v_mfma_f32_16x16x32_bf16 v[88:91], v[44:47], a[140:143], v[88:91]\n"
    "s_waitcnt lgkmcnt(12)\n"
    "v_mfma_f32_16x16x32_bf16 v[196:199], v[48:51], a[128:131], v[196:199]\n"
    "ds_read_b64_tr_b16 v[44:45], v113 offset:8192\n"
    "ds_read_b64_tr_b16 v[46:47], v113 offset:8704\n"
    "v_mfma_f32_16x16x32_bf16 v[212:215], v[48:51], a[132:135], v[212:215]\n"
    "v_mfma_f32_16x16x32_bf16 v[228:231], v[48:51], a[136:139], v[228:231]\n"
    "v_mfma_f32_16x16x32_bf16 v[244:247], v[48:51], a[140:143], v[244:247]\n"
    "s_waitcnt lgkmcnt(12)\n"
    "v_mfma_f32_16x16x32_bf16 v[12:15], v[52:55], a[128:131], v[12:15]\n"
    "ds_read_b64_tr_b16 v[48:49], v114 offset:8192\n"
    "ds_read_b64_tr_b16 v[50:51], v114 offset:8704\n"
    "v_mfma_f32_16x16x32_bf16 v[28:31], v[52:55], a[132:135], v[28:31]\n"
    "v_mfma_f32_16x16x32_bf16 v[76:79], v[52:55], a[136:139], v[76:79]\n"
    "v_mfma_f32_16x16x32_bf16 v[92:95], v[52:55], a[140:143], v[92:95]\n"
    "s_waitcnt lgkmcnt(12)\n"
    "v_mfma_f32_16x16x32_bf16 v[200:203], v[56:59], a[128:131], v[200:203]\n"
    "ds_read_b64_tr_b16 v[52:53], v115 offset:8192\n"
    "buffer_store_dwordx4 v[152:155], v2, s[8:11], s66 offen offset:2048 sc1\n"
    "v_mfma_f32_16x16x32_bf16 v[216:219], v[56:59], a[132:135], v[216:219]\n"
    "ds_read_b64_tr_b16 v[54:55], v115 offset:8704\n"
    "v_mfma_f32_16x16x32_bf16 v[232:235], v[56:59], a[136:139], v[232:235]\n"
    "v_mfma_f32_16x16x32_bf16 v[248:251], v[56:59], a[140:143], v[248:251]\n"
    "s_waitcnt lgkmcnt(12)\n"
    "v_mfma_f32_16x16x32_bf16 v[16:19], v[60:63], a[128:131], v[16:19]\n"
    "ds_read_b64_tr_b16 v[56:57], v112 offset:9216\n"
    "ds_read_b64_tr_b16 v[58:59], v112 offset:9728\n"
    "v_mfma_f32_16x16x32_bf16 v[32:35], v[60:63], a[132:135], v[32:35]\n"
    "v_mfma_f32_16x16x32_bf16 v[80:83], v[60:63], a[136:139], v[80:83]\n"
    "v_mfma_f32_16x16x32_bf16 v[96:99], v[60:63], a[140:143], v[96:99]\n"
    "s_waitcnt lgkmcnt(12)\n"
    "v_mfma_f32_16x16x32_bf16 v[204:207], v[64:67], a[128:131], v[204:207]\n"
    "ds_read_b64_tr_b16 v[60:61], v113 offset:9216\n"
    "ds_read_b64_tr_b16 v[62:63], v113 offset:9728\n"
    "v_mfma_f32_16x16x32_bf16 v[220:223], v[64:67], a[132:135], v[220:223]\n"
    "v_mfma_f32_16x16x32_bf16 v[236:239], v[64:67], a[136:139], v[236:239]\n"
    "v_mfma_f32_16x16x32_bf16 v[252:255], v[64:67], a[140:143], v[252:255]\n"
    "s_waitcnt lgkmcnt(12)\n"
    "v_mfma_f32_16x16x32_bf16 v[20:23], v[68:71], a[128:131], v[20:23]\n"
    "ds_read_b64_tr_b16 v[64:65], v114 offset:9216\n"
    "ds_read_b64_tr_b16 v[66:67], v114 offset:9728\n"
    "v_mfma_f32_16x16x32_bf16 v[36:39], v[68:71], a[132:135], v[36:39]\n"
    "v_mfma_f32_16x16x32_bf16 v[84:87], v[68:71], a[136:139], v[84:87]\n"
    "v_mfma_f32_16x16x32_bf16 v[100:103], v[68:71], a[140:143], v[100:103]\n"
    "s_waitcnt lgkmcnt(12)\n"
    "v_mfma_f32_16x16x32_bf16 v[192:195], v[40:43], a[144:147], v[192:195]\n"
    "ds_read_b64_tr_b16 v[68:69], v115 offset:9216\n"
    "s_add_u32 m0, s91, 0x20000\n"
    "v_mfma_f32_16x16x32_bf16 v[208:211], v[40:43], a[148:151], v[208:211]\n"
    "ds_read_b64_tr_b16 v[70:71], v115 offset:9728\n"
    "v_mfma_f32_16x16x32_bf16 v[224:227], v[40:43], a[152:155], v[224:227]\n"
    "buffer_load_dwordx4 v110, s[8:11], s64 offen sc1 lds\n"
    "v_mfma_f32_16x16x32_bf16 v[240:243], v[40:43], a[156:159], v[240:243]\n"
    "s_waitcnt lgkmcnt(12)\n"
    "v_mfma_f32_16x16x32_bf16 v[8:11], v[44:47], a[144:147], v[8:11]\n"
    "ds_read_b64_tr_b16 v[40:41], v112 offset:16384\n"
    "ds_read_b64_tr_b16 v[42:43], v112 offset:16896\n"
    "v_mfma_f32_16x16x32_bf16 v[24:27], v[44:47], a[148:151], v[24:27]\n"
    "s_add_u32 m0, s91, 0x20400\n"
    "v_mfma_f32_16x16x32_bf16 v[72:75], v[44:47], a[152:155], v[72:75]\n"
    "v_mfma_f32_16x16x32_bf16 v[88:91], v[44:47], a[156:159], v[88:91]\n"
    "s_waitcnt lgkmcnt(12)\n"
    "v_mfma_f32_16x16x32_bf16 v[196:199], v[48:51], a[144:147], v[196:199]\n"
    "ds_read_b64_tr_b16 v[44:45], v113 offset:16384\n"
    "buffer_load_dwordx4 v111, s[8:11], s64 offen sc1 lds\n"
    "v_mfma_f32_16x16x32_bf16 v[212:215], v[48:51], a[148:151], v[212:215]\n"
    "ds_read_b64_tr_b16 v[46:47], v113 offset:16896\n"
    "v_mfma_f32_16x16x32_bf16 v[228:231], v[48:51], a[152:155], v[228:231]\n"
    "s_add_u32 m0, s91, 0x20800\n"
    "v_mfma_f32_16x16x32_bf16 v[244:247], v[48:51], a[156:159], v[244:247]\n"
    "s_waitcnt lgkmcnt(12)\n"
    "v_mfma_f32_16x16x32_bf16 v[12:15], v[52:55], a[144:147], v[12:15]\n"
    "ds_read_b64_tr_b16 v[48:49], v114 offset:16384\n"
    "ds_read_b64_tr_b16 v[50:51], v114 offset:16896\n"
    "v_mfma_f32_16x16x32_bf16 v[28:31], v[52:55], a[148:151], v[28:31]\n"
    "s_add_u32 s83, s64, s62\n"
    "v_mfma_f32_16x16x32_bf16 v[76:79], v[52:55], a[152:155], v[76:79]\n"
    "v_mfma_f32_16x16x32_bf16 v[92:95], v[52:55], a[156:159], v[92:95]\n"
    "s_waitcnt lgkmcnt(12)\n"
    "v_mfma_f32_16x16x32_bf16 v[200:203], v[56:59], a[144:147], v[200:203]\n"
    "ds_read_b64_tr_b16 v[52:53], v115 offset:16384\n"
    "buffer_load_dwordx4 v110, s[8:11], s83 offen sc1 lds\n"
    "v_mfma_f32_16x16x32_bf16 v[216:219], v[56:59], a[148:151], v[216:219]\n"
    "ds_read_b64_tr_b16 v[54:55], v115 offset:16896\n"
    "v_mfma_f32_16x16x32_bf16 v[232:235], v[56:59], a[152:155], v[232:235]\n"
    "s_add_u32 m0, s91, 0x20c00\n"
    "v_mfma_f32_16x16x32_bf16 v[248:251], v[56:59], a[156:159], v[248:251]\n"
    "s_waitcnt lgkmcnt(12)\n"
    "v_mfma_f32_16x16x32_bf16 v[16:19], v[60:63], a[144:147], v[16:19]\n"
    "ds_read_b64_tr_b16 v[56:57], v112 offset:17408\n"
    "ds_read_b64_tr_b16 v[58:59], v112 offset:17920\n"
    "v_mfma_f32_16x16x32_bf16 v[32:35], v[60:63], a[148:151], v[32:35]\n"
    "buffer_load_dwordx4 v111, s[8:11], s83 offen sc1 lds\n"
    "v_mfma_f32_16x16x32_bf16 v[80:83], v[60:63], a[152:155], v[80:83]\n"
    "v_mfma_f32_16x16x32_bf16 v[96:99], v[60:63], a[156:159], v[96:99]\n"
    "s_waitcnt lgkmcnt(12)\n"
    "v_mfma_f32_16x16x32_bf16 v[204:207], v[64:67], a[144:147], v[204:207]\n"
    "ds_read_b64_tr_b16 v[60:61], v113 offset:17408\n"
    "buffer_store_dwordx4 v[156:159], v2, s[8:11], s66 offen offset:3072 sc1\n"
    "v_mfma_f32_16x16x32_bf16 v[220:223], v[64:67], a[148:151], v[220:223]\n"
    "ds_read_b64_tr_b16 v[62:63], v113 offset:17920\n"
    "v_mfma_f32_16x16x32_bf16 v[236:239], v[64:67], a[152:155], v[236:239]\n"
    "s_add_u32 s66, s66, 0x1000\n"
    "v_mfma_f32_16x16x32_bf16 v[252:255], v[64:67], a[156:159], v[252:255]\n"
    "s_waitcnt lgkmcnt(12)\n"
    "v_mfma_f32_16x16x32_bf16 v[20:23], v[68:71], a[144:147], v[20:23]\n"
    "ds_read_b64_tr_b16 v[64:65], v114 offset:17408\n"
    "ds_read_b64_tr_b16 v[66:67], v114 offset:17920\n"
    "v_mfma_f32_16x16x32_bf16 v[36:39], v[68:71], a[148:151], v[36:39]\n"
    "buffer_store_dwordx4 v[160:163], v2, s[8:11], s66 offen sc1\n"
    "v_mfma_f32_16x16x32_bf16 v[84:87], v[68:71], a[152:155], v[84:87]\n"
    "v_mfma_f32_16x16x32_bf16 v[100:103], v[68:71], a[156:159], v[100:103]\n"
    "s_waitcnt lgkmcnt(12)\n"
    "v_mfma_f32_16x16x32_bf16 v[192:195], v[40:43], a[160:163], v[192:195]\n"
    "ds_read_b64_tr_b16 v[68:69], v115 offset:17408\n"
    "s_add_u32 m0, s91, 0x21000\n"
    "v_mfma_f32_16x16x32_bf16 v[208:211], v[40:43], a[164:167], v[208:211]\n"
    "ds_read_b64_tr_b16 v[70:71], v115 offset:17920\n"
    "v_mfma_f32_16x16x32_bf16 v[224:227], v[40:43], a[168:171], v[224:227]\n"
    "buffer_load_dwordx4 v125, s[8:11], s64 offen sc1 lds\n"
    "v_mfma_f32_16x16x32_bf16 v[240:243], v[40:43], a[172:175], v[240:243]\n"
    "s_waitcnt lgkmcnt(12)\n"
    "v_mfma_f32_16x16x32_bf16 v[8:11], v[44:47], a[160:163], v[8:11]\n"
    "ds_read_b64_tr_b16 v[40:41], v112 offset:24576\n"
    "s_add_u32 m0, s91, 0x21400\n"
    "v_mfma_f32_16x16x32_bf16 v[24:27], v[44:47], a[164:167], v[24:27]\n"
    "ds_read_b64_tr_b16 v[42:43], v112 offset:25088\n"
    "v_mfma_f32_16x16x32_bf16 v[72:75], v[44:47], a[168:171], v[72:75]\n"
    "v_mfma_f32_16x16x32_bf16 v[88:91], v[44:47], a[172:175], v[88:91]\n"
    "buffer_load_dwordx4 v126, s[8:11], s64 offen sc1 lds\n"
    "s_waitcnt lgkmcnt(12)\n"
    "v_mfma_f32_16x16x32_bf16 v[196:199], v[48:51], a[160:163], v[196:199]\n"
    "ds_read_b64_tr_b16 v[44:45], v113 offset:24576\n"
    "ds_read_b64_tr_b16 v[46:47], v113 offset:25088\n"
    "v_mfma_f32_16x16x32_bf16 v[212:215], v[48:51], a[164:167], v[212:215]\n"
    "s_add_u32 m0, s91, 0x21800\n"
    "v_mfma_f32_16x16x32_bf16 v[228:231], v[48:51], a[168:171], v[228:231]\n"
    "v_mfma_f32_16x16x32_bf16 v[244:247], v[48:51], a[172:175], v[244:247]\n"
    "s_waitcnt lgkmcnt(12)\n"
    "v_mfma_f32_16x16x32_bf16 v[12:15], v[52:55], a[160:163], v[12:15]\n"
    "ds_read_b64_tr_b16 v[48:49], v114 offset:24576\n"
    "s_add_u32 s83, s64, s62\n"
    "v_mfma_f32_16x16x32_bf16 v[28:31], v[52:55], a[164:167], v[28:31]\n"
    "ds_read_b64_tr_b16 v[50:51], v114 offset:25088\n"
    "v_mfma_f32_16x16x32_bf16 v[76:79], v[52:55], a[168:171], v[76:79]\n"
    "buffer_load_dwordx4 v125, s[8:11], s83 offen sc1 lds\n"
    "v_mfma_f32_16x16x32_bf16 v[92:95], v[52:55], a[172:175], v[92:95]\n"
    "s_waitcnt lgkmcnt(12)\n"
    "v_mfma_f32_16x16x32_bf16 v[200:203], v[56:59], a[160:163], v[200:203]\n"
    "ds_read_b64_tr_b16 v[52:53], v115 offset:24576\n"
    "ds_read_b64_tr_b16 v[54:55], v115 offset:25088\n"
    "v_mfma_f32_16x16x32_bf16 v[216:219], v[56:59], a[164:167], v[216:219]\n"
    "s_add_u32 m0, s91, 0x21c00\n"
    "v_mfma_f32_16x16x32_bf16 v[232:235], v[56:59], a[168:171], v[232:235]\n"
    "v_mfma_f32_16x16x32_bf16 v[248:251], v[56:59], a[172:175], v[248:251]\n"
    "buffer_load_dwordx4 v126, s[8:11], s83 offen sc1 lds\n"
    "s_waitcnt lgkmcnt(12)\n"
    "v_mfma_f32_16x16x32_bf16 v[16:19], v[60:63], a[160:163], v[16:19]\n"
    "ds_read_b64_tr_b16 v[56:57], v112 offset:25600\n"
    "ds_read_b64_tr_b16 v[58:59], v112 offset:26112\n"
    "v_mfma_f32_16x16x32_bf16 v[32:35], v[60:63], a[164:167], v[32:35]\n"
    "v_mfma_f32_16x16x32_bf16 v[80:83], v[60:63], a[168:171], v[80:83]\n"
    "s_add_u32 s64, s64, 0x80\n"
    "v_mfma_f32_16x16x32_bf16 v[96:99], v[60:63], a[172:175], v[96:99]\n"
    "s_waitcnt lgkmcnt(12)\n"
    "v_mfma_f32_16x16x32_bf16 v[204:207], v[64:67], a[160:163], v[204:207]\n"
    "ds_read_b64_tr_b16 v[60:61], v113 offset:25600\n"
    "s_and_b32 s64, s64, 0x3ff\n"
    "v_mfma_f32_16x16x32_bf16 v[220:223], v[64:67], a[164:167], v[220:223]\n"
    "ds_read_b64_tr_b16 v[62:63], v113 offset:26112\n"
    "v_mfma_f32_16x16x32_bf16 v[236:239], v[64:67], a[168:171], v[236:239]\n"
    "v_mfma_f32_16x16x32_bf16 v[252:255], v[64:67], a[172:175], v[252:255]\n"
    "buffer_store_dwordx4 v[164:167], v2, s[8:11], s66 offen offset:1024 sc1\n"
    "s_waitcnt lgkmcnt(12)\n"
    "v_mfma_f32_16x16x32_bf16 v[20:23], v[68:71], a[160:163], v[20:23]\n"
    "ds_read_b64_tr_b16 v[64:65], v114 offset:25600\n"
    "ds_read_b64_tr_b16 v[66:67], v114 offset:26112\n"
    "v_mfma_f32_16x16x32_bf16 v[36:39], v[68:71], a[164:167], v[36:39]\n"
    "buffer_store_dwordx4 v[168:171], v2, s[8:11], s66 offen offset:2048 sc1\n"
    "v_mfma_f32_16x16x32_bf16 v[84:87], v[68:71], a[168:171], v[84:87]\n"
    "v_mfma_f32_16x16x32_bf16 v[100:103], v[68:71], a[172:175], v[100:103]\n"
    "s_waitcnt lgkmcnt(12)\n"
    "v_mfma_f32_16x16x32_bf16 v[192:195], v[40:43], a[176:179], v[192:195]\n"
    "ds_read_b64_tr_b16 v[68:69], v115 offset:25600\n"
    "ds_read_b64_tr_b16 v[70:71], v115 offset:26112\n"
    "v_mfma_f32_16x16x32_bf16 v[208:211], v[40:43], a[180:183], v[208:211]\n"
    "v_mfma_f32_16x16x32_bf16 v[224:227], v[40:43], a[184:187], v[224:227]\n"
    "v_mfma_f32_16x16x32_bf16 v[240:243], v[40:43], a[188:191], v[240:243]\n"
    "s_waitcnt lgkmcnt(12)\n"
    "v_mfma_f32_16x16x32_bf16 v[8:11], v[44:47], a[176:179], v[8:11]\n"
    "v_mfma_f32_16x16x32_bf16 v[24:27], v[44:47], a[180:183], v[24:27]\n"
    "v_mfma_f32_16x16x32_bf16 v[72:75], v[44:47], a[184:187], v[72:75]\n"
    "s_waitcnt vmcnt(14)\n"
    "s_waitcnt lgkmcnt(0)\n"
    "s_barrier\n"
    "v_mfma_f32_16x16x32_bf16 v[88:91], v[44:47], a[188:191], v[88:91]\n"
    "ds_read_b64_tr_b16 v[40:41], v112 offset:32768\n"
    "s_add_u32 m0, s59, 0\n"
    "v_mfma_f32_16x16x32_bf16 v[196:199], v[48:51], a[176:179], v[196:199]\n"
    "ds_read_b64_tr_b16 v[44:45], v113 offset:32768\n"
    "buffer_load_dwordx4 v106, s[16:19], s90 offen lds\n"
    "v_mfma_f32_16x16x32_bf16 v[212:215], v[48:51], a[180:183], v[212:215]\n"
    "ds_read_b64_tr_b16 v[42:43], v112 offset:33280\n"
    "s_add_u32 m0, s59, 0x400\n"
    "v_mfma_f32_16x16x32_bf16 v[228:231], v[48:51], a[184:187], v[228:231]\n"
    "ds_read_b64_tr_b16 v[46:47], v113 offset:33280\n"
    "buffer_load_dwordx4 v107, s[16:19], s90 offen lds\n"
    "v_mfma_f32_16x16x32_bf16 v[244:247], v[48:51], a[188:191], v[244:247]\n"
    "s_add_u32 m0, s59, 0x800\n"
    "v_mfma_f32_16x16x32_bf16 v[12:15], v[52:55], a[176:179], v[12:15]\n"
    "ds_read_b64_tr_b16 v[48:49], v114 offset:32768\n"
    "buffer_load_dwordx4 v121, s[16:19], s90 offen lds\n"
    "v_mfma_f32_16x16x32_bf16 v[28:31], v[52:55], a[180:183], v[28:31]\n"
    "ds_read_b64_tr_b16 v[50:51], v114 offset:33280\n"
    "s_add_u32 m0, s59, 0xc00\n"
    "v_mfma_f32_16x16x32_bf16 v[76:79], v[52:55], a[184:187], v[76:79]\n"
    "buffer_load_dwordx4 v123, s[16:19], s90 offen lds\n"
    "v_mfma_f32_16x16x32_bf16 v[92:95], v[52:55], a[188:191], v[92:95]\n"
    "v_mfma_f32_16x16x32_bf16 v[200:203], v[56:59], a[176:179], v[200:203]\n"
    "ds_read_b64_tr_b16 v[52:53], v115 offset:32768\n"
    "s_add_u32 s90, s90, s69\n"
    "v_mfma_f32_16x16x32_bf16 v[216:219], v[56:59], a[180:183], v[216:219]\n"
    "ds_read_b64_tr_b16 v[54:55], v115 offset:33280\n"
    "s_cmp_eq_u32 s90, s67\n"
    "v_mfma_f32_16x16x32_bf16 v[232:235], v[56:59], a[184:187], v[232:235]\n"
    "s_cselect_b32 s90, 0, s90\n"
    "v_mfma_f32_16x16x32_bf16 v[248:251], v[56:59], a[188:191], v[248:251]\n"
    "s_add_u32 m0, s59, 0x4000\n"
    "v_mfma_f32_16x16x32_bf16 v[16:19], v[60:63], a[176:179], v[16:19]\n"
    "ds_read_b64_tr_b16 v[56:57], v112 offset:33792\n"
    "buffer_load_dwordx4 v106, s[16:19], s90 offen lds\n"
    "v_mfma_f32_16x16x32_bf16 v[32:35], v[60:63], a[180:183], v[32:35]\n"
    "ds_read_b64_tr_b16 v[58:59], v112 offset:34304\n"
    "s_add_u32 m0, s59, 0x4400\n"
    "v_mfma_f32_16x16x32_bf16 v[80:83], v[60:63], a[184:187], v[80:83]\n"
    "buffer_load_dwordx4 v107, s[16:19], s90 offen lds\n"
    "v_mfma_f32_16x16x32_bf16 v[96:99], v[60:63], a[188:191], v[96:99]\n"
    "v_mfma_f32_16x16x32_bf16 v[204:207], v[64:67], a[176:179], v[204:207]\n"
    "ds_read_b64_tr_b16 v[60:61], v113 offset:33792\n"
    "s_add_u32 m0, s59, 0x4800\n"
    "v_mfma_f32_16x16x32_bf16 v[220:223], v[64:67], a[180:183], v[220:223]\n"
    "buffer_load_dwordx4 v121, s[16:19], s90 offen lds\n"
    "ds_read_b64_tr_b16 v[62:63], v113 offset:34304\n"
    "v_mfma_f32_16x16x32_bf16 v[236:239], v[64:67], a[184:187], v[236:239]\n"
    "s_add_u32 m0, s59, 0x4c00\n"
    "v_mfma_f32_16x16x32_bf16 v[252:255], v[64:67], a[188:191], v[252:255]\n"
    "buffer_load_dwordx4 v123, s[16:19], s90 offen lds\n"
    "v_mfma_f32_16x16x32_bf16 v[20:23], v[68:71], a[176:179], v[20:23]\n"
    "ds_read_b64_tr_b16 v[64:65], v114 offset:33792\n"
    "s_add_u32 s90, s90, s69\n"
    "v_mfma_f32_16x16x32_bf16 v[36:39], v[68:71], a[180:183], v[36:39]\n"
    "ds_read_b64_tr_b16 v[66:67], v114 offset:34304\n"
    "s_cmp_eq_u32 s90, s67\n"
    "v_mfma_f32_16x16x32_bf16 v[84:87], v[68:71], a[184:187], v[84:87]\n"
    "s_cselect_b32 s90, 0, s90\n"
    "v_mfma_f32_16x16x32_bf16 v[100:103], v[68:71], a[188:191], v[100:103]\n"
    "ds_read_b64_tr_b16 v[68:69], v115 offset:33792\n"
    "ds_read_b64_tr_b16 v[70:71], v115 offset:34304\n"
    "s_waitcnt lgkmcnt(13)\n"
    "v_mfma_f32_16x16x32_bf16 v[192:195], v[40:43], a[192:195], v[192:195]\n"
    "buffer_store_dwordx4 v[172:175], v2, s[8:11], s66 offen offset:3072 sc1\n"
    "v_mfma_f32_16x16x32_bf16 v[208:211], v[40:43], a[196:199], v[208:211]\n"
    "v_mfma_f32_16x16x32_bf16 v[224:227], v[40:43], a[200:203], v[224:227]\n"
    "v_mfma_f32_16x16x32_bf16 v[240:243], v[40:43], a[204:207], v[240:243]\n"
    "s_waitcnt lgkmcnt(12)\n"
    "v_mfma_f32_16x16x32_bf16 v[8:11], v[44:47], a[192:195], v[8:11]\n"
    "ds_read_b64_tr_b16 v[40:41], v112 offset:40960\n"
    "ds_read_b64_tr_b16 v[42:43], v112 offset:41472\n"
    "v_mfma_f32_16x16x32_bf16 v[24:27], v[44:47], a[196:199], v[24:27]\n"
    "v_mfma_f32_16x16x32_bf16 v[72:75], v[44:47], a[200:203], v[72:75]\n"
    "v_mfma_f32_16x16x32_bf16 v[88:91], v[44:47], a[204:207], v[88:91]\n"
    "s_waitcnt lgkmcnt(12)\n"
    "v_mfma_f32_16x16x32_bf16 v[196:199], v[48:51], a[192:195], v[196:199]\n"
    "ds_read_b64_tr_b16 v[44:45], v113 offset:40960\n"
    "ds_read_b64_tr_b16 v[46:47], v113 offset:41472\n"
    "v_mfma_f32_16x16x32_bf16 v[212:215], v[48:51], a[196:199], v[212:215]\n"
    "v_mfma_f32_16x16x32_bf16 v[228:231], v[48:51], a[200:203], v[228:231]\n"
    "s_add_u32 s66, s66, 0x1000\n"
    "v_mfma_f32_16x16x32_bf16 v[244:247], v[48:51], a[204:207], v[244:247]\n"
    "s_waitcnt lgkmcnt(12)\n"
    "v_mfma_f32_16x16x32_bf16 v[12:15], v[52:55], a[192:195], v[12:15]\n"
    "ds_read_b64_tr_b16 v[48:49], v114 offset:40960\n"
    "ds_read_b64_tr_b16 v[50:51], v114 offset:41472\n"
    "v_mfma_f32_16x16x32_bf16 v[28:31], v[52:55], a[196:199], v[28:31]\n"
    "v_mfma_f32_16x16x32_bf16 v[76:79], v[52:55], a[200:203], v[76:79]\n"
    "v_mfma_f32_16x16x32_bf16 v[92:95], v[52:55], a[204:207], v[92:95]\n"
    "s_waitcnt lgkmcnt(12)\n"
    "v_mfma_f32_16x16x32_bf16 v[200:203], v[56:59], a[192:195], v[200:203]\n"
    "ds_read_b64_tr_b16 v[52:53], v115 offset:40960\n"
    "ds_read_b64_tr_b16 v[54:55], v115 offset:41472\n"
    "v_mfma_f32_16x16x32_bf16 v[216:219], v[56:59], a[196:199], v[216:219]\n"
    "v_mfma_f32_16x16x32_bf16 v[232:235], v[56:59], a[200:203], v[232:235]\n"
    "v_mfma_f32_16x16x32_bf16 v[248:251], v[56:59], a[204:207], v[248:251]\n"
    "s_waitcnt lgkmcnt(12)\n"
    "v_mfma_f32_16x16x32_bf16 v[16:19], v[60:63], a[192:195], v[16:19]\n"
    "ds_read_b64_tr_b16 v[56:57], v112 offset:41984\n"
    "ds_read_b64_tr_b16 v[58:59], v112 offset:42496\n"
    "v_mfma_f32_16x16x32_bf16 v[32:35], v[60:63], a[196:199], v[32:35]\n"
    "buffer_store_dwordx4 v[176:179], v2, s[8:11], s66 offen sc1\n"
    "v_mfma_f32_16x16x32_bf16 v[80:83], v[60:63], a[200:203], v[80:83]\n"
    "v_mfma_f32_16x16x32_bf16 v[96:99], v[60:63], a[204:207], v[96:99]\n"
    "s_waitcnt lgkmcnt(12)\n"
    "v_mfma_f32_16x16x32_bf16 v[204:207], v[64:67], a[192:195], v[204:207]\n"
    "ds_read_b64_tr_b16 v[60:61], v113 offset:41984\n"
    "ds_read_b64_tr_b16 v[62:63], v113 offset:42496\n"
    "v_mfma_f32_16x16x32_bf16 v[220:223], v[64:67], a[196:199], v[220:223]\n"
    "v_mfma_f32_16x16x32_bf16 v[236:239], v[64:67], a[200:203], v[236:239]\n"
    "v_mfma_f32_16x16x32_bf16 v[252:255], v[64:67], a[204:207], v[252:255]\n"
    "s_waitcnt lgkmcnt(12)\n"
    "v_mfma_f32_16x16x32_bf16 v[20:23], v[68:71], a[192:195], v[20:23]\n"
    "ds_read_b64_tr_b16 v[64:65], v114 offset:41984\n"
    "ds_read_b64_tr_b16 v[66:67], v114 offset:42496\n"
    "v_mfma_f32_16x16x32_bf16 v[36:39], v[68:71], a[196:199], v[36:39]\n"
    "v_mfma_f32_16x16x32_bf16 v[84:87], v[68:71], a[200:203], v[84:87]\n"
    "v_mfma_f32_16x16x32_bf16 v[100:103], v[68:71], a[204:207], v[100:103]\n"
    "s_waitcnt lgkmcnt(12)\n"
    "v_mfma_f32_16x16x32_bf16 v[192:195], v[40:43], a[208:211], v[192:195]\n"
    "ds_read_b64_tr_b16 v[68:69], v115 offset:41984\n"
    "s_add_u32 m0, s91, 0x10000\n"
    "v_mfma_f32_16x16x32_bf16 v[208:211], v[40:43], a[212:215], v[208:211]\n"
    "ds_read_b64_tr_b16 v[70:71], v115 offset:42496\n"
    "v_mfma_f32_16x16x32_bf16 v[224:227], v[40:43], a[216:219], v[224:227]\n"
    "buffer_load_dwordx4 v110, s[8:11], s64 offen sc1 lds\n"
    "v_mfma_f32_16x16x32_bf16 v[240:243], v[40:43], a[220:223], v[240:243]\n"
    "s_waitcnt lgkmcnt(12)\n"
    "v_mfma_f32_16x16x32_bf16 v[8:11], v[44:47], a[208:211], v[8:11]\n"
    "ds_read_b64_tr_b16 v[40:41], v112 offset:49152\n"
    "ds_read_b64_tr_b16 v[42:43], v112 offset:49664\n"
    "v_mfma_f32_16x16x32_bf16 v[24:27], v[44:47], a[212:215], v[24:27]\n"
    "s_add_u32 m0, s91, 0x10400\n"
    "v_mfma_f32_16x16x32_bf16 v[72:75], v[44:47], a[216:219], v[72:75]\n"
    "v_mfma_f32_16x16x32_bf16 v[88:91], v[44:47], a[220:223], v[88:91]\n"
    "s_waitcnt lgkmcnt(12)\n"
    "v_mfma_f32_16x16x32_bf16 v[196:199], v[48:51], a[208:211], v[196:199]\n"
    "ds_read_b64_tr_b16 v[44:45], v113 offset:49152\n"
    "buffer_load_dwordx4 v111, s[8:11], s64 offen sc1 lds\n"
    "v_mfma_f32_16x16x32_bf16 v[212:215], v[48:51], a[212:215], v[212:215]\n"
    "ds_read_b64_tr_b16 v[46:47], v113 offset:49664\n"
    "v_mfma_f32_16x16x32_bf16 v[228:231], v[48:51], a[216:219], v[228:231]\n"
    "v_mfma_f32_16x16x32_bf16 v[244:247], v[48:51], a[220:223], v[244:247]\n"
    "s_add_u32 m0, s91, 0x10800\n"
    "s_waitcnt lgkmcnt(12)\n"
    "v_mfma_f32_16x16x32_bf16 v[12:15], v[52:55], a[208:211], v[12:15]\n"
    "ds_read_b64_tr_b16 v[48:49], v114 offset:49152\n"
    "ds_read_b64_tr_b16 v[50:51], v114 offset:49664\n"
    "v_mfma_f32_16x16x32_bf16 v[28:31], v[52:55], a[212:215], v[28:31]\n"
    "v_mfma_f32_16x16x32_bf16 v[76:79], v[52:55], a[216:219], v[76:79]\n"
    "s_add_u32 s83, s64, s62\n"
    "v_mfma_f32_16x16x32_bf16 v[92:95], v[52:55], a[220:223], v[92:95]\n"
    "s_waitcnt lgkmcnt(12)\n"
    "v_mfma_f32_16x16x32_bf16 v[200:203], v[56:59], a[208:211], v[200:203]\n"
    "ds_read_b64_tr_b16 v[52:53], v115 offset:49152\n"
    "ds_read_b64_tr_b16 v[54:55], v115 offset:49664\n"
    "v_mfma_f32_16x16x32_bf16 v[216:219], v[56:59], a[212:215], v[216:219]\n"
    "buffer_load_dwordx4 v110, s[8:11], s83 offen sc1 lds\n"
    "v_mfma_f32_16x16x32_bf16 v[232:235], v[56:59], a[216:219], v[232:235]\n"
    "v_mfma_f32_16x16x32_bf16 v[248:251], v[56:59], a[220:223], v[248:251]\n"
    "s_waitcnt lgkmcnt(12)\n"
    "v_mfma_f32_16x16x32_bf16 v[16:19], v[60:63], a[208:211], v[16:19]\n"
    "ds_read_b64_tr_b16 v[56:57], v112 offset:50176\n"
    "s_add_u32 m0, s91, 0x10c00\n"
    "v_mfma_f32_16x16x32_bf16 v[32:35], v[60:63], a[212:215], v[32:35]\n"
    "ds_read_b64_tr_b16 v[58:59], v112 offset:50688\n"
    "v_mfma_f32_16x16x32_bf16 v[80:83], v[60:63], a[216:219], v[80:83]\n"
    "v_mfma_f32_16x16x32_bf16 v[96:99], v[60:63], a[220:223], v[96:99]\n"
    "buffer_load_dwordx4 v111, s[8:11], s83 offen sc1 lds\n"
    "s_waitcnt lgkmcnt(12)\n"
    "v_mfma_f32_16x16x32_bf16 v[204:207], v[64:67], a[208:211], v[204:207]\n"
    "ds_read_b64_tr_b16 v[60:61], v113 offset:50176\n"
    "ds_read_b64_tr_b16 v[62:63], v113 offset:50688\n"
    "v_mfma_f32_16x16x32_bf16 v[220:223], v[64:67], a[212:215], v[220:223]\n"
    "v_mfma_f32_16x16x32_bf16 v[236:239], v[64:67], a[216:219], v[236:239]\n"
    "buffer_store_dwordx4 v[180:183], v2, s[8:11], s66 offen offset:1024 sc1\n"
    "v_mfma_f32_16x16x32_bf16 v[252:255], v[64:67], a[220:223], v[252:255]\n"
    "s_waitcnt lgkmcnt(12)\n"
    "v_mfma_f32_16x16x32_bf16 v[20:23], v[68:71], a[208:211], v[20:23]\n"
    "ds_read_b64_tr_b16 v[64:65], v114 offset:50176\n"
    "ds_read_b64_tr_b16 v[66:67], v114 offset:50688\n"
    "v_mfma_f32_16x16x32_bf16 v[36:39], v[68:71], a[212:215], v[36:39]\n"
    "buffer_store_dwordx4 v[184:187], v2, s[8:11], s66 offen offset:2048 sc1\n"
    "v_mfma_f32_16x16x32_bf16 v[84:87], v[68:71], a[216:219], v[84:87]\n"
    "v_mfma_f32_16x16x32_bf16 v[100:103], v[68:71], a[220:223], v[100:103]\n"
    "s_waitcnt lgkmcnt(12)\n"
    "v_mfma_f32_16x16x32_bf16 v[192:195], v[40:43], a[224:227], v[192:195]\n"
    "ds_read_b64_tr_b16 v[68:69], v115 offset:50176\n"
    "s_add_u32 m0, s91, 0x11000\n"
    "v_mfma_f32_16x16x32_bf16 v[208:211], v[40:43], a[228:231], v[208:211]\n"
    "ds_read_b64_tr_b16 v[70:71], v115 offset:50688\n"
    "v_mfma_f32_16x16x32_bf16 v[224:227], v[40:43], a[232:235], v[224:227]\n"
    "buffer_load_dwordx4 v125, s[8:11], s64 offen sc1 lds\n"
    "v_mfma_f32_16x16x32_bf16 v[240:243], v[40:43], a[236:239], v[240:243]\n"
    "s_waitcnt lgkmcnt(12)\n"
    "v_mfma_f32_16x16x32_bf16 v[8:11], v[44:47], a[224:227], v[8:11]\n"
    "ds_read_b64_tr_b16 v[40:41], v112 offset:57344\n"
    "s_add_u32 m0, s91, 0x11400\n"
    "v_mfma_f32_16x16x32_bf16 v[24:27], v[44:47], a[228:231], v[24:27]\n"
    "ds_read_b64_tr_b16 v[42:43], v112 offset:57856\n"
    "v_mfma_f32_16x16x32_bf16 v[72:75], v[44:47], a[232:235], v[72:75]\n"
    "buffer_load_dwordx4 v126, s[8:11], s64 offen sc1 lds\n"
    "v_mfma_f32_16x16x32_bf16 v[88:91], v[44:47], a[236:239], v[88:91]\n"
    "s_waitcnt lgkmcnt(12)\n"
    "v_mfma_f32_16x16x32_bf16 v[196:199], v[48:51], a[224:227], v[196:199]\n"
    "ds_read_b64_tr_b16 v[44:45], v113 offset:57344\n"
    "ds_read_b64_tr_b16 v[46:47], v113 offset:57856\n"
    "v_mfma_f32_16x16x32_bf16 v[212:215], v[48:51], a[228:231], v[212:215]\n"
    "s_add_u32 m0, s91, 0x11800\n"
    "v_mfma_f32_16x16x32_bf16 v[228:231], v[48:51], a[232:235], v[228:231]\n"
    "v_mfma_f32_16x16x32_bf16 v[244:247], v[48:51], a[236:239], v[244:247]\n"
    "s_add_u32 s83, s64, s62\n"
    "s_waitcnt lgkmcnt(12)\n"
    "v_mfma_f32_16x16x32_bf16 v[12:15], v[52:55], a[224:227], v[12:15]\n"
    "ds_read_b64_tr_b16 v[48:49], v114 offset:57344\n"
    "ds_read_b64_tr_b16 v[50:51], v114 offset:57856\n"
    "v_mfma_f32_16x16x32_bf16 v[28:31], v[52:55], a[228:231], v[28:31]\n"
    "buffer_load_dwordx4 v125, s[8:11], s83 offen sc1 lds\n"
    "v_mfma_f32_16x16x32_bf16 v[76:79], v[52:55], a[232:235], v[76:79]\n"
    "v_mfma_f32_16x16x32_bf16 v[92:95], v[52:55], a[236:239], v[92:95]\n"
    "s_waitcnt lgkmcnt(12)\n"
    "v_mfma_f32_16x16x32_bf16 v[200:203], v[56:59], a[224:227], v[200:203]\n"
    "ds_read_b64_tr_b16 v[52:53], v115 offset:57344\n"
    "s_add_u32 m0, s91, 0x11c00\n"
    "v_mfma_f32_16x16x32_bf16 v[216:219], v[56:59], a[228:231], v[216:219]\n"
    "ds_read_b64_tr_b16 v[54:55], v115 offset:57856\n"
    "v_mfma_f32_16x16x32_bf16 v[232:235], v[56:59], a[232:235], v[232:235]\n"
    "buffer_load_dwordx4 v126, s[8:11], s83 offen sc1 lds\n"
    "v_mfma_f32_16x16x32_bf16 v[248:251], v[56:59], a[236:239], v[248:251]\n"
    "s_waitcnt lgkmcnt(12)\n"
    "v_mfma_f32_16x16x32_bf16 v[16:19], v[60:63], a[224:227], v[16:19]\n"
    "ds_read_b64_tr_b16 v[56:57], v112 offset:58368\n"
    "s_add_u32 s64, s64, 0x80\n"
    "v_mfma_f32_16x16x32_bf16 v[32:35], v[60:63], a[228:231], v[32:35]\n"
    "ds_read_b64_tr_b16 v[58:59], v112 offset:58880\n"
    "v_mfma_f32_16x16x32_bf16 v[80:83], v[60:63], a[232:235], v[80:83]\n"
    "s_and_b32 s64, s64, 0x3ff\n"
    "v_mfma_f32_16x16x32_bf16 v[96:99], v[60:63], a[236:239], v[96:99]\n"
    "s_waitcnt lgkmcnt(12)\n"
    "v_mfma_f32_16x16x32_bf16 v[204:207], v[64:67], a[224:227], v[204:207]\n"
    "ds_read_b64_tr_b16 v[60:61], v113 offset:58368\n"
    "ds_read_b64_tr_b16 v[62:63], v113 offset:58880\n"
    "v_mfma_f32_16x16x32_bf16 v[220:223], v[64:67], a[228:231], v[220:223]\n"
    "buffer_store_dwordx4 v[188:191], v2, s[8:11], s66 offen offset:3072 sc1\n"
    "v_mfma_f32_16x16x32_bf16 v[236:239], v[64:67], a[232:235], v[236:239]\n"
    "v_mfma_f32_16x16x32_bf16 v[252:255], v[64:67], a[236:239], v[252:255]\n"
    "s_add_u32 s66, s66, 0x1000\n"
    "s_waitcnt lgkmcnt(12)\n"
    "v_mfma_f32_16x16x32_bf16 v[20:23], v[68:71], a[224:227], v[20:23]\n"
    "ds_read_b64_tr_b16 v[64:65], v114 offset:58368\n"
    "ds_read_b64_tr_b16 v[66:67], v114 offset:58880\n"
    "v_mfma_f32_16x16x32_bf16 v[36:39], v[68:71], a[228:231], v[36:39]\n"
    "s_sub_u32 s66, s66, 0x8000\n"
    "v_mfma_f32_16x16x32_bf16 v[84:87], v[68:71], a[232:235], v[84:87]\n"
    "v_mfma_f32_16x16x32_bf16 v[100:103], v[68:71], a[236:239], v[100:103]\n"
    "s_waitcnt lgkmcnt(12)\n"
    "v_mfma_f32_16x16x32_bf16 v[192:195], v[40:43], a[240:243], v[192:195]\n"
    "ds_read_b64_tr_b16 v[68:69], v115 offset:58368\n"
    "ds_read_b64_tr_b16 v[70:71], v115 offset:58880\n"
    "v_mfma_f32_16x16x32_bf16 v[208:211], v[40:43], a[244:247], v[208:211]\n"
    "v_mfma_f32_16x16x32_bf16 v[224:227], v[40:43], a[248:251], v[224:227]\n"
    "v_mfma_f32_16x16x32_bf16 v[240:243], v[40:43], a[252:255], v[240:243]\n"
    "s_waitcnt lgkmcnt(12)\n"
    "v_mfma_f32_16x16x32_bf16 v[8:11], v[44:47], a[240:243], v[8:11]\n"
    "v_mfma_f32_16x16x32_bf16 v[24:27], v[44:47], a[244:247], v[24:27]\n"
    "v_mfma_f32_16x16x32_bf16 v[72:75], v[44:47], a[248:251], v[72:75]\n"
    "s_waitcnt vmcnt(13)\n"
    "s_waitcnt lgkmcnt(0)\n"
    "s_barrier\n"
    "v_mfma_f32_16x16x32_bf16 v[88:91], v[44:47], a[252:255], v[88:91]\n"
    "ds_read_b64_tr_b16 v[40:41], v112\n"
    "s_add_u32 m0, s59, 0x8000\n"
    "v_mfma_f32_16x16x32_bf16 v[196:199], v[48:51], a[240:243], v[196:199]\n"
    "ds_read_b64_tr_b16 v[44:45], v113\n"
    "ds_read_b64_tr_b16 v[42:43], v112 offset:512\n"
    "v_mfma_f32_16x16x32_bf16 v[212:215], v[48:51], a[244:247], v[212:215]\n"
    "buffer_load_dwordx4 v106, s[16:19], s90 offen lds\n"
    "ds_read_b64_tr_b16 v[46:47], v113 offset:512\n"
    "v_mfma_f32_16x16x32_bf16 v[228:231], v[48:51], a[248:251], v[228:231]\n"
    "v_mfma_f32_16x16x32_bf16 v[244:247], v[48:51], a[252:255], v[244:247]\n"
    "ds_read_b128 v[128:131], v108 offset:32768\n"
    "s_add_u32 m0, s59, 0x8400\n"
    "v_mfma_f32_16x16x32_bf16 v[12:15], v[52:55], a[240:243], v[12:15]\n"
    "ds_read_b64_tr_b16 v[48:49], v114\n"
    "ds_read_b128 v[132:135], v108 offset:34816\n"
    "v_mfma_f32_16x16x32_bf16 v[28:31], v[52:55], a[244:247], v[28:31]\n"
    "v_cvt_pk_bf16_f32 v192, v192, v193\n"
    "v_cvt_pk_bf16_f32 v193, v194, v195\n"
    "v_mfma_f32_16x16x32_bf16 v[76:79], v[52:55], a[248:251], v[76:79]\n"
    "ds_read_b64_tr_b16 v[50:51], v114 offset:512\n"
    "v_cvt_pk_bf16_f32 v194, v8, v9\n"
    "v_mfma_f32_16x16x32_bf16 v[92:95], v[52:55], a[252:255], v[92:95]\n"
    "buffer_load_dwordx4 v107, s[16:19], s90 offen lds\n"
    "v_cvt_pk_bf16_f32 v195, v10, v11\n"
    "v_mfma_f32_16x16x32_bf16 v[200:203], v[56:59], a[240:243], v[200:203]\n"
    "ds_read_b64_tr_b16 v[52:53], v115\n"
    "v_cvt_pk_bf16_f32 v208, v208, v209\n"
    "v_mfma_f32_16x16x32_bf16 v[216:219], v[56:59], a[244:247], v[216:219]\n"
    "v_cvt_pk_bf16_f32 v209, v210, v211\n"
    "v_cvt_pk_bf16_f32 v210, v24, v25\n"
    "v_mfma_f32_16x16x32_bf16 v[232:235], v[56:59], a[248:251], v[232:235]\n"
    "v_cvt_pk_bf16_f32 v211, v26, v27\n"
    "s_add_u32 m0, s59, 0x8800\n"
    "v_mfma_f32_16x16x32_bf16 v[248:251], v[56:59], a[252:255], v[248:251]\n"
    "ds_read_b64_tr_b16 v[54:55], v115 offset:512\n"
    "v_cvt_pk_bf16_f32 v224, v224, v225\n"
    "v_mfma_f32_16x16x32_bf16 v[16:19], v[60:63], a[240:243], v[16:19]\n"
    "ds_read_b128 v[136:139], v108 offset:36864\n"
    "v_cvt_pk_bf16_f32 v225, v226, v227\n"
    "v_mfma_f32_16x16x32_bf16 v[32:35], v[60:63], a[244:247], v[32:35]\n"
    "ds_read_b64_tr_b16 v[56:57], v112 offset:1024\n"
    "v_cvt_pk_bf16_f32 v226, v72, v73\n"
    "v_mfma_f32_16x16x32_bf16 v[80:83], v[60:63], a[248:251], v[80:83]\n"
    "v_cvt_pk_bf16_f32 v227, v74, v75\n"
    "v_cvt_pk_bf16_f32 v240, v240, v241\n"
    "ds_read_b128 v[140:143], v108 offset:38912\n"
    "v_cvt_pk_bf16_f32 v241, v242, v243\n"
    "v_cvt_pk_bf16_f32 v242, v88, v89\n"
    "v_cvt_pk_bf16_f32 v243, v90, v91\n"
    "v_mfma_f32_16x16x32_bf16 v[96:99], v[60:63], a[252:255], v[96:99]\n"
    "ds_read_b64_tr_b16 v[58:59], v112 offset:1536\n"
    "v_cvt_pk_bf16_f32 v196, v196, v197\n"
    "v_mfma_f32_16x16x32_bf16 v[204:207], v[64:67], a[240:243], v[204:207]\n"
    "v_cvt_pk_bf16_f32 v197, v198, v199\n"
    "v_cvt_pk_bf16_f32 v198, v12, v13\n"
    "v_mfma_f32_16x16x32_bf16 v[220:223], v[64:67], a[244:247], v[220:223]\n"
    "ds_read_b64_tr_b16 v[60:61], v113 offset:1024\n"
    "v_cvt_pk_bf16_f32 v199, v14, v15\n"
    "v_mfma_f32_16x16x32_bf16 v[236:239], v[64:67], a[248:251], v[236:239]\n"
    "buffer_load_dwordx4 v121, s[16:19], s90 offen lds\n"
    "v_cvt_pk_bf16_f32 v212, v212, v213\n"
    "v_mfma_f32_16x16x32_bf16 v[252:255], v[64:67], a[252:255], v[252:255]\n"
    "v_cvt_pk_bf16_f32 v213, v214, v215\n"
    "v_cvt_pk_bf16_f32 v214, v28, v29\n"
    "v_mfma_f32_16x16x32_bf16 v[20:23], v[68:71], a[240:243], v[20:23]\n"
    "ds_read_b64_tr_b16 v[62:63], v113 offset:1536\n"
    "v_cvt_pk_bf16_f32 v215, v30, v31\n"
    "v_mfma_f32_16x16x32_bf16 v[36:39], v[68:71], a[244:247], v[36:39]\n"
    "ds_read_b64_tr_b16 v[64:65], v114 offset:1024\n"
    "v_cvt_pk_bf16_f32 v228, v228, v229\n"
    "v_mfma_f32_16x16x32_bf16 v[84:87], v[68:71], a[248:251], v[84:87]\n"
    "v_cvt_pk_bf16_f32 v229, v230, v231\n"
    "s_add_u32 m0, s59, 0x8c00\n"
    "v_cvt_pk_bf16_f32 v230, v76, v77\n"
    "v_cvt_pk_bf16_f32 v231, v78, v79\n"
    "v_cvt_pk_bf16_f32 v244, v244, v245\n"
    "v_cvt_pk_bf16_f32 v245, v246, v247\n"
    "v_cvt_pk_bf16_f32 v246, v92, v93\n"
    "v_cvt_pk_bf16_f32 v247, v94, v95\n"
    "v_mfma_f32_16x16x32_bf16 v[100:103], v[68:71], a[252:255], v[100:103]\n"
    "ds_read_b64_tr_b16 v[68:69], v115 offset:1024\n"
    "ds_read_b64_tr_b16 v[70:71], v115 offset:1536\n"
    "ds_read_b64_tr_b16 v[66:67], v114 offset:1536\n"
    "buffer_load_dwordx4 v123, s[16:19], s90 offen lds\n"
    "s_add_u32 s90, s90, s69\n"
    "s_cmp_eq_u32 s90, s67\n"
    "s_cselect_b32 s90, 0, s90\n"
    "s_waitcnt lgkmcnt(15)\n"
    "v_mfma_f32_16x16x32_bf16 a[0:3], v[40:43], v[128:131], 0\n"
    "s_add_u32 m0, s59, 0xc000\n"
    "s_waitcnt lgkmcnt(13)\n"
    "v_mfma_f32_16x16x32_bf16 a[32:35], v[40:43], v[132:135], 0\n"
    "v_cvt_pk_bf16_f32 v200, v200, v201\n"
    "v_cvt_pk_bf16_f32 v201, v202, v203\n"
    "s_waitcnt lgkmcnt(9)\n"
    "v_mfma_f32_16x16x32_bf16 a[64:67], v[40:43], v[136:139], 0\n"
    "v_cvt_pk_bf16_f32 v202, v16, v17\n"
    "v_cvt_pk_bf16_f32 v203, v18, v19\n"
    "s_waitcnt lgkmcnt(7)\n"
    "v_mfma_f32_16x16x32_bf16 a[96:99], v[40:43], v[140:143], 0\n"
    "v_cvt_pk_bf16_f32 v216, v216, v217\n"
    "v_cvt_pk_bf16_f32 v217, v218, v219\n"
    "v_mfma_f32_16x16x32_bf16 a[4:7], v[44:47], v[128:131], 0\n"
    "ds_read_b64_tr_b16 v[40:41], v112 offset:16384\n"
    "v_cvt_pk_bf16_f32 v218, v32, v33\n"
    "v_mfma_f32_16x16x32_bf16 a[36:39], v[44:47], v[132:135], 0\n"
    "buffer_load_dwordx4 v106, s[16:19], s90 offen lds\n"
    "v_cvt_pk_bf16_f32 v219, v34, v35\n"
    "v_mfma_f32_16x16x32_bf16 a[68:71], v[44:47], v[136:139], 0\n"
    "ds_read_b64_tr_b16 v[42:43], v112 offset:16896\n"
    "v_cvt_pk_bf16_f32 v232, v232, v233\n"
    "v_mfma_f32_16x16x32_bf16 a[100:103], v[44:47], v[140:143], 0\n"
    "v_cvt_pk_bf16_f32 v233, v234, v235\n"
    "v_cvt_pk_bf16_f32 v234, v80, v81\n"
    "v_mfma_f32_16x16x32_bf16 a[8:11], v[48:51], v[128:131], 0\n"
    "ds_read_b64_tr_b16 v[44:45], v113 offset:16384\n"
    "v_cvt_pk_bf16_f32 v235, v82, v83\n"
    "v_mfma_f32_16x16x32_bf16 a[40:43], v[48:51], v[132:135], 0\n"
    "v_cvt_pk_bf16_f32 v248, v248, v249\n"
    "v_cvt_pk_bf16_f32 v249, v250, v251\n"
    "v_mfma_f32_16x16x32_bf16 a[72:75], v[48:51], v[136:139], 0\n"
    "ds_read_b64_tr_b16 v[46:47], v113 offset:16896\n"
    "v_cvt_pk_bf16_f32 v250, v96, v97\n"
    "v_mfma_f32_16x16x32_bf16 a[104:107], v[48:51], v[140:143], 0\n"
    "ds_read_b128 v[144:147], v109 offset:32768\n"
    "v_cvt_pk_bf16_f32 v251, v98, v99\n"
    "v_mfma_f32_16x16x32_bf16 a[12:15], v[52:55], v[128:131], 0\n"
    "ds_read_b64_tr_b16 v[48:49], v114 offset:16384\n"
    "v_cvt_pk_bf16_f32 v204, v204, v205\n"
    "v_mfma_f32_16x16x32_bf16 a[44:47], v[52:55], v[132:135], 0\n"
    "v_cvt_pk_bf16_f32 v205, v206, v207\n"
    "v_cvt_pk_bf16_f32 v206, v20, v21\n"
    "v_mfma_f32_16x16x32_bf16 a[76:79], v[52:55], v[136:139], 0\n"
    "ds_read_b64_tr_b16 v[50:51], v114 offset:16896\n"
    "v_cvt_pk_bf16_f32 v207, v22, v23\n"
    "v_mfma_f32_16x16x32_bf16 a[108:111], v[52:55], v[140:143], 0\n"
    "v_cvt_pk_bf16_f32 v220, v220, v221\n"
    "s_add_u32 m0, s59, 0xc400\n"
    "s_waitcnt lgkmcnt(13)\n"
    "v_mfma_f32_16x16x32_bf16 a[16:19], v[56:59], v[128:131], 0\n"
    "ds_read_b64_tr_b16 v[52:53], v115 offset:16384\n"
    "v_cvt_pk_bf16_f32 v221, v222, v223\n"
    "v_mfma_f32_16x16x32_bf16 a[48:51], v[56:59], v[132:135], 0\n"
    "v_cvt_pk_bf16_f32 v222, v36, v37\n"
    "v_cvt_pk_bf16_f32 v223, v38, v39\n"
    "v_mfma_f32_16x16x32_bf16 a[80:83], v[56:59], v[136:139], 0\n"
    "v_cvt_pk_bf16_f32 v236, v236, v237\n"
    "v_cvt_pk_bf16_f32 v237, v238, v239\n"
    "v_mfma_f32_16x16x32_bf16 a[112:115], v[56:59], v[140:143], 0\n"
    "ds_read_b64_tr_b16 v[54:55], v115 offset:16896\n"
    "v_cvt_pk_bf16_f32 v238, v84, v85\n"
    "s_waitcnt lgkmcnt(13)\n"
    "v_mfma_f32_16x16x32_bf16 a[20:23], v[60:63], v[128:131], 0\n"
    "ds_read_b64_tr_b16 v[56:57], v112 offset:17408\n"
    "v_cvt_pk_bf16_f32 v239, v86, v87\n"
    "v_mfma_f32_16x16x32_bf16 a[52:55], v[60:63], v[132:135], 0\n"
    "v_cvt_pk_bf16_f32 v252, v252, v253\n"
    "v_cvt_pk_bf16_f32 v253, v254, v255\n"
    "v_mfma_f32_16x16x32_bf16 a[84:87], v[60:63], v[136:139], 0\n"
    "ds_read_b128 v[148:151], v109 offset:34816\n"
    "v_cvt_pk_bf16_f32 v254, v100, v101\n"
    "v_cvt_pk_bf16_f32 v255, v102, v103\n"
    "v_lshrrev_b32 v3, 4, v1\n"
    "v_lshl_add_u32 v100, v3, 10, v127\n"
    "v_xor_b32 v101, 32, v100\n"
    "v_xor_b32 v102, 64, v100\n"
    "v_mfma_f32_16x16x32_bf16 a[116:119], v[60:63], v[140:143], 0\n"
    "ds_read_b64_tr_b16 v[58:59], v112 offset:17920\n"
    "ds_read_b64_tr_b16 v[72:73], v100 offset:8192\n"
    "s_waitcnt lgkmcnt(13)\n"
    "v_mfma_f32_16x16x32_bf16 a[24:27], v[64:67], v[128:131], 0\n"
    "ds_read_b64_tr_b16 v[76:77], v101 offset:8192\n"
    "ds_read_b64_tr_b16 v[60:61], v113 offset:17408\n"
    "v_mfma_f32_16x16x32_bf16 a[56:59], v[64:67], v[132:135], 0\n"
    "ds_read_b64_tr_b16 v[80:81], v102 offset:8192\n"
    "ds_read_b64_tr_b16 v[74:75], v100 offset:8704\n"
    "v_mfma_f32_16x16x32_bf16 a[88:91], v[64:67], v[136:139], 0\n"
    "ds_read_b64_tr_b16 v[84:85], v100 offset:12288\n"
    "buffer_load_dwordx4 v107, s[16:19], s90 offen lds\n"
    "v_mfma_f32_16x16x32_bf16 a[120:123], v[64:67], v[140:143], 0\n"
    "ds_read_b64_tr_b16 v[62:63], v113 offset:17920\n"
    "ds_read_b64_tr_b16 v[88:89], v101 offset:12288\n"
    "v_mfma_f32_16x16x32_bf16 a[28:31], v[68:71], v[128:131], 0\n"
    "ds_read_b64_tr_b16 v[92:93], v102 offset:12288\n"
    "ds_read_b64_tr_b16 v[78:79], v101 offset:8704\n"
    "v_mfma_f32_16x16x32_bf16 a[60:63], v[68:71], v[132:135], 0\n"
    "ds_read_b64_tr_b16 v[64:65], v114 offset:17408\n"
    "s_add_u32 m0, s59, 0xc800\n"
    "v_mfma_f32_16x16x32_bf16 a[92:95], v[68:71], v[136:139], 0\n"
    "ds_read_b128 v[152:155], v109 offset:36864\n"
    "ds_read_b64_tr_b16 v[82:83], v102 offset:8704\n"
    "v_mfma_f32_16x16x32_bf16 a[124:127], v[68:71], v[140:143], 0\n"
    "ds_read_b64_tr_b16 v[66:67], v114 offset:17920\n"
    "buffer_load_dwordx4 v121, s[16:19], s90 offen lds\n"
    "s_waitcnt lgkmcnt(9)\n"
    "v_mfma_f32_16x16x32_bf16 a[160:163], v[72:75], v[128:131], 0\n"
    "ds_read_b64_tr_b16 v[68:69], v115 offset:17408\n"
    "ds_read_b64_tr_b16 v[86:87], v100 offset:12800\n"
    "v_mfma_f32_16x16x32_bf16 a[184:187], v[72:75], v[132:135], 0\n"
    "ds_read_b64_tr_b16 v[90:91], v101 offset:12800\n"
    "s_add_u32 m0, s59, 0xcc00\n"
    "v_mfma_f32_16x16x32_bf16 a[208:211], v[72:75], v[136:139], 0\n"
    "ds_read_b64_tr_b16 v[70:71], v115 offset:17920\n"
    "ds_read_b128 v[156:159], v109 offset:38912\n"
    "v_mfma_f32_16x16x32_bf16 a[232:235], v[72:75], v[140:143], 0\n"
    "ds_read_b64_tr_b16 v[94:95], v102 offset:12800\n"
    "buffer_load_dwordx4 v123, s[16:19], s90 offen lds\n"
    "s_waitcnt lgkmcnt(10)\n"
    "v_mfma_f32_16x16x32_bf16 a[164:167], v[76:79], v[128:131], 0\n"
    "ds_read_b64_tr_b16 v[72:73], v100 offset:24576\n"
    "ds_read_b64_tr_b16 v[74:75], v100 offset:25088\n"
    "v_mfma_f32_16x16x32_bf16 a[188:191], v[76:79], v[132:135], 0\n"
    "v_mfma_f32_16x16x32_bf16 a[212:215], v[76:79], v[136:139], 0\n"
    "v_mfma_f32_16x16x32_bf16 a[236:239], v[76:79], v[140:143], 0\n"
    "s_waitcnt lgkmcnt(9)\n"
    "v_mfma_f32_16x16x32_bf16 a[168:171], v[80:83], v[128:131], 0\n"
    "ds_read_b64_tr_b16 v[76:77], v101 offset:24576\n"
    "s_add_u32 s90, s90, s69\n"
    "v_mfma_f32_16x16x32_bf16 a[192:195], v[80:83], v[132:135], 0\n"
    "ds_read_b64_tr_b16 v[78:79], v101 offset:25088\n"
    "v_mfma_f32_16x16x32_bf16 a[216:219], v[80:83], v[136:139], 0\n"
    "v_mfma_f32_16x16x32_bf16 a[240:243], v[80:83], v[140:143], 0\n"
    "s_waitcnt lgkmcnt(8)\n"
    "v_mfma_f32_16x16x32_bf16 a[172:175], v[84:87], v[128:131], 0\n"
    "ds_read_b64_tr_b16 v[80:81], v102 offset:24576\n"
    "ds_read_b64_tr_b16 v[82:83], v102 offset:25088\n"
    "v_mfma_f32_16x16x32_bf16 a[196:199], v[84:87], v[132:135], 0\n"
    "s_cmp_eq_u32 s90, s67\n"
    "v_mfma_f32_16x16x32_bf16 a[220:223], v[84:87], v[136:139], 0\n"
    "v_mfma_f32_16x16x32_bf16 a[244:247], v[84:87], v[140:143], 0\n"
    "s_waitcnt lgkmcnt(9)\n"
    "v_mfma_f32_16x16x32_bf16 a[176:179], v[88:91], v[128:131], 0\n"
    "ds_read_b64_tr_b16 v[84:85], v100 offset:28672\n"
    "ds_read_b64_tr_b16 v[86:87], v100 offset:29184\n"
    "v_mfma_f32_16x16x32_bf16 a[200:203], v[88:91], v[132:135], 0\n"
    "v_mfma_f32_16x16x32_bf16 a[224:227], v[88:91], v[136:139], 0\n"
    "s_cselect_b32 s90, 0, s90\n"
    "v_mfma_f32_16x16x32_bf16 a[248:251], v[88:91], v[140:143], 0\n"
    "s_waitcnt lgkmcnt(8)\n"
    "v_mfma_f32_16x16x32_bf16 a[180:183], v[92:95], v[128:131], 0\n"
    "ds_read_b64_tr_b16 v[88:89], v101 offset:28672\n"
    "ds_read_b64_tr_b16 v[90:91], v101 offset:29184\n"
    "v_mfma_f32_16x16x32_bf16 a[204:207], v[92:95], v[132:135], 0\n"
    "v_mfma_f32_16x16x32_bf16 a[228:231], v[92:95], v[136:139], 0\n"
    "v_mfma_f32_16x16x32_bf16 a[252:255], v[92:95], v[140:143], 0\n"
    "v_mfma_f32_16x16x32_bf16 a[0:3], v[40:43], v[144:147], a[0:3]\n"
    "ds_read_b64_tr_b16 v[92:93], v102 offset:28672\n"
    "ds_read_b64_tr_b16 v[94:95], v102 offset:29184\n"
    "v_mfma_f32_16x16x32_bf16 a[32:35], v[40:43], v[148:151], a[32:35]\n"
    "v_mfma_f32_16x16x32_bf16 a[64:67], v[40:43], v[152:155], a[64:67]\n"
    "v_mfma_f32_16x16x32_bf16 a[96:99], v[40:43], v[156:159], a[96:99]\n"
    "v_mfma_f32_16x16x32_bf16 a[4:7], v[44:47], v[144:147], a[4:7]\n"
    "v_mfma_f32_16x16x32_bf16 a[36:39], v[44:47], v[148:151], a[36:39]\n"
    "v_mfma_f32_16x16x32_bf16 a[68:71], v[44:47], v[152:155], a[68:71]\n"
    "s_waitcnt vmcnt(0)\n"
    "s_waitcnt lgkmcnt(0)\n"
    "s_barrier\n"
    "v_mfma_f32_16x16x32_bf16 a[100:103], v[44:47], v[156:159], a[100:103]\n"
    "ds_read_b64_tr_b16 v[40:41], v112 offset:32768\n"
    "buffer_store_dwordx4 v[192:195], v2, s[8:11], s66 offen sc1\n"
    "v_mfma_f32_16x16x32_bf16 a[8:11], v[48:51], v[144:147], a[8:11]\n"
    "ds_read_b64_tr_b16 v[44:45], v113 offset:32768\n"
    "s_add_u32 m0, s59, 0\n"
    "v_mfma_f32_16x16x32_bf16 a[40:43], v[48:51], v[148:151], a[40:43]\n"
    "ds_read_b64_tr_b16 v[42:43], v112 offset:33280\n"
    "ds_read_b64_tr_b16 v[46:47], v113 offset:33280\n"
    "v_mfma_f32_16x16x32_bf16 a[72:75], v[48:51], v[152:155], a[72:75]\n"
    "buffer_load_dwordx4 v106, s[16:19], s90 offen lds\n"
    "v_mfma_f32_16x16x32_bf16 a[104:107], v[48:51], v[156:159], a[104:107]\n"
    "ds_read_b128 v[128:131], v119\n"
    "ds_read_b128 v[132:135], v119 offset:2048\n"
    "v_mfma_f32_16x16x32_bf16 a[12:15], v[52:55], v[144:147], a[12:15]\n"
    "ds_read_b64_tr_b16 v[48:49], v114 offset:32768\n"
    "s_add_u32 m0, s59, 0x400\n"
    "v_mfma_f32_16x16x32_bf16 a[44:47], v[52:55], v[148:151], a[44:47]\n"
    "ds_read_b64_tr_b16 v[50:51], v114 offset:33280\n"
    "ds_read_b128 v[136:139], v119 offset:4096\n"
    "v_mfma_f32_16x16x32_bf16 a[76:79], v[52:55], v[152:155], a[76:79]\n"
    "buffer_load_dwordx4 v107, s[16:19], s90 offen lds\n"
    "ds_read_b128 v[140:143], v119 offset:6144\n"
    "v_mfma_f32_16x16x32_bf16 a[108:111], v[52:55], v[156:159], a[108:111]\n"
    "v_mfma_f32_16x16x32_bf16 a[16:19], v[56:59], v[144:147], a[16:19]\n"
    "ds_read_b64_tr_b16 v[52:53], v115 offset:32768\n"
    "s_add_u32 m0, s59, 0x800\n"
    "v_mfma_f32_16x16x32_bf16 a[48:51], v[56:59], v[148:151], a[48:51]\n"
    "ds_read_b64_tr_b16 v[54:55], v115 offset:33280\n"
    "v_mfma_f32_16x16x32_bf16 a[80:83], v[56:59], v[152:155], a[80:83]\n"
    "buffer_load_dwordx4 v121, s[16:19], s90 offen lds\n"
    "v_mfma_f32_16x16x32_bf16 a[112:115], v[56:59], v[156:159], a[112:115]\n"
    "v_mfma_f32_16x16x32_bf16 a[20:23], v[60:63], v[144:147], a[20:23]\n"
    "ds_read_b64_tr_b16 v[56:57], v112 offset:33792\n"
    "s_add_u32 m0, s59, 0xc00\n"
    "v_mfma_f32_16x16x32_bf16 a[52:55], v[60:63], v[148:151], a[52:55]\n"
    "ds_read_b64_tr_b16 v[58:59], v112 offset:34304\n"
    "v_mfma_f32_16x16x32_bf16 a[84:87], v[60:63], v[152:155], a[84:87]\n"
    "buffer_load_dwordx4 v123, s[16:19], s90 offen lds\n"
    "v_mfma_f32_16x16x32_bf16 a[116:119], v[60:63], v[156:159], a[116:119]\n"
    "v_mfma_f32_16x16x32_bf16 a[24:27], v[64:67], v[144:147], a[24:27]\n"
    "ds_read_b64_tr_b16 v[60:61], v113 offset:33792\n"
    "s_add_u32 s90, s90, s69\n"
    "v_mfma_f32_16x16x32_bf16 a[56:59], v[64:67], v[148:151], a[56:59]\n"
    "ds_read_b64_tr_b16 v[62:63], v113 offset:34304\n"
    "v_mfma_f32_16x16x32_bf16 a[88:91], v[64:67], v[152:155], a[88:91]\n"
    "s_cmp_eq_u32 s90, s67\n"
    "v_mfma_f32_16x16x32_bf16 a[120:123], v[64:67], v[156:159], a[120:123]\n"
    "v_mfma_f32_16x16x32_bf16 a[28:31], v[68:71], v[144:147], a[28:31]\n"
    "ds_read_b64_tr_b16 v[64:65], v114 offset:33792\n"
    "s_cselect_b32 s90, 0, s90\n"
    "v_mfma_f32_16x16x32_bf16 a[60:63], v[68:71], v[148:151], a[60:63]\n"
    "ds_read_b64_tr_b16 v[66:67], v114 offset:34304\n"
    "v_mfma_f32_16x16x32_bf16 a[92:95], v[68:71], v[152:155], a[92:95]\n"
    "buffer_store_dwordx4 v[196:199], v2, s[8:11], s66 offen offset:1024 sc1\n"
    "v_mfma_f32_16x16x32_bf16 a[124:127], v[68:71], v[156:159], a[124:127]\n"
    "v_mfma_f32_16x16x32_bf16 a[160:163], v[72:75], v[144:147], a[160:163]\n"
    "ds_read_b64_tr_b16 v[68:69], v115 offset:33792\n"
    "s_add_u32 m0, s59, 0x4000\n"
    "v_mfma_f32_16x16x32_bf16 a[184:187], v[72:75], v[148:151], a[184:187]\n"
    "ds_read_b64_tr_b16 v[70:71], v115 offset:34304\n"
    "v_mfma_f32_16x16x32_bf16 a[208:211], v[72:75], v[152:155], a[208:211]\n"
    "buffer_load_dwordx4 v106, s[16:19], s90 offen lds\n"
    "v_mfma_f32_16x16x32_bf16 a[232:235], v[72:75], v[156:159], a[232:235]\n"
    "v_mfma_f32_16x16x32_bf16 a[164:167], v[76:79], v[144:147], a[164:167]\n"
    "ds_read_b64_tr_b16 v[72:73], v100 offset:40960\n"
    "s_add_u32 m0, s59, 0x4400\n"
    "v_mfma_f32_16x16x32_bf16 a[188:191], v[76:79], v[148:151], a[188:191]\n"
    "ds_read_b64_tr_b16 v[74:75], v100 offset:41472\n"
    "v_mfma_f32_16x16x32_bf16 a[212:215], v[76:79], v[152:155], a[212:215]\n"
    "buffer_load_dwordx4 v107, s[16:19], s90 offen lds\n"
    "v_mfma_f32_16x16x32_bf16 a[236:239], v[76:79], v[156:159], a[236:239]\n"
    "v_mfma_f32_16x16x32_bf16 a[168:171], v[80:83], v[144:147], a[168:171]\n"
    "ds_read_b64_tr_b16 v[76:77], v101 offset:40960\n"
    "s_add_u32 m0, s59, 0x4800\n"
    "v_mfma_f32_16x16x32_bf16 a[192:195], v[80:83], v[148:151], a[192:195]\n"
    "ds_read_b64_tr_b16 v[78:79], v101 offset:41472\n"
    "v_mfma_f32_16x16x32_bf16 a[216:219], v[80:83], v[152:155], a[216:219]\n"
    "buffer_load_dwordx4 v121, s[16:19], s90 offen lds\n"
    "v_mfma_f32_16x16x32_bf16 a[240:243], v[80:83], v[156:159], a[240:243]\n"
    "v_mfma_f32_16x16x32_bf16 a[172:175], v[84:87], v[144:147], a[172:175]\n"
    "ds_read_b64_tr_b16 v[80:81], v102 offset:40960\n"
    "s_add_u32 m0, s59, 0x4c00\n"
    "v_mfma_f32_16x16x32_bf16 a[196:199], v[84:87], v[148:151], a[196:199]\n"
    "ds_read_b64_tr_b16 v[82:83], v102 offset:41472\n"
    "v_mfma_f32_16x16x32_bf16 a[220:223], v[84:87], v[152:155], a[220:223]\n"
    "buffer_load_dwordx4 v123, s[16:19], s90 offen lds\n"
    "v_mfma_f32_16x16x32_bf16 a[244:247], v[84:87], v[156:159], a[244:247]\n"
    "v_mfma_f32_16x16x32_bf16 a[176:179], v[88:91], v[144:147], a[176:179]\n"
    "ds_read_b64_tr_b16 v[84:85], v100 offset:45056\n"
    "s_add_u32 s90, s90, s69\n"
    "v_mfma_f32_16x16x32_bf16 a[200:203], v[88:91], v[148:151], a[200:203]\n"
    "ds_read_b64_tr_b16 v[86:87], v100 offset:45568\n"
    "v_mfma_f32_16x16x32_bf16 a[224:227], v[88:91], v[152:155], a[224:227]\n"
    "s_cmp_eq_u32 s90, s67\n"
    "v_mfma_f32_16x16x32_bf16 a[248:251], v[88:91], v[156:159], a[248:251]\n"
    "v_mfma_f32_16x16x32_bf16 a[180:183], v[92:95], v[144:147], a[180:183]\n"
    "ds_read_b64_tr_b16 v[88:89], v101 offset:45056\n"
    "s_cselect_b32 s90, 0, s90\n"
    "v_mfma_f32_16x16x32_bf16 a[204:207], v[92:95], v[148:151], a[204:207]\n"
    "ds_read_b64_tr_b16 v[90:91], v101 offset:45568\n"
    "v_mfma_f32_16x16x32_bf16 a[228:231], v[92:95], v[152:155], a[228:231]\n"
    "buffer_store_dwordx4 v[200:203], v2, s[8:11], s66 offen offset:2048 sc1\n"
    "v_mfma_f32_16x16x32_bf16 a[252:255], v[92:95], v[156:159], a[252:255]\n"
    "ds_read_b64_tr_b16 v[92:93], v102 offset:45056\n"
    "ds_read_b64_tr_b16 v[94:95], v102 offset:45568\n"
    "s_waitcnt lgkmcnt(15)\n"
    "v_mfma_f32_16x16x32_bf16 a[0:3], v[40:43], v[128:131], a[0:3]\n"
    "s_add_u32 m0, s91, 0x18000\n"
    "v_mfma_f32_16x16x32_bf16 a[32:35], v[40:43], v[132:135], a[32:35]\n"
    "v_mfma_f32_16x16x32_bf16 a[64:67], v[40:43], v[136:139], a[64:67]\n"
    "buffer_load_dwordx4 v110, s[8:11], s64 offen sc1 lds\n"
    "v_mfma_f32_16x16x32_bf16 a[96:99], v[40:43], v[140:143], a[96:99]\n"
    "v_mfma_f32_16x16x32_bf16 a[4:7], v[44:47], v[128:131], a[4:7]\n"
    "ds_read_b64_tr_b16 v[40:41], v112 offset:49152\n"
    "ds_read_b64_tr_b16 v[42:43], v112 offset:49664\n"
    "v_mfma_f32_16x16x32_bf16 a[36:39], v[44:47], v[132:135], a[36:39]\n"
    "s_add_u32 m0, s91, 0x18400\n"
    "v_mfma_f32_16x16x32_bf16 a[68:71], v[44:47], v[136:139], a[68:71]\n"
    "v_mfma_f32_16x16x32_bf16 a[100:103], v[44:47], v[140:143], a[100:103]\n"
    "buffer_load_dwordx4 v111, s[8:11], s64 offen sc1 lds\n"
    "v_mfma_f32_16x16x32_bf16 a[8:11], v[48:51], v[128:131], a[8:11]\n"
    "ds_read_b64_tr_b16 v[44:45], v113 offset:49152\n"
    "ds_read_b64_tr_b16 v[46:47], v113 offset:49664\n"
    "v_mfma_f32_16x16x32_bf16 a[40:43], v[48:51], v[132:135], a[40:43]\n"
    "v_mfma_f32_16x16x32_bf16 a[72:75], v[48:51], v[136:139], a[72:75]\n"
    "ds_read_b128 v[144:147], v120\n"
    "s_add_u32 m0, s91, 0x18800\n"
    "v_mfma_f32_16x16x32_bf16 a[104:107], v[48:51], v[140:143], a[104:107]\n"
    "ds_read_b128 v[148:151], v120 offset:2048\n"
    "ds_read_b128 v[152:155], v120 offset:4096\n"
    "v_mfma_f32_16x16x32_bf16 a[12:15], v[52:55], v[128:131], a[12:15]\n"
    "ds_read_b64_tr_b16 v[48:49], v114 offset:49152\n"
    "s_add_u32 s83, s64, s62\n"
    "v_mfma_f32_16x16x32_bf16 a[44:47], v[52:55], v[132:135], a[44:47]\n"
    "ds_read_b64_tr_b16 v[50:51], v114 offset:49664\n"
    "ds_read_b128 v[156:159], v120 offset:6144\n"
    "v_mfma_f32_16x16x32_bf16 a[76:79], v[52:55], v[136:139], a[76:79]\n"
    "v_mfma_f32_16x16x32_bf16 a[108:111], v[52:55], v[140:143], a[108:111]\n"
    "buffer_load_dwordx4 v110, s[8:11], s83 offen sc1 lds\n"
    "v_mfma_f32_16x16x32_bf16 a[16:19], v[56:59], v[128:131], a[16:19]\n"
    "ds_read_b64_tr_b16 v[52:53], v115 offset:49152\n"
    "ds_read_b64_tr_b16 v[54:55], v115 offset:49664\n"
    "v_mfma_f32_16x16x32_bf16 a[48:51], v[56:59], v[132:135], a[48:51]\n"
    "s_add_u32 m0, s91, 0x18c00\n"
    "v_mfma_f32_16x16x32_bf16 a[80:83], v[56:59], v[136:139], a[80:83]\n"
    "v_mfma_f32_16x16x32_bf16 a[112:115], v[56:59], v[140:143], a[112:115]\n"
    "v_mfma_f32_16x16x32_bf16 a[20:23], v[60:63], v[128:131], a[20:23]\n"
    "ds_read_b64_tr_b16 v[56:57], v112 offset:50176\n"
    "buffer_load_dwordx4 v111, s[8:11], s83 offen sc1 lds\n"
    "v_mfma_f32_16x16x32_bf16 a[52:55], v[60:63], v[132:135], a[52:55]\n"
    "ds_read_b64_tr_b16 v[58:59], v112 offset:50688\n"
    "v_mfma_f32_16x16x32_bf16 a[84:87], v[60:63], v[136:139], a[84:87]\n"
    "s_add_u32 m0, s91, 0x19000\n"
    "v_mfma_f32_16x16x32_bf16 a[116:119], v[60:63], v[140:143], a[116:119]\n"
    "s_waitcnt lgkmcnt(15)\n"
    "v_mfma_f32_16x16x32_bf16 a[24:27], v[64:67], v[128:131], a[24:27]\n"
    "ds_read_b64_tr_b16 v[60:61], v113 offset:50176\n"
    "ds_read_b64_tr_b16 v[62:63], v113 offset:50688\n"
    "v_mfma_f32_16x16x32_bf16 a[56:59], v[64:67], v[132:135], a[56:59]\n"
    "buffer_load_dwordx4 v125, s[8:11], s64 offen sc1 lds\n"
    "v_mfma_f32_16x16x32_bf16 a[88:91], v[64:67], v[136:139], a[88:91]\n"
    "v_mfma_f32_16x16x32_bf16 a[120:123], v[64:67], v[140:143], a[120:123]\n"
    "v_mfma_f32_16x16x32_bf16 a[28:31], v[68:71], v[128:131], a[28:31]\n"
    "ds_read_b64_tr_b16 v[64:65], v114 offset:50176\n"
    "s_add_u32 m0, s91, 0x19400\n"
    "v_mfma_f32_16x16x32_bf16 a[60:63], v[68:71], v[132:135], a[60:63]\n"
    "ds_read_b64_tr_b16 v[66:67], v114 offset:50688\n"
    "v_mfma_f32_16x16x32_bf16 a[92:95], v[68:71], v[136:139], a[92:95]\n"
    "buffer_load_dwordx4 v126, s[8:11], s64 offen sc1 lds\n"
    "v_mfma_f32_16x16x32_bf16 a[124:127], v[68:71], v[140:143], a[124:127]\n"
    "v_mfma_f32_16x16x32_bf16 a[160:163], v[72:75], v[128:131], a[160:163]\n"
    "ds_read_b64_tr_b16 v[68:69], v115 offset:50176\n"
    "ds_read_b64_tr_b16 v[70:71], v115 offset:50688\n"
    "v_mfma_f32_16x16x32_bf16 a[184:187], v[72:75], v[132:135], a[184:187]\n"
    "s_add_u32 m0, s91, 0x19800\n"
    "v_mfma_f32_16x16x32_bf16 a[208:211], v[72:75], v[136:139], a[208:211]\n"
    "v_mfma_f32_16x16x32_bf16 a[232:235], v[72:75], v[140:143], a[232:235]\n"
    "s_add_u32 s83, s64, s62\n"
    "v_mfma_f32_16x16x32_bf16 a[164:167], v[76:79], v[128:131], a[164:167]\n"
    "ds_read_b64_tr_b16 v[72:73], v100 offset:57344\n"
    "ds_read_b64_tr_b16 v[74:75], v100 offset:57856\n"
    "v_mfma_f32_16x16x32_bf16 a[188:191], v[76:79], v[132:135], a[188:191]\n"
    "v_mfma_f32_16x16x32_bf16 a[212:215], v[76:79], v[136:139], a[212:215]\n"
    "buffer_load_dwordx4 v125, s[8:11], s83 offen sc1 lds\n"
    "v_mfma_f32_16x16x32_bf16 a[236:239], v[76:79], v[140:143], a[236:239]\n"
    "v_mfma_f32_16x16x32_bf16 a[168:171], v[80:83], v[128:131], a[168:171]\n"
    "ds_read_b64_tr_b16 v[76:77], v101 offset:57344\n"
    "s_add_u32 m0, s91, 0x19c00\n"
    "v_mfma_f32_16x16x32_bf16 a[192:195], v[80:83], v[132:135], a[192:195]\n"
    "ds_read_b64_tr_b16 v[78:79], v101 offset:57856\n"
    "v_mfma_f32_16x16x32_bf16 a[216:219], v[80:83], v[136:139], a[216:219]\n"
    "v_mfma_f32_16x16x32_bf16 a[240:243], v[80:83], v[140:143], a[240:243]\n"
    "buffer_load_dwordx4 v126, s[8:11], s83 offen sc1 lds\n"
    "v_mfma_f32_16x16x32_bf16 a[172:175], v[84:87], v[128:131], a[172:175]\n"
    "ds_read_b64_tr_b16 v[80:81], v102 offset:57344\n"
    "ds_read_b64_tr_b16 v[82:83], v102 offset:57856\n"
    "v_mfma_f32_16x16x32_bf16 a[196:199], v[84:87], v[132:135], a[196:199]\n"
    "s_add_u32 s64, s64, 0x80\n"
    "v_mfma_f32_16x16x32_bf16 a[220:223], v[84:87], v[136:139], a[220:223]\n"
    "v_mfma_f32_16x16x32_bf16 a[244:247], v[84:87], v[140:143], a[244:247]\n"
    "v_mfma_f32_16x16x32_bf16 a[176:179], v[88:91], v[128:131], a[176:179]\n"
    "ds_read_b64_tr_b16 v[84:85], v100 offset:61440\n"
    "s_and_b32 s64, s64, 0x3ff\n"
    "v_mfma_f32_16x16x32_bf16 a[200:203], v[88:91], v[132:135], a[200:203]\n"
    "ds_read_b64_tr_b16 v[86:87], v100 offset:61952\n"
    "v_mfma_f32_16x16x32_bf16 a[224:227], v[88:91], v[136:139], a[224:227]\n"
    "buffer_store_dwordx4 v[204:207], v2, s[8:11], s66 offen offset:3072 sc1\n"
    "v_mfma_f32_16x16x32_bf16 a[248:251], v[88:91], v[140:143], a[248:251]\n"
    "s_waitcnt lgkmcnt(15)\n"
    "v_mfma_f32_16x16x32_bf16 a[180:183], v[92:95], v[128:131], a[180:183]\n"
    "ds_read_b64_tr_b16 v[88:89], v101 offset:61440\n"
    "ds_read_b64_tr_b16 v[90:91], v101 offset:61952\n"
    "v_mfma_f32_16x16x32_bf16 a[204:207], v[92:95], v[132:135], a[204:207]\n"
    "s_add_u32 s66, s66, 0x1000\n"
    "v_mfma_f32_16x16x32_bf16 a[228:231], v[92:95], v[136:139], a[228:231]\n"
    "v_mfma_f32_16x16x32_bf16 a[252:255], v[92:95], v[140:143], a[252:255]\n"
    "v_mfma_f32_16x16x32_bf16 a[0:3], v[40:43], v[144:147], a[0:3]\n"
    "ds_read_b64_tr_b16 v[92:93], v102 offset:61440\n"
    "ds_read_b64_tr_b16 v[94:95], v102 offset:61952\n"
    "v_mfma_f32_16x16x32_bf16 a[32:35], v[40:43], v[148:151], a[32:35]\n"
    "v_mfma_f32_16x16x32_bf16 a[64:67], v[40:43], v[152:155], a[64:67]\n"
    "v_mfma_f32_16x16x32_bf16 a[96:99], v[40:43], v[156:159], a[96:99]\n"
    "v_mfma_f32_16x16x32_bf16 a[4:7], v[44:47], v[144:147], a[4:7]\n"
    "v_mfma_f32_16x16x32_bf16 a[36:39], v[44:47], v[148:151], a[36:39]\n"
    "v_mfma_f32_16x16x32_bf16 a[68:71], v[44:47], v[152:155], a[68:71]\n"
    "s_waitcnt vmcnt(10)\n"
    "s_waitcnt lgkmcnt(0)\n"
    "s_barrier\n"
    "v_mfma_f32_16x16x32_bf16 a[100:103], v[44:47], v[156:159], a[100:103]\n"
    "ds_read_b64_tr_b16 v[40:41], v112\n"
    "s_add_u32 m0, s59, 0x8000\n"
    "v_mfma_f32_16x16x32_bf16 a[8:11], v[48:51], v[144:147], a[8:11]\n"
    "ds_read_b64_tr_b16 v[44:45], v113\n"
    "ds_read_b64_tr_b16 v[42:43], v112 offset:512\n"
    "v_mfma_f32_16x16x32_bf16 a[40:43], v[48:51], v[148:151], a[40:43]\n"
    "buffer_load_dwordx4 v106, s[16:19], s90 offen lds\n"
    "ds_read_b64_tr_b16 v[46:47], v113 offset:512\n"
    "v_mfma_f32_16x16x32_bf16 a[72:75], v[48:51], v[152:155], a[72:75]\n"
    "v_mfma_f32_16x16x32_bf16 a[104:107], v[48:51], v[156:159], a[104:107]\n"
    "ds_read_b128 v[128:131], v108\n"
    "s_add_u32 m0, s59, 0x8400\n"
    "v_mfma_f32_16x16x32_bf16 a[12:15], v[52:55], v[144:147], a[12:15]\n"
    "ds_read_b64_tr_b16 v[48:49], v114\n"
    "ds_read_b64_tr_b16 v[50:51], v114 offset:512\n"
    "v_mfma_f32_16x16x32_bf16 a[44:47], v[52:55], v[148:151], a[44:47]\n"
    "buffer_load_dwordx4 v107, s[16:19], s90 offen lds\n"
    "ds_read_b128 v[132:135], v108 offset:2048\n"
    "v_mfma_f32_16x16x32_bf16 a[76:79], v[52:55], v[152:155], a[76:79]\n"
    "ds_read_b128 v[136:139], v108 offset:4096\n"
    "ds_read_b128 v[140:143], v108 offset:6144\n"
    "v_mfma_f32_16x16x32_bf16 a[108:111], v[52:55], v[156:159], a[108:111]\n"
    "s_add_u32 m0, s59, 0x8800\n"
    "v_mfma_f32_16x16x32_bf16 a[16:19], v[56:59], v[144:147], a[16:19]\n"
    "ds_read_b64_tr_b16 v[52:53], v115\n"
    "ds_read_b64_tr_b16 v[54:55], v115 offset:512\n"
    "v_mfma_f32_16x16x32_bf16 a[48:51], v[56:59], v[148:151], a[48:51]\n"
    "buffer_load_dwordx4 v121, s[16:19], s90 offen lds\n"
    "v_mfma_f32_16x16x32_bf16 a[80:83], v[56:59], v[152:155], a[80:83]\n"
    "v_mfma_f32_16x16x32_bf16 a[112:115], v[56:59], v[156:159], a[112:115]\n"
    "s_add_u32 m0, s59, 0x8c00\n"
    "v_mfma_f32_16x16x32_bf16 a[20:23], v[60:63], v[144:147], a[20:23]\n"
    "ds_read_b64_tr_b16 v[56:57], v112 offset:1024\n"
    "ds_read_b64_tr_b16 v[58:59], v112 offset:1536\n"
    "v_mfma_f32_16x16x32_bf16 a[52:55], v[60:63], v[148:151], a[52:55]\n"
    "buffer_load_dwordx4 v123, s[16:19], s90 offen lds\n"
    "v_mfma_f32_16x16x32_bf16 a[84:87], v[60:63], v[152:155], a[84:87]\n"
    "v_mfma_f32_16x16x32_bf16 a[116:119], v[60:63], v[156:159], a[116:119]\n"
    "s_add_u32 s90, s90, s69\n"
    "v_mfma_f32_16x16x32_bf16 a[24:27], v[64:67], v[144:147], a[24:27]\n"
    "ds_read_b64_tr_b16 v[60:61], v113 offset:1024\n"
    "ds_read_b64_tr_b16 v[62:63], v113 offset:1536\n"
    "v_mfma_f32_16x16x32_bf16 a[56:59], v[64:67], v[148:151], a[56:59]\n"
    "s_cmp_eq_u32 s90, s67\n"
    "v_mfma_f32_16x16x32_bf16 a[88:91], v[64:67], v[152:155], a[88:91]\n"
    "v_mfma_f32_16x16x32_bf16 a[120:123], v[64:67], v[156:159], a[120:123]\n"
    "s_cselect_b32 s90, 0, s90\n"
    "v_mfma_f32_16x16x32_bf16 a[28:31], v[68:71], v[144:147], a[28:31]\n"
    "ds_read_b64_tr_b16 v[64:65], v114 offset:1024\n"
    "ds_read_b64_tr_b16 v[66:67], v114 offset:1536\n"
    "v_mfma_f32_16x16x32_bf16 a[60:63], v[68:71], v[148:151], a[60:63]\n"
    "buffer_store_dwordx4 v[208:211], v2, s[8:11], s66 offen sc1\n"
    "v_mfma_f32_16x16x32_bf16 a[92:95], v[68:71], v[152:155], a[92:95]\n"
    "v_mfma_f32_16x16x32_bf16 a[124:127], v[68:71], v[156:159], a[124:127]\n"
    "s_add_u32 m0, s59, 0xc000\n"
    "v_mfma_f32_16x16x32_bf16 a[160:163], v[72:75], v[144:147], a[160:163]\n"
    "ds_read_b64_tr_b16 v[68:69], v115 offset:1024\n"
    "ds_read_b64_tr_b16 v[70:71], v115 offset:1536\n"
    "v_mfma_f32_16x16x32_bf16 a[184:187], v[72:75], v[148:151], a[184:187]\n"
    "buffer_load_dwordx4 v106, s[16:19], s90 offen lds\n"
    "v_mfma_f32_16x16x32_bf16 a[208:211], v[72:75], v[152:155], a[208:211]\n"
    "v_mfma_f32_16x16x32_bf16 a[232:235], v[72:75], v[156:159], a[232:235]\n"
    "s_add_u32 m0, s59, 0xc400\n"
    "v_mfma_f32_16x16x32_bf16 a[164:167], v[76:79], v[144:147], a[164:167]\n"
    "ds_read_b64_tr_b16 v[72:73], v100 offset:8192\n"
    "ds_read_b64_tr_b16 v[74:75], v100 offset:8704\n"
    "v_mfma_f32_16x16x32_bf16 a[188:191], v[76:79], v[148:151], a[188:191]\n"
    "buffer_load_dwordx4 v107, s[16:19], s90 offen lds\n"
    "v_mfma_f32_16x16x32_bf16 a[212:215], v[76:79], v[152:155], a[212:215]\n"
    "v_mfma_f32_16x16x32_bf16 a[236:239], v[76:79], v[156:159], a[236:239]\n"
    "s_add_u32 m0, s59, 0xc800\n"
    "v_mfma_f32_16x16x32_bf16 a[168:171], v[80:83], v[144:147], a[168:171]\n"
    "ds_read_b64_tr_b16 v[76:77], v101 offset:8192\n"
    "ds_read_b64_tr_b16 v[78:79], v101 offset:8704\n"
    "v_mfma_f32_16x16x32_bf16 a[192:195], v[80:83], v[148:151], a[192:195]\n"
    "buffer_load_dwordx4 v121, s[16:19], s90 offen lds\n"
    "v_mfma_f32_16x16x32_bf16 a[216:219], v[80:83], v[152:155], a[216:219]\n"
    "v_mfma_f32_16x16x32_bf16 a[240:243], v[80:83], v[156:159], a[240:243]\n"
    "s_add_u32 m0, s59, 0xcc00\n"
    "v_mfma_f32_16x16x32_bf16 a[172:175], v[84:87], v[144:147], a[172:175]\n"
    "ds_read_b64_tr_b16 v[80:81], v102 offset:8192\n"
    "ds_read_b64_tr_b16 v[82:83], v102 offset:8704\n"
    "v_mfma_f32_16x16x32_bf16 a[196:199], v[84:87], v[148:151], a[196:199]\n"
    "buffer_load_dwordx4 v123, s[16:19], s90 offen lds\n"
    "v_mfma_f32_16x16x32_bf16 a[220:223], v[84:87], v[152:155], a[220:223]\n"
    "v_mfma_f32_16x16x32_bf16 a[244:247], v[84:87], v[156:159], a[244:247]\n"
    "s_add_u32 s90, s90, s69\n"
    "v_mfma_f32_16x16x32_bf16 a[176:179], v[88:91], v[144:147], a[176:179]\n"
    "ds_read_b64_tr_b16 v[84:85], v100 offset:12288\n"
    "ds_read_b64_tr_b16 v[86:87], v100 offset:12800\n"
    "v_mfma_f32_16x16x32_bf16 a[200:203], v[88:91], v[148:151], a[200:203]\n"
    "s_cmp_eq_u32 s90, s67\n"
    "v_mfma_f32_16x16x32_bf16 a[224:227], v[88:91], v[152:155], a[224:227]\n"
    "v_mfma_f32_16x16x32_bf16 a[248:251], v[88:91], v[156:159], a[248:251]\n"
    "s_cselect_b32 s90, 0, s90\n"
    "v_mfma_f32_16x16x32_bf16 a[180:183], v[92:95], v[144:147], a[180:183]\n"
    "ds_read_b64_tr_b16 v[88:89], v101 offset:12288\n"
    "ds_read_b64_tr_b16 v[90:91], v101 offset:12800\n"
    "v_mfma_f32_16x16x32_bf16 a[204:207], v[92:95], v[148:151], a[204:207]\n"
    "buffer_store_dwordx4 v[212:215], v2, s[8:11], s66 offen offset:1024 sc1\n"
    "v_mfma_f32_16x16x32_bf16 a[228:231], v[92:95], v[152:155], a[228:231]\n"
    "v_mfma_f32_16x16x32_bf16 a[252:255], v[92:95], v[156:159], a[252:255]\n"
    "ds_read_b64_tr_b16 v[92:93], v102 offset:12288\n"
    "ds_read_b64_tr_b16 v[94:95], v102 offset:12800\n"
    "s_waitcnt lgkmcnt(15)\n"
    "v_mfma_f32_16x16x32_bf16 a[0:3], v[40:43], v[128:131], a[0:3]\n"
    "s_add_u32 m0, s91, 0x20000\n"
    "v_mfma_f32_16x16x32_bf16 a[32:35], v[40:43], v[132:135], a[32:35]\n"
    "v_mfma_f32_16x16x32_bf16 a[64:67], v[40:43], v[136:139], a[64:67]\n"
    "buffer_load_dwordx4 v110, s[8:11], s64 offen sc1 lds\n"
    "v_mfma_f32_16x16x32_bf16 a[96:99], v[40:43], v[140:143], a[96:99]\n"
    "v_mfma_f32_16x16x32_bf16 a[4:7], v[44:47], v[128:131], a[4:7]\n"
    "ds_read_b64_tr_b16 v[40:41], v112 offset:16384\n"
    "ds_read_b64_tr_b16 v[42:43], v112 offset:16896\n"
    "v_mfma_f32_16x16x32_bf16 a[36:39], v[44:47], v[132:135], a[36:39]\n"
    "s_add_u32 m0, s91, 0x20400\n"
    "v_mfma_f32_16x16x32_bf16 a[68:71], v[44:47], v[136:139], a[68:71]\n"
    "v_mfma_f32_16x16x32_bf16 a[100:103], v[44:47], v[140:143], a[100:103]\n"
    "v_mfma_f32_16x16x32_bf16 a[8:11], v[48:51], v[128:131], a[8:11]\n"
    "ds_read_b64_tr_b16 v[44:45], v113 offset:16384\n"
    "buffer_load_dwordx4 v111, s[8:11], s64 offen sc1 lds\n"
    "v_mfma_f32_16x16x32_bf16 a[40:43], v[48:51], v[132:135], a[40:43]\n"
    "ds_read_b64_tr_b16 v[46:47], v113 offset:16896\n"
    "v_mfma_f32_16x16x32_bf16 a[72:75], v[48:51], v[136:139], a[72:75]\n"
    "ds_read_b128 v[144:147], v109\n"
    "s_add_u32 m0, s91, 0x20800\n"
    "v_mfma_f32_16x16x32_bf16 a[104:107], v[48:51], v[140:143], a[104:107]\n"
    "ds_read_b128 v[148:151], v109 offset:2048\n"
    "ds_read_b128 v[152:155], v109 offset:4096\n"
    "v_mfma_f32_16x16x32_bf16 a[12:15], v[52:55], v[128:131], a[12:15]\n"
    "ds_read_b64_tr_b16 v[48:49], v114 offset:16384\n"
    "ds_read_b64_tr_b16 v[50:51], v114 offset:16896\n"
    "v_mfma_f32_16x16x32_bf16 a[44:47], v[52:55], v[132:135], a[44:47]\n"
    "ds_read_b128 v[156:159], v109 offset:6144\n"
    "s_add_u32 s83, s64, s62\n"
    "v_mfma_f32_16x16x32_bf16 a[76:79], v[52:55], v[136:139], a[76:79]\n"
    "v_mfma_f32_16x16x32_bf16 a[108:111], v[52:55], v[140:143], a[108:111]\n"
    "v_mfma_f32_16x16x32_bf16 a[16:19], v[56:59], v[128:131], a[16:19]\n"
    "ds_read_b64_tr_b16 v[52:53], v115 offset:16384\n"
    "buffer_load_dwordx4 v110, s[8:11], s83 offen sc1 lds\n"
    "v_mfma_f32_16x16x32_bf16 a[48:51], v[56:59], v[132:135], a[48:51]\n"
    "ds_read_b64_tr_b16 v[54:55], v115 offset:16896\n"
    "v_mfma_f32_16x16x32_bf16 a[80:83], v[56:59], v[136:139], a[80:83]\n"
    "s_add_u32 m0, s91, 0x20c00\n"
    "v_mfma_f32_16x16x32_bf16 a[112:115], v[56:59], v[140:143], a[112:115]\n"
    "v_mfma_f32_16x16x32_bf16 a[20:23], v[60:63], v[128:131], a[20:23]\n"
    "ds_read_b64_tr_b16 v[56:57], v112 offset:17408\n"
    "ds_read_b64_tr_b16 v[58:59], v112 offset:17920\n"
    "v_mfma_f32_16x16x32_bf16 a[52:55], v[60:63], v[132:135], a[52:55]\n"
    "buffer_load_dwordx4 v111, s[8:11], s83 offen sc1 lds\n"
    "v_mfma_f32_16x16x32_bf16 a[84:87], v[60:63], v[136:139], a[84:87]\n"
    "v_mfma_f32_16x16x32_bf16 a[116:119], v[60:63], v[140:143], a[116:119]\n"
    "s_waitcnt lgkmcnt(15)\n"
    "v_mfma_f32_16x16x32_bf16 a[24:27], v[64:67], v[128:131], a[24:27]\n"
    "ds_read_b64_tr_b16 v[60:61], v113 offset:17408\n"
    "s_add_u32 m0, s91, 0x21000\n"
    "v_mfma_f32_16x16x32_bf16 a[56:59], v[64:67], v[132:135], a[56:59]\n"
    "ds_read_b64_tr_b16 v[62:63], v113 offset:17920\n"
    "v_mfma_f32_16x16x32_bf16 a[88:91], v[64:67], v[136:139], a[88:91]\n"
    "buffer_load_dwordx4 v125, s[8:11], s64 offen sc1 lds\n"
    "v_mfma_f32_16x16x32_bf16 a[120:123], v[64:67], v[140:143], a[120:123]\n"
    "v_mfma_f32_16x16x32_bf16 a[28:31], v[68:71], v[128:131], a[28:31]\n"
    "ds_read_b64_tr_b16 v[64:65], v114 offset:17408\n"
    "ds_read_b64_tr_b16 v[66:67], v114 offset:17920\n"
    "v_mfma_f32_16x16x32_bf16 a[60:63], v[68:71], v[132:135], a[60:63]\n"
    "s_add_u32 m0, s91, 0x21400\n"
    "v_mfma_f32_16x16x32_bf16 a[92:95], v[68:71], v[136:139], a[92:95]\n"
    "v_mfma_f32_16x16x32_bf16 a[124:127], v[68:71], v[140:143], a[124:127]\n"
    "v_mfma_f32_16x16x32_bf16 a[160:163], v[72:75], v[128:131], a[160:163]\n"
    "ds_read_b64_tr_b16 v[68:69], v115 offset:17408\n"
    "buffer_load_dwordx4 v126, s[8:11], s64 offen sc1 lds\n"
    "v_mfma_f32_16x16x32_bf16 a[184:187], v[72:75], v[132:135], a[184:187]\n"
    "ds_read_b64_tr_b16 v[70:71], v115 offset:17920\n"
    "v_mfma_f32_16x16x32_bf16 a[208:211], v[72:75], v[136:139], a[208:211]\n"
    "s_add_u32 m0, s91, 0x21800\n"
    "v_mfma_f32_16x16x32_bf16 a[232:235], v[72:75], v[140:143], a[232:235]\n"
    "v_mfma_f32_16x16x32_bf16 a[164:167], v[76:79], v[128:131], a[164:167]\n"
    "ds_read_b64_tr_b16 v[72:73], v100 offset:24576\n"
    "ds_read_b64_tr_b16 v[74:75], v100 offset:25088\n"
    "v_mfma_f32_16x16x32_bf16 a[188:191], v[76:79], v[132:135], a[188:191]\n"
    "s_add_u32 s83, s64, s62\n"
    "v_mfma_f32_16x16x32_bf16 a[212:215], v[76:79], v[136:139], a[212:215]\n"
    "v_mfma_f32_16x16x32_bf16 a[236:239], v[76:79], v[140:143], a[236:239]\n"
    "v_mfma_f32_16x16x32_bf16 a[168:171], v[80:83], v[128:131], a[168:171]\n"
    "ds_read_b64_tr_b16 v[76:77], v101 offset:24576\n"
    "buffer_load_dwordx4 v125, s[8:11], s83 offen sc1 lds\n"
    "v_mfma_f32_16x16x32_bf16 a[192:195], v[80:83], v[132:135], a[192:195]\n"
    "ds_read_b64_tr_b16 v[78:79], v101 offset:25088\n"
    "v_mfma_f32_16x16x32_bf16 a[216:219], v[80:83], v[136:139], a[216:219]\n"
    "s_add_u32 m0, s91, 0x21c00\n"
    "v_mfma_f32_16x16x32_bf16 a[240:243], v[80:83], v[140:143], a[240:243]\n"
    "v_mfma_f32_16x16x32_bf16 a[172:175], v[84:87], v[128:131], a[172:175]\n"
    "ds_read_b64_tr_b16 v[80:81], v102 offset:24576\n"
    "ds_read_b64_tr_b16 v[82:83], v102 offset:25088\n"
    "v_mfma_f32_16x16x32_bf16 a[196:199], v[84:87], v[132:135], a[196:199]\n"
    "buffer_load_dwordx4 v126, s[8:11], s83 offen sc1 lds\n"
    "v_mfma_f32_16x16x32_bf16 a[220:223], v[84:87], v[136:139], a[220:223]\n"
    "v_mfma_f32_16x16x32_bf16 a[244:247], v[84:87], v[140:143], a[244:247]\n"
    "v_mfma_f32_16x16x32_bf16 a[176:179], v[88:91], v[128:131], a[176:179]\n"
    "ds_read_b64_tr_b16 v[84:85], v100 offset:28672\n"
    "s_add_u32 s64, s64, 0x80\n"
    "v_mfma_f32_16x16x32_bf16 a[200:203], v[88:91], v[132:135], a[200:203]\n"
    "ds_read_b64_tr_b16 v[86:87], v100 offset:29184\n"
    "v_mfma_f32_16x16x32_bf16 a[224:227], v[88:91], v[136:139], a[224:227]\n"
    "s_and_b32 s64, s64, 0x3ff\n"
    "v_mfma_f32_16x16x32_bf16 a[248:251], v[88:91], v[140:143], a[248:251]\n"
    "s_waitcnt lgkmcnt(15)\n"
    "v_mfma_f32_16x16x32_bf16 a[180:183], v[92:95], v[128:131], a[180:183]\n"
    "ds_read_b64_tr_b16 v[88:89], v101 offset:28672\n"
    "ds_read_b64_tr_b16 v[90:91], v101 offset:29184\n"
    "v_mfma_f32_16x16x32_bf16 a[204:207], v[92:95], v[132:135], a[204:207]\n"
    "buffer_store_dwordx4 v[216:219], v2, s[8:11], s66 offen offset:2048 sc1\n"
    "v_mfma_f32_16x16x32_bf16 a[228:231], v[92:95], v[136:139], a[228:231]\n"
    "v_mfma_f32_16x16x32_bf16 a[252:255], v[92:95], v[140:143], a[252:255]\n"
    "v_mfma_f32_16x16x32_bf16 a[0:3], v[40:43], v[144:147], a[0:3]\n"
    "ds_read_b64_tr_b16 v[92:93], v102 offset:28672\n"
    "ds_read_b64_tr_b16 v[94:95], v102 offset:29184\n"
    "v_mfma_f32_16x16x32_bf16 a[32:35], v[40:43], v[148:151], a[32:35]\n"
    "v_mfma_f32_16x16x32_bf16 a[64:67], v[40:43], v[152:155], a[64:67]\n"
    "v_mfma_f32_16x16x32_bf16 a[96:99], v[40:43], v[156:159], a[96:99]\n"
    "v_mfma_f32_16x16x32_bf16 a[4:7], v[44:47], v[144:147], a[4:7]\n"
    "v_mfma_f32_16x16x32_bf16 a[36:39], v[44:47], v[148:151], a[36:39]\n"
    "v_mfma_f32_16x16x32_bf16 a[68:71], v[44:47], v[152:155], a[68:71]\n"
    "s_waitcnt vmcnt(10)\n"
    "s_waitcnt lgkmcnt(0)\n"
    "s_barrier\n"
    "v_mfma_f32_16x16x32_bf16 a[100:103], v[44:47], v[156:159], a[100:103]\n"
    "ds_read_b64_tr_b16 v[40:41], v112 offset:32768\n"
    "s_add_u32 m0, s59, 0\n"
    "v_mfma_f32_16x16x32_bf16 a[8:11], v[48:51], v[144:147], a[8:11]\n"
    "ds_read_b64_tr_b16 v[44:45], v113 offset:32768\n"
    "buffer_load_dwordx4 v106, s[16:19], s90 offen lds\n"
    "v_mfma_f32_16x16x32_bf16 a[40:43], v[48:51], v[148:151], a[40:43]\n"
    "ds_read_b64_tr_b16 v[42:43], v112 offset:33280\n"
    "ds_read_b64_tr_b16 v[46:47], v113 offset:33280\n"
    "v_mfma_f32_16x16x32_bf16 a[72:75], v[48:51], v[152:155], a[72:75]\n"
    "s_add_u32 m0, s59, 0x400\n"
    "v_mfma_f32_16x16x32_bf16 a[104:107], v[48:51], v[156:159], a[104:107]\n"
    "ds_read_b128 v[128:131], v108 offset:32768\n"
    "ds_read_b128 v[132:135], v108 offset:34816\n"
    "v_mfma_f32_16x16x32_bf16 a[12:15], v[52:55], v[144:147], a[12:15]\n"
    "ds_read_b64_tr_b16 v[48:49], v114 offset:32768\n"
    "buffer_load_dwordx4 v107, s[16:19], s90 offen lds\n"
    "v_mfma_f32_16x16x32_bf16 a[44:47], v[52:55], v[148:151], a[44:47]\n"
    "ds_read_b64_tr_b16 v[50:51], v114 offset:33280\n"
    "ds_read_b128 v[136:139], v108 offset:36864\n"
    "v_mfma_f32_16x16x32_bf16 a[76:79], v[52:55], v[152:155], a[76:79]\n"
    "ds_read_b128 v[140:143], v108 offset:38912\n"
    "s_add_u32 m0, s59, 0x800\n"
    "v_mfma_f32_16x16x32_bf16 a[108:111], v[52:55], v[156:159], a[108:111]\n"
    "v_mfma_f32_16x16x32_bf16 a[16:19], v[56:59], v[144:147], a[16:19]\n"
    "ds_read_b64_tr_b16 v[52:53], v115 offset:32768\n"
    "buffer_load_dwordx4 v121, s[16:19], s90 offen lds\n"
    "v_mfma_f32_16x16x32_bf16 a[48:51], v[56:59], v[148:151], a[48:51]\n"
    "ds_read_b64_tr_b16 v[54:55], v115 offset:33280\n"
    "v_mfma_f32_16x16x32_bf16 a[80:83], v[56:59], v[152:155], a[80:83]\n"
    "s_add_u32 m0, s59, 0xc00\n"
    "v_mfma_f32_16x16x32_bf16 a[112:115], v[56:59], v[156:159], a[112:115]\n"
    "v_mfma_f32_16x16x32_bf16 a[20:23], v[60:63], v[144:147], a[20:23]\n"
    "ds_read_b64_tr_b16 v[56:57], v112 offset:33792\n"
    "buffer_load_dwordx4 v123, s[16:19], s90 offen lds\n"
    "v_mfma_f32_16x16x32_bf16 a[52:55], v[60:63], v[148:151], a[52:55]\n"
    "ds_read_b64_tr_b16 v[58:59], v112 offset:34304\n"
    "v_mfma_f32_16x16x32_bf16 a[84:87], v[60:63], v[152:155], a[84:87]\n"
    "s_add_u32 s90, s90, s69\n"
    "v_mfma_f32_16x16x32_bf16 a[116:119], v[60:63], v[156:159], a[116:119]\n"
    "v_mfma_f32_16x16x32_bf16 a[24:27], v[64:67], v[144:147], a[24:27]\n"
    "ds_read_b64_tr_b16 v[60:61], v113 offset:33792\n"
    "s_cmp_eq_u32 s90, s67\n"
    "v_mfma_f32_16x16x32_bf16 a[56:59], v[64:67], v[148:151], a[56:59]\n"
    "ds_read_b64_tr_b16 v[62:63], v113 offset:34304\n"
    "v_mfma_f32_16x16x32_bf16 a[88:91], v[64:67], v[152:155], a[88:91]\n"
    "s_cselect_b32 s90, 0, s90\n"
    "v_mfma_f32_16x16x32_bf16 a[120:123], v[64:67], v[156:159], a[120:123]\n"
    "v_mfma_f32_16x16x32_bf16 a[28:31], v[68:71], v[144:147], a[28:31]\n"
    "ds_read_b64_tr_b16 v[64:65], v114 offset:33792\n"
    "buffer_store_dwordx4 v[220:223], v2, s[8:11], s66 offen offset:3072 sc1\n"
    "v_mfma_f32_16x16x32_bf16 a[60:63], v[68:71], v[148:151], a[60:63]\n"
    "ds_read_b64_tr_b16 v[66:67], v114 offset:34304\n"
    "v_mfma_f32_16x16x32_bf16 a[92:95], v[68:71], v[152:155], a[92:95]\n"
    "s_add_u32 s66, s66, 0x1000\n"
    "v_mfma_f32_16x16x32_bf16 a[124:127], v[68:71], v[156:159], a[124:127]\n"
    "v_mfma_f32_16x16x32_bf16 a[160:163], v[72:75], v[144:147], a[160:163]\n"
    "ds_read_b64_tr_b16 v[68:69], v115 offset:33792\n"
    "s_add_u32 m0, s59, 0x4000\n"
    "v_mfma_f32_16x16x32_bf16 a[184:187], v[72:75], v[148:151], a[184:187]\n"
    "ds_read_b64_tr_b16 v[70:71], v115 offset:34304\n"
    "v_mfma_f32_16x16x32_bf16 a[208:211], v[72:75], v[152:155], a[208:211]\n"
    "buffer_load_dwordx4 v106, s[16:19], s90 offen lds\n"
    "v_mfma_f32_16x16x32_bf16 a[232:235], v[72:75], v[156:159], a[232:235]\n"
    "v_mfma_f32_16x16x32_bf16 a[164:167], v[76:79], v[144:147], a[164:167]\n"
    "ds_read_b64_tr_b16 v[72:73], v100 offset:40960\n"
    "s_add_u32 m0, s59, 0x4400\n"
    "v_mfma_f32_16x16x32_bf16 a[188:191], v[76:79], v[148:151], a[188:191]\n"
    "ds_read_b64_tr_b16 v[74:75], v100 offset:41472\n"
    "v_mfma_f32_16x16x32_bf16 a[212:215], v[76:79], v[152:155], a[212:215]\n"
    "buffer_load_dwordx4 v107, s[16:19], s90 offen lds\n"
    "v_mfma_f32_16x16x32_bf16 a[236:239], v[76:79], v[156:159], a[236:239]\n"
    "v_mfma_f32_16x16x32_bf16 a[168:171], v[80:83], v[144:147], a[168:171]\n"
    "ds_read_b64_tr_b16 v[76:77], v101 offset:40960\n"
    "s_add_u32 m0, s59, 0x4800\n"
    "v_mfma_f32_16x16x32_bf16 a[192:195], v[80:83], v[148:151], a[192:195]\n"
    "ds_read_b64_tr_b16 v[78:79], v101 offset:41472\n"
    "v_mfma_f32_16x16x32_bf16 a[216:219], v[80:83], v[152:155], a[216:219]\n"
    "buffer_load_dwordx4 v121, s[16:19], s90 offen lds\n"
    "v_mfma_f32_16x16x32_bf16 a[240:243], v[80:83], v[156:159], a[240:243]\n"
    "v_mfma_f32_16x16x32_bf16 a[172:175], v[84:87], v[144:147], a[172:175]\n"
    "ds_read_b64_tr_b16 v[80:81], v102 offset:40960\n"
    "s_add_u32 m0, s59, 0x4c00\n"
    "v_mfma_f32_16x16x32_bf16 a[196:199], v[84:87], v[148:151], a[196:199]\n"
    "ds_read_b64_tr_b16 v[82:83], v102 offset:41472\n"
    "v_mfma_f32_16x16x32_bf16 a[220:223], v[84:87], v[152:155], a[220:223]\n"
    "buffer_load_dwordx4 v123, s[16:19], s90 offen lds\n"
    "v_mfma_f32_16x16x32_bf16 a[244:247], v[84:87], v[156:159], a[244:247]\n"
    "v_mfma_f32_16x16x32_bf16 a[176:179], v[88:91], v[144:147], a[176:179]\n"
    "ds_read_b64_tr_b16 v[84:85], v100 offset:45056\n"
    "s_add_u32 s90, s90, s69\n"
    "v_mfma_f32_16x16x32_bf16 a[200:203], v[88:91], v[148:151], a[200:203]\n"
    "ds_read_b64_tr_b16 v[86:87], v100 offset:45568\n"
    "v_mfma_f32_16x16x32_bf16 a[224:227], v[88:91], v[152:155], a[224:227]\n"
    "s_cmp_eq_u32 s90, s67\n"
    "v_mfma_f32_16x16x32_bf16 a[248:251], v[88:91], v[156:159], a[248:251]\n"
    "v_mfma_f32_16x16x32_bf16 a[180:183], v[92:95], v[144:147], a[180:183]\n"
    "ds_read_b64_tr_b16 v[88:89], v101 offset:45056\n"
    "s_cselect_b32 s90, 0, s90\n"
    "v_mfma_f32_16x16x32_bf16 a[204:207], v[92:95], v[148:151], a[204:207]\n"
    "ds_read_b64_tr_b16 v[90:91], v101 offset:45568\n"
    "v_mfma_f32_16x16x32_bf16 a[228:231], v[92:95], v[152:155], a[228:231]\n"
    "buffer_store_dwordx4 v[224:227], v2, s[8:11], s66 offen sc1\n"
    "v_mfma_f32_16x16x32_bf16 a[252:255], v[92:95], v[156:159], a[252:255]\n"
    "ds_read_b64_tr_b16 v[92:93], v102 offset:45056\n"
    "ds_read_b64_tr_b16 v[94:95], v102 offset:45568\n"
    "s_waitcnt lgkmcnt(15)\n"
    "v_mfma_f32_16x16x32_bf16 a[0:3], v[40:43], v[128:131], a[0:3]\n"
    "s_add_u32 m0, s91, 0x10000\n"
    "v_mfma_f32_16x16x32_bf16 a[32:35], v[40:43], v[132:135], a[32:35]\n"
    "v_mfma_f32_16x16x32_bf16 a[64:67], v[40:43], v[136:139], a[64:67]\n"
    "buffer_load_dwordx4 v110, s[8:11], s64 offen sc1 lds\n"
    "v_mfma_f32_16x16x32_bf16 a[96:99], v[40:43], v[140:143], a[96:99]\n"
    "v_mfma_f32_16x16x32_bf16 a[4:7], v[44:47], v[128:131], a[4:7]\n"
    "ds_read_b64_tr_b16 v[40:41], v112 offset:49152\n"
    "ds_read_b64_tr_b16 v[42:43], v112 offset:49664\n"
    "v_mfma_f32_16x16x32_bf16 a[36:39], v[44:47], v[132:135], a[36:39]\n"
    "s_add_u32 m0, s91, 0x10400\n"
    "v_mfma_f32_16x16x32_bf16 a[68:71], v[44:47], v[136:139], a[68:71]\n"
    "v_mfma_f32_16x16x32_bf16 a[100:103], v[44:47], v[140:143], a[100:103]\n"
    "v_mfma_f32_16x16x32_bf16 a[8:11], v[48:51], v[128:131], a[8:11]\n"
    "ds_read_b64_tr_b16 v[44:45], v113 offset:49152\n"
    "buffer_load_dwordx4 v111, s[8:11], s64 offen sc1 lds\n"
    "v_mfma_f32_16x16x32_bf16 a[40:43], v[48:51], v[132:135], a[40:43]\n"
    "ds_read_b64_tr_b16 v[46:47], v113 offset:49664\n"
    "v_mfma_f32_16x16x32_bf16 a[72:75], v[48:51], v[136:139], a[72:75]\n"
    "ds_read_b128 v[144:147], v109 offset:32768\n"
    "s_add_u32 m0, s91, 0x10800\n"
    "v_mfma_f32_16x16x32_bf16 a[104:107], v[48:51], v[140:143], a[104:107]\n"
    "ds_read_b128 v[148:151], v109 offset:34816\n"
    "ds_read_b128 v[152:155], v109 offset:36864\n"
    "v_mfma_f32_16x16x32_bf16 a[12:15], v[52:55], v[128:131], a[12:15]\n"
    "ds_read_b64_tr_b16 v[48:49], v114 offset:49152\n"
    "ds_read_b64_tr_b16 v[50:51], v114 offset:49664\n"
    "v_mfma_f32_16x16x32_bf16 a[44:47], v[52:55], v[132:135], a[44:47]\n"
    "ds_read_b128 v[156:159], v109 offset:38912\n"
    "s_add_u32 s83, s64, s62\n"
    "v_mfma_f32_16x16x32_bf16 a[76:79], v[52:55], v[136:139], a[76:79]\n"
    "v_mfma_f32_16x16x32_bf16 a[108:111], v[52:55], v[140:143], a[108:111]\n"
    "v_mfma_f32_16x16x32_bf16 a[16:19], v[56:59], v[128:131], a[16:19]\n"
    "ds_read_b64_tr_b16 v[52:53], v115 offset:49152\n"
    "buffer_load_dwordx4 v110, s[8:11], s83 offen sc1 lds\n"
    "v_mfma_f32_16x16x32_bf16 a[48:51], v[56:59], v[132:135], a[48:51]\n"
    "ds_read_b64_tr_b16 v[54:55], v115 offset:49664\n"
    "v_mfma_f32_16x16x32_bf16 a[80:83], v[56:59], v[136:139], a[80:83]\n"
    "s_add_u32 m0, s91, 0x10c00\n"
    "v_mfma_f32_16x16x32_bf16 a[112:115], v[56:59], v[140:143], a[112:115]\n"
    "v_mfma_f32_16x16x32_bf16 a[20:23], v[60:63], v[128:131], a[20:23]\n"
    "ds_read_b64_tr_b16 v[56:57], v112 offset:50176\n"
    "ds_read_b64_tr_b16 v[58:59], v112 offset:50688\n"
    "v_mfma_f32_16x16x32_bf16 a[52:55], v[60:63], v[132:135], a[52:55]\n"
    "buffer_load_dwordx4 v111, s[8:11], s83 offen sc1 lds\n"
    "v_mfma_f32_16x16x32_bf16 a[84:87], v[60:63], v[136:139], a[84:87]\n"
    "v_mfma_f32_16x16x32_bf16 a[116:119], v[60:63], v[140:143], a[116:119]\n"
    "s_waitcnt lgkmcnt(15)\n"
    "v_mfma_f32_16x16x32_bf16 a[24:27], v[64:67], v[128:131], a[24:27]\n"
    "ds_read_b64_tr_b16 v[60:61], v113 offset:50176\n"
    "s_add_u32 m0, s91, 0x11000\n"
    "v_mfma_f32_16x16x32_bf16 a[56:59], v[64:67], v[132:135], a[56:59]\n"
    "ds_read_b64_tr_b16 v[62:63], v113 offset:50688\n"
    "v_mfma_f32_16x16x32_bf16 a[88:91], v[64:67], v[136:139], a[88:91]\n"
    "buffer_load_dwordx4 v125, s[8:11], s64 offen sc1 lds\n"
    "v_mfma_f32_16x16x32_bf16 a[120:123], v[64:67], v[140:143], a[120:123]\n"
    "v_mfma_f32_16x16x32_bf16 a[28:31], v[68:71], v[128:131], a[28:31]\n"
    "ds_read_b64_tr_b16 v[64:65], v114 offset:50176\n"
    "ds_read_b64_tr_b16 v[66:67], v114 offset:50688\n"
    "v_mfma_f32_16x16x32_bf16 a[60:63], v[68:71], v[132:135], a[60:63]\n"
    "s_add_u32 m0, s91, 0x11400\n"
    "v_mfma_f32_16x16x32_bf16 a[92:95], v[68:71], v[136:139], a[92:95]\n"
    "v_mfma_f32_16x16x32_bf16 a[124:127], v[68:71], v[140:143], a[124:127]\n"
    "v_mfma_f32_16x16x32_bf16 a[160:163], v[72:75], v[128:131], a[160:163]\n"
    "ds_read_b64_tr_b16 v[68:69], v115 offset:50176\n"
    "buffer_load_dwordx4 v126, s[8:11], s64 offen sc1 lds\n"
    "v_mfma_f32_16x16x32_bf16 a[184:187], v[72:75], v[132:135], a[184:187]\n"
    "ds_read_b64_tr_b16 v[70:71], v115 offset:50688\n"
    "v_mfma_f32_16x16x32_bf16 a[208:211], v[72:75], v[136:139], a[208:211]\n"
    "s_add_u32 m0, s91, 0x11800\n"
    "v_mfma_f32_16x16x32_bf16 a[232:235], v[72:75], v[140:143], a[232:235]\n"
    "v_mfma_f32_16x16x32_bf16 a[164:167], v[76:79], v[128:131], a[164:167]\n"
    "ds_read_b64_tr_b16 v[72:73], v100 offset:57344\n"
    "ds_read_b64_tr_b16 v[74:75], v100 offset:57856\n"
    "v_mfma_f32_16x16x32_bf16 a[188:191], v[76:79], v[132:135], a[188:191]\n"
    "s_add_u32 s83, s64, s62\n"
    "v_mfma_f32_16x16x32_bf16 a[212:215], v[76:79], v[136:139], a[212:215]\n"
    "v_mfma_f32_16x16x32_bf16 a[236:239], v[76:79], v[140:143], a[236:239]\n"
    "v_mfma_f32_16x16x32_bf16 a[168:171], v[80:83], v[128:131], a[168:171]\n"
    "ds_read_b64_tr_b16 v[76:77], v101 offset:57344\n"
    "buffer_load_dwordx4 v125, s[8:11], s83 offen sc1 lds\n"
    "v_mfma_f32_16x16x32_bf16 a[192:195], v[80:83], v[132:135], a[192:195]\n"
    "ds_read_b64_tr_b16 v[78:79], v101 offset:57856\n"
    "v_mfma_f32_16x16x32_bf16 a[216:219], v[80:83], v[136:139], a[216:219]\n"
    "s_add_u32 m0, s91, 0x11c00\n"
    "v_mfma_f32_16x16x32_bf16 a[240:243], v[80:83], v[140:143], a[240:243]\n"
    "v_mfma_f32_16x16x32_bf16 a[172:175], v[84:87], v[128:131], a[172:175]\n"
    "ds_read_b64_tr_b16 v[80:81], v102 offset:57344\n"
    "ds_read_b64_tr_b16 v[82:83], v102 offset:57856\n"
    "v_mfma_f32_16x16x32_bf16 a[196:199], v[84:87], v[132:135], a[196:199]\n"
    "buffer_load_dwordx4 v126, s[8:11], s83 offen sc1 lds\n"
    "v_mfma_f32_16x16x32_bf16 a[220:223], v[84:87], v[136:139], a[220:223]\n"
    "v_mfma_f32_16x16x32_bf16 a[244:247], v[84:87], v[140:143], a[244:247]\n"
    "v_mfma_f32_16x16x32_bf16 a[176:179], v[88:91], v[128:131], a[176:179]\n"
    "ds_read_b64_tr_b16 v[84:85], v100 offset:61440\n"
    "s_add_u32 s64, s64, 0x80\n"
    "v_mfma_f32_16x16x32_bf16 a[200:203], v[88:91], v[132:135], a[200:203]\n"
    "ds_read_b64_tr_b16 v[86:87], v100 offset:61952\n"
    "v_mfma_f32_16x16x32_bf16 a[224:227], v[88:91], v[136:139], a[224:227]\n"
    "s_and_b32 s64, s64, 0x3ff\n"
    "v_mfma_f32_16x16x32_bf16 a[248:251], v[88:91], v[140:143], a[248:251]\n"
    "s_waitcnt lgkmcnt(15)\n"
    "v_mfma_f32_16x16x32_bf16 a[180:183], v[92:95], v[128:131], a[180:183]\n"
    "ds_read_b64_tr_b16 v[88:89], v101 offset:61440\n"
    "ds_read_b64_tr_b16 v[90:91], v101 offset:61952\n"
    "v_mfma_f32_16x16x32_bf16 a[204:207], v[92:95], v[132:135], a[204:207]\n"
    "buffer_store_dwordx4 v[228:231], v2, s[8:11], s66 offen offset:1024 sc1\n"
    "v_mfma_f32_16x16x32_bf16 a[228:231], v[92:95], v[136:139], a[228:231]\n"
    "v_mfma_f32_16x16x32_bf16 a[252:255], v[92:95], v[140:143], a[252:255]\n"
    "v_mfma_f32_16x16x32_bf16 a[0:3], v[40:43], v[144:147], a[0:3]\n"
    "ds_read_b64_tr_b16 v[92:93], v102 offset:61440\n"
    "ds_read_b64_tr_b16 v[94:95], v102 offset:61952\n"
    "v_mfma_f32_16x16x32_bf16 a[32:35], v[40:43], v[148:151], a[32:35]\n"
    "v_mfma_f32_16x16x32_bf16 a[64:67], v[40:43], v[152:155], a[64:67]\n"
    "v_mfma_f32_16x16x32_bf16 a[96:99], v[40:43], v[156:159], a[96:99]\n"
    "v_mfma_f32_16x16x32_bf16 a[4:7], v[44:47], v[144:147], a[4:7]\n"
    "v_mfma_f32_16x16x32_bf16 a[36:39], v[44:47], v[148:151], a[36:39]\n"
    "v_mfma_f32_16x16x32_bf16 a[68:71], v[44:47], v[152:155], a[68:71]\n"
    "s_waitcnt vmcnt(10)\n"
    "s_waitcnt lgkmcnt(0)\n"
    "s_barrier\n"
    "v_mfma_f32_16x16x32_bf16 a[100:103], v[44:47], v[156:159], a[100:103]\n"
    "ds_read_b64_tr_b16 v[40:41], v112\n"
    "s_add_u32 m0, s59, 0x8000\n"
    "v_mfma_f32_16x16x32_bf16 a[8:11], v[48:51], v[144:147], a[8:11]\n"
    "ds_read_b64_tr_b16 v[44:45], v113\n"
    "buffer_load_dwordx4 v106, s[16:19], s90 offen lds\n"
    "v_mfma_f32_16x16x32_bf16 a[40:43], v[48:51], v[148:151], a[40:43]\n"
    "ds_read_b64_tr_b16 v[42:43], v112 offset:512\n"
    "ds_read_b64_tr_b16 v[46:47], v113 offset:512\n"
    "v_mfma_f32_16x16x32_bf16 a[72:75], v[48:51], v[152:155], a[72:75]\n"
    "s_add_u32 m0, s59, 0x8400\n"
    "v_mfma_f32_16x16x32_bf16 a[104:107], v[48:51], v[156:159], a[104:107]\n"
    "ds_read_b128 v[128:131], v119\n"
    "ds_read_b128 v[132:135], v119 offset:2048\n"
    "v_mfma_f32_16x16x32_bf16 a[12:15], v[52:55], v[144:147], a[12:15]\n"
    "ds_read_b64_tr_b16 v[48:49], v114\n"
    "buffer_load_dwordx4 v107, s[16:19], s90 offen lds\n"
    "v_mfma_f32_16x16x32_bf16 a[44:47], v[52:55], v[148:151], a[44:47]\n"
    "ds_read_b64_tr_b16 v[50:51], v114 offset:512\n"
    "ds_read_b128 v[136:139], v119 offset:4096\n"
    "v_mfma_f32_16x16x32_bf16 a[76:79], v[52:55], v[152:155], a[76:79]\n"
    "ds_read_b128 v[140:143], v119 offset:6144\n"
    "s_add_u32 m0, s59, 0x8800\n"
    "v_mfma_f32_16x16x32_bf16 a[108:111], v[52:55], v[156:159], a[108:111]\n"
    "v_mfma_f32_16x16x32_bf16 a[16:19], v[56:59], v[144:147], a[16:19]\n"
    "ds_read_b64_tr_b16 v[52:53], v115\n"
    "buffer_load_dwordx4 v121, s[16:19], s90 offen lds\n"
    "v_mfma_f32_16x16x32_bf16 a[48:51], v[56:59], v[148:151], a[48:51]\n"
    "ds_read_b64_tr_b16 v[54:55], v115 offset:512\n"
    "v_mfma_f32_16x16x32_bf16 a[80:83], v[56:59], v[152:155], a[80:83]\n"
    "s_add_u32 m0, s59, 0x8c00\n"
    "v_mfma_f32_16x16x32_bf16 a[112:115], v[56:59], v[156:159], a[112:115]\n"
    "v_mfma_f32_16x16x32_bf16 a[20:23], v[60:63], v[144:147], a[20:23]\n"
    "ds_read_b64_tr_b16 v[56:57], v112 offset:1024\n"
    "buffer_load_dwordx4 v123, s[16:19], s90 offen lds\n"
    "v_mfma_f32_16x16x32_bf16 a[52:55], v[60:63], v[148:151], a[52:55]\n"
    "ds_read_b64_tr_b16 v[58:59], v112 offset:1536\n"
    "v_mfma_f32_16x16x32_bf16 a[84:87], v[60:63], v[152:155], a[84:87]\n"
    "s_add_u32 s90, s90, s69\n"
    "v_mfma_f32_16x16x32_bf16 a[116:119], v[60:63], v[156:159], a[116:119]\n"
    "v_mfma_f32_16x16x32_bf16 a[24:27], v[64:67], v[144:147], a[24:27]\n"
    "ds_read_b64_tr_b16 v[60:61], v113 offset:1024\n"
    "s_cmp_eq_u32 s90, s67\n"
    "v_mfma_f32_16x16x32_bf16 a[56:59], v[64:67], v[148:151], a[56:59]\n"
    "ds_read_b64_tr_b16 v[62:63], v113 offset:1536\n"
    "v_mfma_f32_16x16x32_bf16 a[88:91], v[64:67], v[152:155], a[88:91]\n"
    "s_cselect_b32 s90, 0, s90\n"
    "v_mfma_f32_16x16x32_bf16 a[120:123], v[64:67], v[156:159], a[120:123]\n"
    "v_mfma_f32_16x16x32_bf16 a[28:31], v[68:71], v[144:147], a[28:31]\n"
    "ds_read_b64_tr_b16 v[64:65], v114 offset:1024\n"
    "buffer_store_dwordx4 v[232:235], v2, s[8:11], s66 offen offset:2048 sc1\n"
    "v_mfma_f32_16x16x32_bf16 a[60:63], v[68:71], v[148:151], a[60:63]\n"
    "ds_read_b64_tr_b16 v[66:67], v114 offset:1536\n"
    "v_mfma_f32_16x16x32_bf16 a[92:95], v[68:71], v[152:155], a[92:95]\n"
    "s_add_u32 m0, s59, 0xc000\n"
    "v_mfma_f32_16x16x32_bf16 a[124:127], v[68:71], v[156:159], a[124:127]\n"
    "v_mfma_f32_16x16x32_bf16 a[160:163], v[72:75], v[144:147], a[160:163]\n"
    "ds_read_b64_tr_b16 v[68:69], v115 offset:1024\n"
    "buffer_load_dwordx4 v106, s[16:19], s90 offen lds\n"
    "v_mfma_f32_16x16x32_bf16 a[184:187], v[72:75], v[148:151], a[184:187]\n"
    "ds_read_b64_tr_b16 v[70:71], v115 offset:1536\n"
    "v_mfma_f32_16x16x32_bf16 a[208:211], v[72:75], v[152:155], a[208:211]\n"
    "s_add_u32 m0, s59, 0xc400\n"
    "v_mfma_f32_16x16x32_bf16 a[232:235], v[72:75], v[156:159], a[232:235]\n"
    "v_mfma_f32_16x16x32_bf16 a[164:167], v[76:79], v[144:147], a[164:167]\n"
    "ds_read_b64_tr_b16 v[72:73], v100 offset:8192\n"
    "buffer_load_dwordx4 v107, s[16:19], s90 offen lds\n"
    "v_mfma_f32_16x16x32_bf16 a[188:191], v[76:79], v[148:151], a[188:191]\n"
    "ds_read_b64_tr_b16 v[74:75], v100 offset:8704\n"
    "v_mfma_f32_16x16x32_bf16 a[212:215], v[76:79], v[152:155], a[212:215]\n"
    "s_add_u32 m0, s59, 0xc800\n"
    "v_mfma_f32_16x16x32_bf16 a[236:239], v[76:79], v[156:159], a[236:239]\n"
    "v_mfma_f32_16x16x32_bf16 a[168:171], v[80:83], v[144:147], a[168:171]\n"
    "ds_read_b64_tr_b16 v[76:77], v101 offset:8192\n"
    "buffer_load_dwordx4 v121, s[16:19], s90 offen lds\n"
    "v_mfma_f32_16x16x32_bf16 a[192:195], v[80:83], v[148:151], a[192:195]\n"
    "ds_read_b64_tr_b16 v[78:79], v101 offset:8704\n"
    "v_mfma_f32_16x16x32_bf16 a[216:219], v[80:83], v[152:155], a[216:219]\n"
    "s_add_u32 m0, s59, 0xcc00\n"
    "v_mfma_f32_16x16x32_bf16 a[240:243], v[80:83], v[156:159], a[240:243]\n"
    "v_mfma_f32_16x16x32_bf16 a[172:175], v[84:87], v[144:147], a[172:175]\n"
    "ds_read_b64_tr_b16 v[80:81], v102 offset:8192\n"
    "buffer_load_dwordx4 v123, s[16:19], s90 offen lds\n"
    "v_mfma_f32_16x16x32_bf16 a[196:199], v[84:87], v[148:151], a[196:199]\n"
    "ds_read_b64_tr_b16 v[82:83], v102 offset:8704\n"
    "v_mfma_f32_16x16x32_bf16 a[220:223], v[84:87], v[152:155], a[220:223]\n"
    "s_add_u32 s90, s90, s69\n"
    "v_mfma_f32_16x16x32_bf16 a[244:247], v[84:87], v[156:159], a[244:247]\n"
    "v_mfma_f32_16x16x32_bf16 a[176:179], v[88:91], v[144:147], a[176:179]\n"
    "ds_read_b64_tr_b16 v[84:85], v100 offset:12288\n"
    "s_cmp_eq_u32 s90, s67\n"
    "v_mfma_f32_16x16x32_bf16 a[200:203], v[88:91], v[148:151], a[200:203]\n"
    "ds_read_b64_tr_b16 v[86:87], v100 offset:12800\n"
    "v_mfma_f32_16x16x32_bf16 a[224:227], v[88:91], v[152:155], a[224:227]\n"
    "s_cselect_b32 s90, 0, s90\n"
    "v_mfma_f32_16x16x32_bf16 a[248:251], v[88:91], v[156:159], a[248:251]\n"
    "v_mfma_f32_16x16x32_bf16 a[180:183], v[92:95], v[144:147], a[180:183]\n"
    "ds_read_b64_tr_b16 v[88:89], v101 offset:12288\n"
    "buffer_store_dwordx4 v[236:239], v2, s[8:11], s66 offen offset:3072 sc1\n"
    "v_mfma_f32_16x16x32_bf16 a[204:207], v[92:95], v[148:151], a[204:207]\n"
    "ds_read_b64_tr_b16 v[90:91], v101 offset:12800\n"
    "v_mfma_f32_16x16x32_bf16 a[228:231], v[92:95], v[152:155], a[228:231]\n"
    "s_add_u32 s66, s66, 0x1000\n"
    "v_mfma_f32_16x16x32_bf16 a[252:255], v[92:95], v[156:159], a[252:255]\n"
    "ds_read_b64_tr_b16 v[92:93], v102 offset:12288\n"
    "ds_read_b64_tr_b16 v[94:95], v102 offset:12800\n"
    "s_waitcnt lgkmcnt(15)\n"
    "v_mfma_f32_16x16x32_bf16 a[0:3], v[40:43], v[128:131], a[0:3]\n"
    "s_add_u32 m0, s91, 0x18000\n"
    "v_mfma_f32_16x16x32_bf16 a[32:35], v[40:43], v[132:135], a[32:35]\n"
    "v_mfma_f32_16x16x32_bf16 a[64:67], v[40:43], v[136:139], a[64:67]\n"
    "buffer_load_dwordx4 v110, s[8:11], s64 offen sc1 lds\n"
    "v_mfma_f32_16x16x32_bf16 a[96:99], v[40:43], v[140:143], a[96:99]\n"
    "v_mfma_f32_16x16x32_bf16 a[4:7], v[44:47], v[128:131], a[4:7]\n"
    "ds_read_b64_tr_b16 v[40:41], v112 offset:16384\n"
    "ds_read_b64_tr_b16 v[42:43], v112 offset:16896\n"
    "v_mfma_f32_16x16x32_bf16 a[36:39], v[44:47], v[132:135], a[36:39]\n"
    "s_add_u32 m0, s91, 0x18400\n"
    "v_mfma_f32_16x16x32_bf16 a[68:71], v[44:47], v[136:139], a[68:71]\n"
    "v_mfma_f32_16x16x32_bf16 a[100:103], v[44:47], v[140:143], a[100:103]\n"
    "v_mfma_f32_16x16x32_bf16 a[8:11], v[48:51], v[128:131], a[8:11]\n"
    "ds_read_b64_tr_b16 v[44:45], v113 offset:16384\n"
    "buffer_load_dwordx4 v111, s[8:11], s64 offen sc1 lds\n"
    "v_mfma_f32_16x16x32_bf16 a[40:43], v[48:51], v[132:135], a[40:43]\n"
    "ds_read_b64_tr_b16 v[46:47], v113 offset:16896\n"
    "v_mfma_f32_16x16x32_bf16 a[72:75], v[48:51], v[136:139], a[72:75]\n"
    "ds_read_b128 v[144:147], v120\n"
    "s_add_u32 m0, s91, 0x18800\n"
    "v_mfma_f32_16x16x32_bf16 a[104:107], v[48:51], v[140:143], a[104:107]\n"
    "ds_read_b128 v[148:151], v120 offset:2048\n"
    "ds_read_b128 v[152:155], v120 offset:4096\n"
    "v_mfma_f32_16x16x32_bf16 a[12:15], v[52:55], v[128:131], a[12:15]\n"
    "ds_read_b64_tr_b16 v[48:49], v114 offset:16384\n"
    "ds_read_b64_tr_b16 v[50:51], v114 offset:16896\n"
    "v_mfma_f32_16x16x32_bf16 a[44:47], v[52:55], v[132:135], a[44:47]\n"
    "ds_read_b128 v[156:159], v120 offset:6144\n"
    "s_add_u32 s83, s64, s62\n"
    "v_mfma_f32_16x16x32_bf16 a[76:79], v[52:55], v[136:139], a[76:79]\n"
    "v_mfma_f32_16x16x32_bf16 a[108:111], v[52:55], v[140:143], a[108:111]\n"
    "v_mfma_f32_16x16x32_bf16 a[16:19], v[56:59], v[128:131], a[16:19]\n"
    "ds_read_b64_tr_b16 v[52:53], v115 offset:16384\n"
    "buffer_load_dwordx4 v110, s[8:11], s83 offen sc1 lds\n"
    "v_mfma_f32_16x16x32_bf16 a[48:51], v[56:59], v[132:135], a[48:51]\n"
    "ds_read_b64_tr_b16 v[54:55], v115 offset:16896\n"
    "v_mfma_f32_16x16x32_bf16 a[80:83], v[56:59], v[136:139], a[80:83]\n"
    "s_add_u32 m0, s91, 0x18c00\n"
    "v_mfma_f32_16x16x32_bf16 a[112:115], v[56:59], v[140:143], a[112:115]\n"
    "v_mfma_f32_16x16x32_bf16 a[20:23], v[60:63], v[128:131], a[20:23]\n"
    "ds_read_b64_tr_b16 v[56:57], v112 offset:17408\n"
    "ds_read_b64_tr_b16 v[58:59], v112 offset:17920\n"
    "v_mfma_f32_16x16x32_bf16 a[52:55], v[60:63], v[132:135], a[52:55]\n"
    "buffer_load_dwordx4 v111, s[8:11], s83 offen sc1 lds\n"
    "v_mfma_f32_16x16x32_bf16 a[84:87], v[60:63], v[136:139], a[84:87]\n"
    "v_mfma_f32_16x16x32_bf16 a[116:119], v[60:63], v[140:143], a[116:119]\n"
    "s_waitcnt lgkmcnt(15)\n"
    "v_mfma_f32_16x16x32_bf16 a[24:27], v[64:67], v[128:131], a[24:27]\n"
    "ds_read_b64_tr_b16 v[60:61], v113 offset:17408\n"
    "s_add_u32 m0, s91, 0x19000\n"
    "v_mfma_f32_16x16x32_bf16 a[56:59], v[64:67], v[132:135], a[56:59]\n"
    "ds_read_b64_tr_b16 v[62:63], v113 offset:17920\n"
    "v_mfma_f32_16x16x32_bf16 a[88:91], v[64:67], v[136:139], a[88:91]\n"
    "buffer_load_dwordx4 v125, s[8:11], s64 offen sc1 lds\n"
    "v_mfma_f32_16x16x32_bf16 a[120:123], v[64:67], v[140:143], a[120:123]\n"
    "v_mfma_f32_16x16x32_bf16 a[28:31], v[68:71], v[128:131], a[28:31]\n"
    "ds_read_b64_tr_b16 v[64:65], v114 offset:17408\n"
    "ds_read_b64_tr_b16 v[66:67], v114 offset:17920\n"
    "v_mfma_f32_16x16x32_bf16 a[60:63], v[68:71], v[132:135], a[60:63]\n"
    "s_add_u32 m0, s91, 0x19400\n"
    "v_mfma_f32_16x16x32_bf16 a[92:95], v[68:71], v[136:139], a[92:95]\n"
    "v_mfma_f32_16x16x32_bf16 a[124:127], v[68:71], v[140:143], a[124:127]\n"
    "v_mfma_f32_16x16x32_bf16 a[160:163], v[72:75], v[128:131], a[160:163]\n"
    "ds_read_b64_tr_b16 v[68:69], v115 offset:17408\n"
    "buffer_load_dwordx4 v126, s[8:11], s64 offen sc1 lds\n"
    "v_mfma_f32_16x16x32_bf16 a[184:187], v[72:75], v[132:135], a[184:187]\n"
    "ds_read_b64_tr_b16 v[70:71], v115 offset:17920\n"
    "v_mfma_f32_16x16x32_bf16 a[208:211], v[72:75], v[136:139], a[208:211]\n"
    "s_add_u32 m0, s91, 0x19800\n"
    "v_mfma_f32_16x16x32_bf16 a[232:235], v[72:75], v[140:143], a[232:235]\n"
    "v_mfma_f32_16x16x32_bf16 a[164:167], v[76:79], v[128:131], a[164:167]\n"
    "ds_read_b64_tr_b16 v[72:73], v100 offset:24576\n"
    "ds_read_b64_tr_b16 v[74:75], v100 offset:25088\n"
    "v_mfma_f32_16x16x32_bf16 a[188:191], v[76:79], v[132:135], a[188:191]\n"
    "s_add_u32 s83, s64, s62\n"
    "v_mfma_f32_16x16x32_bf16 a[212:215], v[76:79], v[136:139], a[212:215]\n"
    "v_mfma_f32_16x16x32_bf16 a[236:239], v[76:79], v[140:143], a[236:239]\n"
    "v_mfma_f32_16x16x32_bf16 a[168:171], v[80:83], v[128:131], a[168:171]\n"
    "ds_read_b64_tr_b16 v[76:77], v101 offset:24576\n"
    "buffer_load_dwordx4 v125, s[8:11], s83 offen sc1 lds\n"
    "v_mfma_f32_16x16x32_bf16 a[192:195], v[80:83], v[132:135], a[192:195]\n"
    "ds_read_b64_tr_b16 v[78:79], v101 offset:25088\n"
    "v_mfma_f32_16x16x32_bf16 a[216:219], v[80:83], v[136:139], a[216:219]\n"
    "s_add_u32 m0, s91, 0x19c00\n"
    "v_mfma_f32_16x16x32_bf16 a[240:243], v[80:83], v[140:143], a[240:243]\n"
    "v_mfma_f32_16x16x32_bf16 a[172:175], v[84:87], v[128:131], a[172:175]\n"
    "ds_read_b64_tr_b16 v[80:81], v102 offset:24576\n"
    "ds_read_b64_tr_b16 v[82:83], v102 offset:25088\n"
    "v_mfma_f32_16x16x32_bf16 a[196:199], v[84:87], v[132:135], a[196:199]\n"
    "buffer_load_dwordx4 v126, s[8:11], s83 offen sc1 lds\n"
    "v_mfma_f32_16x16x32_bf16 a[220:223], v[84:87], v[136:139], a[220:223]\n"
    "v_mfma_f32_16x16x32_bf16 a[244:247], v[84:87], v[140:143], a[244:247]\n"
    "v_mfma_f32_16x16x32_bf16 a[176:179], v[88:91], v[128:131], a[176:179]\n"
    "ds_read_b64_tr_b16 v[84:85], v100 offset:28672\n"
    "s_add_u32 s64, s64, 0x80\n"
    "v_mfma_f32_16x16x32_bf16 a[200:203], v[88:91], v[132:135], a[200:203]\n"
    "ds_read_b64_tr_b16 v[86:87], v100 offset:29184\n"
    "v_mfma_f32_16x16x32_bf16 a[224:227], v[88:91], v[136:139], a[224:227]\n"
    "s_and_b32 s64, s64, 0x3ff\n"
    "v_mfma_f32_16x16x32_bf16 a[248:251], v[88:91], v[140:143], a[248:251]\n"
    "s_waitcnt lgkmcnt(15)\n"
    "v_mfma_f32_16x16x32_bf16 a[180:183], v[92:95], v[128:131], a[180:183]\n"
    "ds_read_b64_tr_b16 v[88:89], v101 offset:28672\n"
    "ds_read_b64_tr_b16 v[90:91], v101 offset:29184\n"
    "v_mfma_f32_16x16x32_bf16 a[204:207], v[92:95], v[132:135], a[204:207]\n"
    "buffer_store_dwordx4 v[240:243], v2, s[8:11], s66 offen sc1\n"
    "v_mfma_f32_16x16x32_bf16 a[228:231], v[92:95], v[136:139], a[228:231]\n"
    "v_mfma_f32_16x16x32_bf16 a[252:255], v[92:95], v[140:143], a[252:255]\n"
    "v_mfma_f32_16x16x32_bf16 a[0:3], v[40:43], v[144:147], a[0:3]\n"
    "ds_read_b64_tr_b16 v[92:93], v102 offset:28672\n"
    "ds_read_b64_tr_b16 v[94:95], v102 offset:29184\n"
    "v_mfma_f32_16x16x32_bf16 a[32:35], v[40:43], v[148:151], a[32:35]\n"
    "v_mfma_f32_16x16x32_bf16 a[64:67], v[40:43], v[152:155], a[64:67]\n"
    "v_mfma_f32_16x16x32_bf16 a[96:99], v[40:43], v[156:159], a[96:99]\n"
    "v_mfma_f32_16x16x32_bf16 a[4:7], v[44:47], v[144:147], a[4:7]\n"
    "v_mfma_f32_16x16x32_bf16 a[36:39], v[44:47], v[148:151], a[36:39]\n"
    "v_mfma_f32_16x16x32_bf16 a[68:71], v[44:47], v[152:155], a[68:71]\n"
    "s_waitcnt vmcnt(10)\n"
    "s_waitcnt lgkmcnt(0)\n"
    "s_barrier\n"
    "v_mfma_f32_16x16x32_bf16 a[100:103], v[44:47], v[156:159], a[100:103]\n"
    "ds_read_b64_tr_b16 v[40:41], v112 offset:32768\n"
    "s_add_u32 m0, s59, 0\n"
    "v_mfma_f32_16x16x32_bf16 a[8:11], v[48:51], v[144:147], a[8:11]\n"
    "ds_read_b64_tr_b16 v[44:45], v113 offset:32768\n"
    "ds_read_b64_tr_b16 v[42:43], v112 offset:33280\n"
    "v_mfma_f32_16x16x32_bf16 a[40:43], v[48:51], v[148:151], a[40:43]\n"
    "buffer_load_dwordx4 v106, s[16:19], s90 offen lds\n"
    "ds_read_b64_tr_b16 v[46:47], v113 offset:33280\n"
    "v_mfma_f32_16x16x32_bf16 a[72:75], v[48:51], v[152:155], a[72:75]\n"
    "v_mfma_f32_16x16x32_bf16 a[104:107], v[48:51], v[156:159], a[104:107]\n"
    "ds_read_b128 v[128:131], v108\n"
    "s_add_u32 m0, s59, 0x400\n"
    "v_mfma_f32_16x16x32_bf16 a[12:15], v[52:55], v[144:147], a[12:15]\n"
    "ds_read_b64_tr_b16 v[48:49], v114 offset:32768\n"
    "ds_read_b64_tr_b16 v[50:51], v114 offset:33280\n"
    "v_mfma_f32_16x16x32_bf16 a[44:47], v[52:55], v[148:151], a[44:47]\n"
    "buffer_load_dwordx4 v107, s[16:19], s90 offen lds\n"
    "ds_read_b128 v[132:135], v108 offset:2048\n"
    "v_mfma_f32_16x16x32_bf16 a[76:79], v[52:55], v[152:155], a[76:79]\n"
    "ds_read_b128 v[136:139], v108 offset:4096\n"
    "ds_read_b128 v[140:143], v108 offset:6144\n"
    "v_mfma_f32_16x16x32_bf16 a[108:111], v[52:55], v[156:159], a[108:111]\n"
    "s_add_u32 m0, s59, 0x800\n"
    "v_mfma_f32_16x16x32_bf16 a[16:19], v[56:59], v[144:147], a[16:19]\n"
    "ds_read_b64_tr_b16 v[52:53], v115 offset:32768\n"
    "ds_read_b64_tr_b16 v[54:55], v115 offset:33280\n"
    "v_mfma_f32_16x16x32_bf16 a[48:51], v[56:59], v[148:151], a[48:51]\n"
    "buffer_load_dwordx4 v121, s[16:19], s90 offen lds\n"
    "v_mfma_f32_16x16x32_bf16 a[80:83], v[56:59], v[152:155], a[80:83]\n"
    "v_mfma_f32_16x16x32_bf16 a[112:115], v[56:59], v[156:159], a[112:115]\n"
    "s_add_u32 m0, s59, 0xc00\n"
    "v_mfma_f32_16x16x32_bf16 a[20:23], v[60:63], v[144:147], a[20:23]\n"
    "ds_read_b64_tr_b16 v[56:57], v112 offset:33792\n"
    "ds_read_b64_tr_b16 v[58:59], v112 offset:34304\n"
    "v_mfma_f32_16x16x32_bf16 a[52:55], v[60:63], v[148:151], a[52:55]\n"
    "buffer_load_dwordx4 v123, s[16:19], s90 offen lds\n"
    "v_mfma_f32_16x16x32_bf16 a[84:87], v[60:63], v[152:155], a[84:87]\n"
    "v_mfma_f32_16x16x32_bf16 a[116:119], v[60:63], v[156:159], a[116:119]\n"
    "v_mfma_f32_16x16x32_bf16 a[24:27], v[64:67], v[144:147], a[24:27]\n"
    "ds_read_b64_tr_b16 v[60:61], v113 offset:33792\n"
    "s_add_u32 s90, s90, s69\n"
    "v_mfma_f32_16x16x32_bf16 a[56:59], v[64:67], v[148:151], a[56:59]\n"
    "ds_read_b64_tr_b16 v[62:63], v113 offset:34304\n"
    "v_mfma_f32_16x16x32_bf16 a[88:91], v[64:67], v[152:155], a[88:91]\n"
    "s_cmp_eq_u32 s90, s67\n"
    "v_mfma_f32_16x16x32_bf16 a[120:123], v[64:67], v[156:159], a[120:123]\n"
    "v_mfma_f32_16x16x32_bf16 a[28:31], v[68:71], v[144:147], a[28:31]\n"
    "ds_read_b64_tr_b16 v[64:65], v114 offset:33792\n"
    "s_cselect_b32 s90, 0, s90\n"
    "v_mfma_f32_16x16x32_bf16 a[60:63], v[68:71], v[148:151], a[60:63]\n"
    "ds_read_b64_tr_b16 v[66:67], v114 offset:34304\n"
    "v_mfma_f32_16x16x32_bf16 a[92:95], v[68:71], v[152:155], a[92:95]\n"
    "s_add_u32 m0, s59, 0x4000\n"
    "v_mfma_f32_16x16x32_bf16 a[124:127], v[68:71], v[156:159], a[124:127]\n"
    "v_mfma_f32_16x16x32_bf16 a[160:163], v[72:75], v[144:147], a[160:163]\n"
    "ds_read_b64_tr_b16 v[68:69], v115 offset:33792\n"
    "buffer_load_dwordx4 v106, s[16:19], s90 offen lds\n"
    "v_mfma_f32_16x16x32_bf16 a[184:187], v[72:75], v[148:151], a[184:187]\n"
    "ds_read_b64_tr_b16 v[70:71], v115 offset:34304\n"
    "v_mfma_f32_16x16x32_bf16 a[208:211], v[72:75], v[152:155], a[208:211]\n"
    "s_add_u32 m0, s59, 0x4400\n"
    "v_mfma_f32_16x16x32_bf16 a[232:235], v[72:75], v[156:159], a[232:235]\n"
    "v_mfma_f32_16x16x32_bf16 a[164:167], v[76:79], v[144:147], a[164:167]\n"
    "ds_read_b64_tr_b16 v[72:73], v100 offset:40960\n"
    "buffer_load_dwordx4 v107, s[16:19], s90 offen lds\n"
    "v_mfma_f32_16x16x32_bf16 a[188:191], v[76:79], v[148:151], a[188:191]\n"
    "ds_read_b64_tr_b16 v[74:75], v100 offset:41472\n"
    "v_mfma_f32_16x16x32_bf16 a[212:215], v[76:79], v[152:155], a[212:215]\n"
    "s_add_u32 m0, s59, 0x4800\n"
    "v_mfma_f32_16x16x32_bf16 a[236:239], v[76:79], v[156:159], a[236:239]\n"
    "v_mfma_f32_16x16x32_bf16 a[168:171], v[80:83], v[144:147], a[168:171]\n"
    "ds_read_b64_tr_b16 v[76:77], v101 offset:40960\n"
    "ds_read_b64_tr_b16 v[78:79], v101 offset:41472\n"
    "v_mfma_f32_16x16x32_bf16 a[192:195], v[80:83], v[148:151], a[192:195]\n"
    "buffer_load_dwordx4 v121, s[16:19], s90 offen lds\n"
    "v_mfma_f32_16x16x32_bf16 a[216:219], v[80:83], v[152:155], a[216:219]\n"
    "v_mfma_f32_16x16x32_bf16 a[240:243], v[80:83], v[156:159], a[240:243]\n"
    "s_add_u32 m0, s59, 0x4c00\n"
    "v_mfma_f32_16x16x32_bf16 a[172:175], v[84:87], v[144:147], a[172:175]\n"
    "ds_read_b64_tr_b16 v[80:81], v102 offset:40960\n"
    "ds_read_b64_tr_b16 v[82:83], v102 offset:41472\n"
    "v_mfma_f32_16x16x32_bf16 a[196:199], v[84:87], v[148:151], a[196:199]\n"
    "buffer_load_dwordx4 v123, s[16:19], s90 offen lds\n"
    "v_mfma_f32_16x16x32_bf16 a[220:223], v[84:87], v[152:155], a[220:223]\n"
    "v_mfma_f32_16x16x32_bf16 a[244:247], v[84:87], v[156:159], a[244:247]\n"
    "s_add_u32 s90, s90, s69\n"
    "v_mfma_f32_16x16x32_bf16 a[176:179], v[88:91], v[144:147], a[176:179]\n"
    "ds_read_b64_tr_b16 v[84:85], v100 offset:45056\n"
    "ds_read_b64_tr_b16 v[86:87], v100 offset:45568\n"
    "v_mfma_f32_16x16x32_bf16 a[200:203], v[88:91], v[148:151], a[200:203]\n"
    "s_cmp_eq_u32 s90, s67\n"
    "v_mfma_f32_16x16x32_bf16 a[224:227], v[88:91], v[152:155], a[224:227]\n"
    "v_mfma_f32_16x16x32_bf16 a[248:251], v[88:91], v[156:159], a[248:251]\n"
    "s_cselect_b32 s90, 0, s90\n"
    "v_mfma_f32_16x16x32_bf16 a[180:183], v[92:95], v[144:147], a[180:183]\n"
    "ds_read_b64_tr_b16 v[88:89], v101 offset:45056\n"
    "ds_read_b64_tr_b16 v[90:91], v101 offset:45568\n"
    "v_mfma_f32_16x16x32_bf16 a[204:207], v[92:95], v[148:151], a[204:207]\n"
    "buffer_store_dwordx4 v[244:247], v2, s[8:11], s66 offen offset:1024 sc1\n"
    "v_mfma_f32_16x16x32_bf16 a[228:231], v[92:95], v[152:155], a[228:231]\n"
    "v_mfma_f32_16x16x32_bf16 a[252:255], v[92:95], v[156:159], a[252:255]\n"
    "ds_read_b64_tr_b16 v[92:93], v102 offset:45056\n"
    "ds_read_b64_tr_b16 v[94:95], v102 offset:45568\n"
    "s_waitcnt lgkmcnt(15)\n"
    "v_mfma_f32_16x16x32_bf16 a[0:3], v[40:43], v[128:131], a[0:3]\n"
    "s_add_u32 m0, s91, 0x20000\n"
    "v_mfma_f32_16x16x32_bf16 a[32:35], v[40:43], v[132:135], a[32:35]\n"
    "v_mfma_f32_16x16x32_bf16 a[64:67], v[40:43], v[136:139], a[64:67]\n"
    "buffer_load_dwordx4 v110, s[8:11], s64 offen sc1 lds\n"
    "v_mfma_f32_16x16x32_bf16 a[96:99], v[40:43], v[140:143], a[96:99]\n"
    "v_mfma_f32_16x16x32_bf16 a[4:7], v[44:47], v[128:131], a[4:7]\n"
    "ds_read_b64_tr_b16 v[40:41], v112 offset:49152\n"
    "ds_read_b64_tr_b16 v[42:43], v112 offset:49664\n"
    "v_mfma_f32_16x16x32_bf16 a[36:39], v[44:47], v[132:135], a[36:39]\n"
    "s_add_u32 m0, s91, 0x20400\n"
    "v_mfma_f32_16x16x32_bf16 a[68:71], v[44:47], v[136:139], a[68:71]\n"
    "v_mfma_f32_16x16x32_bf16 a[100:103], v[44:47], v[140:143], a[100:103]\n"
    "v_mfma_f32_16x16x32_bf16 a[8:11], v[48:51], v[128:131], a[8:11]\n"
    "ds_read_b64_tr_b16 v[44:45], v113 offset:49152\n"
    "buffer_load_dwordx4 v111, s[8:11], s64 offen sc1 lds\n"
    "v_mfma_f32_16x16x32_bf16 a[40:43], v[48:51], v[132:135], a[40:43]\n"
    "ds_read_b64_tr_b16 v[46:47], v113 offset:49664\n"
    "v_mfma_f32_16x16x32_bf16 a[72:75], v[48:51], v[136:139], a[72:75]\n"
    "ds_read_b128 v[144:147], v109\n"
    "s_add_u32 m0, s91, 0x20800\n"
    "v_mfma_f32_16x16x32_bf16 a[104:107], v[48:51], v[140:143], a[104:107]\n"
    "ds_read_b128 v[148:151], v109 offset:2048\n"
    "ds_read_b128 v[152:155], v109 offset:4096\n"
    "v_mfma_f32_16x16x32_bf16 a[12:15], v[52:55], v[128:131], a[12:15]\n"
    "ds_read_b64_tr_b16 v[48:49], v114 offset:49152\n"
    "ds_read_b64_tr_b16 v[50:51], v114 offset:49664\n"
    "v_mfma_f32_16x16x32_bf16 a[44:47], v[52:55], v[132:135], a[44:47]\n"
    "ds_read_b128 v[156:159], v109 offset:6144\n"
    "s_add_u32 s83, s64, s62\n"
    "v_mfma_f32_16x16x32_bf16 a[76:79], v[52:55], v[136:139], a[76:79]\n"
    "v_mfma_f32_16x16x32_bf16 a[108:111], v[52:55], v[140:143], a[108:111]\n"
    "v_mfma_f32_16x16x32_bf16 a[16:19], v[56:59], v[128:131], a[16:19]\n"
    "ds_read_b64_tr_b16 v[52:53], v115 offset:49152\n"
    "buffer_load_dwordx4 v110, s[8:11], s83 offen sc1 lds\n"
    "v_mfma_f32_16x16x32_bf16 a[48:51], v[56:59], v[132:135], a[48:51]\n"
    "ds_read_b64_tr_b16 v[54:55], v115 offset:49664\n"
    "v_mfma_f32_16x16x32_bf16 a[80:83], v[56:59], v[136:139], a[80:83]\n"
    "s_add_u32 m0, s91, 0x20c00\n"
    "v_mfma_f32_16x16x32_bf16 a[112:115], v[56:59], v[140:143], a[112:115]\n"
    "v_mfma_f32_16x16x32_bf16 a[20:23], v[60:63], v[128:131], a[20:23]\n"
    "ds_read_b64_tr_b16 v[56:57], v112 offset:50176\n"
    "ds_read_b64_tr_b16 v[58:59], v112 offset:50688\n"
    "v_mfma_f32_16x16x32_bf16 a[52:55], v[60:63], v[132:135], a[52:55]\n"
    "buffer_load_dwordx4 v111, s[8:11], s83 offen sc1 lds\n"
    "v_mfma_f32_16x16x32_bf16 a[84:87], v[60:63], v[136:139], a[84:87]\n"
    "v_mfma_f32_16x16x32_bf16 a[116:119], v[60:63], v[140:143], a[116:119]\n"
    "s_waitcnt lgkmcnt(15)\n"
    "v_mfma_f32_16x16x32_bf16 a[24:27], v[64:67], v[128:131], a[24:27]\n"
    "ds_read_b64_tr_b16 v[60:61], v113 offset:50176\n"
    "s_add_u32 m0, s91, 0x21000\n"
    "v_mfma_f32_16x16x32_bf16 a[56:59], v[64:67], v[132:135], a[56:59]\n"
    "ds_read_b64_tr_b16 v[62:63], v113 offset:50688\n"
    "v_mfma_f32_16x16x32_bf16 a[88:91], v[64:67], v[136:139], a[88:91]\n"
    "buffer_load_dwordx4 v125, s[8:11], s64 offen sc1 lds\n"
    "v_mfma_f32_16x16x32_bf16 a[120:123], v[64:67], v[140:143], a[120:123]\n"
    "v_mfma_f32_16x16x32_bf16 a[28:31], v[68:71], v[128:131], a[28:31]\n"
    "ds_read_b64_tr_b16 v[64:65], v114 offset:50176\n"
    "ds_read_b64_tr_b16 v[66:67], v114 offset:50688\n"
    "v_mfma_f32_16x16x32_bf16 a[60:63], v[68:71], v[132:135], a[60:63]\n"
    "s_add_u32 m0, s91, 0x21400\n"
    "v_mfma_f32_16x16x32_bf16 a[92:95], v[68:71], v[136:139], a[92:95]\n"
    "v_mfma_f32_16x16x32_bf16 a[124:127], v[68:71], v[140:143], a[124:127]\n"
    "v_mfma_f32_16x16x32_bf16 a[160:163], v[72:75], v[128:131], a[160:163]\n"
    "ds_read_b64_tr_b16 v[68:69], v115 offset:50176\n"
    "buffer_load_dwordx4 v126, s[8:11], s64 offen sc1 lds\n"
    "v_mfma_f32_16x16x32_bf16 a[184:187], v[72:75], v[132:135], a[184:187]\n"
    "ds_read_b64_tr_b16 v[70:71], v115 offset:50688\n"
    "v_mfma_f32_16x16x32_bf16 a[208:211], v[72:75], v[136:139], a[208:211]\n"
    "s_add_u32 m0, s91, 0x21800\n"
    "v_mfma_f32_16x16x32_bf16 a[232:235], v[72:75], v[140:143], a[232:235]\n"
    "v_mfma_f32_16x16x32_bf16 a[164:167], v[76:79], v[128:131], a[164:167]\n"
    "ds_read_b64_tr_b16 v[72:73], v100 offset:57344\n"
    "ds_read_b64_tr_b16 v[74:75], v100 offset:57856\n"
    "v_mfma_f32_16x16x32_bf16 a[188:191], v[76:79], v[132:135], a[188:191]\n"
    "s_add_u32 s83, s64, s62\n"
    "v_mfma_f32_16x16x32_bf16 a[212:215], v[76:79], v[136:139], a[212:215]\n"
    "v_mfma_f32_16x16x32_bf16 a[236:239], v[76:79], v[140:143], a[236:239]\n"
    "v_mfma_f32_16x16x32_bf16 a[168:171], v[80:83], v[128:131], a[168:171]\n"
    "ds_read_b64_tr_b16 v[76:77], v101 offset:57344\n"
    "buffer_load_dwordx4 v125, s[8:11], s83 offen sc1 lds\n"
    "v_mfma_f32_16x16x32_bf16 a[192:195], v[80:83], v[132:135], a[192:195]\n"
    "ds_read_b64_tr_b16 v[78:79], v101 offset:57856\n"
    "v_mfma_f32_16x16x32_bf16 a[216:219], v[80:83], v[136:139], a[216:219]\n"
    "s_add_u32 m0, s91, 0x21c00\n"
    "v_mfma_f32_16x16x32_bf16 a[240:243], v[80:83], v[140:143], a[240:243]\n"
    "v_mfma_f32_16x16x32_bf16 a[172:175], v[84:87], v[128:131], a[172:175]\n"
    "ds_read_b64_tr_b16 v[80:81], v102 offset:57344\n"
    "ds_read_b64_tr_b16 v[82:83], v102 offset:57856\n"
    "v_mfma_f32_16x16x32_bf16 a[196:199], v[84:87], v[132:135], a[196:199]\n"
    "buffer_load_dwordx4 v126, s[8:11], s83 offen sc1 lds\n"
    "v_mfma_f32_16x16x32_bf16 a[220:223], v[84:87], v[136:139], a[220:223]\n"
    "v_mfma_f32_16x16x32_bf16 a[244:247], v[84:87], v[140:143], a[244:247]\n"
    "v_mfma_f32_16x16x32_bf16 a[176:179], v[88:91], v[128:131], a[176:179]\n"
    "ds_read_b64_tr_b16 v[84:85], v100 offset:61440\n"
    "s_add_u32 s64, s64, 0x80\n"
    "v_mfma_f32_16x16x32_bf16 a[200:203], v[88:91], v[132:135], a[200:203]\n"
    "ds_read_b64_tr_b16 v[86:87], v100 offset:61952\n"
    "v_mfma_f32_16x16x32_bf16 a[224:227], v[88:91], v[136:139], a[224:227]\n"
    "s_and_b32 s64, s64, 0x3ff\n"
    "v_mfma_f32_16x16x32_bf16 a[248:251], v[88:91], v[140:143], a[248:251]\n"
    "s_waitcnt lgkmcnt(15)\n"
    "v_mfma_f32_16x16x32_bf16 a[180:183], v[92:95], v[128:131], a[180:183]\n"
    "ds_read_b64_tr_b16 v[88:89], v101 offset:61440\n"
    "ds_read_b64_tr_b16 v[90:91], v101 offset:61952\n"
    "v_mfma_f32_16x16x32_bf16 a[204:207], v[92:95], v[132:135], a[204:207]\n"
    "buffer_store_dwordx4 v[248:251], v2, s[8:11], s66 offen offset:2048 sc1\n"
    "v_mfma_f32_16x16x32_bf16 a[228:231], v[92:95], v[136:139], a[228:231]\n"
    "v_mfma_f32_16x16x32_bf16 a[252:255], v[92:95], v[140:143], a[252:255]\n"
    "v_mfma_f32_16x16x32_bf16 a[0:3], v[40:43], v[144:147], a[0:3]\n"
    "ds_read_b64_tr_b16 v[92:93], v102 offset:61440\n"
    "ds_read_b64_tr_b16 v[94:95], v102 offset:61952\n"
    "v_mfma_f32_16x16x32_bf16 a[32:35], v[40:43], v[148:151], a[32:35]\n"
    "v_mfma_f32_16x16x32_bf16 a[64:67], v[40:43], v[152:155], a[64:67]\n"
    "v_mfma_f32_16x16x32_bf16 a[96:99], v[40:43], v[156:159], a[96:99]\n"
    "v_mfma_f32_16x16x32_bf16 a[4:7], v[44:47], v[144:147], a[4:7]\n"
    "v_mfma_f32_16x16x32_bf16 a[36:39], v[44:47], v[148:151], a[36:39]\n"
    "v_mfma_f32_16x16x32_bf16 a[68:71], v[44:47], v[152:155], a[68:71]\n"
    "s_waitcnt vmcnt(10)\n"
    "s_waitcnt lgkmcnt(0)\n"
    "s_barrier\n"
    "v_mfma_f32_16x16x32_bf16 a[100:103], v[44:47], v[156:159], a[100:103]\n"
    "ds_read_b64_tr_b16 v[40:41], v112\n"
    "s_add_u32 m0, s59, 0x8000\n"
    "v_mfma_f32_16x16x32_bf16 a[8:11], v[48:51], v[144:147], a[8:11]\n"
    "ds_read_b64_tr_b16 v[44:45], v113\n"
    "buffer_load_dwordx4 v106, s[16:19], s90 offen lds\n"
    "v_mfma_f32_16x16x32_bf16 a[40:43], v[48:51], v[148:151], a[40:43]\n"
    "ds_read_b64_tr_b16 v[42:43], v112 offset:512\n"
    "ds_read_b64_tr_b16 v[46:47], v113 offset:512\n"
    "v_mfma_f32_16x16x32_bf16 a[72:75], v[48:51], v[152:155], a[72:75]\n"
    "s_add_u32 m0, s59, 0x8400\n"
    "v_mfma_f32_16x16x32_bf16 a[104:107], v[48:51], v[156:159], a[104:107]\n"
    "ds_read_b128 v[128:131], v108 offset:32768\n"
    "ds_read_b128 v[132:135], v108 offset:34816\n"
    "v_mfma_f32_16x16x32_bf16 a[12:15], v[52:55], v[144:147], a[12:15]\n"
    "ds_read_b64_tr_b16 v[48:49], v114\n"
    "buffer_load_dwordx4 v107, s[16:19], s90 offen lds\n"
    "v_mfma_f32_16x16x32_bf16 a[44:47], v[52:55], v[148:151], a[44:47]\n"
    "ds_read_b64_tr_b16 v[50:51], v114 offset:512\n"
    "ds_read_b128 v[136:139], v108 offset:36864\n"
    "v_mfma_f32_16x16x32_bf16 a[76:79], v[52:55], v[152:155], a[76:79]\n"
    "ds_read_b128 v[140:143], v108 offset:38912\n"
    "s_add_u32 m0, s59, 0x8800\n"
    "v_mfma_f32_16x16x32_bf16 a[108:111], v[52:55], v[156:159], a[108:111]\n"
    "buffer_load_dwordx4 v121, s[16:19], s90 offen lds\n"
    "v_mfma_f32_16x16x32_bf16 a[16:19], v[56:59], v[144:147], a[16:19]\n"
    "ds_read_b64_tr_b16 v[52:53], v115\n"
    "ds_read_b64_tr_b16 v[54:55], v115 offset:512\n"
    "v_mfma_f32_16x16x32_bf16 a[48:51], v[56:59], v[148:151], a[48:51]\n"
    "s_add_u32 m0, s59, 0x8c00\n"
    "v_mfma_f32_16x16x32_bf16 a[80:83], v[56:59], v[152:155], a[80:83]\n"
    "v_mfma_f32_16x16x32_bf16 a[112:115], v[56:59], v[156:159], a[112:115]\n"
    "buffer_load_dwordx4 v123, s[16:19], s90 offen lds\n"
    "v_mfma_f32_16x16x32_bf16 a[20:23], v[60:63], v[144:147], a[20:23]\n"
    "ds_read_b64_tr_b16 v[56:57], v112 offset:1024\n"
    "ds_read_b64_tr_b16 v[58:59], v112 offset:1536\n"
    "v_mfma_f32_16x16x32_bf16 a[52:55], v[60:63], v[148:151], a[52:55]\n"
    "s_add_u32 s90, s90, s69\n"
    "v_mfma_f32_16x16x32_bf16 a[84:87], v[60:63], v[152:155], a[84:87]\n"
    "s_cmp_eq_u32 s90, s67\n"
    "v_mfma_f32_16x16x32_bf16 a[116:119], v[60:63], v[156:159], a[116:119]\n"
    "v_mfma_f32_16x16x32_bf16 a[24:27], v[64:67], v[144:147], a[24:27]\n"
    "ds_read_b64_tr_b16 v[60:61], v113 offset:1024\n"
    "s_cselect_b32 s90, 0, s90\n"
    "v_mfma_f32_16x16x32_bf16 a[56:59], v[64:67], v[148:151], a[56:59]\n"
    "ds_read_b64_tr_b16 v[62:63], v113 offset:1536\n"
    "v_mfma_f32_16x16x32_bf16 a[88:91], v[64:67], v[152:155], a[88:91]\n"
    "s_add_u32 m0, s59, 0xc000\n"
    "v_mfma_f32_16x16x32_bf16 a[120:123], v[64:67], v[156:159], a[120:123]\n"
    "v_mfma_f32_16x16x32_bf16 a[28:31], v[68:71], v[144:147], a[28:31]\n"
    "ds_read_b64_tr_b16 v[64:65], v114 offset:1024\n"
    "buffer_load_dwordx4 v106, s[16:19], s90 offen lds\n"
    "v_mfma_f32_16x16x32_bf16 a[60:63], v[68:71], v[148:151], a[60:63]\n"
    "ds_read_b64_tr_b16 v[66:67], v114 offset:1536\n"
    "s_add_u32 m0, s59, 0xc400\n"
    "v_mfma_f32_16x16x32_bf16 a[92:95], v[68:71], v[152:155], a[92:95]\n"
    "v_mfma_f32_16x16x32_bf16 a[124:127], v[68:71], v[156:159], a[124:127]\n"
    "buffer_load_dwordx4 v107, s[16:19], s90 offen lds\n"
    "v_mfma_f32_16x16x32_bf16 a[160:163], v[72:75], v[144:147], a[160:163]\n"
    "ds_read_b64_tr_b16 v[68:69], v115 offset:1024\n"
    "ds_read_b64_tr_b16 v[70:71], v115 offset:1536\n"
    "v_mfma_f32_16x16x32_bf16 a[184:187], v[72:75], v[148:151], a[184:187]\n"
    "s_add_u32 m0, s59, 0xc800\n"
    "v_mfma_f32_16x16x32_bf16 a[208:211], v[72:75], v[152:155], a[208:211]\n"
    "v_mfma_f32_16x16x32_bf16 a[232:235], v[72:75], v[156:159], a[232:235]\n"
    "buffer_load_dwordx4 v121, s[16:19], s90 offen lds\n"
    "v_mfma_f32_16x16x32_bf16 a[164:167], v[76:79], v[144:147], a[164:167]\n"
    "ds_read_b64_tr_b16 v[72:73], v100 offset:8192\n"
    "s_add_u32 m0, s59, 0xcc00\n"
    "v_mfma_f32_16x16x32_bf16 a[188:191], v[76:79], v[148:151], a[188:191]\n"
    "ds_read_b64_tr_b16 v[74:75], v100 offset:8704\n"
    "v_mfma_f32_16x16x32_bf16 a[212:215], v[76:79], v[152:155], a[212:215]\n"
    "buffer_load_dwordx4 v123, s[16:19], s90 offen lds\n"
    "v_mfma_f32_16x16x32_bf16 a[236:239], v[76:79], v[156:159], a[236:239]\n"
    "v_mfma_f32_16x16x32_bf16 a[168:171], v[80:83], v[144:147], a[168:171]\n"
    "ds_read_b64_tr_b16 v[76:77], v101 offset:8192\n"
    "s_add_u32 s90, s90, s69\n"
    "v_mfma_f32_16x16x32_bf16 a[192:195], v[80:83], v[148:151], a[192:195]\n"
    "ds_read_b64_tr_b16 v[78:79], v101 offset:8704\n"
    "v_mfma_f32_16x16x32_bf16 a[216:219], v[80:83], v[152:155], a[216:219]\n"
    "s_cmp_eq_u32 s90, s67\n"
    "v_mfma_f32_16x16x32_bf16 a[240:243], v[80:83], v[156:159], a[240:243]\n"
    "s_cselect_b32 s90, 0, s90\n"
    "v_mfma_f32_16x16x32_bf16 a[172:175], v[84:87], v[144:147], a[172:175]\n"
    "ds_read_b64_tr_b16 v[80:81], v102 offset:8192\n"
    "ds_read_b64_tr_b16 v[82:83], v102 offset:8704\n"
    "v_mfma_f32_16x16x32_bf16 a[196:199], v[84:87], v[148:151], a[196:199]\n"
    "buffer_store_dwordx4 v[252:255], v2, s[8:11], s66 offen offset:3072 sc1\n"
    "v_mfma_f32_16x16x32_bf16 a[220:223], v[84:87], v[152:155], a[220:223]\n"
    "v_mfma_f32_16x16x32_bf16 a[244:247], v[84:87], v[156:159], a[244:247]\n"
    "s_add_u32 s66, s66, 0x1000\n"
    "v_mfma_f32_16x16x32_bf16 a[176:179], v[88:91], v[144:147], a[176:179]\n"
    "ds_read_b64_tr_b16 v[84:85], v100 offset:12288\n"
    "ds_read_b64_tr_b16 v[86:87], v100 offset:12800\n"
    "v_mfma_f32_16x16x32_bf16 a[200:203], v[88:91], v[148:151], a[200:203]\n"
    "s_mov_b64 s[28:29], s[44:45]\n"
    "v_mfma_f32_16x16x32_bf16 a[224:227], v[88:91], v[152:155], a[224:227]\n"
    "s_mov_b32 s30, Lcodeend-Lstart\n"
    "v_mfma_f32_16x16x32_bf16 a[248:251], v[88:91], v[156:159], a[248:251]\n"
    "v_mfma_f32_16x16x32_bf16 a[180:183], v[92:95], v[144:147], a[180:183]\n"
    "ds_read_b64_tr_b16 v[88:89], v101 offset:12288\n"
    "s_mov_b32 s31, 0x20000\n"
    "v_mfma_f32_16x16x32_bf16 a[204:207], v[92:95], v[148:151], a[204:207]\n"
    "ds_read_b64_tr_b16 v[90:91], v101 offset:12800\n"
    "v_mfma_f32_16x16x32_bf16 a[228:231], v[92:95], v[152:155], a[228:231]\n"
    "buffer_load_dword v124, v2, s[28:31], s32 offen\n"
    "v_mfma_f32_16x16x32_bf16 a[252:255], v[92:95], v[156:159], a[252:255]\n"
    "ds_read_b64_tr_b16 v[92:93], v102 offset:12288\n"
    "ds_read_b64_tr_b16 v[94:95], v102 offset:12800\n"
    "s_waitcnt lgkmcnt(15)\n"
    "v_mfma_f32_16x16x32_bf16 a[0:3], v[40:43], v[128:131], a[0:3]\n"
    "s_lshl_b32 s83, s56, 20\n"
    "v_mfma_f32_16x16x32_bf16 a[32:35], v[40:43], v[132:135], a[32:35]\n"
    "v_mfma_f32_16x16x32_bf16 a[64:67], v[40:43], v[136:139], a[64:67]\n"
    "s_add_u32 s83, s83, 0x220000\n"
    "v_mfma_f32_16x16x32_bf16 a[96:99], v[40:43], v[140:143], a[96:99]\n"
    "v_mfma_f32_16x16x32_bf16 a[4:7], v[44:47], v[128:131], a[4:7]\n"
    "ds_read_b64_tr_b16 v[40:41], v112 offset:16384\n"
    "s_sub_u32 s4, 16, s57\n"
    "v_mfma_f32_16x16x32_bf16 a[36:39], v[44:47], v[132:135], a[36:39]\n"
    "ds_read_b64_tr_b16 v[42:43], v112 offset:16896\n"
    "v_mfma_f32_16x16x32_bf16 a[68:71], v[44:47], v[136:139], a[68:71]\n"
    "v_mfma_f32_16x16x32_bf16 a[100:103], v[44:47], v[140:143], a[100:103]\n"
    "s_lshl_b32 s4, s4, 16\n"
    "v_mfma_f32_16x16x32_bf16 a[8:11], v[48:51], v[128:131], a[8:11]\n"
    "ds_read_b64_tr_b16 v[44:45], v113 offset:16384\n"
    "ds_read_b64_tr_b16 v[46:47], v113 offset:16896\n"
    "v_mfma_f32_16x16x32_bf16 a[40:43], v[48:51], v[132:135], a[40:43]\n"
    "s_mov_b64 s[24:25], s[8:9]\n"
    "v_mfma_f32_16x16x32_bf16 a[72:75], v[48:51], v[136:139], a[72:75]\n"
    "ds_read_b128 v[144:147], v109 offset:32768\n"
    "ds_read_b128 v[148:151], v109 offset:34816\n"
    "v_mfma_f32_16x16x32_bf16 a[104:107], v[48:51], v[140:143], a[104:107]\n"
    "ds_read_b128 v[152:155], v109 offset:36864\n"
    "s_add_u32 s26, s83, s4\n"
    "v_mfma_f32_16x16x32_bf16 a[12:15], v[52:55], v[128:131], a[12:15]\n"
    "ds_read_b64_tr_b16 v[48:49], v114 offset:16384\n"
    "ds_read_b64_tr_b16 v[50:51], v114 offset:16896\n"
    "v_mfma_f32_16x16x32_bf16 a[44:47], v[52:55], v[132:135], a[44:47]\n"
    "ds_read_b128 v[156:159], v109 offset:38912\n"
    "v_mfma_f32_16x16x32_bf16 a[76:79], v[52:55], v[136:139], a[76:79]\n"
    "s_mov_b32 s27, 0x20000\n"
    "v_mfma_f32_16x16x32_bf16 a[108:111], v[52:55], v[140:143], a[108:111]\n"
    "v_mfma_f32_16x16x32_bf16 a[16:19], v[56:59], v[128:131], a[16:19]\n"
    "ds_read_b64_tr_b16 v[52:53], v115 offset:16384\n"
    "s_add_u32 s83, s83, s59\n"
    "v_mfma_f32_16x16x32_bf16 a[48:51], v[56:59], v[132:135], a[48:51]\n"
    "ds_read_b64_tr_b16 v[54:55], v115 offset:16896\n"
    "v_mfma_f32_16x16x32_bf16 a[80:83], v[56:59], v[136:139], a[80:83]\n"
    "s_add_u32 s75, s83, 0x4000\n"
    "v_mfma_f32_16x16x32_bf16 a[112:115], v[56:59], v[140:143], a[112:115]\n"
    "v_mfma_f32_16x16x32_bf16 a[20:23], v[60:63], v[128:131], a[20:23]\n"
    "ds_read_b64_tr_b16 v[56:57], v112 offset:17408\n"
    "ds_read_b64_tr_b16 v[58:59], v112 offset:17920\n"
    "v_mfma_f32_16x16x32_bf16 a[52:55], v[60:63], v[132:135], a[52:55]\n"
    "v_and_b32 v5, 31, v1\n"
    "v_mfma_f32_16x16x32_bf16 a[84:87], v[60:63], v[136:139], a[84:87]\n"
    "v_mfma_f32_16x16x32_bf16 a[116:119], v[60:63], v[140:143], a[116:119]\n"
    "v_lshlrev_b32 v5, 2, v5\n"
    "s_waitcnt lgkmcnt(15)\n"
    "v_mfma_f32_16x16x32_bf16 a[24:27], v[64:67], v[128:131], a[24:27]\n"
    "ds_read_b64_tr_b16 v[60:61], v113 offset:17408\n"
    "ds_read_b64_tr_b16 v[62:63], v113 offset:17920\n"
    "v_mfma_f32_16x16x32_bf16 a[56:59], v[64:67], v[132:135], a[56:59]\n"
    "s_lshl_b32 s7, s56, 7\n"
    "v_mfma_f32_16x16x32_bf16 a[88:91], v[64:67], v[136:139], a[88:91]\n"
    "v_mfma_f32_16x16x32_bf16 a[120:123], v[64:67], v[140:143], a[120:123]\n"
    "v_mfma_f32_16x16x32_bf16 a[28:31], v[68:71], v[128:131], a[28:31]\n"
    "ds_read_b64_tr_b16 v[64:65], v114 offset:17408\n"
    "s_add_u32 s7, s7, 0x2000\n"
    "v_mfma_f32_16x16x32_bf16 a[60:63], v[68:71], v[132:135], a[60:63]\n"
    "ds_read_b64_tr_b16 v[66:67], v114 offset:17920\n"
    "v_mfma_f32_16x16x32_bf16 a[92:95], v[68:71], v[136:139], a[92:95]\n"
    "s_waitcnt vmcnt(1)\n"
    "s_lshl_b32 s4, s94, 5\n"
    "s_lshl_b32 s5, s95, 2\n"
    "s_add_u32 s4, s4, s5\n"
    "s_add_u32 s4, s4, s58\n"
    "s_lshl_b32 s4, s4, 2\n"
    "s_add_u32 s4, s4, 0x2000\n"
    "v_mov_b32 v6, 0\n"
    "s_mov_b64 exec, 1\n"
    "buffer_store_dword v7, v6, s[8:11], s4 offen sc1\n"
    "s_mov_b64 exec, -1\n"
    "v_mfma_f32_16x16x32_bf16 a[124:127], v[68:71], v[140:143], a[124:127]\n"
    "v_mfma_f32_16x16x32_bf16 a[160:163], v[72:75], v[128:131], a[160:163]\n"
    "ds_read_b64_tr_b16 v[68:69], v115 offset:17408\n"
    "ds_read_b64_tr_b16 v[70:71], v115 offset:17920\n"
    "v_mfma_f32_16x16x32_bf16 a[184:187], v[72:75], v[132:135], a[184:187]\n"
    "v_mfma_f32_16x16x32_bf16 a[208:211], v[72:75], v[136:139], a[208:211]\n"
    "v_mfma_f32_16x16x32_bf16 a[232:235], v[72:75], v[140:143], a[232:235]\n"
    "v_mfma_f32_16x16x32_bf16 a[164:167], v[76:79], v[128:131], a[164:167]\n"
    "ds_read_b64_tr_b16 v[72:73], v100 offset:24576\n"
    "ds_read_b64_tr_b16 v[74:75], v100 offset:25088\n"
    "v_mfma_f32_16x16x32_bf16 a[188:191], v[76:79], v[132:135], a[188:191]\n"
    "v_mfma_f32_16x16x32_bf16 a[212:215], v[76:79], v[136:139], a[212:215]\n"
    "v_mfma_f32_16x16x32_bf16 a[236:239], v[76:79], v[140:143], a[236:239]\n"
    "v_mfma_f32_16x16x32_bf16 a[168:171], v[80:83], v[128:131], a[168:171]\n"
    "ds_read_b64_tr_b16 v[76:77], v101 offset:24576\n"
    "ds_read_b64_tr_b16 v[78:79], v101 offset:25088\n"
    "v_mfma_f32_16x16x32_bf16 a[192:195], v[80:83], v[132:135], a[192:195]\n"
    "v_mfma_f32_16x16x32_bf16 a[216:219], v[80:83], v[136:139], a[216:219]\n"
    "v_mfma_f32_16x16x32_bf16 a[240:243], v[80:83], v[140:143], a[240:243]\n"
    "v_mfma_f32_16x16x32_bf16 a[172:175], v[84:87], v[128:131], a[172:175]\n"
    "ds_read_b64_tr_b16 v[80:81], v102 offset:24576\n"
    "ds_read_b64_tr_b16 v[82:83], v102 offset:25088\n"
    "v_mfma_f32_16x16x32_bf16 a[196:199], v[84:87], v[132:135], a[196:199]\n"
    "v_mfma_f32_16x16x32_bf16 a[220:223], v[84:87], v[136:139], a[220:223]\n"
    "v_mfma_f32_16x16x32_bf16 a[244:247], v[84:87], v[140:143], a[244:247]\n"
    "v_mfma_f32_16x16x32_bf16 a[176:179], v[88:91], v[128:131], a[176:179]\n"
    "ds_read_b64_tr_b16 v[84:85], v100 offset:28672\n"
    "ds_read_b64_tr_b16 v[86:87], v100 offset:29184\n"
    "v_mfma_f32_16x16x32_bf16 a[200:203], v[88:91], v[132:135], a[200:203]\n"
    "v_mfma_f32_16x16x32_bf16 a[224:227], v[88:91], v[136:139], a[224:227]\n"
    "v_mfma_f32_16x16x32_bf16 a[248:251], v[88:91], v[140:143], a[248:251]\n"
    "s_waitcnt lgkmcnt(15)\n"
    "v_mfma_f32_16x16x32_bf16 a[180:183], v[92:95], v[128:131], a[180:183]\n"
    "ds_read_b64_tr_b16 v[88:89], v101 offset:28672\n"
    "ds_read_b64_tr_b16 v[90:91], v101 offset:29184\n"
    "v_mfma_f32_16x16x32_bf16 a[204:207], v[92:95], v[132:135], a[204:207]\n"
    "v_mfma_f32_16x16x32_bf16 a[228:231], v[92:95], v[136:139], a[228:231]\n"
    "v_mfma_f32_16x16x32_bf16 a[252:255], v[92:95], v[140:143], a[252:255]\n"
    "v_mfma_f32_16x16x32_bf16 a[0:3], v[40:43], v[144:147], a[0:3]\n"
    "ds_read_b64_tr_b16 v[92:93], v102 offset:28672\n"
    "ds_read_b64_tr_b16 v[94:95], v102 offset:29184\n"
    "v_mfma_f32_16x16x32_bf16 a[32:35], v[40:43], v[148:151], a[32:35]\n"
    "v_mfma_f32_16x16x32_bf16 a[64:67], v[40:43], v[152:155], a[64:67]\n"
    "v_mfma_f32_16x16x32_bf16 a[96:99], v[40:43], v[156:159], a[96:99]\n"
    "v_mfma_f32_16x16x32_bf16 a[4:7], v[44:47], v[144:147], a[4:7]\n"
    "v_mfma_f32_16x16x32_bf16 a[36:39], v[44:47], v[148:151], a[36:39]\n"
    "v_mfma_f32_16x16x32_bf16 a[68:71], v[44:47], v[152:155], a[68:71]\n"
    "s_waitcnt lgkmcnt(0)\n"
    "s_barrier\n"
    "v_mfma_f32_16x16x32_bf16 a[100:103], v[44:47], v[156:159], a[100:103]\n"
    "ds_read_b64_tr_b16 v[40:41], v112 offset:32768\n"
    "s_lshr_b32 s63, s60, 1\n"
    "v_mfma_f32_16x16x32_bf16 a[8:11], v[48:51], v[144:147], a[8:11]\n"
    "ds_read_b64_tr_b16 v[44:45], v113 offset:32768\n"
    "s_and_b32 s64, s56, 3\n"
    "v_mfma_f32_16x16x32_bf16 a[40:43], v[48:51], v[148:151], a[40:43]\n"
    "ds_read_b64_tr_b16 v[42:43], v112 offset:33280\n"
    "s_sub_u32 s4, 15, s57\n"
    "v_mfma_f32_16x16x32_bf16 a[72:75], v[48:51], v[152:155], a[72:75]\n"
    "ds_read_b64_tr_b16 v[46:47], v113 offset:33280\n"
    "v_mfma_f32_16x16x32_bf16 a[104:107], v[48:51], v[156:159], a[104:107]\n"
    "s_xor_b32 s60, s60, 0\n"
    "s_bitcmp1_b32 s60, 0\n"
    "v_mfma_f32_16x16x32_bf16 a[12:15], v[52:55], v[144:147], a[12:15]\n"
    "ds_read_b64_tr_b16 v[48:49], v114 offset:32768\n"
    "s_cselect_b32 s65, s4, s57\n"
    "v_mfma_f32_16x16x32_bf16 a[44:47], v[52:55], v[148:151], a[44:47]\n"
    "ds_read_b128 v[128:131], v119\n"
    "s_lshl_b32 s65, s65, 2\n"
    "v_mfma_f32_16x16x32_bf16 a[76:79], v[52:55], v[152:155], a[76:79]\n"
    "ds_read_b64_tr_b16 v[50:51], v114 offset:33280\n"
    "s_add_u32 s65, s65, s58\n"
    "v_mfma_f32_16x16x32_bf16 a[108:111], v[52:55], v[156:159], a[108:111]\n"
    "s_lshl_b32 s6, s64, 8\n"
    "s_sub_u32 s5, 0x158000, s6\n"
    "v_mfma_f32_16x16x32_bf16 a[16:19], v[56:59], v[144:147], a[16:19]\n"
    "ds_read_b64_tr_b16 v[52:53], v115 offset:32768\n"
    "s_add_u32 s16, s50, s6\n"
    "v_mfma_f32_16x16x32_bf16 a[48:51], v[56:59], v[148:151], a[48:51]\n"
    "ds_read_b64_tr_b16 v[54:55], v115 offset:33280\n"
    "ds_read_b128 v[132:135], v119 offset:2048\n"
    "v_mfma_f32_16x16x32_bf16 a[80:83], v[56:59], v[152:155], a[80:83]\n"
    "s_addc_u32 s17, s51, 0\n"
    "s_and_b32 s17, s17, 0xffff\n"
    "v_mfma_f32_16x16x32_bf16 a[112:115], v[56:59], v[156:159], a[112:115]\n"
    "s_mov_b32 s18, s5\n"
    "s_mov_b32 s19, 0x20000\n"
    "v_mfma_f32_16x16x32_bf16 a[20:23], v[60:63], v[144:147], a[20:23]\n"
    "ds_read_b64_tr_b16 v[56:57], v112 offset:33792\n"
    "s_sub_u32 s5, 0x400000, s6\n"
    "v_mfma_f32_16x16x32_bf16 a[52:55], v[60:63], v[148:151], a[52:55]\n"
    "ds_read_b64_tr_b16 v[58:59], v112 offset:34304\n"
    "ds_read_b128 v[136:139], v119 offset:4096\n"
    "v_mfma_f32_16x16x32_bf16 a[84:87], v[60:63], v[152:155], a[84:87]\n"
    "s_add_u32 s12, s42, s6\n"
    "s_addc_u32 s13, s43, 0\n"
    "v_mfma_f32_16x16x32_bf16 a[116:119], v[60:63], v[156:159], a[116:119]\n"
    "s_and_b32 s13, s13, 0xffff\n"
    "s_mov_b32 s14, s5\n"
    "v_mfma_f32_16x16x32_bf16 a[24:27], v[64:67], v[144:147], a[24:27]\n"
    "ds_read_b64_tr_b16 v[60:61], v113 offset:33792\n"
    "ds_read_b64_tr_b16 v[62:63], v113 offset:34304\n"
    "v_mfma_f32_16x16x32_bf16 a[56:59], v[64:67], v[148:151], a[56:59]\n"
    "ds_read_b128 v[140:143], v119 offset:6144\n"
    "s_mov_b32 s15, 0x20000\n"
    "v_mfma_f32_16x16x32_bf16 a[88:91], v[64:67], v[152:155], a[88:91]\n"
    "v_bfe_u32 v3, v1, 4, 2\n"
    "v_lshlrev_b32 v3, 1, v3\n"
    "v_mfma_f32_16x16x32_bf16 a[120:123], v[64:67], v[156:159], a[120:123]\n"
    "v_and_b32 v4, 7, v1\n"
    "v_xor_b32 v3, v3, v4\n"
    "v_mfma_f32_16x16x32_bf16 a[28:31], v[68:71], v[144:147], a[28:31]\n"
    "ds_read_b64_tr_b16 v[64:65], v114 offset:33792\n"
    "ds_read_b64_tr_b16 v[66:67], v114 offset:34304\n"
    "v_mfma_f32_16x16x32_bf16 a[60:63], v[68:71], v[148:151], a[60:63]\n"
    "v_lshrrev_b32 v4, 2, v3\n"
    "v_xor_b32 v4, s63, v4\n"
    "v_mfma_f32_16x16x32_bf16 a[92:95], v[68:71], v[152:155], a[92:95]\n"
    "v_and_b32 v3, 3, v3\n"
    "v_lshlrev_b32 v3, 4, v3\n"
    "v_mfma_f32_16x16x32_bf16 a[124:127], v[68:71], v[156:159], a[124:127]\n"
    "v_mfma_f32_16x16x32_bf16 a[160:163], v[72:75], v[144:147], a[160:163]\n"
    "ds_read_b64_tr_b16 v[68:69], v115 offset:33792\n"
    "ds_read_b64_tr_b16 v[70:71], v115 offset:34304\n"
    "v_mfma_f32_16x16x32_bf16 a[184:187], v[72:75], v[148:151], a[184:187]\n"
    "v_mfma_f32_16x16x32_bf16 a[208:211], v[72:75], v[152:155], a[208:211]\n"
    "v_mfma_f32_16x16x32_bf16 a[232:235], v[72:75], v[156:159], a[232:235]\n"
    "v_mfma_f32_16x16x32_bf16 a[164:167], v[76:79], v[144:147], a[164:167]\n"
    "ds_read_b64_tr_b16 v[72:73], v100 offset:40960\n"
    "ds_read_b64_tr_b16 v[74:75], v100 offset:41472\n"
    "v_mfma_f32_16x16x32_bf16 a[188:191], v[76:79], v[148:151], a[188:191]\n"
    "v_mfma_f32_16x16x32_bf16 a[212:215], v[76:79], v[152:155], a[212:215]\n"
    "v_mfma_f32_16x16x32_bf16 a[236:239], v[76:79], v[156:159], a[236:239]\n"
    "v_mfma_f32_16x16x32_bf16 a[168:171], v[80:83], v[144:147], a[168:171]\n"
    "ds_read_b64_tr_b16 v[76:77], v101 offset:40960\n"
    "ds_read_b64_tr_b16 v[78:79], v101 offset:41472\n"
    "v_mfma_f32_16x16x32_bf16 a[192:195], v[80:83], v[148:151], a[192:195]\n"
    "v_mfma_f32_16x16x32_bf16 a[216:219], v[80:83], v[152:155], a[216:219]\n"
    "v_mfma_f32_16x16x32_bf16 a[240:243], v[80:83], v[156:159], a[240:243]\n"
    "v_mfma_f32_16x16x32_bf16 a[172:175], v[84:87], v[144:147], a[172:175]\n"
    "ds_read_b64_tr_b16 v[80:81], v102 offset:40960\n"
    "ds_read_b64_tr_b16 v[82:83], v102 offset:41472\n"
    "v_mfma_f32_16x16x32_bf16 a[196:199], v[84:87], v[148:151], a[196:199]\n"
    "v_mfma_f32_16x16x32_bf16 a[220:223], v[84:87], v[152:155], a[220:223]\n"
    "v_mfma_f32_16x16x32_bf16 a[244:247], v[84:87], v[156:159], a[244:247]\n"
    "v_mfma_f32_16x16x32_bf16 a[176:179], v[88:91], v[144:147], a[176:179]\n"
    "ds_read_b64_tr_b16 v[84:85], v100 offset:45056\n"
    "ds_read_b64_tr_b16 v[86:87], v100 offset:45568\n"
    "v_mfma_f32_16x16x32_bf16 a[200:203], v[88:91], v[148:151], a[200:203]\n"
    "v_mfma_f32_16x16x32_bf16 a[224:227], v[88:91], v[152:155], a[224:227]\n"
    "v_mfma_f32_16x16x32_bf16 a[248:251], v[88:91], v[156:159], a[248:251]\n"
    "v_mfma_f32_16x16x32_bf16 a[180:183], v[92:95], v[144:147], a[180:183]\n"
    "ds_read_b64_tr_b16 v[88:89], v101 offset:45056\n"
    "ds_read_b64_tr_b16 v[90:91], v101 offset:45568\n"
    "v_mfma_f32_16x16x32_bf16 a[204:207], v[92:95], v[148:151], a[204:207]\n"
    "v_mfma_f32_16x16x32_bf16 a[228:231], v[92:95], v[152:155], a[228:231]\n"
    "v_mfma_f32_16x16x32_bf16 a[252:255], v[92:95], v[156:159], a[252:255]\n"
    "ds_read_b64_tr_b16 v[92:93], v102 offset:45056\n"
    "ds_read_b64_tr_b16 v[94:95], v102 offset:45568\n"
    "s_waitcnt lgkmcnt(10)\n"
    "v_mfma_f32_16x16x32_bf16 a[160:163], v[72:75], v[128:131], a[160:163]\n"
    "ds_read_b128 v[144:147], v120\n"
    "v_lshl_add_u32 v3, v4, 6, v3\n"
    "v_mfma_f32_16x16x32_bf16 a[184:187], v[72:75], v[132:135], a[184:187]\n"
    "v_lshrrev_b32 v4, 3, v1\n"
    "v_lshl_add_u32 v121, v4, 10, v3\n"
    "v_mfma_f32_16x16x32_bf16 a[208:211], v[72:75], v[136:139], a[208:211]\n"
    "ds_read_b128 v[148:151], v120 offset:2048\n"
    "v_xor_b32 v123, 0x80, v121\n"
    "v_mfma_f32_16x16x32_bf16 a[232:235], v[72:75], v[140:143], a[232:235]\n"
    "s_lshl_b32 s68, s65, 5\n"
    "s_lshl_b32 s4, s63, 3\n"
    "s_waitcnt lgkmcnt(10)\n"
    "v_mfma_f32_16x16x32_bf16 a[164:167], v[76:79], v[128:131], a[164:167]\n"
    "ds_read_b64_tr_b16 v[72:73], v100 offset:57344\n"
    "s_add_u32 s68, s68, s4\n"
    "v_mfma_f32_16x16x32_bf16 a[188:191], v[76:79], v[132:135], a[188:191]\n"
    "ds_read_b128 v[152:155], v120 offset:4096\n"
    "s_lshl_b32 s68, s68, 2\n"
    "v_mfma_f32_16x16x32_bf16 a[212:215], v[76:79], v[136:139], a[212:215]\n"
    "ds_read_b64_tr_b16 v[74:75], v100 offset:57856\n"
    "s_mul_i32 s74, s65, 0x58000\n"
    "v_mfma_f32_16x16x32_bf16 a[236:239], v[76:79], v[140:143], a[236:239]\n"
    "ds_read_b128 v[156:159], v120 offset:6144\n"
    "s_add_u32 s74, s74, 0x2220000\n"
    "s_waitcnt lgkmcnt(12)\n"
    "v_mfma_f32_16x16x32_bf16 a[168:171], v[80:83], v[128:131], a[168:171]\n"
    "ds_read_b64_tr_b16 v[76:77], v101 offset:57344\n"
    "s_mul_i32 s4, s63, 0x3000\n"
    "v_mfma_f32_16x16x32_bf16 a[192:195], v[80:83], v[132:135], a[192:195]\n"
    "s_add_u32 s66, s74, s4\n"
    "s_add_u32 s66, s66, 0x20000\n"
    "v_mfma_f32_16x16x32_bf16 a[216:219], v[80:83], v[136:139], a[216:219]\n"
    "ds_read_b64_tr_b16 v[78:79], v101 offset:57856\n"
    "s_lshl_b32 s4, s63, 15\n"
    "v_mfma_f32_16x16x32_bf16 a[240:243], v[80:83], v[140:143], a[240:243]\n"
    "s_add_u32 s74, s74, s4\n"
    "s_lshl_b32 s4, s58, 14\n"
    "s_waitcnt lgkmcnt(12)\n"
    "v_mfma_f32_16x16x32_bf16 a[172:175], v[84:87], v[128:131], a[172:175]\n"
    "buffer_load_dword v122, v5, s[8:11], s7 offen sc1\n"
    "ds_read_b64_tr_b16 v[80:81], v102 offset:57344\n"
    "v_mfma_f32_16x16x32_bf16 a[196:199], v[84:87], v[132:135], a[196:199]\n"
    "s_mul_i32 s67, s63, 0x60000\n"
    "s_add_u32 s67, s67, s4\n"
    "v_mfma_f32_16x16x32_bf16 a[220:223], v[84:87], v[136:139], a[220:223]\n"
    "ds_read_b64_tr_b16 v[82:83], v102 offset:57856\n"
    "s_lshl_b32 s90, s63, 20\n"
    "v_mfma_f32_16x16x32_bf16 a[244:247], v[84:87], v[140:143], a[244:247]\n"
    "s_add_u32 s90, s90, s4\n"
    "s_lshl_b32 s3, s64, 1\n"
    "s_waitcnt lgkmcnt(12)\n"
    "v_mfma_f32_16x16x32_bf16 a[176:179], v[88:91], v[128:131], a[176:179]\n"
    "ds_read_b64_tr_b16 v[84:85], v100 offset:61440\n"
    "s_add_u32 s3, s3, s57\n"
    "v_mfma_f32_16x16x32_bf16 a[200:203], v[88:91], v[132:135], a[200:203]\n"
    "s_and_b32 s4, s56, 7\n"
    "s_cmp_eq_u32 s57, 7\n"
    "v_mfma_f32_16x16x32_bf16 a[224:227], v[88:91], v[136:139], a[224:227]\n"
    "ds_read_b64_tr_b16 v[86:87], v100 offset:61952\n"
    "s_cselect_b32 s69, 0, s57\n"
    "v_mfma_f32_16x16x32_bf16 a[248:251], v[88:91], v[140:143], a[248:251]\n"
    "s_bitcmp1_b32 s60, 0\n"
    "s_cselect_b32 s3, s4, s3\n"
    "s_waitcnt lgkmcnt(12)\n"
    "v_mfma_f32_16x16x32_bf16 a[180:183], v[92:95], v[128:131], a[180:183]\n"
    "ds_read_b64_tr_b16 v[88:89], v101 offset:61440\n"
    "s_cselect_b32 s69, s69, 0\n"
    "v_mfma_f32_16x16x32_bf16 a[204:207], v[92:95], v[132:135], a[204:207]\n"
    "ds_read_b64_tr_b16 v[90:91], v101 offset:61952\n"
    "s_add_u32 s3, s3, 1\n"
    "v_mfma_f32_16x16x32_bf16 a[228:231], v[92:95], v[136:139], a[228:231]\n"
    "s_mov_b32 s71, s69\n"
    "v_mfma_f32_16x16x32_bf16 a[252:255], v[92:95], v[140:143], a[252:255]\n"
    "s_waitcnt lgkmcnt(9)\n"
    "v_mfma_f32_16x16x32_bf16 a[160:163], v[72:75], v[144:147], a[160:163]\n"
    "ds_read_b64_tr_b16 v[92:93], v102 offset:61440\n"
    "ds_read_b64_tr_b16 v[94:95], v102 offset:61952\n"
    "v_mfma_f32_16x16x32_bf16 a[184:187], v[72:75], v[148:151], a[184:187]\n"
    "v_mfma_f32_16x16x32_bf16 a[208:211], v[72:75], v[152:155], a[208:211]\n"
    "s_waitcnt lgkmcnt(10)\n"
    "v_mfma_f32_16x16x32_bf16 a[232:235], v[72:75], v[156:159], a[232:235]\n"
    "s_waitcnt lgkmcnt(8)\n"
    "v_mfma_f32_16x16x32_bf16 a[164:167], v[76:79], v[144:147], a[164:167]\n"
    "v_mfma_f32_16x16x32_bf16 a[188:191], v[76:79], v[148:151], a[188:191]\n"
    "v_mfma_f32_16x16x32_bf16 a[212:215], v[76:79], v[152:155], a[212:215]\n"
    "v_mfma_f32_16x16x32_bf16 a[236:239], v[76:79], v[156:159], a[236:239]\n"
    "s_waitcnt lgkmcnt(6)\n"
    "v_mfma_f32_16x16x32_bf16 a[168:171], v[80:83], v[144:147], a[168:171]\n"
    "v_mfma_f32_16x16x32_bf16 a[192:195], v[80:83], v[148:151], a[192:195]\n"
    "v_mfma_f32_16x16x32_bf16 a[216:219], v[80:83], v[152:155], a[216:219]\n"
    "v_mfma_f32_16x16x32_bf16 a[240:243], v[80:83], v[156:159], a[240:243]\n"
    "s_waitcnt lgkmcnt(4)\n"
    "v_mfma_f32_16x16x32_bf16 a[172:175], v[84:87], v[144:147], a[172:175]\n"
    "v_mfma_f32_16x16x32_bf16 a[196:199], v[84:87], v[148:151], a[196:199]\n"
    "v_mfma_f32_16x16x32_bf16 a[220:223], v[84:87], v[152:155], a[220:223]\n"
    "v_mfma_f32_16x16x32_bf16 a[244:247], v[84:87], v[156:159], a[244:247]\n"
    "s_waitcnt lgkmcnt(2)\n"
    "v_mfma_f32_16x16x32_bf16 a[176:179], v[88:91], v[144:147], a[176:179]\n"
    "v_mfma_f32_16x16x32_bf16 a[200:203], v[88:91], v[148:151], a[200:203]\n"
    "v_mfma_f32_16x16x32_bf16 a[224:227], v[88:91], v[152:155], a[224:227]\n"
    "v_mfma_f32_16x16x32_bf16 a[248:251], v[88:91], v[156:159], a[248:251]\n"
    "s_waitcnt lgkmcnt(0)\n"
    "v_mfma_f32_16x16x32_bf16 a[180:183], v[92:95], v[144:147], a[180:183]\n"
    "s_waitcnt vmcnt(0)\n"
    "v_cmp_ne_u32 vcc, v122, v7\n"
    "s_nop 0\n"
    "s_cbranch_vccnz Lspin2\n"
    "Lspinret2:\n"
    "v_mfma_f32_16x16x32_bf16 a[204:207], v[92:95], v[148:151], a[204:207]\n"
    "v_mfma_f32_16x16x32_bf16 a[228:231], v[92:95], v[152:155], a[228:231]\n"
    "v_mfma_f32_16x16x32_bf16 a[252:255], v[92:95], v[156:159], a[252:255]\n"
    "v_mfma_f32_16x16x32_bf16 a[0:3], v[40:43], v[128:131], a[0:3]\n"
    "v_mfma_f32_16x16x32_bf16 a[32:35], v[40:43], v[132:135], a[32:35]\n"
    "s_add_u32 m0, s59, 0x10000\n"
    "v_mfma_f32_16x16x32_bf16 a[64:67], v[40:43], v[136:139], a[64:67]\n"
    "v_mfma_f32_16x16x32_bf16 a[96:99], v[40:43], v[140:143], a[96:99]\n"
    "v_mfma_f32_16x16x32_bf16 a[4:7], v[44:47], v[128:131], a[4:7]\n"
    "ds_read_b64_tr_b16 v[40:41], v112 offset:49152\n"
    "buffer_load_dwordx4 v2, s[24:27], s83 offen sc1 lds\n"
    "v_mfma_f32_16x16x32_bf16 a[36:39], v[44:47], v[132:135], a[36:39]\n"
    "ds_read_b64_tr_b16 v[42:43], v112 offset:49664\n"
    "v_mfma_f32_16x16x32_bf16 a[68:71], v[44:47], v[136:139], a[68:71]\n"
    "s_mul_i32 s5, s56, 3\n"
    "s_lshr_b32 s5, s5, 1\n"
    "v_mfma_f32_16x16x32_bf16 a[100:103], v[44:47], v[140:143], a[100:103]\n"
    "buffer_load_dwordx4 v2, s[24:27], s83 offen offset:1024 sc1 lds\n"
    "s_lshl_b32 s84, s5, 10\n"
    "v_mfma_f32_16x16x32_bf16 a[8:11], v[48:51], v[128:131], a[8:11]\n"
    "ds_read_b64_tr_b16 v[44:45], v113 offset:49152\n"
    "v_add_u32 v120, 0x2c000, v2\n"
    "v_mfma_f32_16x16x32_bf16 a[40:43], v[48:51], v[132:135], a[40:43]\n"
    "s_lshl_b32 s85, s57, 2\n"
    "s_add_u32 s85, s85, s58\n"
    "v_mfma_f32_16x16x32_bf16 a[72:75], v[48:51], v[136:139], a[72:75]\n"
    "ds_read_b64_tr_b16 v[46:47], v113 offset:49664\n"
    "s_mul_i32 s85, s85, 0x58000\n"
    "v_mfma_f32_16x16x32_bf16 a[104:107], v[48:51], v[140:143], a[104:107]\n"
    "s_add_u32 s85, s85, 0x2240000\n"
    "s_add_u32 s85, s85, s84\n"
    "v_mfma_f32_16x16x32_bf16 a[12:15], v[52:55], v[128:131], a[12:15]\n"
    "ds_read_b64_tr_b16 v[48:49], v114 offset:49152\n"
    "buffer_load_dwordx4 v2, s[24:27], s83 offen offset:2048 sc1 lds\n"
    "v_mfma_f32_16x16x32_bf16 a[44:47], v[52:55], v[132:135], a[44:47]\n"
    "s_sub_u32 s86, 15, s57\n"
    "s_lshl_b32 s86, s86, 2\n"
    "v_mfma_f32_16x16x32_bf16 a[76:79], v[52:55], v[136:139], a[76:79]\n"
    "ds_read_b64_tr_b16 v[50:51], v114 offset:49664\n"
    "s_add_u32 s86, s86, s58\n"
    "v_mfma_f32_16x16x32_bf16 a[108:111], v[52:55], v[140:143], a[108:111]\n"
    "s_mul_i32 s86, s86, 0x58000\n"
    "s_add_u32 s86, s86, 0x2240000\n"
    "v_mfma_f32_16x16x32_bf16 a[16:19], v[56:59], v[128:131], a[16:19]\n"
    "ds_read_b64_tr_b16 v[52:53], v115 offset:49152\n"
    "buffer_load_dwordx4 v2, s[24:27], s83 offen offset:3072 sc1 lds\n"
    "v_mfma_f32_16x16x32_bf16 a[48:51], v[56:59], v[132:135], a[48:51]\n"
    "s_add_u32 s86, s86, s84\n"
    "v_accvgpr_read_b32 v224, a160\n"
    "v_mfma_f32_16x16x32_bf16 a[80:83], v[56:59], v[136:139], a[80:83]\n"
    "ds_read_b64_tr_b16 v[54:55], v115 offset:49664\n"
    "v_accvgpr_read_b32 v225, a161\n"
    "v_mfma_f32_16x16x32_bf16 a[112:115], v[56:59], v[140:143], a[112:115]\n"
    "v_accvgpr_read_b32 v226, a162\n"
    "v_accvgpr_read_b32 v227, a163\n"
    "v_mfma_f32_16x16x32_bf16 a[20:23], v[60:63], v[128:131], a[20:23]\n"
    "ds_read_b64_tr_b16 v[56:57], v112 offset:50176\n"
    "s_add_u32 s83, s83, 0x8000\n"
    "v_mfma_f32_16x16x32_bf16 a[52:55], v[60:63], v[132:135], a[52:55]\n"
    "v_accvgpr_read_b32 v228, a164\n"
    "v_accvgpr_read_b32 v229, a165\n"
    "v_mfma_f32_16x16x32_bf16 a[84:87], v[60:63], v[136:139], a[84:87]\n"
    "ds_read_b64_tr_b16 v[58:59], v112 offset:50688\n"
    "v_accvgpr_read_b32 v230, a166\n"
    "v_mfma_f32_16x16x32_bf16 a[116:119], v[60:63], v[140:143], a[116:119]\n"
    "v_accvgpr_read_b32 v231, a167\n"
    "v_accvgpr_read_b32 v232, a168\n"
    "v_mfma_f32_16x16x32_bf16 a[24:27], v[64:67], v[128:131], a[24:27]\n"
    "ds_read_b64_tr_b16 v[60:61], v113 offset:50176\n"
    "v_accvgpr_read_b32 v233, a169\n"
    "v_mfma_f32_16x16x32_bf16 a[56:59], v[64:67], v[132:135], a[56:59]\n"
    "s_add_u32 m0, s59, 0x14000\n"
    "v_accvgpr_read_b32 v234, a170\n"
    "v_mfma_f32_16x16x32_bf16 a[88:91], v[64:67], v[136:139], a[88:91]\n"
    "v_accvgpr_read_b32 v235, a171\n"
    "v_accvgpr_read_b32 v236, a172\n"
    "v_mfma_f32_16x16x32_bf16 a[120:123], v[64:67], v[140:143], a[120:123]\n"
    "ds_read_b64_tr_b16 v[62:63], v113 offset:50688\n"
    "v_accvgpr_read_b32 v237, a173\n"
    "v_mfma_f32_16x16x32_bf16 a[28:31], v[68:71], v[128:131], a[28:31]\n"
    "ds_read_b64_tr_b16 v[64:65], v114 offset:50176\n"
    "v_accvgpr_read_b32 v238, a174\n"
    "v_mfma_f32_16x16x32_bf16 a[60:63], v[68:71], v[132:135], a[60:63]\n"
    "buffer_load_dwordx4 v2, s[24:27], s75 offen sc1 lds\n"
    "v_accvgpr_read_b32 v239, a175\n"
    "v_mfma_f32_16x16x32_bf16 a[92:95], v[68:71], v[136:139], a[92:95]\n"
    "v_accvgpr_read_b32 v240, a176\n"
    "v_accvgpr_read_b32 v241, a177\n"
    "v_mfma_f32_16x16x32_bf16 a[124:127], v[68:71], v[140:143], a[124:127]\n"
    "ds_read_b64_tr_b16 v[66:67], v114 offset:50688\n"
    "v_accvgpr_read_b32 v242, a178\n"
    "s_waitcnt lgkmcnt(12)\n"
    "v_mfma_f32_16x16x32_bf16 a[0:3], v[40:43], v[144:147], a[0:3]\n"
    "ds_read_b64_tr_b16 v[68:69], v115 offset:50176\n"
    "v_accvgpr_read_b32 v243, a179\n"
    "v_mfma_f32_16x16x32_bf16 a[32:35], v[40:43], v[148:151], a[32:35]\n"
    "buffer_load_dwordx4 v2, s[24:27], s75 offen offset:1024 sc1 lds\n"
    "v_accvgpr_read_b32 v244, a180\n"
    "v_mfma_f32_16x16x32_bf16 a[64:67], v[40:43], v[152:155], a[64:67]\n"
    "v_accvgpr_read_b32 v245, a181\n"
    "v_accvgpr_read_b32 v246, a182\n"
    "v_mfma_f32_16x16x32_bf16 a[96:99], v[40:43], v[156:159], a[96:99]\n"
    "ds_read_b64_tr_b16 v[70:71], v115 offset:50688\n"
    "v_accvgpr_read_b32 v247, a183\n"
    "s_waitcnt lgkmcnt(12)\n"
    "v_mfma_f32_16x16x32_bf16 a[4:7], v[44:47], v[144:147], a[4:7]\n"
    "v_max_f32 v224, 0, v224\n"
    "v_max_f32 v225, 0, v225\n"
    "v_mfma_f32_16x16x32_bf16 a[36:39], v[44:47], v[148:151], a[36:39]\n"
    "buffer_load_dwordx4 v2, s[24:27], s75 offen offset:2048 sc1 lds\n"
    "v_max_f32 v226, 0, v226\n"
    "v_mfma_f32_16x16x32_bf16 a[68:71], v[44:47], v[152:155], a[68:71]\n"
    "v_max_f32 v227, 0, v227\n"
    "v_max_f32 v228, 0, v228\n"
    "v_mfma_f32_16x16x32_bf16 a[100:103], v[44:47], v[156:159], a[100:103]\n"
    "v_max_f32 v229, 0, v229\n"
    "v_max_f32 v230, 0, v230\n"
    "s_waitcnt lgkmcnt(10)\n"
    "v_mfma_f32_16x16x32_bf16 a[8:11], v[48:51], v[144:147], a[8:11]\n"
    "buffer_load_dwordx4 v2, s[24:27], s75 offen offset:3072 sc1 lds\n"
    "v_max_f32 v231, 0, v231\n"
    "v_mfma_f32_16x16x32_bf16 a[40:43], v[48:51], v[148:151], a[40:43]\n"
    "v_max_f32 v232, 0, v232\n"
    "v_max_f32 v233, 0, v233\n"
    "v_mfma_f32_16x16x32_bf16 a[72:75], v[48:51], v[152:155], a[72:75]\n"
    "v_max_f32 v234, 0, v234\n"
    "v_max_f32 v235, 0, v235\n"
    "v_mfma_f32_16x16x32_bf16 a[104:107], v[48:51], v[156:159], a[104:107]\n"
    "s_add_u32 s75, s75, 0x8000\n"
    "v_mul_f32 v224, v224, v236\n"
    "s_waitcnt lgkmcnt(8)\n"
    "v_mfma_f32_16x16x32_bf16 a[12:15], v[52:55], v[144:147], a[12:15]\n"
    "v_mul_f32 v225, v225, v237\n"
    "v_mul_f32 v226, v226, v238\n"
    "v_mfma_f32_16x16x32_bf16 a[44:47], v[52:55], v[148:151], a[44:47]\n"
    "v_mul_f32 v227, v227, v239\n"
    "v_mul_f32 v228, v228, v240\n"
    "v_mfma_f32_16x16x32_bf16 a[76:79], v[52:55], v[152:155], a[76:79]\n"
    "s_waitcnt lgkmcnt(0)\n"
    "s_barrier\n"
    "v_mfma_f32_16x16x32_bf16 a[108:111], v[52:55], v[156:159], a[108:111]\n"
    "v_mul_f32 v229, v229, v241\n"
    "s_add_u32 m0, s59, 0x18000\n"
    "v_mfma_f32_16x16x32_bf16 a[16:19], v[56:59], v[144:147], a[16:19]\n"
    "v_mul_f32 v230, v230, v242\n"
    "v_mul_f32 v231, v231, v243\n"
    "v_mfma_f32_16x16x32_bf16 a[48:51], v[56:59], v[148:151], a[48:51]\n"
    "v_mul_f32 v232, v232, v244\n"
    "v_mul_f32 v233, v233, v245\n"
    "v_mfma_f32_16x16x32_bf16 a[80:83], v[56:59], v[152:155], a[80:83]\n"
    "buffer_load_dwordx4 v2, s[24:27], s83 offen sc1 lds\n"
    "v_mul_f32 v234, v234, v246\n"
    "v_mfma_f32_16x16x32_bf16 a[112:115], v[56:59], v[156:159], a[112:115]\n"
    "v_mul_f32 v235, v235, v247\n"
    "v_cvt_pk_bf16_f32 v8, v224, v225\n"
    "v_mfma_f32_16x16x32_bf16 a[20:23], v[60:63], v[144:147], a[20:23]\n"
    "v_cvt_pk_bf16_f32 v9, v226, v227\n"
    "v_cvt_pk_bf16_f32 v10, v228, v229\n"
    "buffer_load_dwordx4 v2, s[24:27], s83 offen offset:1024 sc1 lds\n"
    "buffer_load_dwordx4 v2, s[24:27], s83 offen offset:2048 sc1 lds\n"
    "buffer_load_dwordx4 v2, s[24:27], s83 offen offset:3072 sc1 lds\n"
    "s_add_u32 s83, s83, 0x8000\n"
    "v_mfma_f32_16x16x32_bf16 a[52:55], v[60:63], v[148:151], a[52:55]\n"
    "v_cvt_pk_bf16_f32 v11, v230, v231\n"
    "v_cvt_pk_bf16_f32 v12, v232, v233\n"
    "v_mfma_f32_16x16x32_bf16 a[84:87], v[60:63], v[152:155], a[84:87]\n"
    "v_cvt_pk_bf16_f32 v13, v234, v235\n"
    "v_accvgpr_read_b32 v224, a184\n"
    "v_mfma_f32_16x16x32_bf16 a[116:119], v[60:63], v[156:159], a[116:119]\n"
    "v_accvgpr_read_b32 v225, a185\n"
    "v_accvgpr_read_b32 v226, a186\n"
    "v_mfma_f32_16x16x32_bf16 a[24:27], v[64:67], v[144:147], a[24:27]\n"
    "v_accvgpr_read_b32 v227, a187\n"
    "v_accvgpr_read_b32 v228, a188\n"
    "v_mfma_f32_16x16x32_bf16 a[56:59], v[64:67], v[148:151], a[56:59]\n"
    "v_accvgpr_read_b32 v229, a189\n"
    "v_accvgpr_read_b32 v230, a190\n"
    "v_mfma_f32_16x16x32_bf16 a[88:91], v[64:67], v[152:155], a[88:91]\n"
    "v_accvgpr_read_b32 v231, a191\n"
    "v_accvgpr_read_b32 v232, a192\n"
    "v_mfma_f32_16x16x32_bf16 a[120:123], v[64:67], v[156:159], a[120:123]\n"
    "v_accvgpr_read_b32 v233, a193\n"
    "v_accvgpr_read_b32 v234, a194\n"
    "v_mfma_f32_16x16x32_bf16 a[28:31], v[68:71], v[144:147], a[28:31]\n"
    "v_accvgpr_read_b32 v235, a195\n"
    "v_accvgpr_read_b32 v236, a196\n"
    "v_mfma_f32_16x16x32_bf16 a[60:63], v[68:71], v[148:151], a[60:63]\n"
    "v_accvgpr_read_b32 v237, a197\n"
    "v_accvgpr_read_b32 v238, a198\n"
    "v_mfma_f32_16x16x32_bf16 a[92:95], v[68:71], v[152:155], a[92:95]\n"
    "v_accvgpr_read_b32 v239, a199\n"
    "v_accvgpr_read_b32 v240, a200\n"
    "v_mfma_f32_16x16x32_bf16 a[124:127], v[68:71], v[156:159], a[124:127]\n"
    "v_accvgpr_read_b32 v241, a201\n"
    "v_accvgpr_read_b32 v242, a202\n"
    "v_accvgpr_read_b32 v243, a203\n"
    "v_accvgpr_read_b32 v244, a204\n"
    "v_accvgpr_read_b32 v245, a205\n"
    "v_accvgpr_read_b32 v246, a206\n"
    "v_accvgpr_read_b32 v247, a207\n"
    "v_max_f32 v224, 0, v224\n"
    "v_max_f32 v225, 0, v225\n"
    "v_max_f32 v226, 0, v226\n"
    "v_max_f32 v227, 0, v227\n"
    "v_max_f32 v228, 0, v228\n"
    "v_max_f32 v229, 0, v229\n"
    "v_max_f32 v230, 0, v230\n"
    "v_max_f32 v231, 0, v231\n"
    "v_max_f32 v232, 0, v232\n"
    "v_max_f32 v233, 0, v233\n"
    "v_max_f32 v234, 0, v234\n"
    "v_max_f32 v235, 0, v235\n"
    "v_mul_f32 v224, v224, v236\n"
    "v_mul_f32 v225, v225, v237\n"
    "v_mul_f32 v226, v226, v238\n"
    "v_mul_f32 v227, v227, v239\n"
    "v_mul_f32 v228, v228, v240\n"
    "s_mov_b32 s72, 0x3e0293ee\n"
    "s_mov_b32 s73, 0x3e0293ee\n"
    ".p2align 3\n"
    "v_accvgpr_read_b32 v192, a0\n"
    "v_accvgpr_read_b32 v193, a1\n"
    "v_accvgpr_read_b32 v194, a2\n"
    "v_accvgpr_read_b32 v195, a3\n"
    "v_accvgpr_read_b32 v196, a4\n"
    "v_accvgpr_read_b32 v197, a5\n"
    "v_accvgpr_read_b32 v198, a6\n"
    "v_accvgpr_read_b32 v199, a7\n"
    "v_accvgpr_read_b32 v200, a8\n"
    "v_accvgpr_read_b32 v201, a9\n"
    "v_accvgpr_read_b32 v202, a10\n"
    "v_accvgpr_read_b32 v203, a11\n"
    "v_accvgpr_read_b32 v204, a12\n"
    "v_accvgpr_read_b32 v205, a13\n"
    "v_accvgpr_read_b32 v206, a14\n"
    "v_accvgpr_read_b32 v207, a15\n"
    "s_add_u32 m0, s59, 0x1c000\n"
    "s_nop 0\n"
    "buffer_load_dwordx4 v2, s[24:27], s75 offen sc1 lds\n"
    "buffer_load_dwordx4 v2, s[24:27], s75 offen offset:1024 sc1 lds\n"
    "buffer_load_dwordx4 v2, s[24:27], s75 offen offset:2048 sc1 lds\n"
    "buffer_load_dwordx4 v2, s[24:27], s75 offen offset:3072 sc1 lds\n"
    "s_add_u32 s75, s75, 0x8000\n"
    ".p2align 3\n"
    "v_pk_mul_f32 v[192:193], v[192:193], s[72:73]\n"
    "v_pk_mul_f32 v[194:195], v[194:195], s[72:73]\n"
    "v_pk_mul_f32 v[196:197], v[196:197], s[72:73]\n"
    "v_pk_mul_f32 v[198:199], v[198:199], s[72:73]\n"
    "v_pk_mul_f32 v[200:201], v[200:201], s[72:73]\n"
    "v_pk_mul_f32 v[202:203], v[202:203], s[72:73]\n"
    "v_pk_mul_f32 v[204:205], v[204:205], s[72:73]\n"
    "v_pk_mul_f32 v[206:207], v[206:207], s[72:73]\n"
    "v_cvt_pk_bf16_f32 v128, v192, v193\n"
    "v_cvt_pk_bf16_f32 v129, v194, v195\n"
    "v_cvt_pk_bf16_f32 v130, v196, v197\n"
    "v_cvt_pk_bf16_f32 v131, v198, v199\n"
    "v_cvt_pk_bf16_f32 v132, v200, v201\n"
    "v_cvt_pk_bf16_f32 v133, v202, v203\n"
    "v_cvt_pk_bf16_f32 v134, v204, v205\n"
    "v_cvt_pk_bf16_f32 v135, v206, v207\n"
    "v_accvgpr_read_b32 v192, a16\n"
    "v_accvgpr_read_b32 v193, a17\n"
    "v_accvgpr_read_b32 v194, a18\n"
    "v_accvgpr_read_b32 v195, a19\n"
    "v_accvgpr_read_b32 v196, a20\n"
    "v_accvgpr_read_b32 v197, a21\n"
    "v_accvgpr_read_b32 v198, a22\n"
    "v_accvgpr_read_b32 v199, a23\n"
    "v_accvgpr_read_b32 v200, a24\n"
    "v_accvgpr_read_b32 v201, a25\n"
    "v_accvgpr_read_b32 v202, a26\n"
    "v_accvgpr_read_b32 v203, a27\n"
    "v_accvgpr_read_b32 v204, a28\n"
    "v_accvgpr_read_b32 v205, a29\n"
    "v_accvgpr_read_b32 v206, a30\n"
    "v_accvgpr_read_b32 v207, a31\n"
    "v_pk_mul_f32 v[192:193], v[192:193], s[72:73]\n"
    "v_pk_mul_f32 v[194:195], v[194:195], s[72:73]\n"
    "v_pk_mul_f32 v[196:197], v[196:197], s[72:73]\n"
    "v_pk_mul_f32 v[198:199], v[198:199], s[72:73]\n"
    "v_pk_mul_f32 v[200:201], v[200:201], s[72:73]\n"
    "v_pk_mul_f32 v[202:203], v[202:203], s[72:73]\n"
    "v_pk_mul_f32 v[204:205], v[204:205], s[72:73]\n"
    "v_pk_mul_f32 v[206:207], v[206:207], s[72:73]\n"
    "v_cvt_pk_bf16_f32 v136, v192, v193\n"
    "v_cvt_pk_bf16_f32 v137, v194, v195\n"
    "v_cvt_pk_bf16_f32 v138, v196, v197\n"
    "v_cvt_pk_bf16_f32 v139, v198, v199\n"
    "v_cvt_pk_bf16_f32 v140, v200, v201\n"
    "v_cvt_pk_bf16_f32 v141, v202, v203\n"
    "v_cvt_pk_bf16_f32 v142, v204, v205\n"
    "v_cvt_pk_bf16_f32 v143, v206, v207\n"
    "v_accvgpr_read_b32 v192, a32\n"
    "v_accvgpr_read_b32 v193, a33\n"
    "v_accvgpr_read_b32 v194, a34\n"
    "v_accvgpr_read_b32 v195, a35\n"
    "v_accvgpr_read_b32 v196, a36\n"
    "v_accvgpr_read_b32 v197, a37\n"
    "v_accvgpr_read_b32 v198, a38\n"
    "v_accvgpr_read_b32 v199, a39\n"
    "v_accvgpr_read_b32 v200, a40\n"
    "v_accvgpr_read_b32 v201, a41\n"
    "v_accvgpr_read_b32 v202, a42\n"
    "v_accvgpr_read_b32 v203, a43\n"
    "v_accvgpr_read_b32 v204, a44\n"
    "v_accvgpr_read_b32 v205, a45\n"
    "v_accvgpr_read_b32 v206, a46\n"
    "v_accvgpr_read_b32 v207, a47\n"
    "s_add_u32 m0, s59, 0\n"
    "s_nop 0\n"
    "buffer_load_dwordx4 v2, s[24:27], s83 offen sc1 lds\n"
    "buffer_load_dwordx4 v2, s[24:27], s83 offen offset:1024 sc1 lds\n"
    "buffer_load_dwordx4 v2, s[24:27], s83 offen offset:2048 sc1 lds\n"
    "buffer_load_dwordx4 v2, s[24:27], s83 offen offset:3072 sc1 lds\n"
    "s_add_u32 s83, s83, 0x8000\n"
    ".p2align 3\n"
    "v_pk_mul_f32 v[192:193], v[192:193], s[72:73]\n"
    "v_pk_mul_f32 v[194:195], v[194:195], s[72:73]\n"
    "v_pk_mul_f32 v[196:197], v[196:197], s[72:73]\n"
    "v_pk_mul_f32 v[198:199], v[198:199], s[72:73]\n"
    "v_pk_mul_f32 v[200:201], v[200:201], s[72:73]\n"
    "v_pk_mul_f32 v[202:203], v[202:203], s[72:73]\n"
    "v_pk_mul_f32 v[204:205], v[204:205], s[72:73]\n"
    "v_pk_mul_f32 v[206:207], v[206:207], s[72:73]\n"
    "v_cvt_pk_bf16_f32 v144, v192, v193\n"
    "v_cvt_pk_bf16_f32 v145, v194, v195\n"
    "v_cvt_pk_bf16_f32 v146, v196, v197\n"
    "v_cvt_pk_bf16_f32 v147, v198, v199\n"
    "v_cvt_pk_bf16_f32 v148, v200, v201\n"
    "v_cvt_pk_bf16_f32 v149, v202, v203\n"
    "v_cvt_pk_bf16_f32 v150, v204, v205\n"
    "v_cvt_pk_bf16_f32 v151, v206, v207\n"
    "v_accvgpr_read_b32 v192, a48\n"
    "v_accvgpr_read_b32 v193, a49\n"
    "v_accvgpr_read_b32 v194, a50\n"
    "v_accvgpr_read_b32 v195, a51\n"
    "v_accvgpr_read_b32 v196, a52\n"
    "v_accvgpr_read_b32 v197, a53\n"
    "v_accvgpr_read_b32 v198, a54\n"
    "v_accvgpr_read_b32 v199, a55\n"
    "v_accvgpr_read_b32 v200, a56\n"
    "v_accvgpr_read_b32 v201, a57\n"
    "v_accvgpr_read_b32 v202, a58\n"
    "v_accvgpr_read_b32 v203, a59\n"
    "v_accvgpr_read_b32 v204, a60\n"
    "v_accvgpr_read_b32 v205, a61\n"
    "v_accvgpr_read_b32 v206, a62\n"
    "v_accvgpr_read_b32 v207, a63\n"
    "v_pk_mul_f32 v[192:193], v[192:193], s[72:73]\n"
    "v_pk_mul_f32 v[194:195], v[194:195], s[72:73]\n"
    "v_pk_mul_f32 v[196:197], v[196:197], s[72:73]\n"
    "v_pk_mul_f32 v[198:199], v[198:199], s[72:73]\n"
    "v_pk_mul_f32 v[200:201], v[200:201], s[72:73]\n"
    "v_pk_mul_f32 v[202:203], v[202:203], s[72:73]\n"
    "v_pk_mul_f32 v[204:205], v[204:205], s[72:73]\n"
    "v_pk_mul_f32 v[206:207], v[206:207], s[72:73]\n"
    "v_cvt_pk_bf16_f32 v152, v192, v193\n"
    "v_cvt_pk_bf16_f32 v153, v194, v195\n"
    "v_cvt_pk_bf16_f32 v154, v196, v197\n"
    "v_cvt_pk_bf16_f32 v155, v198, v199\n"
    "v_cvt_pk_bf16_f32 v156, v200, v201\n"
    "v_cvt_pk_bf16_f32 v157, v202, v203\n"
    "v_cvt_pk_bf16_f32 v158, v204, v205\n"
    "v_cvt_pk_bf16_f32 v159, v206, v207\n"
    "v_accvgpr_read_b32 v192, a64\n"
    "v_accvgpr_read_b32 v193, a65\n"
    "v_accvgpr_read_b32 v194, a66\n"
    "v_accvgpr_read_b32 v195, a67\n"
    "v_accvgpr_read_b32 v196, a68\n"
    "v_accvgpr_read_b32 v197, a69\n"
    "v_accvgpr_read_b32 v198, a70\n"
    "v_accvgpr_read_b32 v199, a71\n"
    "v_accvgpr_read_b32 v200, a72\n"
    "v_accvgpr_read_b32 v201, a73\n"
    "v_accvgpr_read_b32 v202, a74\n"
    "v_accvgpr_read_b32 v203, a75\n"
    "v_accvgpr_read_b32 v204, a76\n"
    "v_accvgpr_read_b32 v205, a77\n"
    "v_accvgpr_read_b32 v206, a78\n"
    "v_accvgpr_read_b32 v207, a79\n"
    "s_add_u32 m0, s59, 0x4000\n"
    "s_nop 0\n"
    "buffer_load_dwordx4 v2, s[24:27], s75 offen sc1 lds\n"
    "buffer_load_dwordx4 v2, s[24:27], s75 offen offset:1024 sc1 lds\n"
    "buffer_load_dwordx4 v2, s[24:27], s75 offen offset:2048 sc1 lds\n"
    "buffer_load_dwordx4 v2, s[24:27], s75 offen offset:3072 sc1 lds\n"
    "s_add_u32 s75, s75, 0x8000\n"
    ".p2align 3\n"
    "v_pk_mul_f32 v[192:193], v[192:193], s[72:73]\n"
    "v_pk_mul_f32 v[194:195], v[194:195], s[72:73]\n"
    "v_pk_mul_f32 v[196:197], v[196:197], s[72:73]\n"
    "v_pk_mul_f32 v[198:199], v[198:199], s[72:73]\n"
    "v_pk_mul_f32 v[200:201], v[200:201], s[72:73]\n"
    "v_pk_mul_f32 v[202:203], v[202:203], s[72:73]\n"
    "v_pk_mul_f32 v[204:205], v[204:205], s[72:73]\n"
    "v_pk_mul_f32 v[206:207], v[206:207], s[72:73]\n"
    "v_cvt_pk_bf16_f32 v160, v192, v193\n"
    "v_cvt_pk_bf16_f32 v161, v194, v195\n"
    "v_cvt_pk_bf16_f32 v162, v196, v197\n"
    "v_cvt_pk_bf16_f32 v163, v198, v199\n"
    "v_cvt_pk_bf16_f32 v164, v200, v201\n"
    "v_cvt_pk_bf16_f32 v165, v202, v203\n"
    "v_cvt_pk_bf16_f32 v166, v204, v205\n"
    "v_cvt_pk_bf16_f32 v167, v206, v207\n"
    "v_accvgpr_read_b32 v192, a80\n"
    "v_accvgpr_read_b32 v193, a81\n"
    "v_accvgpr_read_b32 v194, a82\n"
    "v_accvgpr_read_b32 v195, a83\n"
    "v_accvgpr_read_b32 v196, a84\n"
    "v_accvgpr_read_b32 v197, a85\n"
    "v_accvgpr_read_b32 v198, a86\n"
    "v_accvgpr_read_b32 v199, a87\n"
    "v_accvgpr_read_b32 v200, a88\n"
    "v_accvgpr_read_b32 v201, a89\n"
    "v_accvgpr_read_b32 v202, a90\n"
    "v_accvgpr_read_b32 v203, a91\n"
    "v_accvgpr_read_b32 v204, a92\n"
    "v_accvgpr_read_b32 v205, a93\n"
    "v_accvgpr_read_b32 v206, a94\n"
    "v_accvgpr_read_b32 v207, a95\n"
    "v_pk_mul_f32 v[192:193], v[192:193], s[72:73]\n"
    "v_pk_mul_f32 v[194:195], v[194:195], s[72:73]\n"
    "v_pk_mul_f32 v[196:197], v[196:197], s[72:73]\n"
    "v_pk_mul_f32 v[198:199], v[198:199], s[72:73]\n"
    "v_pk_mul_f32 v[200:201], v[200:201], s[72:73]\n"
    "v_pk_mul_f32 v[202:203], v[202:203], s[72:73]\n"
    "v_pk_mul_f32 v[204:205], v[204:205], s[72:73]\n"
    "v_pk_mul_f32 v[206:207], v[206:207], s[72:73]\n"
    "v_cvt_pk_bf16_f32 v168, v192, v193\n"
    "v_cvt_pk_bf16_f32 v169, v194, v195\n"
    "v_cvt_pk_bf16_f32 v170, v196, v197\n"
    "v_cvt_pk_bf16_f32 v171, v198, v199\n"
    "v_cvt_pk_bf16_f32 v172, v200, v201\n"
    "v_cvt_pk_bf16_f32 v173, v202, v203\n"
    "v_cvt_pk_bf16_f32 v174, v204, v205\n"
    "v_cvt_pk_bf16_f32 v175, v206, v207\n"
    "v_accvgpr_read_b32 v192, a96\n"
    "v_accvgpr_read_b32 v193, a97\n"
    "v_accvgpr_read_b32 v194, a98\n"
    "v_accvgpr_read_b32 v195, a99\n"
    "v_accvgpr_read_b32 v196, a100\n"
    "v_accvgpr_read_b32 v197, a101\n"
    "v_accvgpr_read_b32 v198, a102\n"
    "v_accvgpr_read_b32 v199, a103\n"
    "v_accvgpr_read_b32 v200, a104\n"
    "v_accvgpr_read_b32 v201, a105\n"
    "v_accvgpr_read_b32 v202, a106\n"
    "v_accvgpr_read_b32 v203, a107\n"
    "v_accvgpr_read_b32 v204, a108\n"
    "v_accvgpr_read_b32 v205, a109\n"
    "v_accvgpr_read_b32 v206, a110\n"
    "v_accvgpr_read_b32 v207, a111\n"
    "s_add_u32 m0, s59, 0x8000\n"
    "s_nop 0\n"
    "buffer_load_dwordx4 v2, s[24:27], s83 offen sc1 lds\n"
    "buffer_load_dwordx4 v2, s[24:27], s83 offen offset:1024 sc1 lds\n"
    "buffer_load_dwordx4 v2, s[24:27], s83 offen offset:2048 sc1 lds\n"
    "buffer_load_dwordx4 v2, s[24:27], s83 offen offset:3072 sc1 lds\n"
    "s_add_u32 s83, s83, 0x8000\n"
    ".p2align 3\n"
    "v_pk_mul_f32 v[192:193], v[192:193], s[72:73]\n"
    "v_pk_mul_f32 v[194:195], v[194:195], s[72:73]\n"
    "v_pk_mul_f32 v[196:197], v[196:197], s[72:73]\n"
    "v_pk_mul_f32 v[198:199], v[198:199], s[72:73]\n"
    "v_pk_mul_f32 v[200:201], v[200:201], s[72:73]\n"
    "v_pk_mul_f32 v[202:203], v[202:203], s[72:73]\n"
    "v_pk_mul_f32 v[204:205], v[204:205], s[72:73]\n"
    "v_pk_mul_f32 v[206:207], v[206:207], s[72:73]\n"
    "v_cvt_pk_bf16_f32 v176, v192, v193\n"
    "v_cvt_pk_bf16_f32 v177, v194, v195\n"
    "v_cvt_pk_bf16_f32 v178, v196, v197\n"
    "v_cvt_pk_bf16_f32 v179, v198, v199\n"
    "v_cvt_pk_bf16_f32 v180, v200, v201\n"
    "v_cvt_pk_bf16_f32 v181, v202, v203\n"
    "v_cvt_pk_bf16_f32 v182, v204, v205\n"
    "v_cvt_pk_bf16_f32 v183, v206, v207\n"
    "v_accvgpr_read_b32 v192, a112\n"
    "v_accvgpr_read_b32 v193, a113\n"
    "v_accvgpr_read_b32 v194, a114\n"
    "v_accvgpr_read_b32 v195, a115\n"
    "v_accvgpr_read_b32 v196, a116\n"
    "v_accvgpr_read_b32 v197, a117\n"
    "v_accvgpr_read_b32 v198, a118\n"
    "v_accvgpr_read_b32 v199, a119\n"
    "v_accvgpr_read_b32 v200, a120\n"
    "v_accvgpr_read_b32 v201, a121\n"
    "v_accvgpr_read_b32 v202, a122\n"
    "v_accvgpr_read_b32 v203, a123\n"
    "v_accvgpr_read_b32 v204, a124\n"
    "v_accvgpr_read_b32 v205, a125\n"
    "v_accvgpr_read_b32 v206, a126\n"
    "v_accvgpr_read_b32 v207, a127\n"
    "v_pk_mul_f32 v[192:193], v[192:193], s[72:73]\n"
    "v_pk_mul_f32 v[194:195], v[194:195], s[72:73]\n"
    "v_pk_mul_f32 v[196:197], v[196:197], s[72:73]\n"
    "v_pk_mul_f32 v[198:199], v[198:199], s[72:73]\n"
    "v_pk_mul_f32 v[200:201], v[200:201], s[72:73]\n"
    "v_pk_mul_f32 v[202:203], v[202:203], s[72:73]\n"
    "v_pk_mul_f32 v[204:205], v[204:205], s[72:73]\n"
    "v_pk_mul_f32 v[206:207], v[206:207], s[72:73]\n"
    "v_cvt_pk_bf16_f32 v184, v192, v193\n"
    "v_cvt_pk_bf16_f32 v185, v194, v195\n"
    "v_cvt_pk_bf16_f32 v186, v196, v197\n"
    "v_cvt_pk_bf16_f32 v187, v198, v199\n"
    "v_cvt_pk_bf16_f32 v188, v200, v201\n"
    "v_cvt_pk_bf16_f32 v189, v202, v203\n"
    "v_cvt_pk_bf16_f32 v190, v204, v205\n"
    "v_cvt_pk_bf16_f32 v191, v206, v207\n"
    "v_add_u32 v114, 0x4000, v2\n"
    "v_subrev_u32 v115, s59, v2\n"
    "v_add_u32 v109, 0x10000, v2\n"
    "s_add_u32 s76, s59, 0x10000\n"
    "s_add_u32 s77, s59, 0xc000\n"
    "s_min_u32 s82, s57, 1\n"
    "s_add_u32 s82, s82, 1\n"
    "s_waitcnt vmcnt(24)\n"
    "s_barrier\n"
    "ds_read_b128 a[128:131], v109\n"
    "ds_read_b128 a[144:147], v109 offset:4096\n"
    "ds_read_b128 a[132:135], v109 offset:1024\n"
    "ds_read_b128 a[148:151], v109 offset:5120\n"
    "ds_read_b128 a[136:139], v109 offset:2048\n"
    "ds_read_b128 a[152:155], v109 offset:6144\n"
    "ds_read_b128 a[140:143], v109 offset:3072\n"
    "ds_read_b128 a[156:159], v109 offset:7168\n"
    "v_mov_b32 v100, 0\n"
    "v_mov_b32 v101, 0\n"
    "v_mov_b32 v102, 0\n"
    "v_mov_b32 v103, 0\n"
    "s_nop 1\n"
    "v_mfma_f32_32x32x16_bf16 a[0:15], v[100:103], v[100:103], 0\n"
    "v_mul_f32 v229, v229, v241\n"
    "v_mul_f32 v230, v230, v242\n"
    "v_mul_f32 v231, v231, v243\n"
    "v_mul_f32 v232, v232, v244\n"
    "v_mul_f32 v233, v233, v245\n"
    "v_mul_f32 v234, v234, v246\n"
    "v_mfma_f32_32x32x16_bf16 a[16:31], v[100:103], v[100:103], 0\n"
    "v_mul_f32 v235, v235, v247\n"
    "v_cvt_pk_bf16_f32 v14, v224, v225\n"
    "v_cvt_pk_bf16_f32 v15, v226, v227\n"
    "v_cvt_pk_bf16_f32 v16, v228, v229\n"
    "v_cvt_pk_bf16_f32 v17, v230, v231\n"
    "v_cvt_pk_bf16_f32 v18, v232, v233\n"
    "v_mfma_f32_32x32x16_bf16 a[32:47], v[100:103], v[100:103], 0\n"
    "v_cvt_pk_bf16_f32 v19, v234, v235\n"
    "v_accvgpr_read_b32 v224, a208\n"
    "v_accvgpr_read_b32 v225, a209\n"
    "v_accvgpr_read_b32 v226, a210\n"
    "v_accvgpr_read_b32 v227, a211\n"
    "v_accvgpr_read_b32 v228, a212\n"
    "v_mfma_f32_32x32x16_bf16 a[48:63], v[100:103], v[100:103], 0\n"
    "v_accvgpr_read_b32 v229, a213\n"
    "v_accvgpr_read_b32 v230, a214\n"
    "v_accvgpr_read_b32 v231, a215\n"
    "v_accvgpr_read_b32 v232, a216\n"
    "v_accvgpr_read_b32 v233, a217\n"
    "v_accvgpr_read_b32 v234, a218\n"
    "v_mfma_f32_32x32x16_bf16 a[64:79], v[100:103], v[100:103], 0\n"
    "v_accvgpr_read_b32 v235, a219\n"
    "v_accvgpr_read_b32 v236, a220\n"
    "v_accvgpr_read_b32 v237, a221\n"
    "v_accvgpr_read_b32 v238, a222\n"
    "v_accvgpr_read_b32 v239, a223\n"
    "v_accvgpr_read_b32 v240, a224\n"
    "v_mfma_f32_32x32x16_bf16 a[80:95], v[100:103], v[100:103], 0\n"
    "v_accvgpr_read_b32 v241, a225\n"
    "v_accvgpr_read_b32 v242, a226\n"
    "v_accvgpr_read_b32 v243, a227\n"
    "v_accvgpr_read_b32 v244, a228\n"
    "v_accvgpr_read_b32 v245, a229\n"
    "v_accvgpr_read_b32 v246, a230\n"
    "v_mfma_f32_32x32x16_bf16 a[96:111], v[100:103], v[100:103], 0\n"
    "v_accvgpr_read_b32 v247, a231\n"
    "v_max_f32 v224, 0, v224\n"
    "v_max_f32 v225, 0, v225\n"
    "v_max_f32 v226, 0, v226\n"
    "v_max_f32 v227, 0, v227\n"
    "v_max_f32 v228, 0, v228\n"
    "v_mfma_f32_32x32x16_bf16 a[112:127], v[100:103], v[100:103], 0\n"
    "v_max_f32 v229, 0, v229\n"
    "v_max_f32 v230, 0, v230\n"
    "v_max_f32 v231, 0, v231\n"
    "v_max_f32 v232, 0, v232\n"
    "v_max_f32 v233, 0, v233\n"
    "v_max_f32 v234, 0, v234\n"
    "s_waitcnt lgkmcnt(0)\n"
    "v_mfma_f32_16x16x32_bf16 v[192:195], a[128:131], v[128:131], 0\n"
    "v_max_f32 v235, 0, v235\n"
    "v_mul_f32 v224, v224, v236\n"
    "v_mfma_f32_16x16x32_bf16 v[200:203], a[128:131], v[144:147], 0\n"
    "v_mul_f32 v225, v225, v237\n"
    "v_mul_f32 v226, v226, v238\n"
    "v_mfma_f32_16x16x32_bf16 v[208:211], a[128:131], v[160:163], 0\n"
    "v_mul_f32 v227, v227, v239\n"
    "v_mul_f32 v228, v228, v240\n"
    "v_mfma_f32_16x16x32_bf16 v[216:219], a[128:131], v[176:179], 0\n"
    "v_mul_f32 v229, v229, v241\n"
    "v_mul_f32 v230, v230, v242\n"
    "v_mfma_f32_16x16x32_bf16 v[196:199], a[144:147], v[128:131], 0\n"
    "v_mul_f32 v231, v231, v243\n"
    "v_mul_f32 v232, v232, v244\n"
    "v_mfma_f32_16x16x32_bf16 v[204:207], a[144:147], v[144:147], 0\n"
    "v_mul_f32 v233, v233, v245\n"
    "v_mul_f32 v234, v234, v246\n"
    "v_mfma_f32_16x16x32_bf16 v[212:215], a[144:147], v[160:163], 0\n"
    "v_mul_f32 v235, v235, v247\n"
    "v_cvt_pk_bf16_f32 v20, v224, v225\n"
    "v_mfma_f32_16x16x32_bf16 v[220:223], a[144:147], v[176:179], 0\n"
    "v_cvt_pk_bf16_f32 v21, v226, v227\n"
    "v_cvt_pk_bf16_f32 v22, v228, v229\n"
    "v_mfma_f32_16x16x32_bf16 v[192:195], a[132:135], v[132:135], v[192:195]\n"
    "v_cvt_pk_bf16_f32 v23, v230, v231\n"
    "v_cvt_pk_bf16_f32 v24, v232, v233\n"
    "v_mfma_f32_16x16x32_bf16 v[200:203], a[132:135], v[148:151], v[200:203]\n"
    "v_cvt_pk_bf16_f32 v25, v234, v235\n"
    "v_accvgpr_read_b32 v224, a232\n"
    "v_mfma_f32_16x16x32_bf16 v[208:211], a[132:135], v[164:167], v[208:211]\n"
    "v_accvgpr_read_b32 v225, a233\n"
    "v_accvgpr_read_b32 v226, a234\n"
    "v_mfma_f32_16x16x32_bf16 v[216:219], a[132:135], v[180:183], v[216:219]\n"
    "v_accvgpr_read_b32 v227, a235\n"
    "v_accvgpr_read_b32 v228, a236\n"
    "v_mfma_f32_16x16x32_bf16 v[196:199], a[148:151], v[132:135], v[196:199]\n"
    "v_accvgpr_read_b32 v229, a237\n"
    "v_accvgpr_read_b32 v230, a238\n"
    "v_mfma_f32_16x16x32_bf16 v[204:207], a[148:151], v[148:151], v[204:207]\n"
    "v_accvgpr_read_b32 v231, a239\n"
    "v_accvgpr_read_b32 v232, a240\n"
    "v_mfma_f32_16x16x32_bf16 v[212:215], a[148:151], v[164:167], v[212:215]\n"
    "v_accvgpr_read_b32 v233, a241\n"
    "v_accvgpr_read_b32 v234, a242\n"
    "v_mfma_f32_16x16x32_bf16 v[220:223], a[148:151], v[180:183], v[220:223]\n"
    "v_accvgpr_read_b32 v235, a243\n"
    "v_accvgpr_read_b32 v236, a244\n"
    "v_mfma_f32_16x16x32_bf16 v[192:195], a[136:139], v[136:139], v[192:195]\n"
    "v_accvgpr_read_b32 v237, a245\n"
    "v_accvgpr_read_b32 v238, a246\n"
    "v_mfma_f32_16x16x32_bf16 v[200:203], a[136:139], v[152:155], v[200:203]\n"
    "v_accvgpr_read_b32 v239, a247\n"
    "v_accvgpr_read_b32 v240, a248\n"
    "v_mfma_f32_16x16x32_bf16 v[208:211], a[136:139], v[168:171], v[208:211]\n"
    "v_accvgpr_read_b32 v241, a249\n"
    "v_accvgpr_read_b32 v242, a250\n"
    "v_mfma_f32_16x16x32_bf16 v[216:219], a[136:139], v[184:187], v[216:219]\n"
    "v_accvgpr_read_b32 v243, a251\n"
    "v_accvgpr_read_b32 v244, a252\n"
    "v_mfma_f32_16x16x32_bf16 v[196:199], a[152:155], v[136:139], v[196:199]\n"
    "v_accvgpr_read_b32 v245, a253\n"
    "v_accvgpr_read_b32 v246, a254\n"
    "v_mfma_f32_16x16x32_bf16 v[204:207], a[152:155], v[152:155], v[204:207]\n"
    "v_accvgpr_read_b32 v247, a255\n"
    "v_max_f32 v224, 0, v224\n"
    "v_mfma_f32_16x16x32_bf16 v[212:215], a[152:155], v[168:171], v[212:215]\n"
    "v_max_f32 v225, 0, v225\n"
    "v_max_f32 v226, 0, v226\n"
    "v_mfma_f32_16x16x32_bf16 v[220:223], a[152:155], v[184:187], v[220:223]\n"
    "v_max_f32 v227, 0, v227\n"
    "v_max_f32 v228, 0, v228\n"
    "v_mfma_f32_16x16x32_bf16 v[192:195], a[140:143], v[140:143], v[192:195]\n"
    "v_max_f32 v229, 0, v229\n"
    "v_max_f32 v230, 0, v230\n"
    "v_mfma_f32_16x16x32_bf16 v[200:203], a[140:143], v[156:159], v[200:203]\n"
    "v_max_f32 v231, 0, v231\n"
    "v_max_f32 v232, 0, v232\n"
    "v_mfma_f32_16x16x32_bf16 v[208:211], a[140:143], v[172:175], v[208:211]\n"
    "v_max_f32 v233, 0, v233\n"
    "v_max_f32 v234, 0, v234\n"
    "v_mfma_f32_16x16x32_bf16 v[216:219], a[140:143], v[188:191], v[216:219]\n"
    "v_max_f32 v235, 0, v235\n"
    "v_mul_f32 v224, v224, v236\n"
    "v_mfma_f32_16x16x32_bf16 v[196:199], a[156:159], v[140:143], v[196:199]\n"
    "v_mul_f32 v225, v225, v237\n"
    "v_mul_f32 v226, v226, v238\n"
    "v_mfma_f32_16x16x32_bf16 v[204:207], a[156:159], v[156:159], v[204:207]\n"
    "v_mul_f32 v227, v227, v239\n"
    "v_mul_f32 v228, v228, v240\n"
    "v_mfma_f32_16x16x32_bf16 v[212:215], a[156:159], v[172:175], v[212:215]\n"
    "v_mul_f32 v229, v229, v241\n"
    "v_mul_f32 v230, v230, v242\n"
    "v_mfma_f32_16x16x32_bf16 v[220:223], a[156:159], v[188:191], v[220:223]\n"
    "v_mul_f32 v231, v231, v243\n"
    "v_mul_f32 v232, v232, v244\n"
    "ds_read_b128 a[128:131], v109 offset:8192\n"
    "ds_read_b128 a[144:147], v109 offset:12288\n"
    "ds_read_b128 a[132:135], v109 offset:9216\n"
    "ds_read_b128 a[148:151], v109 offset:13312\n"
    "ds_read_b128 a[136:139], v109 offset:10240\n"
    "ds_read_b128 a[152:155], v109 offset:14336\n"
    "ds_read_b128 a[140:143], v109 offset:11264\n"
    "ds_read_b128 a[156:159], v109 offset:15360\n"
    "v_mul_f32 v233, v233, v245\n"
    "v_mul_f32 v234, v234, v246\n"
    "v_mul_f32 v235, v235, v247\n"
    "v_cvt_pk_bf16_f32 v26, v224, v225\n"
    "v_cvt_pk_bf16_f32 v27, v226, v227\n"
    "v_cvt_pk_bf16_f32 v28, v228, v229\n"
    "v_cvt_pk_bf16_f32 v29, v230, v231\n"
    "v_cvt_pk_bf16_f32 v30, v232, v233\n"
    "v_cvt_pk_bf16_f32 v31, v234, v235\n"
    "v_mov_b32 v113, 0xff800000\n"
    "v_mov_b32 v80, 0x3f803f80\n"
    "v_mov_b32 v81, 0x3f803f80\n"
    "v_mov_b32 v82, 0x3f803f80\n"
    "v_mov_b32 v83, 0x3f803f80\n"
    "v_and_b32 v3, 15, v1\n"
    "v_lshrrev_b32 v4, 4, v1\n"
    "v_lshlrev_b32 v4, 2, v4\n"
    "v_sub_u32 v3, v3, v4\n"
    "s_lshl_b32 s4, s58, 4\n"
    "v_add_u32 v111, s4, v3\n"
    "v_mov_b32 v84, 0\n"
    "v_mov_b32 v88, 0\n"
    "v_mov_b32 v92, 0\n"
    "v_mov_b32 v96, 0\n"
    "s_bitcmp1_b32 s56, 0\n"
    "s_cbranch_scc1 Lhodd\n"
    "buffer_store_dwordx4 v[8:11], v2, s[8:11], s85 offen sc1\n"
    "buffer_store_dwordx2 v[12:13], v2, s[8:11], s85 offen offset:1024 sc1\n"
    "buffer_store_dwordx4 v[14:17], v120, s[8:11], s85 offen sc1\n"
    "buffer_store_dwordx2 v[18:19], v120, s[8:11], s85 offen offset:1024 sc1\n"
    "buffer_store_dwordx4 v[20:23], v2, s[8:11], s86 offen sc1\n"
    "buffer_store_dwordx2 v[24:25], v2, s[8:11], s86 offen offset:1024 sc1\n"
    "buffer_store_dwordx4 v[26:29], v120, s[8:11], s86 offen sc1\n"
    "buffer_store_dwordx2 v[30:31], v120, s[8:11], s86 offen offset:1024 sc1\n"
    "Lhret:\n"
    "s_mov_b32 m0, s77\n"
    "s_add_u32 s77, s77, 0x8000\n"
    "s_and_b32 s77, s77, 0x1ffff\n"
    "v_add_u32 v110, s77, v115\n"
    "s_cmp_lg_u32 s57, 0\n"
    "s_cbranch_scc1 Lpm0\n"
    "v_mov_b32 v112, v111\n"
    "v_cmp_gt_i32 s[84:85], 0, v112\n"
    "v_cmp_gt_i32 s[86:87], 1, v112\n"
    "v_cmp_gt_i32 s[92:93], 2, v112\n"
    "v_cndmask_b32 v192, v192, v113, s[84:85]\n"
    "v_cmp_gt_i32 s[94:95], 3, v112\n"
    "v_cndmask_b32 v193, v193, v113, s[86:87]\n"
    "v_cmp_gt_i32 s[84:85], 16, v112\n"
    "v_cndmask_b32 v194, v194, v113, s[92:93]\n"
    "v_cmp_gt_i32 s[86:87], 17, v112\n"
    "v_cndmask_b32 v195, v195, v113, s[94:95]\n"
    "v_cmp_gt_i32 s[92:93], 18, v112\n"
    "v_cndmask_b32 v196, v196, v113, s[84:85]\n"
    "v_cmp_gt_i32 s[94:95], 19, v112\n"
    "v_cndmask_b32 v197, v197, v113, s[86:87]\n"
    "v_cndmask_b32 v198, v198, v113, s[92:93]\n"
    "v_cndmask_b32 v199, v199, v113, s[94:95]\n"
    "Lpm0:\n"
    "v_max3_f32 v116, v192, v193, v194\n"
    "v_max3_f32 v116, v116, v195, v196\n"
    "v_max3_f32 v116, v116, v197, v198\n"
    "v_max_f32 v116, v116, v199\n"
    "v_max3_f32 v117, v200, v201, v202\n"
    "v_max3_f32 v117, v117, v203, v204\n"
    "v_max3_f32 v117, v117, v205, v206\n"
    "v_max_f32 v117, v117, v207\n"
    "v_max3_f32 v118, v208, v209, v210\n"
    "v_max3_f32 v118, v118, v211, v212\n"
    "v_max3_f32 v118, v118, v213, v214\n"
    "v_max_f32 v118, v118, v215\n"
    "v_max3_f32 v119, v216, v217, v218\n"
    "v_max3_f32 v119, v119, v219, v220\n"
    "v_max3_f32 v119, v119, v221, v222\n"
    "v_max_f32 v119, v119, v223\n"
    "v_mov_b32 v72, v116\n"
    "v_mov_b32 v73, v117\n"
    "v_mov_b32 v74, v118\n"
    "v_mov_b32 v75, v119\n"
    "v_permlane16_swap_b32 v72, v116\n"
    "v_permlane16_swap_b32 v73, v117\n"
    "v_permlane16_swap_b32 v74, v118\n"
    "v_permlane16_swap_b32 v75, v119\n"
    "v_max_f32 v116, v116, v72\n"
    "v_max_f32 v117, v117, v73\n"
    "v_max_f32 v118, v118, v74\n"
    "v_max_f32 v119, v119, v75\n"
    "v_mov_b32 v72, v116\n"
    "v_mov_b32 v73, v117\n"
    "v_mov_b32 v74, v118\n"
    "v_mov_b32 v75, v119\n"
    "v_permlane32_swap_b32 v72, v116\n"
    "v_permlane32_swap_b32 v73, v117\n"
    "v_permlane32_swap_b32 v74, v118\n"
    "v_permlane32_swap_b32 v75, v119\n"
    "v_max_f32 v116, v116, v72\n"
    "v_max_f32 v117, v117, v73\n"
    "v_max_f32 v118, v118, v74\n"
    "v_max_f32 v119, v119, v75\n"
    "v_add_f32 v116, 0x41000000, v116\n"
    "v_add_f32 v117, 0x41000000, v117\n"
    "v_add_f32 v118, 0x41000000, v118\n"
    "v_add_f32 v119, 0x41000000, v119\n"
    "v_sub_f32 v192, v192, v116\n"
    "v_sub_f32 v193, v193, v116\n"
    "v_sub_f32 v194, v194, v116\n"
    "v_sub_f32 v195, v195, v116\n"
    "v_sub_f32 v196, v196, v116\n"
    "v_sub_f32 v197, v197, v116\n"
    "v_sub_f32 v198, v198, v116\n"
    "v_sub_f32 v199, v199, v116\n"
    "v_sub_f32 v200, v200, v117\n"
    "v_sub_f32 v201, v201, v117\n"
    "v_sub_f32 v202, v202, v117\n"
    "v_sub_f32 v203, v203, v117\n"
    "v_sub_f32 v204, v204, v117\n"
    "v_sub_f32 v205, v205, v117\n"
    "v_sub_f32 v206, v206, v117\n"
    "v_sub_f32 v207, v207, v117\n"
    "v_sub_f32 v208, v208, v118\n"
    "v_sub_f32 v209, v209, v118\n"
    "v_sub_f32 v210, v210, v118\n"
    "v_sub_f32 v211, v211, v118\n"
    "v_sub_f32 v212, v212, v118\n"
    "v_sub_f32 v213, v213, v118\n"
    "v_sub_f32 v214, v214, v118\n"
    "v_sub_f32 v215, v215, v118\n"
    "v_sub_f32 v216, v216, v119\n"
    "v_sub_f32 v217, v217, v119\n"
    "v_sub_f32 v218, v218, v119\n"
    "v_sub_f32 v219, v219, v119\n"
    "v_sub_f32 v220, v220, v119\n"
    "v_sub_f32 v221, v221, v119\n"
    "v_sub_f32 v222, v222, v119\n"
    "v_sub_f32 v223, v223, v119\n"
    "v_sub_f32 v40, 0, v116\n"
    "v_sub_f32 v41, 0, v116\n"
    "v_sub_f32 v42, 0, v116\n"
    "v_sub_f32 v43, 0, v116\n"
    "v_sub_f32 v44, 0, v116\n"
    "v_sub_f32 v45, 0, v116\n"
    "v_sub_f32 v46, 0, v116\n"
    "v_sub_f32 v47, 0, v116\n"
    "v_sub_f32 v48, 0, v117\n"
    "v_sub_f32 v49, 0, v117\n"
    "v_sub_f32 v50, 0, v117\n"
    "v_sub_f32 v51, 0, v117\n"
    "v_sub_f32 v52, 0, v117\n"
    "v_sub_f32 v53, 0, v117\n"
    "v_sub_f32 v54, 0, v117\n"
    "v_sub_f32 v55, 0, v117\n"
    "v_sub_f32 v56, 0, v118\n"
    "v_sub_f32 v57, 0, v118\n"
    "v_sub_f32 v58, 0, v118\n"
    "v_sub_f32 v59, 0, v118\n"
    "v_sub_f32 v60, 0, v118\n"
    "v_sub_f32 v61, 0, v118\n"
    "v_sub_f32 v62, 0, v118\n"
    "v_sub_f32 v63, 0, v118\n"
    "v_sub_f32 v64, 0, v119\n"
    "v_sub_f32 v65, 0, v119\n"
    "v_sub_f32 v66, 0, v119\n"
    "v_sub_f32 v67, 0, v119\n"
    "v_sub_f32 v68, 0, v119\n"
    "v_sub_f32 v69, 0, v119\n"
    "v_sub_f32 v70, 0, v119\n"
    "v_sub_f32 v71, 0, v119\n"
    "s_cmp_lg_u32 s57, 0\n"
    "s_cbranch_scc1 Lpm1\n"
    "v_subrev_u32 v112, 32, v111\n"
    "v_cmp_gt_i32 s[84:85], 0, v112\n"
    "v_cmp_gt_i32 s[86:87], 1, v112\n"
    "v_cmp_gt_i32 s[92:93], 2, v112\n"
    "v_cndmask_b32 v40, v40, v113, s[84:85]\n"
    "v_cmp_gt_i32 s[94:95], 3, v112\n"
    "v_cndmask_b32 v41, v41, v113, s[86:87]\n"
    "v_cmp_gt_i32 s[84:85], 16, v112\n"
    "v_cndmask_b32 v42, v42, v113, s[92:93]\n"
    "v_cmp_gt_i32 s[86:87], 17, v112\n"
    "v_cndmask_b32 v43, v43, v113, s[94:95]\n"
    "v_cmp_gt_i32 s[92:93], 18, v112\n"
    "v_cndmask_b32 v44, v44, v113, s[84:85]\n"
    "v_cmp_gt_i32 s[94:95], 19, v112\n"
    "v_cndmask_b32 v45, v45, v113, s[86:87]\n"
    "v_cndmask_b32 v46, v46, v113, s[92:93]\n"
    "v_cndmask_b32 v47, v47, v113, s[94:95]\n"
    "Lpm1:\n"
    "v_exp_f32 v72, v192\n"
    "v_exp_f32 v73, v193\n"
    "v_exp_f32 v74, v194\n"
    "v_exp_f32 v75, v195\n"
    "v_cvt_pk_bf16_f32 v8, v72, v73\n"
    "v_exp_f32 v76, v200\n"
    "v_exp_f32 v77, v201\n"
    "v_cvt_pk_bf16_f32 v9, v74, v75\n"
    "v_exp_f32 v78, v202\n"
    "v_exp_f32 v79, v203\n"
    "v_cvt_pk_bf16_f32 v12, v76, v77\n"
    "v_or_b32 v108, v8, v9\n"
    "v_exp_f32 v72, v208\n"
    "v_exp_f32 v73, v209\n"
    "v_cvt_pk_bf16_f32 v13, v78, v79\n"
    "v_exp_f32 v74, v210\n"
    "v_exp_f32 v75, v211\n"
    "v_cvt_pk_bf16_f32 v16, v72, v73\n"
    "v_or3_b32 v108, v108, v12, v13\n"
    "v_exp_f32 v76, v216\n"
    "v_exp_f32 v77, v217\n"
    "v_cvt_pk_bf16_f32 v17, v74, v75\n"
    "v_exp_f32 v78, v218\n"
    "v_exp_f32 v79, v219\n"
    "v_cvt_pk_bf16_f32 v20, v76, v77\n"
    "v_or3_b32 v108, v108, v16, v17\n"
    "v_exp_f32 v72, v196\n"
    "v_exp_f32 v73, v197\n"
    "v_cvt_pk_bf16_f32 v21, v78, v79\n"
    "v_exp_f32 v74, v198\n"
    "v_exp_f32 v75, v199\n"
    "v_cvt_pk_bf16_f32 v10, v72, v73\n"
    "v_or3_b32 v108, v108, v20, v21\n"
    "v_exp_f32 v76, v204\n"
    "s_lshl_b32 s78, s57, 1\n"
    "s_sub_u32 s78, s78, 1\n"
    "s_cbranch_scc1 LP1\n"
    ".p2align 6\n"
    "LloopF:\n"
    "s_waitcnt lgkmcnt(0)\n"
    "v_mfma_f32_16x16x32_bf16 v[224:227], a[128:131], v[128:131], v[40:43]\n"
    "s_waitcnt vmcnt(16)\n"
    "s_barrier\n"
    "v_mfma_f32_16x16x32_bf16 v[232:235], a[128:131], v[144:147], v[48:51]\n"
    "s_sub_u32 s82, s82, 1\n"
    "s_cbranch_scc1 Levs1\n"
    "Levr1:\n"
    "v_mfma_f32_16x16x32_bf16 v[240:243], a[128:131], v[160:163], v[56:59]\n"
    "v_exp_f32 v77, v205\n"
    "v_mfma_f32_16x16x32_bf16 v[248:251], a[128:131], v[176:179], v[64:67]\n"
    "ds_read_b128 a[192:195], v110\n"
    "v_cvt_pk_bf16_f32 v11, v74, v75\n"
    "v_mfma_f32_16x16x32_bf16 v[228:231], a[144:147], v[128:131], v[44:47]\n"
    "v_exp_f32 v78, v206\n"
    "v_mfma_f32_16x16x32_bf16 v[236:239], a[144:147], v[144:147], v[52:55]\n"
    "ds_read_b128 a[196:199], v110 offset:1024\n"
    "buffer_load_dwordx4 v2, s[24:27], s75 offen sc1 lds\n"
    "v_mfma_f32_16x16x32_bf16 v[244:247], a[144:147], v[160:163], v[60:63]\n"
    "v_exp_f32 v79, v207\n"
    "v_mfma_f32_16x16x32_bf16 v[252:255], a[144:147], v[176:179], v[68:71]\n"
    "ds_read_b128 a[200:203], v110 offset:2048\n"
    "v_cvt_pk_bf16_f32 v14, v76, v77\n"
    "v_mfma_f32_16x16x32_bf16 v[224:227], a[132:135], v[132:135], v[224:227]\n"
    "buffer_load_dwordx4 v2, s[24:27], s75 offen offset:1024 sc1 lds\n"
    "v_or3_b32 v108, v108, v10, v11\n"
    "v_mfma_f32_16x16x32_bf16 v[232:235], a[132:135], v[148:151], v[232:235]\n"
    "v_exp_f32 v72, v212\n"
    "v_mfma_f32_16x16x32_bf16 v[240:243], a[132:135], v[164:167], v[240:243]\n"
    "buffer_load_dwordx4 v2, s[24:27], s75 offen offset:2048 sc1 lds\n"
    "ds_read_b128 a[204:207], v110 offset:3072\n"
    "v_mfma_f32_16x16x32_bf16 v[248:251], a[132:135], v[180:183], v[248:251]\n"
    "v_exp_f32 v73, v213\n"
    "v_mfma_f32_16x16x32_bf16 v[228:231], a[148:151], v[132:135], v[228:231]\n"
    "buffer_load_dwordx4 v2, s[24:27], s75 offen offset:3072 sc1 lds\n"
    "v_cvt_pk_bf16_f32 v15, v78, v79\n"
    "v_mfma_f32_16x16x32_bf16 v[236:239], a[148:151], v[148:151], v[236:239]\n"
    "v_exp_f32 v74, v214\n"
    "v_mfma_f32_16x16x32_bf16 v[244:247], a[148:151], v[164:167], v[244:247]\n"
    "ds_read_b128 a[208:211], v110 offset:4096\n"
    "s_mov_b32 m0, s76\n"
    "v_mfma_f32_16x16x32_bf16 v[252:255], a[148:151], v[180:183], v[252:255]\n"
    "v_exp_f32 v75, v215\n"
    "v_mfma_f32_16x16x32_bf16 v[224:227], a[136:139], v[136:139], v[224:227]\n"
    "buffer_load_dwordx4 v114, s[24:27], s75 offen sc1 lds\n"
    "v_cvt_pk_bf16_f32 v18, v72, v73\n"
    "v_mfma_f32_16x16x32_bf16 v[232:235], a[136:139], v[152:155], v[232:235]\n"
    "ds_read_b128 a[212:215], v110 offset:5120\n"
    "v_or3_b32 v108, v108, v14, v15\n"
    "v_mfma_f32_16x16x32_bf16 v[240:243], a[136:139], v[168:171], v[240:243]\n"
    "v_exp_f32 v76, v220\n"
    "v_mfma_f32_16x16x32_bf16 v[248:251], a[136:139], v[184:187], v[248:251]\n"
    "buffer_load_dwordx4 v114, s[24:27], s75 offen offset:1024 sc1 lds\n"
    "ds_read_b128 a[216:219], v110 offset:6144\n"
    "v_mfma_f32_16x16x32_bf16 v[228:231], a[152:155], v[136:139], v[228:231]\n"
    "v_exp_f32 v77, v221\n"
    "v_mfma_f32_16x16x32_bf16 v[236:239], a[152:155], v[152:155], v[236:239]\n"
    "buffer_load_dwordx4 v114, s[24:27], s75 offen offset:2048 sc1 lds\n"
    "v_cvt_pk_bf16_f32 v19, v74, v75\n"
    "v_mfma_f32_16x16x32_bf16 v[244:247], a[152:155], v[168:171], v[244:247]\n"
    "v_exp_f32 v78, v222\n"
    "v_mfma_f32_16x16x32_bf16 v[252:255], a[152:155], v[184:187], v[252:255]\n"
    "buffer_load_dwordx4 v114, s[24:27], s75 offen offset:3072 sc1 lds\n"
    "ds_read_b128 a[220:223], v110 offset:7168\n"
    "v_mfma_f32_16x16x32_bf16 v[224:227], a[140:143], v[140:143], v[224:227]\n"
    "v_exp_f32 v79, v223\n"
    "v_mfma_f32_16x16x32_bf16 v[232:235], a[140:143], v[156:159], v[232:235]\n"
    "s_add_u32 s76, s76, 0x8000\n"
    "v_cvt_pk_bf16_f32 v22, v76, v77\n"
    "v_mfma_f32_16x16x32_bf16 v[240:243], a[140:143], v[172:175], v[240:243]\n"
    "s_and_b32 s76, s76, 0x1ffff\n"
    "v_or3_b32 v108, v108, v18, v19\n"
    "v_mfma_f32_16x16x32_bf16 v[248:251], a[140:143], v[188:191], v[248:251]\n"
    "v_cvt_pk_bf16_f32 v23, v78, v79\n"
    "v_or3_b32 v108, v108, v22, v23\n"
    "v_mfma_f32_16x16x32_bf16 v[228:231], a[156:159], v[140:143], v[228:231]\n"
    "v_add_u32 v109, s76, v115\n"
    "v_and_b32 v3, 0x40004000, v108\n"
    "v_mfma_f32_16x16x32_bf16 v[236:239], a[156:159], v[156:159], v[236:239]\n"
    "v_cmp_ne_u32 vcc, 0, v3\n"
    "v_mfma_f32_16x16x32_bf16 v[244:247], a[156:159], v[172:175], v[244:247]\n"
    "v_mfma_f32_16x16x32_bf16 v[252:255], a[156:159], v[188:191], v[252:255]\n"
    "s_cbranch_vccnz Lrs1\n"
    "Lrr1:\n"
    "s_waitcnt lgkmcnt(0)\n"
    "v_mfma_f32_16x16x32_bf16 a[0:3], a[192:195], v[8:11], a[0:3]\n"
    "ds_read_b128 a[128:131], v109\n"
    "s_add_u32 s75, s75, 0x8000\n"
    "v_mfma_f32_16x16x32_bf16 a[32:35], a[192:195], v[12:15], a[32:35]\n"
    "ds_read_b128 a[144:147], v109 offset:4096\n"
    "ds_read_b128 a[132:135], v109 offset:1024\n"
    "v_mfma_f32_16x16x32_bf16 a[64:67], a[192:195], v[16:19], a[64:67]\n"
    "v_exp_f32 v72, v224\n"
    "v_mfma_f32_16x16x32_bf16 a[96:99], a[192:195], v[20:23], a[96:99]\n"
    "v_exp_f32 v73, v225\n"
    "v_mfma_f32_16x16x32_bf16 a[4:7], a[196:199], v[8:11], a[4:7]\n"
    "v_exp_f32 v74, v226\n"
    "v_mfma_f32_16x16x32_bf16 a[36:39], a[196:199], v[12:15], a[36:39]\n"
    "v_exp_f32 v75, v227\n"
    "v_mfma_f32_16x16x32_bf16 a[68:71], a[196:199], v[16:19], a[68:71]\n"
    "ds_read_b128 a[148:151], v109 offset:5120\n"
    "v_cvt_pk_bf16_f32 v24, v72, v73\n"
    "v_mfma_f32_16x16x32_bf16 a[100:103], a[196:199], v[20:23], a[100:103]\n"
    "v_exp_f32 v76, v232\n"
    "v_mfma_f32_16x16x32_bf16 v[84:87], v[80:83], v[8:11], v[84:87]\n"
    "v_exp_f32 v77, v233\n"
    "v_mfma_f32_16x16x32_bf16 a[8:11], a[200:203], v[8:11], a[8:11]\n"
    "ds_read_b128 a[136:139], v109 offset:2048\n"
    "v_cvt_pk_bf16_f32 v25, v74, v75\n"
    "v_mfma_f32_16x16x32_bf16 a[40:43], a[200:203], v[12:15], a[40:43]\n"
    "v_exp_f32 v78, v234\n"
    "v_mfma_f32_16x16x32_bf16 a[72:75], a[200:203], v[16:19], a[72:75]\n"
    "v_exp_f32 v79, v235\n"
    "v_mfma_f32_16x16x32_bf16 a[104:107], a[200:203], v[20:23], a[104:107]\n"
    "v_cvt_pk_bf16_f32 v28, v76, v77\n"
    "v_or_b32 v108, v24, v25\n"
    "v_mfma_f32_16x16x32_bf16 a[12:15], a[204:207], v[8:11], a[12:15]\n"
    "v_exp_f32 v72, v240\n"
    "v_mfma_f32_16x16x32_bf16 a[44:47], a[204:207], v[12:15], a[44:47]\n"
    "v_exp_f32 v73, v241\n"
    "v_mfma_f32_16x16x32_bf16 a[76:79], a[204:207], v[16:19], a[76:79]\n"
    "ds_read_b128 a[152:155], v109 offset:6144\n"
    "v_cvt_pk_bf16_f32 v29, v78, v79\n"
    "v_mfma_f32_16x16x32_bf16 a[108:111], a[204:207], v[20:23], a[108:111]\n"
    "v_exp_f32 v74, v242\n"
    "v_mfma_f32_16x16x32_bf16 v[88:91], v[80:83], v[12:15], v[88:91]\n"
    "v_exp_f32 v75, v243\n"
    "v_mfma_f32_16x16x32_bf16 a[16:19], a[208:211], v[8:11], a[16:19]\n"
    "v_cvt_pk_bf16_f32 v32, v72, v73\n"
    "v_or3_b32 v108, v108, v28, v29\n"
    "v_mfma_f32_16x16x32_bf16 a[48:51], a[208:211], v[12:15], a[48:51]\n"
    "v_exp_f32 v76, v248\n"
    "v_mfma_f32_16x16x32_bf16 a[80:83], a[208:211], v[16:19], a[80:83]\n"
    "v_exp_f32 v77, v249\n"
    "v_mfma_f32_16x16x32_bf16 a[112:115], a[208:211], v[20:23], a[112:115]\n"
    "ds_read_b128 a[140:143], v109 offset:3072\n"
    "v_cvt_pk_bf16_f32 v33, v74, v75\n"
    "v_mfma_f32_16x16x32_bf16 a[20:23], a[212:215], v[8:11], a[20:23]\n"
    "v_exp_f32 v78, v250\n"
    "v_mfma_f32_16x16x32_bf16 a[52:55], a[212:215], v[12:15], a[52:55]\n"
    "v_exp_f32 v79, v251\n"
    "v_mfma_f32_16x16x32_bf16 a[84:87], a[212:215], v[16:19], a[84:87]\n"
    "ds_read_b128 a[156:159], v109 offset:7168\n"
    "v_cvt_pk_bf16_f32 v36, v76, v77\n"
    "v_mfma_f32_16x16x32_bf16 a[116:119], a[212:215], v[20:23], a[116:119]\n"
    "v_or3_b32 v108, v108, v32, v33\n"
    "v_mfma_f32_16x16x32_bf16 v[92:95], v[80:83], v[16:19], v[92:95]\n"
    "v_exp_f32 v72, v228\n"
    "v_mfma_f32_16x16x32_bf16 a[24:27], a[216:219], v[8:11], a[24:27]\n"
    "v_exp_f32 v73, v229\n"
    "v_mfma_f32_16x16x32_bf16 a[56:59], a[216:219], v[12:15], a[56:59]\n"
    "v_cvt_pk_bf16_f32 v37, v78, v79\n"
    "v_mfma_f32_16x16x32_bf16 a[88:91], a[216:219], v[16:19], a[88:91]\n"
    "v_exp_f32 v74, v230\n"
    "v_mfma_f32_16x16x32_bf16 a[120:123], a[216:219], v[20:23], a[120:123]\n"
    "v_exp_f32 v75, v231\n"
    "v_mfma_f32_16x16x32_bf16 a[28:31], a[220:223], v[8:11], a[28:31]\n"
    "v_cvt_pk_bf16_f32 v26, v72, v73\n"
    "v_or3_b32 v108, v108, v36, v37\n"
    "v_mfma_f32_16x16x32_bf16 a[60:63], a[220:223], v[12:15], a[60:63]\n"
    "v_exp_f32 v76, v236\n"
    "v_mfma_f32_16x16x32_bf16 a[92:95], a[220:223], v[16:19], a[92:95]\n"
    "v_mfma_f32_16x16x32_bf16 a[124:127], a[220:223], v[20:23], a[124:127]\n"
    "v_mfma_f32_16x16x32_bf16 v[96:99], v[80:83], v[20:23], v[96:99]\n"
    "s_waitcnt lgkmcnt(0)\n"
    "v_mfma_f32_16x16x32_bf16 v[192:195], a[128:131], v[128:131], v[40:43]\n"
    "v_exp_f32 v77, v237\n"
    "v_mfma_f32_16x16x32_bf16 v[200:203], a[128:131], v[144:147], v[48:51]\n"
    "ds_read_b128 a[192:195], v110 offset:8192\n"
    "v_cvt_pk_bf16_f32 v27, v74, v75\n"
    "v_mfma_f32_16x16x32_bf16 v[208:211], a[128:131], v[160:163], v[56:59]\n"
    "v_exp_f32 v78, v238\n"
    "v_mfma_f32_16x16x32_bf16 v[216:219], a[128:131], v[176:179], v[64:67]\n"
    "v_exp_f32 v79, v239\n"
    "v_mfma_f32_16x16x32_bf16 v[196:199], a[144:147], v[128:131], v[44:47]\n"
    "ds_read_b128 a[196:199], v110 offset:9216\n"
    "v_cvt_pk_bf16_f32 v30, v76, v77\n"
    "v_mfma_f32_16x16x32_bf16 v[204:207], a[144:147], v[144:147], v[52:55]\n"
    "ds_read_b128 a[200:203], v110 offset:10240\n"
    "v_or3_b32 v108, v108, v26, v27\n"
    "v_mfma_f32_16x16x32_bf16 v[212:215], a[144:147], v[160:163], v[60:63]\n"
    "v_exp_f32 v72, v244\n"
    "v_mfma_f32_16x16x32_bf16 v[220:223], a[144:147], v[176:179], v[68:71]\n"
    "v_exp_f32 v73, v245\n"
    "v_mfma_f32_16x16x32_bf16 v[192:195], a[132:135], v[132:135], v[192:195]\n"
    "ds_read_b128 a[204:207], v110 offset:11264\n"
    "v_cvt_pk_bf16_f32 v31, v78, v79\n"
    "v_mfma_f32_16x16x32_bf16 v[200:203], a[132:135], v[148:151], v[200:203]\n"
    "v_exp_f32 v74, v246\n"
    "v_mfma_f32_16x16x32_bf16 v[208:211], a[132:135], v[164:167], v[208:211]\n"
    "v_exp_f32 v75, v247\n"
    "v_mfma_f32_16x16x32_bf16 v[216:219], a[132:135], v[180:183], v[216:219]\n"
    "ds_read_b128 a[208:211], v110 offset:12288\n"
    "v_cvt_pk_bf16_f32 v34, v72, v73\n"
    "v_mfma_f32_16x16x32_bf16 v[196:199], a[148:151], v[132:135], v[196:199]\n"
    "ds_read_b128 a[212:215], v110 offset:13312\n"
    "v_or3_b32 v108, v108, v30, v31\n"
    "v_mfma_f32_16x16x32_bf16 v[204:207], a[148:151], v[148:151], v[204:207]\n"
    "v_exp_f32 v76, v252\n"
    "v_mfma_f32_16x16x32_bf16 v[212:215], a[148:151], v[164:167], v[212:215]\n"
    "v_exp_f32 v77, v253\n"
    "v_mfma_f32_16x16x32_bf16 v[220:223], a[148:151], v[180:183], v[220:223]\n"
    "ds_read_b128 a[216:219], v110 offset:14336\n"
    "v_cvt_pk_bf16_f32 v35, v74, v75\n"
    "v_mfma_f32_16x16x32_bf16 v[192:195], a[136:139], v[136:139], v[192:195]\n"
    "v_exp_f32 v78, v254\n"
    "v_mfma_f32_16x16x32_bf16 v[200:203], a[136:139], v[152:155], v[200:203]\n"
    "v_exp_f32 v79, v255\n"
    "v_mfma_f32_16x16x32_bf16 v[208:211], a[136:139], v[168:171], v[208:211]\n"
    "ds_read_b128 a[220:223], v110 offset:15360\n"
    "v_cvt_pk_bf16_f32 v38, v76, v77\n"
    "v_mfma_f32_16x16x32_bf16 v[216:219], a[136:139], v[184:187], v[216:219]\n"
    "v_or3_b32 v108, v108, v34, v35\n"
    "v_cvt_pk_bf16_f32 v39, v78, v79\n"
    "v_mfma_f32_16x16x32_bf16 v[196:199], a[152:155], v[136:139], v[196:199]\n"
    "v_or3_b32 v108, v108, v38, v39\n"
    "v_and_b32 v3, 0x40004000, v108\n"
    "v_mfma_f32_16x16x32_bf16 v[204:207], a[152:155], v[152:155], v[204:207]\n"
    "v_cmp_ne_u32 vcc, 0, v3\n"
    "v_mfma_f32_16x16x32_bf16 v[212:215], a[152:155], v[168:171], v[212:215]\n"
    "v_mfma_f32_16x16x32_bf16 v[220:223], a[152:155], v[184:187], v[220:223]\n"
    "v_mfma_f32_16x16x32_bf16 v[192:195], a[140:143], v[140:143], v[192:195]\n"
    "v_mfma_f32_16x16x32_bf16 v[200:203], a[140:143], v[156:159], v[200:203]\n"
    "v_mfma_f32_16x16x32_bf16 v[208:211], a[140:143], v[172:175], v[208:211]\n"
    "v_mfma_f32_16x16x32_bf16 v[216:219], a[140:143], v[188:191], v[216:219]\n"
    "v_mfma_f32_16x16x32_bf16 v[196:199], a[156:159], v[140:143], v[196:199]\n"
    "v_mfma_f32_16x16x32_bf16 v[204:207], a[156:159], v[156:159], v[204:207]\n"
    "v_mfma_f32_16x16x32_bf16 v[212:215], a[156:159], v[172:175], v[212:215]\n"
    "v_mfma_f32_16x16x32_bf16 v[220:223], a[156:159], v[188:191], v[220:223]\n"
    "s_cbranch_vccnz Lrs2\n"
    "Lrr2:\n"
    "s_waitcnt lgkmcnt(0)\n"
    "v_mfma_f32_16x16x32_bf16 a[0:3], a[192:195], v[24:27], a[0:3]\n"
    "ds_read_b128 a[128:131], v109 offset:8192\n"
    "s_mov_b32 m0, s77\n"
    "v_mfma_f32_16x16x32_bf16 a[32:35], a[192:195], v[28:31], a[32:35]\n"
    "ds_read_b128 a[144:147], v109 offset:12288\n"
    "s_add_u32 s77, s77, 0x8000\n"
    "v_mfma_f32_16x16x32_bf16 a[64:67], a[192:195], v[32:35], a[64:67]\n"
    "v_exp_f32 v72, v192\n"
    "v_mfma_f32_16x16x32_bf16 a[96:99], a[192:195], v[36:39], a[96:99]\n"
    "v_exp_f32 v73, v193\n"
    "v_mfma_f32_16x16x32_bf16 a[4:7], a[196:199], v[24:27], a[4:7]\n"
    "v_exp_f32 v74, v194\n"
    "v_mfma_f32_16x16x32_bf16 a[36:39], a[196:199], v[28:31], a[36:39]\n"
    "v_exp_f32 v75, v195\n"
    "v_mfma_f32_16x16x32_bf16 a[68:71], a[196:199], v[32:35], a[68:71]\n"
    "ds_read_b128 a[132:135], v109 offset:9216\n"
    "v_cvt_pk_bf16_f32 v8, v72, v73\n"
    "v_mfma_f32_16x16x32_bf16 a[100:103], a[196:199], v[36:39], a[100:103]\n"
    "v_exp_f32 v76, v200\n"
    "v_mfma_f32_16x16x32_bf16 v[84:87], v[80:83], v[24:27], v[84:87]\n"
    "v_exp_f32 v77, v201\n"
    "v_mfma_f32_16x16x32_bf16 a[8:11], a[200:203], v[24:27], a[8:11]\n"
    "ds_read_b128 a[148:151], v109 offset:13312\n"
    "v_cvt_pk_bf16_f32 v9, v74, v75\n"
    "v_mfma_f32_16x16x32_bf16 a[40:43], a[200:203], v[28:31], a[40:43]\n"
    "v_exp_f32 v78, v202\n"
    "v_mfma_f32_16x16x32_bf16 a[72:75], a[200:203], v[32:35], a[72:75]\n"
    "v_exp_f32 v79, v203\n"
    "v_mfma_f32_16x16x32_bf16 a[104:107], a[200:203], v[36:39], a[104:107]\n"
    "s_and_b32 s77, s77, 0x1ffff\n"
    "v_cvt_pk_bf16_f32 v12, v76, v77\n"
    "v_mfma_f32_16x16x32_bf16 a[12:15], a[204:207], v[24:27], a[12:15]\n"
    "ds_read_b128 a[136:139], v109 offset:10240\n"
    "v_or_b32 v108, v8, v9\n"
    "v_mfma_f32_16x16x32_bf16 a[44:47], a[204:207], v[28:31], a[44:47]\n"
    "v_exp_f32 v72, v208\n"
    "v_mfma_f32_16x16x32_bf16 a[76:79], a[204:207], v[32:35], a[76:79]\n"
    "v_exp_f32 v73, v209\n"
    "v_mfma_f32_16x16x32_bf16 a[108:111], a[204:207], v[36:39], a[108:111]\n"
    "v_cvt_pk_bf16_f32 v13, v78, v79\n"
    "v_add_u32 v110, s77, v115\n"
    "v_mfma_f32_16x16x32_bf16 v[88:91], v[80:83], v[28:31], v[88:91]\n"
    "v_exp_f32 v74, v210\n"
    "v_mfma_f32_16x16x32_bf16 a[16:19], a[208:211], v[24:27], a[16:19]\n"
    "v_exp_f32 v75, v211\n"
    "v_mfma_f32_16x16x32_bf16 a[48:51], a[208:211], v[28:31], a[48:51]\n"
    "ds_read_b128 a[152:155], v109 offset:14336\n"
    "v_cvt_pk_bf16_f32 v16, v72, v73\n"
    "v_mfma_f32_16x16x32_bf16 a[80:83], a[208:211], v[32:35], a[80:83]\n"
    "ds_read_b128 a[140:143], v109 offset:11264\n"
    "v_or3_b32 v108, v108, v12, v13\n"
    "v_mfma_f32_16x16x32_bf16 a[112:115], a[208:211], v[36:39], a[112:115]\n"
    "v_exp_f32 v76, v216\n"
    "v_mfma_f32_16x16x32_bf16 a[20:23], a[212:215], v[24:27], a[20:23]\n"
    "v_exp_f32 v77, v217\n"
    "v_mfma_f32_16x16x32_bf16 a[52:55], a[212:215], v[28:31], a[52:55]\n"
    "v_cvt_pk_bf16_f32 v17, v74, v75\n"
    "s_sub_u32 s78, s78, 1\n"
    "v_mfma_f32_16x16x32_bf16 a[84:87], a[212:215], v[32:35], a[84:87]\n"
    "v_exp_f32 v78, v218\n"
    "v_mfma_f32_16x16x32_bf16 a[116:119], a[212:215], v[36:39], a[116:119]\n"
    "v_exp_f32 v79, v219\n"
    "v_mfma_f32_16x16x32_bf16 v[92:95], v[80:83], v[32:35], v[92:95]\n"
    "ds_read_b128 a[156:159], v109 offset:15360\n"
    "v_cvt_pk_bf16_f32 v20, v76, v77\n"
    "v_mfma_f32_16x16x32_bf16 a[24:27], a[216:219], v[24:27], a[24:27]\n"
    "v_or3_b32 v108, v108, v16, v17\n"
    "s_cmp_lg_u32 s78, 0\n"
    "v_mfma_f32_16x16x32_bf16 a[56:59], a[216:219], v[28:31], a[56:59]\n"
    "v_exp_f32 v72, v196\n"
    "v_mfma_f32_16x16x32_bf16 a[88:91], a[216:219], v[32:35], a[88:91]\n"
    "v_exp_f32 v73, v197\n"
    "v_mfma_f32_16x16x32_bf16 a[120:123], a[216:219], v[36:39], a[120:123]\n"
    "v_cvt_pk_bf16_f32 v21, v78, v79\n"
    "v_mfma_f32_16x16x32_bf16 a[28:31], a[220:223], v[24:27], a[28:31]\n"
    "v_exp_f32 v74, v198\n"
    "v_mfma_f32_16x16x32_bf16 a[60:63], a[220:223], v[28:31], a[60:63]\n"
    "v_exp_f32 v75, v199\n"
    "v_mfma_f32_16x16x32_bf16 a[92:95], a[220:223], v[32:35], a[92:95]\n"
    "v_cvt_pk_bf16_f32 v10, v72, v73\n"
    "v_or3_b32 v108, v108, v20, v21\n"
    "v_mfma_f32_16x16x32_bf16 a[124:127], a[220:223], v[36:39], a[124:127]\n"
    "v_exp_f32 v76, v204\n"
    "v_mfma_f32_16x16x32_bf16 v[96:99], v[80:83], v[36:39], v[96:99]\n"
    "s_cbranch_scc1 LloopF\n"
    "LloopFe:\n"
    "s_waitcnt lgkmcnt(0)\n"
    "v_mfma_f32_16x16x32_bf16 v[224:227], a[128:131], v[128:131], v[40:43]\n"
    "s_waitcnt vmcnt(16)\n"
    "s_barrier\n"
    "v_mfma_f32_16x16x32_bf16 v[232:235], a[128:131], v[144:147], v[48:51]\n"
    "s_sub_u32 s82, s82, 1\n"
    "s_cbranch_scc1 Levs3\n"
    "Levr3:\n"
    "v_mfma_f32_16x16x32_bf16 v[240:243], a[128:131], v[160:163], v[56:59]\n"
    "v_exp_f32 v77, v205\n"
    "v_mfma_f32_16x16x32_bf16 v[248:251], a[128:131], v[176:179], v[64:67]\n"
    "ds_read_b128 a[192:195], v110\n"
    "v_cvt_pk_bf16_f32 v11, v74, v75\n"
    "v_mfma_f32_16x16x32_bf16 v[228:231], a[144:147], v[128:131], v[44:47]\n"
    "v_exp_f32 v78, v206\n"
    "v_mfma_f32_16x16x32_bf16 v[236:239], a[144:147], v[144:147], v[52:55]\n"
    "ds_read_b128 a[196:199], v110 offset:1024\n"
    "buffer_load_dwordx4 v2, s[24:27], s75 offen sc1 lds\n"
    "v_mfma_f32_16x16x32_bf16 v[244:247], a[144:147], v[160:163], v[60:63]\n"
    "v_exp_f32 v79, v207\n"
    "v_mfma_f32_16x16x32_bf16 v[252:255], a[144:147], v[176:179], v[68:71]\n"
    "ds_read_b128 a[200:203], v110 offset:2048\n"
    "v_cvt_pk_bf16_f32 v14, v76, v77\n"
    "v_mfma_f32_16x16x32_bf16 v[224:227], a[132:135], v[132:135], v[224:227]\n"
    "buffer_load_dwordx4 v2, s[24:27], s75 offen offset:1024 sc1 lds\n"
    "v_or3_b32 v108, v108, v10, v11\n"
    "v_mfma_f32_16x16x32_bf16 v[232:235], a[132:135], v[148:151], v[232:235]\n"
    "v_exp_f32 v72, v212\n"
    "v_mfma_f32_16x16x32_bf16 v[240:243], a[132:135], v[164:167], v[240:243]\n"
    "buffer_load_dwordx4 v2, s[24:27], s75 offen offset:2048 sc1 lds\n"
    "ds_read_b128 a[204:207], v110 offset:3072\n"
    "v_mfma_f32_16x16x32_bf16 v[248:251], a[132:135], v[180:183], v[248:251]\n"
    "v_exp_f32 v73, v213\n"
    "v_mfma_f32_16x16x32_bf16 v[228:231], a[148:151], v[132:135], v[228:231]\n"
    "buffer_load_dwordx4 v2, s[24:27], s75 offen offset:3072 sc1 lds\n"
    "v_cvt_pk_bf16_f32 v15, v78, v79\n"
    "v_mfma_f32_16x16x32_bf16 v[236:239], a[148:151], v[148:151], v[236:239]\n"
    "v_exp_f32 v74, v214\n"
    "v_mfma_f32_16x16x32_bf16 v[244:247], a[148:151], v[164:167], v[244:247]\n"
    "ds_read_b128 a[208:211], v110 offset:4096\n"
    "s_mov_b32 m0, s76\n"
    "v_mfma_f32_16x16x32_bf16 v[252:255], a[148:151], v[180:183], v[252:255]\n"
    "v_exp_f32 v75, v215\n"
    "v_mfma_f32_16x16x32_bf16 v[224:227], a[136:139], v[136:139], v[224:227]\n"
    "buffer_load_dwordx4 v114, s[24:27], s75 offen sc1 lds\n"
    "v_cvt_pk_bf16_f32 v18, v72, v73\n"
    "v_mfma_f32_16x16x32_bf16 v[232:235], a[136:139], v[152:155], v[232:235]\n"
    "ds_read_b128 a[212:215], v110 offset:5120\n"
    "v_or3_b32 v108, v108, v14, v15\n"
    "v_mfma_f32_16x16x32_bf16 v[240:243], a[136:139], v[168:171], v[240:243]\n"
    "v_exp_f32 v76, v220\n"
    "v_mfma_f32_16x16x32_bf16 v[248:251], a[136:139], v[184:187], v[248:251]\n"
    "buffer_load_dwordx4 v114, s[24:27], s75 offen offset:1024 sc1 lds\n"
    "ds_read_b128 a[216:219], v110 offset:6144\n"
    "v_mfma_f32_16x16x32_bf16 v[228:231], a[152:155], v[136:139], v[228:231]\n"
    "v_exp_f32 v77, v221\n"
    "v_mfma_f32_16x16x32_bf16 v[236:239], a[152:155], v[152:155], v[236:239]\n"
    "buffer_load_dwordx4 v114, s[24:27], s75 offen offset:2048 sc1 lds\n"
    "v_cvt_pk_bf16_f32 v19, v74, v75\n"
    "v_mfma_f32_16x16x32_bf16 v[244:247], a[152:155], v[168:171], v[244:247]\n"
    "v_exp_f32 v78, v222\n"
    "v_mfma_f32_16x16x32_bf16 v[252:255], a[152:155], v[184:187], v[252:255]\n"
    "buffer_load_dwordx4 v114, s[24:27], s75 offen offset:3072 sc1 lds\n"
    "ds_read_b128 a[220:223], v110 offset:7168\n"
    "v_mfma_f32_16x16x32_bf16 v[224:227], a[140:143], v[140:143], v[224:227]\n"
    "v_exp_f32 v79, v223\n"
    "v_mfma_f32_16x16x32_bf16 v[232:235], a[140:143], v[156:159], v[232:235]\n"
    "s_add_u32 s76, s76, 0x8000\n"
    "v_cvt_pk_bf16_f32 v22, v76, v77\n"
    "v_mfma_f32_16x16x32_bf16 v[240:243], a[140:143], v[172:175], v[240:243]\n"
    "s_and_b32 s76, s76, 0x1ffff\n"
    "v_or3_b32 v108, v108, v18, v19\n"
    "v_mfma_f32_16x16x32_bf16 v[248:251], a[140:143], v[188:191], v[248:251]\n"
    "v_cvt_pk_bf16_f32 v23, v78, v79\n"
    "v_or3_b32 v108, v108, v22, v23\n"
    "v_mfma_f32_16x16x32_bf16 v[228:231], a[156:159], v[140:143], v[228:231]\n"
    "v_add_u32 v109, s76, v115\n"
    "v_and_b32 v3, 0x40004000, v108\n"
    "v_mfma_f32_16x16x32_bf16 v[236:239], a[156:159], v[156:159], v[236:239]\n"
    "v_cmp_ne_u32 vcc, 0, v3\n"
    "v_mfma_f32_16x16x32_bf16 v[244:247], a[156:159], v[172:175], v[244:247]\n"
    "v_mfma_f32_16x16x32_bf16 v[252:255], a[156:159], v[188:191], v[252:255]\n"
    "s_cbranch_vccnz Lrs3\n"
    "Lrr3:\n"
    "s_waitcnt lgkmcnt(0)\n"
    "v_mfma_f32_16x16x32_bf16 a[0:3], a[192:195], v[8:11], a[0:3]\n"
    "ds_read_b128 a[128:131], v109\n"
    "s_add_u32 s75, s75, 0x8000\n"
    "v_mfma_f32_16x16x32_bf16 a[32:35], a[192:195], v[12:15], a[32:35]\n"
    "v_mov_b32 v112, v111\n"
    "v_cmp_gt_i32 s[84:85], 0, v112\n"
    "v_mfma_f32_16x16x32_bf16 a[64:67], a[192:195], v[16:19], a[64:67]\n"
    "v_exp_f32 v72, v224\n"
    "v_mfma_f32_16x16x32_bf16 a[96:99], a[192:195], v[20:23], a[96:99]\n"
    "v_exp_f32 v73, v225\n"
    "v_mfma_f32_16x16x32_bf16 a[4:7], a[196:199], v[8:11], a[4:7]\n"
    "v_exp_f32 v74, v226\n"
    "v_mfma_f32_16x16x32_bf16 a[36:39], a[196:199], v[12:15], a[36:39]\n"
    "ds_read_b128 a[144:147], v109 offset:4096\n"
    "v_cmp_gt_i32 s[86:87], 1, v112\n"
    "v_mfma_f32_16x16x32_bf16 a[68:71], a[196:199], v[16:19], a[68:71]\n"
    "v_exp_f32 v75, v227\n"
    "v_mfma_f32_16x16x32_bf16 a[100:103], a[196:199], v[20:23], a[100:103]\n"
    "ds_read_b128 a[132:135], v109 offset:1024\n"
    "v_cvt_pk_bf16_f32 v24, v72, v73\n"
    "v_mfma_f32_16x16x32_bf16 v[84:87], v[80:83], v[8:11], v[84:87]\n"
    "v_exp_f32 v76, v232\n"
    "v_mfma_f32_16x16x32_bf16 a[8:11], a[200:203], v[8:11], a[8:11]\n"
    "v_exp_f32 v77, v233\n"
    "v_mfma_f32_16x16x32_bf16 a[40:43], a[200:203], v[12:15], a[40:43]\n"
    "v_cmp_gt_i32 s[92:93], 2, v112\n"
    "v_cvt_pk_bf16_f32 v25, v74, v75\n"
    "v_mfma_f32_16x16x32_bf16 a[72:75], a[200:203], v[16:19], a[72:75]\n"
    "ds_read_b128 a[148:151], v109 offset:5120\n"
    "v_cndmask_b32 v40, v40, v113, s[84:85]\n"
    "v_mfma_f32_16x16x32_bf16 a[104:107], a[200:203], v[20:23], a[104:107]\n"
    "v_exp_f32 v78, v234\n"
    "v_mfma_f32_16x16x32_bf16 a[12:15], a[204:207], v[8:11], a[12:15]\n"
    "v_exp_f32 v79, v235\n"
    "v_mfma_f32_16x16x32_bf16 a[44:47], a[204:207], v[12:15], a[44:47]\n"
    "v_cmp_gt_i32 s[94:95], 3, v112\n"
    "v_cvt_pk_bf16_f32 v28, v76, v77\n"
    "v_mfma_f32_16x16x32_bf16 a[76:79], a[204:207], v[16:19], a[76:79]\n"
    "v_or_b32 v108, v24, v25\n"
    "v_cndmask_b32 v41, v41, v113, s[86:87]\n"
    "v_mfma_f32_16x16x32_bf16 a[108:111], a[204:207], v[20:23], a[108:111]\n"
    "v_exp_f32 v72, v240\n"
    "v_mfma_f32_16x16x32_bf16 v[88:91], v[80:83], v[12:15], v[88:91]\n"
    "ds_read_b128 a[136:139], v109 offset:2048\n"
    "v_cmp_gt_i32 s[84:85], 16, v112\n"
    "v_mfma_f32_16x16x32_bf16 a[16:19], a[208:211], v[8:11], a[16:19]\n"
    "v_exp_f32 v73, v241\n"
    "v_mfma_f32_16x16x32_bf16 a[48:51], a[208:211], v[12:15], a[48:51]\n"
    "ds_read_b128 a[152:155], v109 offset:6144\n"
    "v_cvt_pk_bf16_f32 v29, v78, v79\n"
    "v_mfma_f32_16x16x32_bf16 a[80:83], a[208:211], v[16:19], a[80:83]\n"
    "v_exp_f32 v74, v242\n"
    "v_mfma_f32_16x16x32_bf16 a[112:115], a[208:211], v[20:23], a[112:115]\n"
    "v_exp_f32 v75, v243\n"
    "v_mfma_f32_16x16x32_bf16 a[20:23], a[212:215], v[8:11], a[20:23]\n"
    "v_cndmask_b32 v42, v42, v113, s[92:93]\n"
    "v_cvt_pk_bf16_f32 v32, v72, v73\n"
    "v_mfma_f32_16x16x32_bf16 a[52:55], a[212:215], v[12:15], a[52:55]\n"
    "v_cmp_gt_i32 s[86:87], 17, v112\n"
    "v_or3_b32 v108, v108, v28, v29\n"
    "v_mfma_f32_16x16x32_bf16 a[84:87], a[212:215], v[16:19], a[84:87]\n"
    "v_exp_f32 v76, v248\n"
    "v_mfma_f32_16x16x32_bf16 a[116:119], a[212:215], v[20:23], a[116:119]\n"
    "ds_read_b128 a[140:143], v109 offset:3072\n"
    "v_cndmask_b32 v43, v43, v113, s[94:95]\n"
    "v_mfma_f32_16x16x32_bf16 v[92:95], v[80:83], v[16:19], v[92:95]\n"
    "v_exp_f32 v77, v249\n"
    "v_mfma_f32_16x16x32_bf16 a[24:27], a[216:219], v[8:11], a[24:27]\n"
    "v_cvt_pk_bf16_f32 v33, v74, v75\n"
    "v_cmp_gt_i32 s[92:93], 18, v112\n"
    "v_mfma_f32_16x16x32_bf16 a[56:59], a[216:219], v[12:15], a[56:59]\n"
    "v_exp_f32 v78, v250\n"
    "ds_read_b128 a[156:159], v109 offset:7168\n"
    "v_mfma_f32_16x16x32_bf16 a[88:91], a[216:219], v[16:19], a[88:91]\n"
    "v_exp_f32 v79, v251\n"
    "v_mfma_f32_16x16x32_bf16 a[120:123], a[216:219], v[20:23], a[120:123]\n"
    "v_cndmask_b32 v44, v44, v113, s[84:85]\n"
    "v_cvt_pk_bf16_f32 v36, v76, v77\n"
    "v_mfma_f32_16x16x32_bf16 a[28:31], a[220:223], v[8:11], a[28:31]\n"
    "v_or3_b32 v108, v108, v32, v33\n"
    "v_cmp_gt_i32 s[94:95], 19, v112\n"
    "v_mfma_f32_16x16x32_bf16 a[60:63], a[220:223], v[12:15], a[60:63]\n"
    "v_exp_f32 v72, v228\n"
    "v_mfma_f32_16x16x32_bf16 a[92:95], a[220:223], v[16:19], a[92:95]\n"
    "v_exp_f32 v73, v229\n"
    "v_mfma_f32_16x16x32_bf16 a[124:127], a[220:223], v[20:23], a[124:127]\n"
    "v_cndmask_b32 v45, v45, v113, s[86:87]\n"
    "v_cvt_pk_bf16_f32 v37, v78, v79\n"
    "v_mfma_f32_16x16x32_bf16 v[96:99], v[80:83], v[20:23], v[96:99]\n"
    "v_exp_f32 v74, v230\n"
    "v_exp_f32 v75, v231\n"
    "v_cvt_pk_bf16_f32 v26, v72, v73\n"
    "v_or3_b32 v108, v108, v36, v37\n"
    "v_exp_f32 v76, v236\n"
    "v_cndmask_b32 v46, v46, v113, s[92:93]\n"
    "v_cndmask_b32 v47, v47, v113, s[94:95]\n"
    "s_waitcnt lgkmcnt(0)\n"
    "v_mfma_f32_16x16x32_bf16 v[192:195], a[128:131], v[128:131], v[40:43]\n"
    "v_exp_f32 v77, v237\n"
    "v_mfma_f32_16x16x32_bf16 v[200:203], a[128:131], v[144:147], v[48:51]\n"
    "ds_read_b128 a[192:195], v110 offset:8192\n"
    "v_cvt_pk_bf16_f32 v27, v74, v75\n"
    "v_mfma_f32_16x16x32_bf16 v[208:211], a[128:131], v[160:163], v[56:59]\n"
    "v_exp_f32 v78, v238\n"
    "v_mfma_f32_16x16x32_bf16 v[216:219], a[128:131], v[176:179], v[64:67]\n"
    "v_exp_f32 v79, v239\n"
    "v_mfma_f32_16x16x32_bf16 v[196:199], a[144:147], v[128:131], v[44:47]\n"
    "ds_read_b128 a[196:199], v110 offset:9216\n"
    "v_cvt_pk_bf16_f32 v30, v76, v77\n"
    "v_mfma_f32_16x16x32_bf16 v[204:207], a[144:147], v[144:147], v[52:55]\n"
    "ds_read_b128 a[200:203], v110 offset:10240\n"
    "v_or3_b32 v108, v108, v26, v27\n"
    "v_mfma_f32_16x16x32_bf16 v[212:215], a[144:147], v[160:163], v[60:63]\n"
    "v_exp_f32 v72, v244\n"
    "v_mfma_f32_16x16x32_bf16 v[220:223], a[144:147], v[176:179], v[68:71]\n"
    "v_exp_f32 v73, v245\n"
    "v_mfma_f32_16x16x32_bf16 v[192:195], a[132:135], v[132:135], v[192:195]\n"
    "ds_read_b128 a[204:207], v110 offset:11264\n"
    "v_cvt_pk_bf16_f32 v31, v78, v79\n"
    "v_mfma_f32_16x16x32_bf16 v[200:203], a[132:135], v[148:151], v[200:203]\n"
    "v_exp_f32 v74, v246\n"
    "v_mfma_f32_16x16x32_bf16 v[208:211], a[132:135], v[164:167], v[208:211]\n"
    "v_exp_f32 v75, v247\n"
    "v_mfma_f32_16x16x32_bf16 v[216:219], a[132:135], v[180:183], v[216:219]\n"
    "ds_read_b128 a[208:211], v110 offset:12288\n"
    "v_cvt_pk_bf16_f32 v34, v72, v73\n"
    "v_mfma_f32_16x16x32_bf16 v[196:199], a[148:151], v[132:135], v[196:199]\n"
    "ds_read_b128 a[212:215], v110 offset:13312\n"
    "v_or3_b32 v108, v108, v30, v31\n"
    "v_mfma_f32_16x16x32_bf16 v[204:207], a[148:151], v[148:151], v[204:207]\n"
    "v_exp_f32 v76, v252\n"
    "v_mfma_f32_16x16x32_bf16 v[212:215], a[148:151], v[164:167], v[212:215]\n"
    "v_exp_f32 v77, v253\n"
    "v_mfma_f32_16x16x32_bf16 v[220:223], a[148:151], v[180:183], v[220:223]\n"
    "ds_read_b128 a[216:219], v110 offset:14336\n"
    "v_cvt_pk_bf16_f32 v35, v74, v75\n"
    "v_mfma_f32_16x16x32_bf16 v[192:195], a[136:139], v[136:139], v[192:195]\n"
    "v_exp_f32 v78, v254\n"
    "v_mfma_f32_16x16x32_bf16 v[200:203], a[136:139], v[152:155], v[200:203]\n"
    "v_exp_f32 v79, v255\n"
    "v_mfma_f32_16x16x32_bf16 v[208:211], a[136:139], v[168:171], v[208:211]\n"
    "ds_read_b128 a[220:223], v110 offset:15360\n"
    "v_cvt_pk_bf16_f32 v38, v76, v77\n"
    "v_mfma_f32_16x16x32_bf16 v[216:219], a[136:139], v[184:187], v[216:219]\n"
    "v_or3_b32 v108, v108, v34, v35\n"
    "v_cvt_pk_bf16_f32 v39, v78, v79\n"
    "v_mfma_f32_16x16x32_bf16 v[196:199], a[152:155], v[136:139], v[196:199]\n"
    "v_or3_b32 v108, v108, v38, v39\n"
    "v_and_b32 v3, 0x40004000, v108\n"
    "v_mfma_f32_16x16x32_bf16 v[204:207], a[152:155], v[152:155], v[204:207]\n"
    "v_cmp_ne_u32 vcc, 0, v3\n"
    "v_mfma_f32_16x16x32_bf16 v[212:215], a[152:155], v[168:171], v[212:215]\n"
    "v_mfma_f32_16x16x32_bf16 v[220:223], a[152:155], v[184:187], v[220:223]\n"
    "v_mfma_f32_16x16x32_bf16 v[192:195], a[140:143], v[140:143], v[192:195]\n"
    "v_mfma_f32_16x16x32_bf16 v[200:203], a[140:143], v[156:159], v[200:203]\n"
    "v_mfma_f32_16x16x32_bf16 v[208:211], a[140:143], v[172:175], v[208:211]\n"
    "v_mfma_f32_16x16x32_bf16 v[216:219], a[140:143], v[188:191], v[216:219]\n"
    "v_mfma_f32_16x16x32_bf16 v[196:199], a[156:159], v[140:143], v[196:199]\n"
    "v_mfma_f32_16x16x32_bf16 v[204:207], a[156:159], v[156:159], v[204:207]\n"
    "v_mfma_f32_16x16x32_bf16 v[212:215], a[156:159], v[172:175], v[212:215]\n"
    "v_mfma_f32_16x16x32_bf16 v[220:223], a[156:159], v[188:191], v[220:223]\n"
    "s_cbranch_vccnz Lrs4\n"
    "Lrr4:\n"
    "s_waitcnt lgkmcnt(0)\n"
    "v_mfma_f32_16x16x32_bf16 a[0:3], a[192:195], v[24:27], a[0:3]\n"
    "ds_read_b128 a[128:131], v109 offset:8192\n"
    "v_subrev_u32 v112, 32, v111\n"
    "v_mfma_f32_16x16x32_bf16 a[32:35], a[192:195], v[28:31], a[32:35]\n"
    "v_cmp_gt_i32 s[84:85], 0, v112\n"
    "v_cmp_gt_i32 s[86:87], 1, v112\n"
    "v_mfma_f32_16x16x32_bf16 a[64:67], a[192:195], v[32:35], a[64:67]\n"
    "v_exp_f32 v72, v192\n"
    "v_mfma_f32_16x16x32_bf16 a[96:99], a[192:195], v[36:39], a[96:99]\n"
    "v_exp_f32 v73, v193\n"
    "v_mfma_f32_16x16x32_bf16 a[4:7], a[196:199], v[24:27], a[4:7]\n"
    "ds_read_b128 a[144:147], v109 offset:12288\n"
    "v_cmp_gt_i32 s[92:93], 2, v112\n"
    "v_mfma_f32_16x16x32_bf16 a[36:39], a[196:199], v[28:31], a[36:39]\n"
    "v_exp_f32 v74, v194\n"
    "v_mfma_f32_16x16x32_bf16 a[68:71], a[196:199], v[32:35], a[68:71]\n"
    "v_exp_f32 v75, v195\n"
    "v_mfma_f32_16x16x32_bf16 a[100:103], a[196:199], v[36:39], a[100:103]\n"
    "v_cndmask_b32 v40, v40, v113, s[84:85]\n"
    "v_cvt_pk_bf16_f32 v8, v72, v73\n"
    "v_mfma_f32_16x16x32_bf16 v[84:87], v[80:83], v[24:27], v[84:87]\n"
    "v_exp_f32 v76, v200\n"
    "v_mfma_f32_16x16x32_bf16 a[8:11], a[200:203], v[24:27], a[8:11]\n"
    "ds_read_b128 a[132:135], v109 offset:9216\n"
    "v_cmp_gt_i32 s[94:95], 3, v112\n"
    "v_mfma_f32_16x16x32_bf16 a[40:43], a[200:203], v[28:31], a[40:43]\n"
    "v_exp_f32 v77, v201\n"
    "v_mfma_f32_16x16x32_bf16 a[72:75], a[200:203], v[32:35], a[72:75]\n"
    "v_cvt_pk_bf16_f32 v9, v74, v75\n"
    "v_cndmask_b32 v41, v41, v113, s[86:87]\n"
    "v_mfma_f32_16x16x32_bf16 a[104:107], a[200:203], v[36:39], a[104:107]\n"
    "v_exp_f32 v78, v202\n"
    "v_mfma_f32_16x16x32_bf16 a[12:15], a[204:207], v[24:27], a[12:15]\n"
    "ds_read_b128 a[148:151], v109 offset:13312\n"
    "v_cmp_gt_i32 s[84:85], 16, v112\n"
    "v_mfma_f32_16x16x32_bf16 a[44:47], a[204:207], v[28:31], a[44:47]\n"
    "v_exp_f32 v79, v203\n"
    "v_mfma_f32_16x16x32_bf16 a[76:79], a[204:207], v[32:35], a[76:79]\n"
    "v_cvt_pk_bf16_f32 v12, v76, v77\n"
    "v_or_b32 v108, v8, v9\n"
    "v_mfma_f32_16x16x32_bf16 a[108:111], a[204:207], v[36:39], a[108:111]\n"
    "ds_read_b128 a[136:139], v109 offset:10240\n"
    "v_cndmask_b32 v42, v42, v113, s[92:93]\n"
    "v_mfma_f32_16x16x32_bf16 v[88:91], v[80:83], v[28:31], v[88:91]\n"
    "v_exp_f32 v72, v208\n"
    "v_mfma_f32_16x16x32_bf16 a[16:19], a[208:211], v[24:27], a[16:19]\n"
    "v_exp_f32 v73, v209\n"
    "v_mfma_f32_16x16x32_bf16 a[48:51], a[208:211], v[28:31], a[48:51]\n"
    "v_cmp_gt_i32 s[86:87], 17, v112\n"
    "v_cvt_pk_bf16_f32 v13, v78, v79\n"
    "v_mfma_f32_16x16x32_bf16 a[80:83], a[208:211], v[32:35], a[80:83]\n"
    "v_exp_f32 v74, v210\n"
    "v_mfma_f32_16x16x32_bf16 a[112:115], a[208:211], v[36:39], a[112:115]\n"
    "ds_read_b128 a[152:155], v109 offset:14336\n"
    "v_cndmask_b32 v43, v43, v113, s[94:95]\n"
    "v_mfma_f32_16x16x32_bf16 a[20:23], a[212:215], v[24:27], a[20:23]\n"
    "v_exp_f32 v75, v211\n"
    "v_mfma_f32_16x16x32_bf16 a[52:55], a[212:215], v[28:31], a[52:55]\n"
    "v_cmp_gt_i32 s[92:93], 18, v112\n"
    "v_cvt_pk_bf16_f32 v16, v72, v73\n"
    "v_mfma_f32_16x16x32_bf16 a[84:87], a[212:215], v[32:35], a[84:87]\n"
    "v_or3_b32 v108, v108, v12, v13\n"
    "v_cndmask_b32 v44, v44, v113, s[84:85]\n"
    "v_mfma_f32_16x16x32_bf16 a[116:119], a[212:215], v[36:39], a[116:119]\n"
    "v_exp_f32 v76, v216\n"
    "v_mfma_f32_16x16x32_bf16 v[92:95], v[80:83], v[32:35], v[92:95]\n"
    "ds_read_b128 a[140:143], v109 offset:11264\n"
    "v_cmp_gt_i32 s[94:95], 19, v112\n"
    "v_mfma_f32_16x16x32_bf16 a[24:27], a[216:219], v[24:27], a[24:27]\n"
    "v_exp_f32 v77, v217\n"
    "v_mfma_f32_16x16x32_bf16 a[56:59], a[216:219], v[28:31], a[56:59]\n"
    "v_cvt_pk_bf16_f32 v17, v74, v75\n"
    "v_cndmask_b32 v45, v45, v113, s[86:87]\n"
    "ds_read_b128 a[156:159], v109 offset:15360\n"
    "v_mfma_f32_16x16x32_bf16 a[88:91], a[216:219], v[32:35], a[88:91]\n"
    "v_exp_f32 v78, v218\n"
    "v_mfma_f32_16x16x32_bf16 a[120:123], a[216:219], v[36:39], a[120:123]\n"
    "v_exp_f32 v79, v219\n"
    "v_mfma_f32_16x16x32_bf16 a[28:31], a[220:223], v[24:27], a[28:31]\n"
    "v_cndmask_b32 v46, v46, v113, s[92:93]\n"
    "v_cvt_pk_bf16_f32 v20, v76, v77\n"
    "v_mfma_f32_16x16x32_bf16 a[60:63], a[220:223], v[28:31], a[60:63]\n"
    "v_or3_b32 v108, v108, v16, v17\n"
    "v_cndmask_b32 v47, v47, v113, s[94:95]\n"
    "v_mfma_f32_16x16x32_bf16 a[92:95], a[220:223], v[32:35], a[92:95]\n"
    "v_exp_f32 v72, v196\n"
    "v_mfma_f32_16x16x32_bf16 a[124:127], a[220:223], v[36:39], a[124:127]\n"
    "v_exp_f32 v73, v197\n"
    "v_mfma_f32_16x16x32_bf16 v[96:99], v[80:83], v[36:39], v[96:99]\n"
    "s_mov_b32 m0, s77\n"
    "v_cvt_pk_bf16_f32 v21, v78, v79\n"
    "v_exp_f32 v74, v198\n"
    "v_exp_f32 v75, v199\n"
    "v_cvt_pk_bf16_f32 v10, v72, v73\n"
    "v_or3_b32 v108, v108, v20, v21\n"
    "v_exp_f32 v76, v204\n"
    "s_add_u32 s77, s77, 0x8000\n"
    "s_and_b32 s77, s77, 0x1ffff\n"
    "v_add_u32 v110, s77, v115\n"
    "LP1:\n"
    "s_waitcnt lgkmcnt(0)\n"
    "v_mfma_f32_16x16x32_bf16 v[224:227], a[128:131], v[128:131], v[40:43]\n"
    "s_waitcnt vmcnt(16)\n"
    "s_barrier\n"
    "v_mfma_f32_16x16x32_bf16 v[232:235], a[128:131], v[144:147], v[48:51]\n"
    "s_sub_u32 s82, s82, 1\n"
    "s_cbranch_scc1 Levs5\n"
    "Levr5:\n"
    "v_mfma_f32_16x16x32_bf16 v[240:243], a[128:131], v[160:163], v[56:59]\n"
    "v_exp_f32 v77, v205\n"
    "v_mfma_f32_16x16x32_bf16 v[248:251], a[128:131], v[176:179], v[64:67]\n"
    "ds_read_b128 a[192:195], v110\n"
    "v_cvt_pk_bf16_f32 v11, v74, v75\n"
    "v_mfma_f32_16x16x32_bf16 v[228:231], a[144:147], v[128:131], v[44:47]\n"
    "v_exp_f32 v78, v206\n"
    "v_mfma_f32_16x16x32_bf16 v[236:239], a[144:147], v[144:147], v[52:55]\n"
    "ds_read_b128 a[196:199], v110 offset:1024\n"
    "buffer_load_dwordx4 v2, s[24:27], s75 offen sc1 lds\n"
    "v_mfma_f32_16x16x32_bf16 v[244:247], a[144:147], v[160:163], v[60:63]\n"
    "v_exp_f32 v79, v207\n"
    "v_mfma_f32_16x16x32_bf16 v[252:255], a[144:147], v[176:179], v[68:71]\n"
    "ds_read_b128 a[200:203], v110 offset:2048\n"
    "v_cvt_pk_bf16_f32 v14, v76, v77\n"
    "v_mfma_f32_16x16x32_bf16 v[224:227], a[132:135], v[132:135], v[224:227]\n"
    "buffer_load_dwordx4 v2, s[24:27], s75 offen offset:1024 sc1 lds\n"
    "v_or3_b32 v108, v108, v10, v11\n"
    "v_mfma_f32_16x16x32_bf16 v[232:235], a[132:135], v[148:151], v[232:235]\n"
    "v_exp_f32 v72, v212\n"
    "v_mfma_f32_16x16x32_bf16 v[240:243], a[132:135], v[164:167], v[240:243]\n"
    "buffer_load_dwordx4 v2, s[24:27], s75 offen offset:2048 sc1 lds\n"
    "ds_read_b128 a[204:207], v110 offset:3072\n"
    "v_mfma_f32_16x16x32_bf16 v[248:251], a[132:135], v[180:183], v[248:251]\n"
    "v_exp_f32 v73, v213\n"
    "v_mfma_f32_16x16x32_bf16 v[228:231], a[148:151], v[132:135], v[228:231]\n"
    "buffer_load_dwordx4 v2, s[24:27], s75 offen offset:3072 sc1 lds\n"
    "v_cvt_pk_bf16_f32 v15, v78, v79\n"
    "v_mfma_f32_16x16x32_bf16 v[236:239], a[148:151], v[148:151], v[236:239]\n"
    "v_exp_f32 v74, v214\n"
    "v_mfma_f32_16x16x32_bf16 v[244:247], a[148:151], v[164:167], v[244:247]\n"
    "ds_read_b128 a[208:211], v110 offset:4096\n"
    "s_mov_b32 m0, s76\n"
    "v_mfma_f32_16x16x32_bf16 v[252:255], a[148:151], v[180:183], v[252:255]\n"
    "v_exp_f32 v75, v215\n"
    "v_mfma_f32_16x16x32_bf16 v[224:227], a[136:139], v[136:139], v[224:227]\n"
    "buffer_load_dwordx4 v114, s[24:27], s75 offen sc1 lds\n"
    "v_cvt_pk_bf16_f32 v18, v72, v73\n"
    "v_mfma_f32_16x16x32_bf16 v[232:235], a[136:139], v[152:155], v[232:235]\n"
    "ds_read_b128 a[212:215], v110 offset:5120\n"
    "v_or3_b32 v108, v108, v14, v15\n"
    "v_mfma_f32_16x16x32_bf16 v[240:243], a[136:139], v[168:171], v[240:243]\n"
    "v_exp_f32 v76, v220\n"
    "v_mfma_f32_16x16x32_bf16 v[248:251], a[136:139], v[184:187], v[248:251]\n"
    "buffer_load_dwordx4 v114, s[24:27], s75 offen offset:1024 sc1 lds\n"
    "ds_read_b128 a[216:219], v110 offset:6144\n"
    "v_mfma_f32_16x16x32_bf16 v[228:231], a[152:155], v[136:139], v[228:231]\n"
    "v_exp_f32 v77, v221\n"
    "v_mfma_f32_16x16x32_bf16 v[236:239], a[152:155], v[152:155], v[236:239]\n"
    "buffer_load_dwordx4 v114, s[24:27], s75 offen offset:2048 sc1 lds\n"
    "v_cvt_pk_bf16_f32 v19, v74, v75\n"
    "v_mfma_f32_16x16x32_bf16 v[244:247], a[152:155], v[168:171], v[244:247]\n"
    "v_exp_f32 v78, v222\n"
    "v_mfma_f32_16x16x32_bf16 v[252:255], a[152:155], v[184:187], v[252:255]\n"
    "buffer_load_dwordx4 v114, s[24:27], s75 offen offset:3072 sc1 lds\n"
    "ds_read_b128 a[220:223], v110 offset:7168\n"
    "v_mfma_f32_16x16x32_bf16 v[224:227], a[140:143], v[140:143], v[224:227]\n"
    "v_exp_f32 v79, v223\n"
    "v_mfma_f32_16x16x32_bf16 v[232:235], a[140:143], v[156:159], v[232:235]\n"
    "s_add_u32 s76, s76, 0x8000\n"
    "v_cvt_pk_bf16_f32 v22, v76, v77\n"
    "v_mfma_f32_16x16x32_bf16 v[240:243], a[140:143], v[172:175], v[240:243]\n"
    "s_and_b32 s76, s76, 0x1ffff\n"
    "v_or3_b32 v108, v108, v18, v19\n"
    "v_mfma_f32_16x16x32_bf16 v[248:251], a[140:143], v[188:191], v[248:251]\n"
    "v_cvt_pk_bf16_f32 v23, v78, v79\n"
    "v_or3_b32 v108, v108, v22, v23\n"
    "v_mfma_f32_16x16x32_bf16 v[228:231], a[156:159], v[140:143], v[228:231]\n"
    "v_add_u32 v109, s76, v115\n"
    "v_and_b32 v3, 0x40004000, v108\n"
    "v_mfma_f32_16x16x32_bf16 v[236:239], a[156:159], v[156:159], v[236:239]\n"
    "v_cmp_ne_u32 vcc, 0, v3\n"
    "v_mfma_f32_16x16x32_bf16 v[244:247], a[156:159], v[172:175], v[244:247]\n"
    "v_mfma_f32_16x16x32_bf16 v[252:255], a[156:159], v[188:191], v[252:255]\n"
    "s_cbranch_vccnz Lrs5\n"
    "Lrr5:\n"
    "s_waitcnt lgkmcnt(0)\n"
    "v_mfma_f32_16x16x32_bf16 a[0:3], a[192:195], v[8:11], a[0:3]\n"
    "ds_read_b128 a[128:131], v109\n"
    "s_add_u32 s75, s75, 0x8000\n"
    "v_mfma_f32_16x16x32_bf16 a[32:35], a[192:195], v[12:15], a[32:35]\n"
    "s_lshr_b32 s6, s62, 10\n"
    "s_sub_u32 s6, s6, 64\n"
    "v_mfma_f32_16x16x32_bf16 a[64:67], a[192:195], v[16:19], a[64:67]\n"
    "v_exp_f32 v72, v224\n"
    "v_mfma_f32_16x16x32_bf16 a[96:99], a[192:195], v[20:23], a[96:99]\n"
    "v_exp_f32 v73, v225\n"
    "v_mfma_f32_16x16x32_bf16 a[4:7], a[196:199], v[8:11], a[4:7]\n"
    "ds_read_b128 a[144:147], v109 offset:4096\n"
    "v_add_u32 v112, s6, v111\n"
    "v_mfma_f32_16x16x32_bf16 a[36:39], a[196:199], v[12:15], a[36:39]\n"
    "v_exp_f32 v74, v226\n"
    "v_mfma_f32_16x16x32_bf16 a[68:71], a[196:199], v[16:19], a[68:71]\n"
    "v_exp_f32 v75, v227\n"
    "v_mfma_f32_16x16x32_bf16 a[100:103], a[196:199], v[20:23], a[100:103]\n"
    "ds_read_b128 a[132:135], v109 offset:1024\n"
    "v_cvt_pk_bf16_f32 v24, v72, v73\n"
    "v_mfma_f32_16x16x32_bf16 v[84:87], v[80:83], v[8:11], v[84:87]\n"
    "v_exp_f32 v76, v232\n"
    "v_mfma_f32_16x16x32_bf16 a[8:11], a[200:203], v[8:11], a[8:11]\n"
    "v_cmp_gt_i32 s[84:85], 0, v112\n"
    "v_cmp_gt_i32 s[86:87], 1, v112\n"
    "v_mfma_f32_16x16x32_bf16 a[40:43], a[200:203], v[12:15], a[40:43]\n"
    "v_exp_f32 v77, v233\n"
    "v_mfma_f32_16x16x32_bf16 a[72:75], a[200:203], v[16:19], a[72:75]\n"
    "ds_read_b128 a[148:151], v109 offset:5120\n"
    "v_cvt_pk_bf16_f32 v25, v74, v75\n"
    "v_mfma_f32_16x16x32_bf16 a[104:107], a[200:203], v[20:23], a[104:107]\n"
    "v_exp_f32 v78, v234\n"
    "v_mfma_f32_16x16x32_bf16 a[12:15], a[204:207], v[8:11], a[12:15]\n"
    "v_cmp_gt_i32 s[92:93], 2, v112\n"
    "v_cndmask_b32 v48, v48, v113, s[84:85]\n"
    "v_mfma_f32_16x16x32_bf16 a[44:47], a[204:207], v[12:15], a[44:47]\n"
    "v_exp_f32 v79, v235\n"
    "v_mfma_f32_16x16x32_bf16 a[76:79], a[204:207], v[16:19], a[76:79]\n"
    "v_cvt_pk_bf16_f32 v28, v76, v77\n"
    "v_or_b32 v108, v24, v25\n"
    "v_mfma_f32_16x16x32_bf16 a[108:111], a[204:207], v[20:23], a[108:111]\n"
    "ds_read_b128 a[136:139], v109 offset:2048\n"
    "v_cmp_gt_i32 s[94:95], 3, v112\n"
    "v_mfma_f32_16x16x32_bf16 v[88:91], v[80:83], v[12:15], v[88:91]\n"
    "v_exp_f32 v72, v240\n"
    "v_mfma_f32_16x16x32_bf16 a[16:19], a[208:211], v[8:11], a[16:19]\n"
    "v_exp_f32 v73, v241\n"
    "v_mfma_f32_16x16x32_bf16 a[48:51], a[208:211], v[12:15], a[48:51]\n"
    "v_cndmask_b32 v49, v49, v113, s[86:87]\n"
    "v_cvt_pk_bf16_f32 v29, v78, v79\n"
    "v_mfma_f32_16x16x32_bf16 a[80:83], a[208:211], v[16:19], a[80:83]\n"
    "v_exp_f32 v74, v242\n"
    "v_mfma_f32_16x16x32_bf16 a[112:115], a[208:211], v[20:23], a[112:115]\n"
    "ds_read_b128 a[152:155], v109 offset:6144\n"
    "v_cmp_gt_i32 s[84:85], 16, v112\n"
    "v_mfma_f32_16x16x32_bf16 a[20:23], a[212:215], v[8:11], a[20:23]\n"
    "v_exp_f32 v75, v243\n"
    "v_mfma_f32_16x16x32_bf16 a[52:55], a[212:215], v[12:15], a[52:55]\n"
    "v_cvt_pk_bf16_f32 v32, v72, v73\n"
    "v_cndmask_b32 v50, v50, v113, s[92:93]\n"
    "v_mfma_f32_16x16x32_bf16 a[84:87], a[212:215], v[16:19], a[84:87]\n"
    "ds_read_b128 a[140:143], v109 offset:3072\n"
    "v_or3_b32 v108, v108, v28, v29\n"
    "v_mfma_f32_16x16x32_bf16 a[116:119], a[212:215], v[20:23], a[116:119]\n"
    "v_exp_f32 v76, v248\n"
    "v_mfma_f32_16x16x32_bf16 v[92:95], v[80:83], v[16:19], v[92:95]\n"
    "v_exp_f32 v77, v249\n"
    "v_mfma_f32_16x16x32_bf16 a[24:27], a[216:219], v[8:11], a[24:27]\n"
    "v_cmp_gt_i32 s[86:87], 17, v112\n"
    "v_cvt_pk_bf16_f32 v33, v74, v75\n"
    "v_mfma_f32_16x16x32_bf16 a[56:59], a[216:219], v[12:15], a[56:59]\n"
    "ds_read_b128 a[156:159], v109 offset:7168\n"
    "v_cndmask_b32 v51, v51, v113, s[94:95]\n"
    "v_mfma_f32_16x16x32_bf16 a[88:91], a[216:219], v[16:19], a[88:91]\n"
    "v_exp_f32 v78, v250\n"
    "v_mfma_f32_16x16x32_bf16 a[120:123], a[216:219], v[20:23], a[120:123]\n"
    "v_exp_f32 v79, v251\n"
    "v_mfma_f32_16x16x32_bf16 a[28:31], a[220:223], v[8:11], a[28:31]\n"
    "v_cmp_gt_i32 s[92:93], 18, v112\n"
    "v_cvt_pk_bf16_f32 v36, v76, v77\n"
    "v_mfma_f32_16x16x32_bf16 a[60:63], a[220:223], v[12:15], a[60:63]\n"
    "v_cndmask_b32 v52, v52, v113, s[84:85]\n"
    "v_or3_b32 v108, v108, v32, v33\n"
    "v_mfma_f32_16x16x32_bf16 a[92:95], a[220:223], v[16:19], a[92:95]\n"
    "v_exp_f32 v72, v228\n"
    "v_mfma_f32_16x16x32_bf16 a[124:127], a[220:223], v[20:23], a[124:127]\n"
    "v_exp_f32 v73, v229\n"
    "v_mfma_f32_16x16x32_bf16 v[96:99], v[80:83], v[20:23], v[96:99]\n"
    "v_cmp_gt_i32 s[94:95], 19, v112\n"
    "v_cvt_pk_bf16_f32 v37, v78, v79\n"
    "v_exp_f32 v74, v230\n"
    "v_exp_f32 v75, v231\n"
    "v_cvt_pk_bf16_f32 v26, v72, v73\n"
    "v_or3_b32 v108, v108, v36, v37\n"
    "v_exp_f32 v76, v236\n"
    "v_cndmask_b32 v53, v53, v113, s[86:87]\n"
    "v_cndmask_b32 v54, v54, v113, s[92:93]\n"
    "v_cndmask_b32 v55, v55, v113, s[94:95]\n"
    "s_waitcnt lgkmcnt(0)\n"
    "v_mfma_f32_16x16x32_bf16 v[200:203], a[128:131], v[144:147], v[48:51]\n"
    "ds_read_b128 a[192:195], v110 offset:8192\n"
    "s_min_u32 s5, s57, 5\n"
    "v_mfma_f32_16x16x32_bf16 v[208:211], a[128:131], v[160:163], v[56:59]\n"
    "v_exp_f32 v77, v237\n"
    "v_mfma_f32_16x16x32_bf16 v[216:219], a[128:131], v[176:179], v[64:67]\n"
    "ds_read_b128 a[196:199], v110 offset:9216\n"
    "v_cvt_pk_bf16_f32 v27, v74, v75\n"
    "v_mfma_f32_16x16x32_bf16 v[204:207], a[144:147], v[144:147], v[52:55]\n"
    "v_exp_f32 v78, v238\n"
    "v_mfma_f32_16x16x32_bf16 v[212:215], a[144:147], v[160:163], v[60:63]\n"
    "v_exp_f32 v79, v239\n"
    "v_mfma_f32_16x16x32_bf16 v[220:223], a[144:147], v[176:179], v[68:71]\n"
    "ds_read_b128 a[200:203], v110 offset:10240\n"
    "s_lshl_b32 s5, s5, 16\n"
    "v_mfma_f32_16x16x32_bf16 v[200:203], a[132:135], v[148:151], v[200:203]\n"
    "v_cvt_pk_bf16_f32 v30, v76, v77\n"
    "v_or3_b32 v108, v108, v26, v27\n"
    "v_mfma_f32_16x16x32_bf16 v[208:211], a[132:135], v[164:167], v[208:211]\n"
    "v_exp_f32 v72, v244\n"
    "v_mfma_f32_16x16x32_bf16 v[216:219], a[132:135], v[180:183], v[216:219]\n"
    "ds_read_b128 a[204:207], v110 offset:11264\n"
    "s_add_u32 s5, s5, s67\n"
    "v_mfma_f32_16x16x32_bf16 v[204:207], a[148:151], v[148:151], v[204:207]\n"
    "v_exp_f32 v73, v245\n"
    "v_mfma_f32_16x16x32_bf16 v[212:215], a[148:151], v[164:167], v[212:215]\n"
    "ds_read_b128 a[208:211], v110 offset:12288\n"
    "v_cvt_pk_bf16_f32 v31, v78, v79\n"
    "v_mfma_f32_16x16x32_bf16 v[220:223], a[148:151], v[180:183], v[220:223]\n"
    "v_exp_f32 v74, v246\n"
    "v_mfma_f32_16x16x32_bf16 v[200:203], a[136:139], v[152:155], v[200:203]\n"
    "ds_read_b128 a[212:215], v110 offset:13312\n"
    "s_add_u32 s6, s5, 0x2000\n"
    "v_mfma_f32_16x16x32_bf16 v[208:211], a[136:139], v[168:171], v[208:211]\n"
    "v_exp_f32 v75, v247\n"
    "v_mfma_f32_16x16x32_bf16 v[216:219], a[136:139], v[184:187], v[216:219]\n"
    "buffer_load_dword v86, v121, s[16:19], s5 offen\n"
    "v_cvt_pk_bf16_f32 v34, v72, v73\n"
    "v_mfma_f32_16x16x32_bf16 v[204:207], a[152:155], v[152:155], v[204:207]\n"
    "ds_read_b128 a[216:219], v110 offset:14336\n"
    "v_or3_b32 v108, v108, v30, v31\n"
    "v_mfma_f32_16x16x32_bf16 v[212:215], a[152:155], v[168:171], v[212:215]\n"
    "v_exp_f32 v76, v252\n"
    "v_mfma_f32_16x16x32_bf16 v[220:223], a[152:155], v[184:187], v[220:223]\n"
    "v_exp_f32 v77, v253\n"
    "ds_read_b128 a[220:223], v110 offset:15360\n"
    "v_mfma_f32_16x16x32_bf16 v[200:203], a[140:143], v[156:159], v[200:203]\n"
    "buffer_load_dword v86, v123, s[16:19], s5 offen\n"
    "v_cvt_pk_bf16_f32 v35, v74, v75\n"
    "v_mfma_f32_16x16x32_bf16 v[208:211], a[140:143], v[172:175], v[208:211]\n"
    "v_exp_f32 v78, v254\n"
    "v_mfma_f32_16x16x32_bf16 v[216:219], a[140:143], v[188:191], v[216:219]\n"
    "v_exp_f32 v79, v255\n"
    "v_mfma_f32_16x16x32_bf16 v[204:207], a[156:159], v[156:159], v[204:207]\n"
    "buffer_load_dword v86, v121, s[16:19], s6 offen\n"
    "v_cvt_pk_bf16_f32 v38, v76, v77\n"
    "v_mfma_f32_16x16x32_bf16 v[212:215], a[156:159], v[172:175], v[212:215]\n"
    "buffer_load_dword v86, v123, s[16:19], s6 offen\n"
    "v_or3_b32 v108, v108, v34, v35\n"
    "v_cvt_pk_bf16_f32 v39, v78, v79\n"
    "v_or3_b32 v108, v108, v38, v39\n"
    "v_and_b32 v3, 0x40004000, v108\n"
    "v_cmp_ne_u32 vcc, 0, v3\n"
    "v_mfma_f32_16x16x32_bf16 v[220:223], a[156:159], v[188:191], v[220:223]\n"
    "s_cbranch_vccnz Lrs6\n"
    "Lrr6:\n"
    "s_waitcnt lgkmcnt(0)\n"
    "v_mfma_f32_16x16x32_bf16 a[0:3], a[192:195], v[24:27], a[0:3]\n"
    "ds_read_b128 a[128:131], v109 offset:8192\n"
    "s_lshr_b32 s6, s62, 10\n"
    "v_mfma_f32_16x16x32_bf16 a[32:35], a[192:195], v[28:31], a[32:35]\n"
    "s_sub_u32 s6, s6, 0x60\n"
    "v_add_u32 v112, s6, v111\n"
    "v_mfma_f32_16x16x32_bf16 a[64:67], a[192:195], v[32:35], a[64:67]\n"
    "v_exp_f32 v72, v200\n"
    "v_mfma_f32_16x16x32_bf16 a[96:99], a[192:195], v[36:39], a[96:99]\n"
    "v_exp_f32 v73, v201\n"
    "v_mfma_f32_16x16x32_bf16 a[4:7], a[196:199], v[24:27], a[4:7]\n"
    "ds_read_b128 a[144:147], v109 offset:12288\n"
    "v_cmp_gt_i32 s[84:85], 0, v112\n"
    "v_mfma_f32_16x16x32_bf16 a[36:39], a[196:199], v[28:31], a[36:39]\n"
    "v_exp_f32 v74, v202\n"
    "v_mfma_f32_16x16x32_bf16 a[68:71], a[196:199], v[32:35], a[68:71]\n"
    "ds_read_b128 a[132:135], v109 offset:9216\n"
    "v_cmp_gt_i32 s[86:87], 1, v112\n"
    "v_mfma_f32_16x16x32_bf16 a[100:103], a[196:199], v[36:39], a[100:103]\n"
    "v_exp_f32 v75, v203\n"
    "v_mfma_f32_16x16x32_bf16 v[84:87], v[80:83], v[24:27], v[84:87]\n"
    "v_cmp_gt_i32 s[92:93], 2, v112\n"
    "v_cvt_pk_bf16_f32 v12, v72, v73\n"
    "v_mfma_f32_16x16x32_bf16 a[8:11], a[200:203], v[24:27], a[8:11]\n"
    "v_exp_f32 v76, v208\n"
    "v_mfma_f32_16x16x32_bf16 a[40:43], a[200:203], v[28:31], a[40:43]\n"
    "ds_read_b128 a[148:151], v109 offset:13312\n"
    "v_cndmask_b32 v48, v48, v113, s[84:85]\n"
    "v_mfma_f32_16x16x32_bf16 a[72:75], a[200:203], v[32:35], a[72:75]\n"
    "v_exp_f32 v77, v209\n"
    "v_mfma_f32_16x16x32_bf16 a[104:107], a[200:203], v[36:39], a[104:107]\n"
    "v_cmp_gt_i32 s[94:95], 3, v112\n"
    "v_cndmask_b32 v49, v49, v113, s[86:87]\n"
    "v_mfma_f32_16x16x32_bf16 a[12:15], a[204:207], v[24:27], a[12:15]\n"
    "v_cvt_pk_bf16_f32 v13, v74, v75\n"
    "v_cmp_gt_i32 s[84:85], 16, v112\n"
    "v_mfma_f32_16x16x32_bf16 a[44:47], a[204:207], v[28:31], a[44:47]\n"
    "v_exp_f32 v78, v210\n"
    "v_mfma_f32_16x16x32_bf16 a[76:79], a[204:207], v[32:35], a[76:79]\n"
    "ds_read_b128 a[136:139], v109 offset:10240\n"
    "v_cndmask_b32 v50, v50, v113, s[92:93]\n"
    "v_mfma_f32_16x16x32_bf16 a[108:111], a[204:207], v[36:39], a[108:111]\n"
    "v_exp_f32 v79, v211\n"
    "v_mfma_f32_16x16x32_bf16 v[88:91], v[80:83], v[28:31], v[88:91]\n"
    "v_cmp_gt_i32 s[86:87], 17, v112\n"
    "v_cvt_pk_bf16_f32 v16, v76, v77\n"
    "v_mfma_f32_16x16x32_bf16 a[16:19], a[208:211], v[24:27], a[16:19]\n"
    "ds_read_b128 a[152:155], v109 offset:14336\n"
    "v_or_b32 v108, v12, v13\n"
    "v_mfma_f32_16x16x32_bf16 a[48:51], a[208:211], v[28:31], a[48:51]\n"
    "v_exp_f32 v72, v216\n"
    "v_mfma_f32_16x16x32_bf16 a[80:83], a[208:211], v[32:35], a[80:83]\n"
    "v_cndmask_b32 v51, v51, v113, s[94:95]\n"
    "v_cmp_gt_i32 s[92:93], 18, v112\n"
    "v_mfma_f32_16x16x32_bf16 a[112:115], a[208:211], v[36:39], a[112:115]\n"
    "v_exp_f32 v73, v217\n"
    "v_mfma_f32_16x16x32_bf16 a[20:23], a[212:215], v[24:27], a[20:23]\n"
    "ds_read_b128 a[140:143], v109 offset:11264\n"
    "v_cndmask_b32 v52, v52, v113, s[84:85]\n"
    "v_mfma_f32_16x16x32_bf16 a[52:55], a[212:215], v[28:31], a[52:55]\n"
    "v_cvt_pk_bf16_f32 v17, v78, v79\n"
    "v_cmp_gt_i32 s[94:95], 19, v112\n"
    "v_mfma_f32_16x16x32_bf16 a[84:87], a[212:215], v[32:35], a[84:87]\n"
    "v_exp_f32 v74, v218\n"
    "v_mfma_f32_16x16x32_bf16 a[116:119], a[212:215], v[36:39], a[116:119]\n"
    "v_exp_f32 v75, v219\n"
    "v_mfma_f32_16x16x32_bf16 v[92:95], v[80:83], v[32:35], v[92:95]\n"
    "ds_read_b128 a[156:159], v109 offset:15360\n"
    "v_cndmask_b32 v53, v53, v113, s[86:87]\n"
    "v_mfma_f32_16x16x32_bf16 a[24:27], a[216:219], v[24:27], a[24:27]\n"
    "v_cvt_pk_bf16_f32 v20, v72, v73\n"
    "v_cndmask_b32 v54, v54, v113, s[92:93]\n"
    "v_mfma_f32_16x16x32_bf16 a[56:59], a[216:219], v[28:31], a[56:59]\n"
    "v_or3_b32 v108, v108, v16, v17\n"
    "v_cndmask_b32 v55, v55, v113, s[94:95]\n"
    "v_mfma_f32_16x16x32_bf16 a[88:91], a[216:219], v[32:35], a[88:91]\n"
    "v_exp_f32 v76, v204\n"
    "v_mfma_f32_16x16x32_bf16 a[120:123], a[216:219], v[36:39], a[120:123]\n"
    "v_exp_f32 v77, v205\n"
    "v_mfma_f32_16x16x32_bf16 a[28:31], a[220:223], v[24:27], a[28:31]\n"
    "s_mov_b32 m0, s77\n"
    "s_add_u32 s77, s77, 0x8000\n"
    "v_mfma_f32_16x16x32_bf16 a[60:63], a[220:223], v[28:31], a[60:63]\n"
    "v_cvt_pk_bf16_f32 v21, v74, v75\n"
    "s_and_b32 s77, s77, 0x1ffff\n"
    "v_mfma_f32_16x16x32_bf16 a[92:95], a[220:223], v[32:35], a[92:95]\n"
    "v_exp_f32 v78, v206\n"
    "v_mfma_f32_16x16x32_bf16 a[124:127], a[220:223], v[36:39], a[124:127]\n"
    "v_exp_f32 v79, v207\n"
    "v_mfma_f32_16x16x32_bf16 v[96:99], v[80:83], v[36:39], v[96:99]\n"
    "v_add_u32 v110, s77, v115\n"
    "s_waitcnt lgkmcnt(0)\n"
    "v_mfma_f32_16x16x32_bf16 v[232:235], a[128:131], v[144:147], v[48:51]\n"
    "s_waitcnt vmcnt(16)\n"
    "s_barrier\n"
    "v_mfma_f32_16x16x32_bf16 v[240:243], a[128:131], v[160:163], v[56:59]\n"
    "ds_read_b128 a[192:195], v110\n"
    "v_cvt_pk_bf16_f32 v14, v76, v77\n"
    "v_mfma_f32_16x16x32_bf16 v[248:251], a[128:131], v[176:179], v[64:67]\n"
    "s_sub_u32 s82, s82, 1\n"
    "s_cbranch_scc1 Levs7\n"
    "Levr7:\n"
    "v_mfma_f32_16x16x32_bf16 v[236:239], a[144:147], v[144:147], v[52:55]\n"
    "ds_read_b128 a[196:199], v110 offset:1024\n"
    "v_or3_b32 v108, v108, v20, v21\n"
    "v_mfma_f32_16x16x32_bf16 v[244:247], a[144:147], v[160:163], v[60:63]\n"
    "v_exp_f32 v72, v212\n"
    "v_mfma_f32_16x16x32_bf16 v[252:255], a[144:147], v[176:179], v[68:71]\n"
    "v_exp_f32 v73, v213\n"
    "v_mfma_f32_16x16x32_bf16 v[232:235], a[132:135], v[148:151], v[232:235]\n"
    "buffer_load_dwordx4 v2, s[24:27], s75 offen sc1 lds\n"
    "ds_read_b128 a[200:203], v110 offset:2048\n"
    "v_mfma_f32_16x16x32_bf16 v[240:243], a[132:135], v[164:167], v[240:243]\n"
    "buffer_load_dwordx4 v2, s[24:27], s75 offen offset:1024 sc1 lds\n"
    "v_cvt_pk_bf16_f32 v15, v78, v79\n"
    "v_mfma_f32_16x16x32_bf16 v[248:251], a[132:135], v[180:183], v[248:251]\n"
    "v_exp_f32 v74, v214\n"
    "v_mfma_f32_16x16x32_bf16 v[236:239], a[148:151], v[148:151], v[236:239]\n"
    "ds_read_b128 a[204:207], v110 offset:3072\n"
    "buffer_load_dwordx4 v2, s[24:27], s75 offen offset:2048 sc1 lds\n"
    "v_mfma_f32_16x16x32_bf16 v[244:247], a[148:151], v[164:167], v[244:247]\n"
    "v_exp_f32 v75, v215\n"
    "v_mfma_f32_16x16x32_bf16 v[252:255], a[148:151], v[180:183], v[252:255]\n"
    "ds_read_b128 a[208:211], v110 offset:4096\n"
    "buffer_load_dwordx4 v2, s[24:27], s75 offen offset:3072 sc1 lds\n"
    "v_mfma_f32_16x16x32_bf16 v[232:235], a[136:139], v[152:155], v[232:235]\n"
    "v_cvt_pk_bf16_f32 v18, v72, v73\n"
    "s_mov_b32 m0, s76\n"
    "v_mfma_f32_16x16x32_bf16 v[240:243], a[136:139], v[168:171], v[240:243]\n"
    "ds_read_b128 a[212:215], v110 offset:5120\n"
    "v_or3_b32 v108, v108, v14, v15\n"
    "v_mfma_f32_16x16x32_bf16 v[248:251], a[136:139], v[184:187], v[248:251]\n"
    "v_exp_f32 v76, v220\n"
    "v_mfma_f32_16x16x32_bf16 v[236:239], a[152:155], v[152:155], v[236:239]\n"
    "buffer_load_dwordx4 v114, s[24:27], s75 offen sc1 lds\n"
    "ds_read_b128 a[216:219], v110 offset:6144\n"
    "v_mfma_f32_16x16x32_bf16 v[244:247], a[152:155], v[168:171], v[244:247]\n"
    "v_exp_f32 v77, v221\n"
    "v_mfma_f32_16x16x32_bf16 v[252:255], a[152:155], v[184:187], v[252:255]\n"
    "buffer_load_dwordx4 v114, s[24:27], s75 offen offset:1024 sc1 lds\n"
    "v_cvt_pk_bf16_f32 v19, v74, v75\n"
    "ds_read_b128 a[220:223], v110 offset:7168\n"
    "v_mfma_f32_16x16x32_bf16 v[232:235], a[140:143], v[156:159], v[232:235]\n"
    "v_exp_f32 v78, v222\n"
    "v_mfma_f32_16x16x32_bf16 v[240:243], a[140:143], v[172:175], v[240:243]\n"
    "buffer_load_dwordx4 v114, s[24:27], s75 offen offset:2048 sc1 lds\n"
    "buffer_load_dwordx4 v114, s[24:27], s75 offen offset:3072 sc1 lds\n"
    "v_mfma_f32_16x16x32_bf16 v[248:251], a[140:143], v[188:191], v[248:251]\n"
    "v_exp_f32 v79, v223\n"
    "v_mfma_f32_16x16x32_bf16 v[236:239], a[156:159], v[156:159], v[236:239]\n"
    "s_add_u32 s76, s76, 0x8000\n"
    "v_cvt_pk_bf16_f32 v22, v76, v77\n"
    "v_mfma_f32_16x16x32_bf16 v[244:247], a[156:159], v[172:175], v[244:247]\n"
    "v_or3_b32 v108, v108, v18, v19\n"
    "s_and_b32 s76, s76, 0x1ffff\n"
    "v_cvt_pk_bf16_f32 v23, v78, v79\n"
    "v_or3_b32 v108, v108, v22, v23\n"
    "v_and_b32 v3, 0x40004000, v108\n"
    "v_cmp_ne_u32 vcc, 0, v3\n"
    "v_add_u32 v109, s76, v115\n"
    "v_mfma_f32_16x16x32_bf16 v[252:255], a[156:159], v[188:191], v[252:255]\n"
    "s_cbranch_vccnz Lrs7\n"
    "Lrr7:\n"
    "s_waitcnt lgkmcnt(0)\n"
    "v_mfma_f32_16x16x32_bf16 a[32:35], a[192:195], v[12:15], a[32:35]\n"
    "ds_read_b128 a[128:131], v109\n"
    "s_add_u32 s75, s75, 0x8000\n"
    "v_mfma_f32_16x16x32_bf16 a[64:67], a[192:195], v[16:19], a[64:67]\n"
    "ds_read_b128 a[160:163], v109 offset:8192\n"
    "v_mov_b32 v40, v56\n"
    "v_mfma_f32_16x16x32_bf16 a[96:99], a[192:195], v[20:23], a[96:99]\n"
    "v_exp_f32 v72, v232\n"
    "v_mfma_f32_16x16x32_bf16 a[36:39], a[196:199], v[12:15], a[36:39]\n"
    "ds_read_b128 a[144:147], v109 offset:4096\n"
    "v_mov_b32 v41, v57\n"
    "v_mfma_f32_16x16x32_bf16 a[68:71], a[196:199], v[16:19], a[68:71]\n"
    "ds_read_b128 a[176:179], v109 offset:12288\n"
    "v_mov_b32 v42, v58\n"
    "v_mfma_f32_16x16x32_bf16 a[100:103], a[196:199], v[20:23], a[100:103]\n"
    "v_exp_f32 v73, v233\n"
    "v_mfma_f32_16x16x32_bf16 a[40:43], a[200:203], v[12:15], a[40:43]\n"
    "ds_read_b128 a[132:135], v109 offset:1024\n"
    "v_mov_b32 v43, v59\n"
    "v_mfma_f32_16x16x32_bf16 a[72:75], a[200:203], v[16:19], a[72:75]\n"
    "v_exp_f32 v74, v234\n"
    "v_mfma_f32_16x16x32_bf16 a[104:107], a[200:203], v[20:23], a[104:107]\n"
    "ds_read_b128 a[164:167], v109 offset:9216\n"
    "v_mov_b32 v44, v60\n"
    "v_mfma_f32_16x16x32_bf16 a[44:47], a[204:207], v[12:15], a[44:47]\n"
    "v_exp_f32 v75, v235\n"
    "v_mfma_f32_16x16x32_bf16 a[76:79], a[204:207], v[16:19], a[76:79]\n"
    "ds_read_b128 a[148:151], v109 offset:5120\n"
    "v_mov_b32 v45, v61\n"
    "v_mfma_f32_16x16x32_bf16 a[108:111], a[204:207], v[20:23], a[108:111]\n"
    "v_cvt_pk_bf16_f32 v28, v72, v73\n"
    "v_mov_b32 v46, v62\n"
    "v_mfma_f32_16x16x32_bf16 v[88:91], v[80:83], v[12:15], v[88:91]\n"
    "v_exp_f32 v76, v240\n"
    "v_mfma_f32_16x16x32_bf16 a[48:51], a[208:211], v[12:15], a[48:51]\n"
    "ds_read_b128 a[180:183], v109 offset:13312\n"
    "v_mov_b32 v47, v63\n"
    "v_mfma_f32_16x16x32_bf16 a[80:83], a[208:211], v[16:19], a[80:83]\n"
    "v_exp_f32 v77, v241\n"
    "v_mfma_f32_16x16x32_bf16 a[112:115], a[208:211], v[20:23], a[112:115]\n"
    "ds_read_b128 a[136:139], v109 offset:2048\n"
    "v_mov_b32 v48, v64\n"
    "v_mfma_f32_16x16x32_bf16 a[52:55], a[212:215], v[12:15], a[52:55]\n"
    "ds_read_b128 a[168:171], v109 offset:10240\n"
    "v_cvt_pk_bf16_f32 v29, v74, v75\n"
    "v_mfma_f32_16x16x32_bf16 a[84:87], a[212:215], v[16:19], a[84:87]\n"
    "ds_read_b128 a[152:155], v109 offset:6144\n"
    "v_mov_b32 v49, v65\n"
    "v_mfma_f32_16x16x32_bf16 a[116:119], a[212:215], v[20:23], a[116:119]\n"
    "v_exp_f32 v78, v242\n"
    "v_mfma_f32_16x16x32_bf16 v[92:95], v[80:83], v[16:19], v[92:95]\n"
    "ds_read_b128 a[184:187], v109 offset:14336\n"
    "v_mov_b32 v50, v66\n"
    "ds_read_b128 a[140:143], v109 offset:3072\n"
    "ds_read_b128 a[172:175], v109 offset:11264\n"
    "ds_read_b128 a[156:159], v109 offset:7168\n"
    "ds_read_b128 a[188:191], v109 offset:15360\n"
    "v_mfma_f32_16x16x32_bf16 a[56:59], a[216:219], v[12:15], a[56:59]\n"
    "v_exp_f32 v79, v243\n"
    "v_mfma_f32_16x16x32_bf16 a[88:91], a[216:219], v[16:19], a[88:91]\n"
    "v_mov_b32 v51, v67\n"
    "v_mov_b32 v52, v68\n"
    "v_mfma_f32_16x16x32_bf16 a[120:123], a[216:219], v[20:23], a[120:123]\n"
    "v_cvt_pk_bf16_f32 v32, v76, v77\n"
    "v_or_b32 v108, v28, v29\n"
    "v_mfma_f32_16x16x32_bf16 a[60:63], a[220:223], v[12:15], a[60:63]\n"
    "v_exp_f32 v72, v248\n"
    "v_mfma_f32_16x16x32_bf16 a[92:95], a[220:223], v[16:19], a[92:95]\n"
    "v_mov_b32 v53, v69\n"
    "v_mov_b32 v54, v70\n"
    "v_mfma_f32_16x16x32_bf16 a[124:127], a[220:223], v[20:23], a[124:127]\n"
    "v_exp_f32 v73, v249\n"
    "v_mfma_f32_16x16x32_bf16 v[96:99], v[80:83], v[20:23], v[96:99]\n"
    "v_mov_b32 v55, v71\n"
    "v_cvt_pk_bf16_f32 v33, v78, v79\n"
    "v_exp_f32 v74, v250\n"
    "v_exp_f32 v75, v251\n"
    "v_cvt_pk_bf16_f32 v36, v72, v73\n"
    "v_or3_b32 v108, v108, v32, v33\n"
    "v_exp_f32 v76, v236\n"
    "v_exp_f32 v77, v237\n"
    "v_cvt_pk_bf16_f32 v37, v74, v75\n"
    "v_exp_f32 v78, v238\n"
    "v_exp_f32 v79, v239\n"
    "s_lshl_b32 s5, s64, 1\n"
    "s_add_u32 s5, s5, s57\n"
    "s_and_b32 s5, s5, 7\n"
    "s_lshl_b32 s5, s5, 17\n"
    "s_add_u32 s5, s5, s90\n"
    "s_add_u32 s5, s5, 0\n"
    "s_add_u32 s6, s5, 0x2000\n"
    "buffer_load_dword v86, v121, s[12:15], s5 offen\n"
    "buffer_load_dword v86, v123, s[12:15], s5 offen\n"
    "buffer_load_dword v86, v121, s[12:15], s6 offen\n"
    "buffer_load_dword v86, v123, s[12:15], s6 offen\n"
    "s_cmp_lg_u32 s57, 7\n"
    "s_cbranch_scc1 Lm7a\n"
    "v_mov_b32 v112, v111\n"
    "v_cmp_gt_i32 s[84:85], 0, v112\n"
    "v_cmp_gt_i32 s[86:87], 1, v112\n"
    "v_cmp_gt_i32 s[92:93], 2, v112\n"
    "v_cndmask_b32 v40, v40, v113, s[84:85]\n"
    "v_cmp_gt_i32 s[94:95], 3, v112\n"
    "v_cndmask_b32 v41, v41, v113, s[86:87]\n"
    "v_cmp_gt_i32 s[84:85], 16, v112\n"
    "v_cndmask_b32 v42, v42, v113, s[92:93]\n"
    "v_cmp_gt_i32 s[86:87], 17, v112\n"
    "v_cndmask_b32 v43, v43, v113, s[94:95]\n"
    "v_cmp_gt_i32 s[92:93], 18, v112\n"
    "v_cndmask_b32 v44, v44, v113, s[84:85]\n"
    "v_cmp_gt_i32 s[94:95], 19, v112\n"
    "v_cndmask_b32 v45, v45, v113, s[86:87]\n"
    "v_cndmask_b32 v46, v46, v113, s[92:93]\n"
    "v_cndmask_b32 v47, v47, v113, s[94:95]\n"
    "v_subrev_u32 v112, 32, v111\n"
    "v_cmp_gt_i32 s[84:85], 0, v112\n"
    "v_cmp_gt_i32 s[86:87], 1, v112\n"
    "v_cmp_gt_i32 s[92:93], 2, v112\n"
    "v_cndmask_b32 v56, v56, v113, s[84:85]\n"
    "v_cmp_gt_i32 s[94:95], 3, v112\n"
    "v_cndmask_b32 v57, v57, v113, s[86:87]\n"
    "v_cmp_gt_i32 s[84:85], 16, v112\n"
    "v_cndmask_b32 v58, v58, v113, s[92:93]\n"
    "v_cmp_gt_i32 s[86:87], 17, v112\n"
    "v_cndmask_b32 v59, v59, v113, s[94:95]\n"
    "v_cmp_gt_i32 s[92:93], 18, v112\n"
    "v_cndmask_b32 v60, v60, v113, s[84:85]\n"
    "v_cmp_gt_i32 s[94:95], 19, v112\n"
    "v_cndmask_b32 v61, v61, v113, s[86:87]\n"
    "v_cndmask_b32 v62, v62, v113, s[92:93]\n"
    "v_cndmask_b32 v63, v63, v113, s[94:95]\n"
    "Lm7a:\n"
    "s_waitcnt lgkmcnt(0)\n"
    "v_mfma_f32_16x16x32_bf16 v[192:195], a[128:131], v[160:163], v[40:43]\n"
    "s_waitcnt vmcnt(16)\n"
    "s_barrier\n"
    "v_mfma_f32_16x16x32_bf16 v[200:203], a[128:131], v[176:179], v[48:51]\n"
    "ds_read_b128 a[192:195], v110 offset:8192\n"
    "s_mov_b32 m0, s76\n"
    "v_mfma_f32_16x16x32_bf16 v[208:211], a[160:163], v[160:163], v[56:59]\n"
    "v_cvt_pk_bf16_f32 v30, v76, v77\n"
    "v_or3_b32 v108, v108, v36, v37\n"
    "v_mfma_f32_16x16x32_bf16 v[216:219], a[160:163], v[176:179], v[64:67]\n"
    "v_exp_f32 v72, v244\n"
    "v_mfma_f32_16x16x32_bf16 v[196:199], a[144:147], v[160:163], v[44:47]\n"
    "buffer_load_dwordx4 v114, s[24:27], s75 offen sc1 lds\n"
    "ds_read_b128 a[196:199], v110 offset:9216\n"
    "v_mfma_f32_16x16x32_bf16 v[204:207], a[144:147], v[176:179], v[52:55]\n"
    "buffer_load_dwordx4 v114, s[24:27], s75 offen offset:1024 sc1 lds\n"
    "buffer_load_dwordx4 v114, s[24:27], s75 offen offset:2048 sc1 lds\n"
    "v_mfma_f32_16x16x32_bf16 v[212:215], a[176:179], v[160:163], v[60:63]\n"
    "v_exp_f32 v73, v245\n"
    "v_mfma_f32_16x16x32_bf16 v[220:223], a[176:179], v[176:179], v[68:71]\n"
    "ds_read_b128 a[200:203], v110 offset:10240\n"
    "buffer_load_dwordx4 v114, s[24:27], s75 offen offset:3072 sc1 lds\n"
    "v_mfma_f32_16x16x32_bf16 v[192:195], a[132:135], v[164:167], v[192:195]\n"
    "v_cvt_pk_bf16_f32 v31, v78, v79\n"
    "s_add_u32 s76, s76, 0x8000\n"
    "v_mfma_f32_16x16x32_bf16 v[200:203], a[132:135], v[180:183], v[200:203]\n"
    "v_exp_f32 v74, v246\n"
    "v_mfma_f32_16x16x32_bf16 v[208:211], a[164:167], v[164:167], v[208:211]\n"
    "ds_read_b128 a[204:207], v110 offset:11264\n"
    "s_and_b32 s76, s76, 0x1ffff\n"
    "v_mfma_f32_16x16x32_bf16 v[216:219], a[164:167], v[180:183], v[216:219]\n"
    "v_exp_f32 v75, v247\n"
    "v_mfma_f32_16x16x32_bf16 v[196:199], a[148:151], v[164:167], v[196:199]\n"
    "v_add_u32 v109, s76, v115\n"
    "v_cvt_pk_bf16_f32 v34, v72, v73\n"
    "v_mfma_f32_16x16x32_bf16 v[204:207], a[148:151], v[180:183], v[204:207]\n"
    "ds_read_b128 a[208:211], v110 offset:12288\n"
    "s_lshl_b32 s5, s64, 1\n"
    "v_mfma_f32_16x16x32_bf16 v[212:215], a[180:183], v[164:167], v[212:215]\n"
    "ds_read_b128 a[212:215], v110 offset:13312\n"
    "v_or3_b32 v108, v108, v30, v31\n"
    "v_mfma_f32_16x16x32_bf16 v[220:223], a[180:183], v[180:183], v[220:223]\n"
    "v_exp_f32 v76, v252\n"
    "v_mfma_f32_16x16x32_bf16 v[192:195], a[136:139], v[168:171], v[192:195]\n"
    "s_add_u32 s5, s5, s57\n"
    "s_and_b32 s5, s5, 7\n"
    "v_mfma_f32_16x16x32_bf16 v[200:203], a[136:139], v[184:187], v[200:203]\n"
    "v_exp_f32 v77, v253\n"
    "v_mfma_f32_16x16x32_bf16 v[208:211], a[168:171], v[168:171], v[208:211]\n"
    "s_lshl_b32 s5, s5, 17\n"
    "v_cvt_pk_bf16_f32 v35, v74, v75\n"
    "v_mfma_f32_16x16x32_bf16 v[216:219], a[168:171], v[184:187], v[216:219]\n"
    "ds_read_b128 a[216:219], v110 offset:14336\n"
    "s_add_u32 s5, s5, s90\n"
    "v_mfma_f32_16x16x32_bf16 v[196:199], a[152:155], v[168:171], v[196:199]\n"
    "v_exp_f32 v78, v254\n"
    "v_mfma_f32_16x16x32_bf16 v[204:207], a[152:155], v[184:187], v[204:207]\n"
    "ds_read_b128 a[220:223], v110 offset:15360\n"
    "s_add_u32 s5, s5, 0x10000\n"
    "v_mfma_f32_16x16x32_bf16 v[212:215], a[184:187], v[168:171], v[212:215]\n"
    "v_exp_f32 v79, v255\n"
    "v_mfma_f32_16x16x32_bf16 v[220:223], a[184:187], v[184:187], v[220:223]\n"
    "s_add_u32 s6, s5, 0x2000\n"
    "v_cvt_pk_bf16_f32 v38, v76, v77\n"
    "v_mfma_f32_16x16x32_bf16 v[192:195], a[140:143], v[172:175], v[192:195]\n"
    "buffer_load_dword v86, v121, s[12:15], s5 offen\n"
    "v_or3_b32 v108, v108, v34, v35\n"
    "v_mfma_f32_16x16x32_bf16 v[200:203], a[140:143], v[188:191], v[200:203]\n"
    "v_cvt_pk_bf16_f32 v39, v78, v79\n"
    "v_or3_b32 v108, v108, v38, v39\n"
    "v_mfma_f32_16x16x32_bf16 v[208:211], a[172:175], v[172:175], v[208:211]\n"
    "buffer_load_dword v86, v123, s[12:15], s5 offen\n"
    "v_and_b32 v3, 0x40004000, v108\n"
    "v_mfma_f32_16x16x32_bf16 v[216:219], a[172:175], v[188:191], v[216:219]\n"
    "buffer_load_dword v86, v121, s[12:15], s6 offen\n"
    "v_cmp_ne_u32 vcc, 0, v3\n"
    "v_mfma_f32_16x16x32_bf16 v[196:199], a[156:159], v[172:175], v[196:199]\n"
    "v_mfma_f32_16x16x32_bf16 v[204:207], a[156:159], v[188:191], v[204:207]\n"
    "buffer_load_dword v86, v123, s[12:15], s6 offen\n"
    "v_mfma_f32_16x16x32_bf16 v[212:215], a[188:191], v[172:175], v[212:215]\n"
    "v_mfma_f32_16x16x32_bf16 v[220:223], a[188:191], v[188:191], v[220:223]\n"
    "s_cbranch_vccnz Lrs8\n"
    "Lrr8:\n"
    "s_waitcnt lgkmcnt(0)\n"
    "v_mfma_f32_16x16x32_bf16 a[32:35], a[192:195], v[28:31], a[32:35]\n"
    "ds_read_b128 a[128:131], v109\n"
    "v_add_u32 v114, 0x8000, v114\n"
    "v_mfma_f32_16x16x32_bf16 a[64:67], a[192:195], v[32:35], a[64:67]\n"
    "ds_read_b128 a[160:163], v109 offset:8192\n"
    "s_lshl_b32 s4, s57, 2\n"
    "v_mfma_f32_16x16x32_bf16 a[96:99], a[192:195], v[36:39], a[96:99]\n"
    "s_add_u32 s4, s4, s58\n"
    "s_mul_i32 s4, s4, 0x58000\n"
    "v_mfma_f32_16x16x32_bf16 a[36:39], a[196:199], v[28:31], a[36:39]\n"
    "v_exp_f32 v72, v192\n"
    "v_mfma_f32_16x16x32_bf16 a[68:71], a[196:199], v[32:35], a[68:71]\n"
    "ds_read_b128 a[144:147], v109 offset:4096\n"
    "s_lshl_b32 s5, s56, 12\n"
    "v_mfma_f32_16x16x32_bf16 a[100:103], a[196:199], v[36:39], a[100:103]\n"
    "v_exp_f32 v73, v193\n"
    "v_mfma_f32_16x16x32_bf16 a[40:43], a[200:203], v[28:31], a[40:43]\n"
    "ds_read_b128 a[176:179], v109 offset:12288\n"
    "s_add_u32 s4, s4, s5\n"
    "v_mfma_f32_16x16x32_bf16 a[72:75], a[200:203], v[32:35], a[72:75]\n"
    "v_exp_f32 v74, v194\n"
    "v_mfma_f32_16x16x32_bf16 a[104:107], a[200:203], v[36:39], a[104:107]\n"
    "ds_read_b128 a[132:135], v109 offset:1024\n"
    "s_add_u32 s4, s4, 0x2220000\n"
    "v_mfma_f32_16x16x32_bf16 a[44:47], a[204:207], v[28:31], a[44:47]\n"
    "v_rcp_f32 v124, v84\n"
    "v_mfma_f32_16x16x32_bf16 a[76:79], a[204:207], v[32:35], a[76:79]\n"
    "v_exp_f32 v75, v195\n"
    "v_mfma_f32_16x16x32_bf16 a[108:111], a[204:207], v[36:39], a[108:111]\n"
    "ds_read_b128 a[164:167], v109 offset:9216\n"
    "v_accvgpr_read_b32 v100, a0\n"
    "v_mfma_f32_16x16x32_bf16 v[88:91], v[80:83], v[28:31], v[88:91]\n"
    "v_cvt_pk_bf16_f32 v8, v72, v73\n"
    "v_accvgpr_read_b32 v101, a1\n"
    "v_mfma_f32_16x16x32_bf16 a[48:51], a[208:211], v[28:31], a[48:51]\n"
    "v_exp_f32 v76, v200\n"
    "v_mfma_f32_16x16x32_bf16 a[80:83], a[208:211], v[32:35], a[80:83]\n"
    "ds_read_b128 a[148:151], v109 offset:5120\n"
    "v_accvgpr_read_b32 v102, a2\n"
    "v_mfma_f32_16x16x32_bf16 a[112:115], a[208:211], v[36:39], a[112:115]\n"
    "v_exp_f32 v77, v201\n"
    "v_mfma_f32_16x16x32_bf16 a[52:55], a[212:215], v[28:31], a[52:55]\n"
    "ds_read_b128 a[180:183], v109 offset:13312\n"
    "v_accvgpr_read_b32 v103, a3\n"
    "v_mfma_f32_16x16x32_bf16 a[84:87], a[212:215], v[32:35], a[84:87]\n"
    "v_accvgpr_read_b32 v104, a4\n"
    "v_cvt_pk_bf16_f32 v9, v74, v75\n"
    "v_mfma_f32_16x16x32_bf16 a[116:119], a[212:215], v[36:39], a[116:119]\n"
    "ds_read_b128 a[136:139], v109 offset:2048\n"
    "v_accvgpr_read_b32 v105, a5\n"
    "v_mfma_f32_16x16x32_bf16 v[92:95], v[80:83], v[32:35], v[92:95]\n"
    "v_exp_f32 v78, v202\n"
    "ds_read_b128 a[168:171], v109 offset:10240\n"
    "ds_read_b128 a[152:155], v109 offset:6144\n"
    "ds_read_b128 a[184:187], v109 offset:14336\n"
    "ds_read_b128 a[140:143], v109 offset:3072\n"
    "ds_read_b128 a[172:175], v109 offset:11264\n"
    "ds_read_b128 a[156:159], v109 offset:7168\n"
    "ds_read_b128 a[188:191], v109 offset:15360\n"
    "v_mfma_f32_16x16x32_bf16 a[56:59], a[216:219], v[28:31], a[56:59]\n"
    "v_accvgpr_read_b32 v106, a6\n"
    "v_accvgpr_read_b32 v107, a7\n"
    "v_mfma_f32_16x16x32_bf16 a[88:91], a[216:219], v[32:35], a[88:91]\n"
    "v_exp_f32 v79, v203\n"
    "v_mfma_f32_16x16x32_bf16 a[120:123], a[216:219], v[36:39], a[120:123]\n"
    "v_mul_f32 v100, v100, v124\n"
    "v_cvt_pk_bf16_f32 v12, v76, v77\n"
    "v_mfma_f32_16x16x32_bf16 a[60:63], a[220:223], v[28:31], a[60:63]\n"
    "v_mul_f32 v101, v101, v124\n"
    "v_or_b32 v108, v8, v9\n"
    "v_mfma_f32_16x16x32_bf16 a[92:95], a[220:223], v[32:35], a[92:95]\n"
    "v_exp_f32 v72, v208\n"
    "v_mfma_f32_16x16x32_bf16 a[124:127], a[220:223], v[36:39], a[124:127]\n"
    "v_mul_f32 v102, v102, v124\n"
    "v_mul_f32 v103, v103, v124\n"
    "v_mfma_f32_16x16x32_bf16 v[96:99], v[80:83], v[36:39], v[96:99]\n"
    "v_exp_f32 v73, v209\n"
    "v_cvt_pk_bf16_f32 v13, v78, v79\n"
    "v_exp_f32 v74, v210\n"
    "v_exp_f32 v75, v211\n"
    "v_cvt_pk_bf16_f32 v16, v72, v73\n"
    "v_or3_b32 v108, v108, v12, v13\n"
    "v_exp_f32 v76, v216\n"
    "v_exp_f32 v77, v217\n"
    "v_cvt_pk_bf16_f32 v17, v74, v75\n"
    "v_exp_f32 v78, v218\n"
    "v_exp_f32 v79, v219\n"
    "v_cvt_pk_bf16_f32 v20, v76, v77\n"
    "v_or3_b32 v108, v108, v16, v17\n"
    "v_exp_f32 v72, v196\n"
    "v_exp_f32 v73, v197\n"
    "v_cvt_pk_bf16_f32 v21, v78, v79\n"
    "v_exp_f32 v74, v198\n"
    "v_exp_f32 v75, v199\n"
    "v_cvt_pk_bf16_f32 v10, v72, v73\n"
    "v_or3_b32 v108, v108, v20, v21\n"
    "v_exp_f32 v76, v204\n"
    "v_mul_f32 v104, v104, v124\n"
    "v_mul_f32 v105, v105, v124\n"
    "v_mul_f32 v106, v106, v124\n"
    "v_mul_f32 v107, v107, v124\n"
    "v_cvt_pk_bf16_f32 v116, v100, v101\n"
    "v_cvt_pk_bf16_f32 v117, v102, v103\n"
    "v_cvt_pk_bf16_f32 v118, v104, v105\n"
    "v_cvt_pk_bf16_f32 v119, v106, v107\n"
    "buffer_store_dwordx4 v[116:119], v2, s[8:11], s4 offen sc1\n"
    "v_accvgpr_read_b32 v100, a8\n"
    "v_accvgpr_read_b32 v101, a9\n"
    "v_accvgpr_read_b32 v102, a10\n"
    "v_accvgpr_read_b32 v103, a11\n"
    "v_accvgpr_read_b32 v104, a12\n"
    "v_accvgpr_read_b32 v105, a13\n"
    "v_accvgpr_read_b32 v106, a14\n"
    "v_accvgpr_read_b32 v107, a15\n"
    "v_mul_f32 v100, v100, v124\n"
    "v_mul_f32 v101, v101, v124\n"
    "v_mul_f32 v102, v102, v124\n"
    "v_mul_f32 v103, v103, v124\n"
    "v_mul_f32 v104, v104, v124\n"
    "v_mul_f32 v105, v105, v124\n"
    "v_mul_f32 v106, v106, v124\n"
    "v_mul_f32 v107, v107, v124\n"
    "v_cvt_pk_bf16_f32 v116, v100, v101\n"
    "v_cvt_pk_bf16_f32 v117, v102, v103\n"
    "v_cvt_pk_bf16_f32 v118, v104, v105\n"
    "v_cvt_pk_bf16_f32 v119, v106, v107\n"
    "buffer_store_dwordx4 v[116:119], v2, s[8:11], s4 offen offset:1024 sc1\n"
    "v_rcp_f32 v126, v88\n"
    "s_nop 0\n"
    "v_mov_b32 v127, v126\n"
    ".p2align 3\n"
    "v_accvgpr_read_b32 v100, a32\n"
    "v_accvgpr_read_b32 v101, a33\n"
    "v_accvgpr_read_b32 v102, a34\n"
    "v_accvgpr_read_b32 v103, a35\n"
    "v_accvgpr_read_b32 v104, a36\n"
    "v_accvgpr_read_b32 v105, a37\n"
    "v_accvgpr_read_b32 v106, a38\n"
    "v_accvgpr_read_b32 v107, a39\n"
    "v_pk_mul_f32 v[100:101], v[100:101], v[126:127]\n"
    "v_pk_mul_f32 v[102:103], v[102:103], v[126:127]\n"
    "v_pk_mul_f32 v[104:105], v[104:105], v[126:127]\n"
    "v_pk_mul_f32 v[106:107], v[106:107], v[126:127]\n"
    "v_cvt_pk_bf16_f32 v156, v100, v101\n"
    "v_cvt_pk_bf16_f32 v157, v102, v103\n"
    "v_cvt_pk_bf16_f32 v158, v104, v105\n"
    "v_cvt_pk_bf16_f32 v159, v106, v107\n"
    "buffer_store_dwordx4 v[156:159], v120, s[8:11], s4 offen sc1\n"
    "v_accvgpr_read_b32 v100, a40\n"
    "v_accvgpr_read_b32 v101, a41\n"
    "v_accvgpr_read_b32 v102, a42\n"
    "v_accvgpr_read_b32 v103, a43\n"
    "v_accvgpr_read_b32 v104, a44\n"
    "v_accvgpr_read_b32 v105, a45\n"
    "v_accvgpr_read_b32 v106, a46\n"
    "v_accvgpr_read_b32 v107, a47\n"
    "v_pk_mul_f32 v[100:101], v[100:101], v[126:127]\n"
    "v_pk_mul_f32 v[102:103], v[102:103], v[126:127]\n"
    "v_pk_mul_f32 v[104:105], v[104:105], v[126:127]\n"
    "v_pk_mul_f32 v[106:107], v[106:107], v[126:127]\n"
    "v_cvt_pk_bf16_f32 v156, v100, v101\n"
    "v_cvt_pk_bf16_f32 v157, v102, v103\n"
    "v_cvt_pk_bf16_f32 v158, v104, v105\n"
    "v_cvt_pk_bf16_f32 v159, v106, v107\n"
    "buffer_store_dwordx4 v[156:159], v120, s[8:11], s4 offen offset:1024 sc1\n"
    "v_accvgpr_read_b32 v100, a48\n"
    "v_accvgpr_read_b32 v101, a49\n"
    "v_accvgpr_read_b32 v102, a50\n"
    "v_accvgpr_read_b32 v103, a51\n"
    "v_accvgpr_read_b32 v104, a52\n"
    "v_accvgpr_read_b32 v105, a53\n"
    "v_accvgpr_read_b32 v106, a54\n"
    "v_accvgpr_read_b32 v107, a55\n"
    "v_pk_mul_f32 v[100:101], v[100:101], v[126:127]\n"
    "v_pk_mul_f32 v[102:103], v[102:103], v[126:127]\n"
    "v_pk_mul_f32 v[104:105], v[104:105], v[126:127]\n"
    "v_pk_mul_f32 v[106:107], v[106:107], v[126:127]\n"
    "v_cvt_pk_bf16_f32 v156, v100, v101\n"
    "v_cvt_pk_bf16_f32 v157, v102, v103\n"
    "v_cvt_pk_bf16_f32 v158, v104, v105\n"
    "v_cvt_pk_bf16_f32 v159, v106, v107\n"
    "buffer_store_dwordx4 v[156:159], v120, s[8:11], s4 offen offset:2048 sc1\n"
    "v_accvgpr_read_b32 v100, a16\n"
    "v_accvgpr_read_b32 v101, a17\n"
    "v_accvgpr_read_b32 v102, a18\n"
    "v_accvgpr_read_b32 v103, a19\n"
    "v_accvgpr_read_b32 v104, a20\n"
    "v_accvgpr_read_b32 v105, a21\n"
    "v_accvgpr_read_b32 v106, a22\n"
    "v_accvgpr_read_b32 v107, a23\n"
    "v_mul_f32 v100, v100, v124\n"
    "v_mul_f32 v101, v101, v124\n"
    "v_mul_f32 v102, v102, v124\n"
    "v_mul_f32 v103, v103, v124\n"
    "v_mul_f32 v104, v104, v124\n"
    "v_mul_f32 v105, v105, v124\n"
    "v_mul_f32 v106, v106, v124\n"
    "v_mul_f32 v107, v107, v124\n"
    "v_cvt_pk_bf16_f32 v116, v100, v101\n"
    "v_cvt_pk_bf16_f32 v117, v102, v103\n"
    "v_cvt_pk_bf16_f32 v118, v104, v105\n"
    "v_cvt_pk_bf16_f32 v119, v106, v107\n"
    "buffer_store_dwordx4 v[116:119], v2, s[8:11], s4 offen offset:2048 sc1\n"
    "v_accvgpr_read_b32 v100, a56\n"
    "v_accvgpr_read_b32 v101, a57\n"
    "v_accvgpr_read_b32 v102, a58\n"
    "v_accvgpr_read_b32 v103, a59\n"
    "v_accvgpr_read_b32 v104, a60\n"
    "v_accvgpr_read_b32 v105, a61\n"
    "v_accvgpr_read_b32 v106, a62\n"
    "v_accvgpr_read_b32 v107, a63\n"
    "v_pk_mul_f32 v[100:101], v[100:101], v[126:127]\n"
    "v_pk_mul_f32 v[102:103], v[102:103], v[126:127]\n"
    "v_pk_mul_f32 v[104:105], v[104:105], v[126:127]\n"
    "v_pk_mul_f32 v[106:107], v[106:107], v[126:127]\n"
    "v_cvt_pk_bf16_f32 v156, v100, v101\n"
    "v_cvt_pk_bf16_f32 v157, v102, v103\n"
    "v_cvt_pk_bf16_f32 v158, v104, v105\n"
    "v_cvt_pk_bf16_f32 v159, v106, v107\n"
    "buffer_store_dwordx4 v[156:159], v120, s[8:11], s4 offen offset:3072 sc1\n"
    "v_accvgpr_read_b32 v100, a24\n"
    "v_accvgpr_read_b32 v101, a25\n"
    "v_accvgpr_read_b32 v102, a26\n"
    "v_accvgpr_read_b32 v103, a27\n"
    "v_accvgpr_read_b32 v104, a28\n"
    "v_accvgpr_read_b32 v105, a29\n"
    "v_accvgpr_read_b32 v106, a30\n"
    "v_accvgpr_read_b32 v107, a31\n"
    "v_mul_f32 v100, v100, v124\n"
    "v_mul_f32 v101, v101, v124\n"
    "v_mul_f32 v102, v102, v124\n"
    "v_mul_f32 v103, v103, v124\n"
    "v_mul_f32 v104, v104, v124\n"
    "v_mul_f32 v105, v105, v124\n"
    "v_mul_f32 v106, v106, v124\n"
    "v_mul_f32 v107, v107, v124\n"
    "v_cvt_pk_bf16_f32 v116, v100, v101\n"
    "v_cvt_pk_bf16_f32 v117, v102, v103\n"
    "v_cvt_pk_bf16_f32 v118, v104, v105\n"
    "v_cvt_pk_bf16_f32 v119, v106, v107\n"
    "buffer_store_dwordx4 v[116:119], v2, s[8:11], s4 offen offset:3072 sc1\n"
    "s_cmp_lg_u32 s57, 7\n"
    "s_cbranch_scc1 LH0\n"
    "s_lshr_b32 s6, s62, 10\n"
    "s_sub_u32 s6, s6, 64\n"
    "v_add_u32 v112, s6, v111\n"
    "v_cmp_gt_i32 s[84:85], 0, v112\n"
    "v_cmp_gt_i32 s[86:87], 1, v112\n"
    "v_cmp_gt_i32 s[92:93], 2, v112\n"
    "v_cndmask_b32 v48, v48, v113, s[84:85]\n"
    "v_cmp_gt_i32 s[94:95], 3, v112\n"
    "v_cndmask_b32 v49, v49, v113, s[86:87]\n"
    "v_cmp_gt_i32 s[84:85], 16, v112\n"
    "v_cndmask_b32 v50, v50, v113, s[92:93]\n"
    "v_cmp_gt_i32 s[86:87], 17, v112\n"
    "v_cndmask_b32 v51, v51, v113, s[94:95]\n"
    "v_cmp_gt_i32 s[92:93], 18, v112\n"
    "v_cndmask_b32 v52, v52, v113, s[84:85]\n"
    "v_cmp_gt_i32 s[94:95], 19, v112\n"
    "v_cndmask_b32 v53, v53, v113, s[86:87]\n"
    "v_cndmask_b32 v54, v54, v113, s[92:93]\n"
    "v_cndmask_b32 v55, v55, v113, s[94:95]\n"
    "s_lshr_b32 s6, s62, 10\n"
    "s_sub_u32 s6, s6, 0x60\n"
    "v_add_u32 v112, s6, v111\n"
    "v_cmp_gt_i32 s[84:85], 0, v112\n"
    "v_cmp_gt_i32 s[86:87], 1, v112\n"
    "v_cmp_gt_i32 s[92:93], 2, v112\n"
    "v_cndmask_b32 v64, v64, v113, s[84:85]\n"
    "v_cmp_gt_i32 s[94:95], 3, v112\n"
    "v_cndmask_b32 v65, v65, v113, s[86:87]\n"
    "v_cmp_gt_i32 s[84:85], 16, v112\n"
    "v_cndmask_b32 v66, v66, v113, s[92:93]\n"
    "v_cmp_gt_i32 s[86:87], 17, v112\n"
    "v_cndmask_b32 v67, v67, v113, s[94:95]\n"
    "v_cmp_gt_i32 s[92:93], 18, v112\n"
    "v_cndmask_b32 v68, v68, v113, s[84:85]\n"
    "v_cmp_gt_i32 s[94:95], 19, v112\n"
    "v_cndmask_b32 v69, v69, v113, s[86:87]\n"
    "v_cndmask_b32 v70, v70, v113, s[92:93]\n"
    "v_cndmask_b32 v71, v71, v113, s[94:95]\n"
    "v_and_b32 v5, 7, v1\n"
    "v_lshlrev_b32 v5, 2, v5\n"
    "s_add_u32 s7, s68, 0x3000\n"
    "buffer_load_dword v122, v5, s[8:11], s7 offen sc1\n"
    "s_add_u32 s70, s67, 0x2000\n"
    "s_add_u32 m0, s59, 0x20000\n"
    "s_nop 0\n"
    "buffer_load_dwordx4 v121, s[16:19], s67 offen lds\n"
    "s_add_u32 m0, s59, 0x20400\n"
    "s_nop 0\n"
    "buffer_load_dwordx4 v123, s[16:19], s67 offen lds\n"
    "s_add_u32 m0, s59, 0x20800\n"
    "s_nop 0\n"
    "buffer_load_dwordx4 v121, s[16:19], s70 offen lds\n"
    "s_add_u32 m0, s59, 0x20c00\n"
    "s_nop 0\n"
    "buffer_load_dwordx4 v123, s[16:19], s70 offen lds\n"
    "s_add_u32 s67, s67, 0x10000\n"
    "s_add_u32 s70, s67, 0x2000\n"
    "s_add_u32 m0, s59, 0x24000\n"
    "s_nop 0\n"
    "buffer_load_dwordx4 v121, s[16:19], s67 offen lds\n"
    "s_add_u32 m0, s59, 0x24400\n"
    "s_nop 0\n"
    "buffer_load_dwordx4 v123, s[16:19], s67 offen lds\n"
    "s_add_u32 m0, s59, 0x24800\n"
    "s_nop 0\n"
    "buffer_load_dwordx4 v121, s[16:19], s70 offen lds\n"
    "s_add_u32 m0, s59, 0x24c00\n"
    "s_nop 0\n"
    "buffer_load_dwordx4 v123, s[16:19], s70 offen lds\n"
    "s_add_u32 s67, s67, 0x10000\n"
    "s_waitcnt vmcnt(8)\n"
    "v_cmp_ne_u32 vcc, v122, v7\n"
    "s_nop 0\n"
    "s_cbranch_vccnz Lspin3\n"
    "Lspinret3:\n"
    "buffer_load_dwordx4 v[128:131], v2, s[8:11], s66 offen sc1\n"
    "buffer_load_dwordx4 v[132:135], v120, s[8:11], s66 offen sc1\n"
    "buffer_load_dwordx4 v[136:139], v2, s[8:11], s66 offen offset:1024 sc1\n"
    "buffer_load_dwordx4 v[140:143], v120, s[8:11], s66 offen offset:1024 sc1\n"
    "buffer_load_dwordx4 v[144:147], v2, s[8:11], s66 offen offset:2048 sc1\n"
    "buffer_load_dwordx4 v[148:151], v120, s[8:11], s66 offen offset:2048 sc1\n"
    "buffer_load_dwordx4 v[152:155], v2, s[8:11], s66 offen offset:3072 sc1\n"
    "buffer_load_dwordx4 v[156:159], v120, s[8:11], s66 offen offset:3072 sc1\n"
    "s_add_u32 s66, s66, 0x1000\n"
    "buffer_load_dwordx4 a[0:3], v2, s[8:11], s66 offen sc1\n"
    "buffer_load_dwordx4 a[4:7], v120, s[8:11], s66 offen sc1\n"
    "buffer_load_dwordx4 a[8:11], v2, s[8:11], s66 offen offset:1024 sc1\n"
    "buffer_load_dwordx4 a[12:15], v120, s[8:11], s66 offen offset:1024 sc1\n"
    "s_add_u32 s77, s77, 0x8000\n"
    "s_and_b32 s77, s77, 0x1ffff\n"
    "v_add_u32 v110, s77, v115\n"
    "s_branch LT1\n"
    "LH0:\n"
    "s_mov_b32 m0, s77\n"
    "s_add_u32 s77, s77, 0x8000\n"
    "s_and_b32 s77, s77, 0x1ffff\n"
    "v_add_u32 v110, s77, v115\n"
    "s_lshl_b32 s78, s57, 1\n"
    "s_sub_u32 s78, 13, s78\n"
    ".p2align 6\n"
    "LloopH:\n"
    "s_waitcnt lgkmcnt(0)\n"
    "v_mfma_f32_16x16x32_bf16 v[224:227], a[128:131], v[160:163], v[40:43]\n"
    "s_waitcnt vmcnt(12)\n"
    "s_barrier\n"
    "v_mfma_f32_16x16x32_bf16 v[232:235], a[128:131], v[176:179], v[48:51]\n"
    "ds_read_b128 a[192:195], v110\n"
    "buffer_load_dwordx4 v2, s[24:27], s75 offen sc1 lds\n"
    "v_mfma_f32_16x16x32_bf16 v[240:243], a[160:163], v[160:163], v[56:59]\n"
    "v_exp_f32 v77, v205\n"
    "v_mfma_f32_16x16x32_bf16 v[248:251], a[160:163], v[176:179], v[64:67]\n"
    "ds_read_b128 a[224:227], v110 offset:8192\n"
    "v_cvt_pk_bf16_f32 v11, v74, v75\n"
    "v_mfma_f32_16x16x32_bf16 v[228:231], a[144:147], v[160:163], v[44:47]\n"
    "v_exp_f32 v78, v206\n"
    "v_mfma_f32_16x16x32_bf16 v[236:239], a[144:147], v[176:179], v[52:55]\n"
    "buffer_load_dwordx4 v2, s[24:27], s75 offen offset:1024 sc1 lds\n"
    "ds_read_b128 a[196:199], v110 offset:1024\n"
    "v_mfma_f32_16x16x32_bf16 v[244:247], a[176:179], v[160:163], v[60:63]\n"
    "v_exp_f32 v79, v207\n"
    "v_mfma_f32_16x16x32_bf16 v[252:255], a[176:179], v[176:179], v[68:71]\n"
    "ds_read_b128 a[228:231], v110 offset:9216\n"
    "buffer_load_dwordx4 v2, s[24:27], s75 offen offset:2048 sc1 lds\n"
    "v_mfma_f32_16x16x32_bf16 v[224:227], a[132:135], v[164:167], v[224:227]\n"
    "ds_read_b128 a[200:203], v110 offset:2048\n"
    "v_cvt_pk_bf16_f32 v14, v76, v77\n"
    "v_mfma_f32_16x16x32_bf16 v[232:235], a[132:135], v[180:183], v[232:235]\n"
    "buffer_load_dwordx4 v2, s[24:27], s75 offen offset:3072 sc1 lds\n"
    "v_or3_b32 v108, v108, v10, v11\n"
    "v_mfma_f32_16x16x32_bf16 v[240:243], a[164:167], v[164:167], v[240:243]\n"
    "v_exp_f32 v72, v212\n"
    "v_mfma_f32_16x16x32_bf16 v[248:251], a[164:167], v[180:183], v[248:251]\n"
    "ds_read_b128 a[232:235], v110 offset:10240\n"
    "ds_read_b128 a[204:207], v110 offset:3072\n"
    "v_mfma_f32_16x16x32_bf16 v[228:231], a[148:151], v[164:167], v[228:231]\n"
    "v_exp_f32 v73, v213\n"
    "v_mfma_f32_16x16x32_bf16 v[236:239], a[148:151], v[180:183], v[236:239]\n"
    "ds_read_b128 a[236:239], v110 offset:11264\n"
    "s_mov_b32 m0, s76\n"
    "v_mfma_f32_16x16x32_bf16 v[244:247], a[180:183], v[164:167], v[244:247]\n"
    "ds_read_b128 a[208:211], v110 offset:4096\n"
    "v_cvt_pk_bf16_f32 v15, v78, v79\n"
    "v_mfma_f32_16x16x32_bf16 v[252:255], a[180:183], v[180:183], v[252:255]\n"
    "v_exp_f32 v74, v214\n"
    "v_mfma_f32_16x16x32_bf16 v[224:227], a[136:139], v[168:171], v[224:227]\n"
    "buffer_load_dwordx4 v114, s[24:27], s75 offen sc1 lds\n"
    "ds_read_b128 a[240:243], v110 offset:12288\n"
    "v_mfma_f32_16x16x32_bf16 v[232:235], a[136:139], v[184:187], v[232:235]\n"
    "v_exp_f32 v75, v215\n"
    "v_mfma_f32_16x16x32_bf16 v[240:243], a[168:171], v[168:171], v[240:243]\n"
    "buffer_load_dwordx4 v114, s[24:27], s75 offen offset:1024 sc1 lds\n"
    "ds_read_b128 a[212:215], v110 offset:5120\n"
    "v_mfma_f32_16x16x32_bf16 v[248:251], a[168:171], v[184:187], v[248:251]\n"
    "v_cvt_pk_bf16_f32 v18, v72, v73\n"
    "v_or3_b32 v108, v108, v14, v15\n"
    "v_mfma_f32_16x16x32_bf16 v[228:231], a[152:155], v[168:171], v[228:231]\n"
    "ds_read_b128 a[244:247], v110 offset:13312\n"
    "buffer_load_dwordx4 v114, s[24:27], s75 offen offset:2048 sc1 lds\n"
    "v_mfma_f32_16x16x32_bf16 v[236:239], a[152:155], v[184:187], v[236:239]\n"
    "v_exp_f32 v76, v220\n"
    "v_mfma_f32_16x16x32_bf16 v[244:247], a[184:187], v[168:171], v[244:247]\n"
    "v_exp_f32 v77, v221\n"
    "v_mfma_f32_16x16x32_bf16 v[252:255], a[184:187], v[184:187], v[252:255]\n"
    "ds_read_b128 a[216:219], v110 offset:6144\n"
    "ds_read_b128 a[248:251], v110 offset:14336\n"
    "v_mfma_f32_16x16x32_bf16 v[224:227], a[140:143], v[172:175], v[224:227]\n"
    "buffer_load_dwordx4 v114, s[24:27], s75 offen offset:3072 sc1 lds\n"
    "v_cvt_pk_bf16_f32 v19, v74, v75\n"
    "v_mfma_f32_16x16x32_bf16 v[232:235], a[140:143], v[188:191], v[232:235]\n"
    "v_exp_f32 v78, v222\n"
    "ds_read_b128 a[220:223], v110 offset:7168\n"
    "ds_read_b128 a[252:255], v110 offset:15360\n"
    "v_mfma_f32_16x16x32_bf16 v[240:243], a[172:175], v[172:175], v[240:243]\n"
    "v_exp_f32 v79, v223\n"
    "v_mfma_f32_16x16x32_bf16 v[248:251], a[172:175], v[188:191], v[248:251]\n"
    "s_add_u32 s76, s76, 0x8000\n"
    "v_cvt_pk_bf16_f32 v22, v76, v77\n"
    "v_mfma_f32_16x16x32_bf16 v[228:231], a[156:159], v[172:175], v[228:231]\n"
    "s_and_b32 s76, s76, 0x1ffff\n"
    "v_or3_b32 v108, v108, v18, v19\n"
    "v_mfma_f32_16x16x32_bf16 v[236:239], a[156:159], v[188:191], v[236:239]\n"
    "v_cvt_pk_bf16_f32 v23, v78, v79\n"
    "v_or3_b32 v108, v108, v22, v23\n"
    "v_mfma_f32_16x16x32_bf16 v[244:247], a[188:191], v[172:175], v[244:247]\n"
    "v_add_u32 v109, s76, v115\n"
    "v_and_b32 v3, 0x40004000, v108\n"
    "v_cmp_ne_u32 vcc, 0, v3\n"
    "v_mfma_f32_16x16x32_bf16 v[252:255], a[188:191], v[188:191], v[252:255]\n"
    "s_cbranch_vccnz Lrs9\n"
    "Lrr9:\n"
    "s_waitcnt lgkmcnt(0)\n"
    "v_mfma_f32_16x16x32_bf16 a[64:67], a[192:195], v[8:11], a[64:67]\n"
    "ds_read_b128 a[128:131], v109\n"
    "s_add_u32 s75, s75, 0x8000\n"
    "v_mfma_f32_16x16x32_bf16 a[96:99], a[192:195], v[12:15], a[96:99]\n"
    "ds_read_b128 a[160:163], v109 offset:8192\n"
    "ds_read_b128 a[144:147], v109 offset:4096\n"
    "v_mfma_f32_16x16x32_bf16 a[64:67], a[224:227], v[16:19], a[64:67]\n"
    "v_exp_f32 v72, v224\n"
    "v_mfma_f32_16x16x32_bf16 a[96:99], a[224:227], v[20:23], a[96:99]\n"
    "v_exp_f32 v73, v225\n"
    "v_mfma_f32_16x16x32_bf16 a[68:71], a[196:199], v[8:11], a[68:71]\n"
    "v_exp_f32 v74, v226\n"
    "v_mfma_f32_16x16x32_bf16 a[100:103], a[196:199], v[12:15], a[100:103]\n"
    "v_exp_f32 v75, v227\n"
    "v_mfma_f32_16x16x32_bf16 a[68:71], a[228:231], v[16:19], a[68:71]\n"
    "ds_read_b128 a[176:179], v109 offset:12288\n"
    "v_cvt_pk_bf16_f32 v24, v72, v73\n"
    "v_mfma_f32_16x16x32_bf16 a[100:103], a[228:231], v[20:23], a[100:103]\n"
    "ds_read_b128 a[132:135], v109 offset:1024\n"
    "s_mov_b32 m0, s77\n"
    "v_mfma_f32_16x16x32_bf16 v[92:95], v[80:83], v[8:11], v[92:95]\n"
    "v_exp_f32 v76, v232\n"
    "v_mfma_f32_16x16x32_bf16 a[72:75], a[200:203], v[8:11], a[72:75]\n"
    "v_exp_f32 v77, v233\n"
    "v_mfma_f32_16x16x32_bf16 a[104:107], a[200:203], v[12:15], a[104:107]\n"
    "ds_read_b128 a[164:167], v109 offset:9216\n"
    "v_cvt_pk_bf16_f32 v25, v74, v75\n"
    "v_mfma_f32_16x16x32_bf16 a[72:75], a[232:235], v[16:19], a[72:75]\n"
    "v_exp_f32 v78, v234\n"
    "v_mfma_f32_16x16x32_bf16 a[104:107], a[232:235], v[20:23], a[104:107]\n"
    "ds_read_b128 a[148:151], v109 offset:5120\n"
    "ds_read_b128 a[180:183], v109 offset:13312\n"
    "v_mfma_f32_16x16x32_bf16 a[76:79], a[204:207], v[8:11], a[76:79]\n"
    "v_exp_f32 v79, v235\n"
    "v_mfma_f32_16x16x32_bf16 a[108:111], a[204:207], v[12:15], a[108:111]\n"
    "v_cvt_pk_bf16_f32 v28, v76, v77\n"
    "v_or_b32 v108, v24, v25\n"
    "v_mfma_f32_16x16x32_bf16 a[76:79], a[236:239], v[16:19], a[76:79]\n"
    "v_exp_f32 v72, v240\n"
    "v_mfma_f32_16x16x32_bf16 a[108:111], a[236:239], v[20:23], a[108:111]\n"
    "ds_read_b128 a[136:139], v109 offset:2048\n"
    "s_add_u32 s77, s77, 0x8000\n"
    "v_mfma_f32_16x16x32_bf16 v[96:99], v[80:83], v[12:15], v[96:99]\n"
    "v_exp_f32 v73, v241\n"
    "v_mfma_f32_16x16x32_bf16 a[80:83], a[208:211], v[8:11], a[80:83]\n"
    "ds_read_b128 a[168:171], v109 offset:10240\n"
    "v_cvt_pk_bf16_f32 v29, v78, v79\n"
    "v_mfma_f32_16x16x32_bf16 a[112:115], a[208:211], v[12:15], a[112:115]\n"
    "v_exp_f32 v74, v242\n"
    "v_mfma_f32_16x16x32_bf16 a[80:83], a[240:243], v[16:19], a[80:83]\n"
    "v_exp_f32 v75, v243\n"
    "v_mfma_f32_16x16x32_bf16 a[112:115], a[240:243], v[20:23], a[112:115]\n"
    "ds_read_b128 a[152:155], v109 offset:6144\n"
    "ds_read_b128 a[184:187], v109 offset:14336\n"
    "v_mfma_f32_16x16x32_bf16 a[84:87], a[212:215], v[8:11], a[84:87]\n"
    "v_cvt_pk_bf16_f32 v32, v72, v73\n"
    "v_or3_b32 v108, v108, v28, v29\n"
    "v_mfma_f32_16x16x32_bf16 a[116:119], a[212:215], v[12:15], a[116:119]\n"
    "v_exp_f32 v76, v248\n"
    "v_mfma_f32_16x16x32_bf16 a[84:87], a[244:247], v[16:19], a[84:87]\n"
    "ds_read_b128 a[140:143], v109 offset:3072\n"
    "s_and_b32 s77, s77, 0x1ffff\n"
    "v_mfma_f32_16x16x32_bf16 a[116:119], a[244:247], v[20:23], a[116:119]\n"
    "v_exp_f32 v77, v249\n"
    "v_mfma_f32_16x16x32_bf16 v[92:95], v[80:83], v[16:19], v[92:95]\n"
    "ds_read_b128 a[172:175], v109 offset:11264\n"
    "v_cvt_pk_bf16_f32 v33, v74, v75\n"
    "v_mfma_f32_16x16x32_bf16 a[88:91], a[216:219], v[8:11], a[88:91]\n"
    "v_exp_f32 v78, v250\n"
    "v_mfma_f32_16x16x32_bf16 a[120:123], a[216:219], v[12:15], a[120:123]\n"
    "v_exp_f32 v79, v251\n"
    "ds_read_b128 a[156:159], v109 offset:7168\n"
    "ds_read_b128 a[188:191], v109 offset:15360\n"
    "v_mfma_f32_16x16x32_bf16 a[88:91], a[248:251], v[16:19], a[88:91]\n"
    "v_cvt_pk_bf16_f32 v36, v76, v77\n"
    "v_or3_b32 v108, v108, v32, v33\n"
    "v_mfma_f32_16x16x32_bf16 a[120:123], a[248:251], v[20:23], a[120:123]\n"
    "v_exp_f32 v72, v228\n"
    "v_mfma_f32_16x16x32_bf16 a[92:95], a[220:223], v[8:11], a[92:95]\n"
    "v_exp_f32 v73, v229\n"
    "v_mfma_f32_16x16x32_bf16 a[124:127], a[220:223], v[12:15], a[124:127]\n"
    "v_add_u32 v110, s77, v115\n"
    "v_cvt_pk_bf16_f32 v37, v78, v79\n"
    "v_mfma_f32_16x16x32_bf16 a[92:95], a[252:255], v[16:19], a[92:95]\n"
    "v_exp_f32 v74, v230\n"
    "v_mfma_f32_16x16x32_bf16 a[124:127], a[252:255], v[20:23], a[124:127]\n"
    "v_exp_f32 v75, v231\n"
    "v_mfma_f32_16x16x32_bf16 v[96:99], v[80:83], v[20:23], v[96:99]\n"
    "v_cvt_pk_bf16_f32 v26, v72, v73\n"
    "v_or3_b32 v108, v108, v36, v37\n"
    "v_exp_f32 v76, v236\n"
    "s_waitcnt lgkmcnt(0)\n"
    "v_mfma_f32_16x16x32_bf16 v[192:195], a[128:131], v[160:163], v[40:43]\n"
    "s_waitcnt vmcnt(12)\n"
    "s_barrier\n"
    "v_mfma_f32_16x16x32_bf16 v[200:203], a[128:131], v[176:179], v[48:51]\n"
    "ds_read_b128 a[192:195], v110\n"
    "buffer_load_dwordx4 v2, s[24:27], s75 offen sc1 lds\n"
    "v_mfma_f32_16x16x32_bf16 v[208:211], a[160:163], v[160:163], v[56:59]\n"
    "v_exp_f32 v77, v237\n"
    "v_mfma_f32_16x16x32_bf16 v[216:219], a[160:163], v[176:179], v[64:67]\n"
    "ds_read_b128 a[224:227], v110 offset:8192\n"
    "v_cvt_pk_bf16_f32 v27, v74, v75\n"
    "v_mfma_f32_16x16x32_bf16 v[196:199], a[144:147], v[160:163], v[44:47]\n"
    "v_exp_f32 v78, v238\n"
    "v_mfma_f32_16x16x32_bf16 v[204:207], a[144:147], v[176:179], v[52:55]\n"
    "buffer_load_dwordx4 v2, s[24:27], s75 offen offset:1024 sc1 lds\n"
    "ds_read_b128 a[196:199], v110 offset:1024\n"
    "v_mfma_f32_16x16x32_bf16 v[212:215], a[176:179], v[160:163], v[60:63]\n"
    "v_exp_f32 v79, v239\n"
    "v_mfma_f32_16x16x32_bf16 v[220:223], a[176:179], v[176:179], v[68:71]\n"
    "ds_read_b128 a[228:231], v110 offset:9216\n"
    "buffer_load_dwordx4 v2, s[24:27], s75 offen offset:2048 sc1 lds\n"
    "v_mfma_f32_16x16x32_bf16 v[192:195], a[132:135], v[164:167], v[192:195]\n"
    "ds_read_b128 a[200:203], v110 offset:2048\n"
    "v_cvt_pk_bf16_f32 v30, v76, v77\n"
    "v_mfma_f32_16x16x32_bf16 v[200:203], a[132:135], v[180:183], v[200:203]\n"
    "buffer_load_dwordx4 v2, s[24:27], s75 offen offset:3072 sc1 lds\n"
    "v_or3_b32 v108, v108, v26, v27\n"
    "v_mfma_f32_16x16x32_bf16 v[208:211], a[164:167], v[164:167], v[208:211]\n"
    "v_exp_f32 v72, v244\n"
    "v_mfma_f32_16x16x32_bf16 v[216:219], a[164:167], v[180:183], v[216:219]\n"
    "ds_read_b128 a[232:235], v110 offset:10240\n"
    "ds_read_b128 a[204:207], v110 offset:3072\n"
    "v_mfma_f32_16x16x32_bf16 v[196:199], a[148:151], v[164:167], v[196:199]\n"
    "v_exp_f32 v73, v245\n"
    "v_mfma_f32_16x16x32_bf16 v[204:207], a[148:151], v[180:183], v[204:207]\n"
    "ds_read_b128 a[236:239], v110 offset:11264\n"
    "s_mov_b32 m0, s76\n"
    "v_mfma_f32_16x16x32_bf16 v[212:215], a[180:183], v[164:167], v[212:215]\n"
    "ds_read_b128 a[208:211], v110 offset:4096\n"
    "v_cvt_pk_bf16_f32 v31, v78, v79\n"
    "v_mfma_f32_16x16x32_bf16 v[220:223], a[180:183], v[180:183], v[220:223]\n"
    "v_exp_f32 v74, v246\n"
    "v_mfma_f32_16x16x32_bf16 v[192:195], a[136:139], v[168:171], v[192:195]\n"
    "buffer_load_dwordx4 v114, s[24:27], s75 offen sc1 lds\n"
    "ds_read_b128 a[240:243], v110 offset:12288\n"
    "v_mfma_f32_16x16x32_bf16 v[200:203], a[136:139], v[184:187], v[200:203]\n"
    "v_exp_f32 v75, v247\n"
    "v_mfma_f32_16x16x32_bf16 v[208:211], a[168:171], v[168:171], v[208:211]\n"
    "buffer_load_dwordx4 v114, s[24:27], s75 offen offset:1024 sc1 lds\n"
    "ds_read_b128 a[212:215], v110 offset:5120\n"
    "v_mfma_f32_16x16x32_bf16 v[216:219], a[168:171], v[184:187], v[216:219]\n"
    "v_cvt_pk_bf16_f32 v34, v72, v73\n"
    "v_or3_b32 v108, v108, v30, v31\n"
    "v_mfma_f32_16x16x32_bf16 v[196:199], a[152:155], v[168:171], v[196:199]\n"
    "ds_read_b128 a[244:247], v110 offset:13312\n"
    "buffer_load_dwordx4 v114, s[24:27], s75 offen offset:2048 sc1 lds\n"
    "v_mfma_f32_16x16x32_bf16 v[204:207], a[152:155], v[184:187], v[204:207]\n"
    "v_exp_f32 v76, v252\n"
    "v_mfma_f32_16x16x32_bf16 v[212:215], a[184:187], v[168:171], v[212:215]\n"
    "v_exp_f32 v77, v253\n"
    "v_mfma_f32_16x16x32_bf16 v[220:223], a[184:187], v[184:187], v[220:223]\n"
    "ds_read_b128 a[216:219], v110 offset:6144\n"
    "ds_read_b128 a[248:251], v110 offset:14336\n"
    "v_mfma_f32_16x16x32_bf16 v[192:195], a[140:143], v[172:175], v[192:195]\n"
    "buffer_load_dwordx4 v114, s[24:27], s75 offen offset:3072 sc1 lds\n"
    "v_cvt_pk_bf16_f32 v35, v74, v75\n"
    "v_mfma_f32_16x16x32_bf16 v[200:203], a[140:143], v[188:191], v[200:203]\n"
    "v_exp_f32 v78, v254\n"
    "ds_read_b128 a[220:223], v110 offset:7168\n"
    "ds_read_b128 a[252:255], v110 offset:15360\n"
    "v_mfma_f32_16x16x32_bf16 v[208:211], a[172:175], v[172:175], v[208:211]\n"
    "v_exp_f32 v79, v255\n"
    "v_mfma_f32_16x16x32_bf16 v[216:219], a[172:175], v[188:191], v[216:219]\n"
    "s_add_u32 s76, s76, 0x8000\n"
    "v_cvt_pk_bf16_f32 v38, v76, v77\n"
    "v_mfma_f32_16x16x32_bf16 v[196:199], a[156:159], v[172:175], v[196:199]\n"
    "s_and_b32 s76, s76, 0x1ffff\n"
    "v_or3_b32 v108, v108, v34, v35\n"
    "v_mfma_f32_16x16x32_bf16 v[204:207], a[156:159], v[188:191], v[204:207]\n"
    "v_cvt_pk_bf16_f32 v39, v78, v79\n"
    "v_or3_b32 v108, v108, v38, v39\n"
    "v_mfma_f32_16x16x32_bf16 v[212:215], a[188:191], v[172:175], v[212:215]\n"
    "v_add_u32 v109, s76, v115\n"
    "v_and_b32 v3, 0x40004000, v108\n"
    "v_cmp_ne_u32 vcc, 0, v3\n"
    "v_mfma_f32_16x16x32_bf16 v[220:223], a[188:191], v[188:191], v[220:223]\n"
    "s_cbranch_vccnz Lrs10\n"
    "Lrr10:\n"
    "s_waitcnt lgkmcnt(0)\n"
    "v_mfma_f32_16x16x32_bf16 a[64:67], a[192:195], v[24:27], a[64:67]\n"
    "ds_read_b128 a[128:131], v109\n"
    "s_add_u32 s75, s75, 0x8000\n"
    "v_mfma_f32_16x16x32_bf16 a[96:99], a[192:195], v[28:31], a[96:99]\n"
    "ds_read_b128 a[160:163], v109 offset:8192\n"
    "ds_read_b128 a[144:147], v109 offset:4096\n"
    "v_mfma_f32_16x16x32_bf16 a[64:67], a[224:227], v[32:35], a[64:67]\n"
    "v_exp_f32 v72, v192\n"
    "v_mfma_f32_16x16x32_bf16 a[96:99], a[224:227], v[36:39], a[96:99]\n"
    "v_exp_f32 v73, v193\n"
    "v_mfma_f32_16x16x32_bf16 a[68:71], a[196:199], v[24:27], a[68:71]\n"
    "v_exp_f32 v74, v194\n"
    "v_mfma_f32_16x16x32_bf16 a[100:103], a[196:199], v[28:31], a[100:103]\n"
    "ds_read_b128 a[176:179], v109 offset:12288\n"
    "s_mov_b32 m0, s77\n"
    "v_mfma_f32_16x16x32_bf16 a[68:71], a[228:231], v[32:35], a[68:71]\n"
    "v_exp_f32 v75, v195\n"
    "v_mfma_f32_16x16x32_bf16 a[100:103], a[228:231], v[36:39], a[100:103]\n"
    "ds_read_b128 a[132:135], v109 offset:1024\n"
    "v_cvt_pk_bf16_f32 v8, v72, v73\n"
    "v_mfma_f32_16x16x32_bf16 v[92:95], v[80:83], v[24:27], v[92:95]\n"
    "v_exp_f32 v76, v200\n"
    "v_mfma_f32_16x16x32_bf16 a[72:75], a[200:203], v[24:27], a[72:75]\n"
    "v_exp_f32 v77, v201\n"
    "v_mfma_f32_16x16x32_bf16 a[104:107], a[200:203], v[28:31], a[104:107]\n"
    "ds_read_b128 a[164:167], v109 offset:9216\n"
    "v_cvt_pk_bf16_f32 v9, v74, v75\n"
    "v_mfma_f32_16x16x32_bf16 a[72:75], a[232:235], v[32:35], a[72:75]\n"
    "ds_read_b128 a[148:151], v109 offset:5120\n"
    "s_add_u32 s77, s77, 0x8000\n"
    "v_mfma_f32_16x16x32_bf16 a[104:107], a[232:235], v[36:39], a[104:107]\n"
    "v_exp_f32 v78, v202\n"
    "v_mfma_f32_16x16x32_bf16 a[76:79], a[204:207], v[24:27], a[76:79]\n"
    "v_exp_f32 v79, v203\n"
    "v_mfma_f32_16x16x32_bf16 a[108:111], a[204:207], v[28:31], a[108:111]\n"
    "ds_read_b128 a[180:183], v109 offset:13312\n"
    "v_cvt_pk_bf16_f32 v12, v76, v77\n"
    "v_mfma_f32_16x16x32_bf16 a[76:79], a[236:239], v[32:35], a[76:79]\n"
    "ds_read_b128 a[136:139], v109 offset:2048\n"
    "v_or_b32 v108, v8, v9\n"
    "v_mfma_f32_16x16x32_bf16 a[108:111], a[236:239], v[36:39], a[108:111]\n"
    "v_exp_f32 v72, v208\n"
    "v_mfma_f32_16x16x32_bf16 v[96:99], v[80:83], v[28:31], v[96:99]\n"
    "v_exp_f32 v73, v209\n"
    "v_mfma_f32_16x16x32_bf16 a[80:83], a[208:211], v[24:27], a[80:83]\n"
    "ds_read_b128 a[168:171], v109 offset:10240\n"
    "s_and_b32 s77, s77, 0x1ffff\n"
    "v_mfma_f32_16x16x32_bf16 a[112:115], a[208:211], v[28:31], a[112:115]\n"
    "ds_read_b128 a[152:155], v109 offset:6144\n"
    "v_cvt_pk_bf16_f32 v13, v78, v79\n"
    "v_mfma_f32_16x16x32_bf16 a[80:83], a[240:243], v[32:35], a[80:83]\n"
    "v_exp_f32 v74, v210\n"
    "v_mfma_f32_16x16x32_bf16 a[112:115], a[240:243], v[36:39], a[112:115]\n"
    "v_exp_f32 v75, v211\n"
    "v_mfma_f32_16x16x32_bf16 a[84:87], a[212:215], v[24:27], a[84:87]\n"
    "ds_read_b128 a[184:187], v109 offset:14336\n"
    "v_cvt_pk_bf16_f32 v16, v72, v73\n"
    "v_mfma_f32_16x16x32_bf16 a[116:119], a[212:215], v[28:31], a[116:119]\n"
    "v_or3_b32 v108, v108, v12, v13\n"
    "v_add_u32 v110, s77, v115\n"
    "v_mfma_f32_16x16x32_bf16 a[84:87], a[244:247], v[32:35], a[84:87]\n"
    "v_exp_f32 v76, v216\n"
    "v_mfma_f32_16x16x32_bf16 a[116:119], a[244:247], v[36:39], a[116:119]\n"
    "ds_read_b128 a[140:143], v109 offset:3072\n"
    "ds_read_b128 a[172:175], v109 offset:11264\n"
    "v_mfma_f32_16x16x32_bf16 v[92:95], v[80:83], v[32:35], v[92:95]\n"
    "v_exp_f32 v77, v217\n"
    "v_mfma_f32_16x16x32_bf16 a[88:91], a[216:219], v[24:27], a[88:91]\n"
    "ds_read_b128 a[156:159], v109 offset:7168\n"
    "v_cvt_pk_bf16_f32 v17, v74, v75\n"
    "v_mfma_f32_16x16x32_bf16 a[120:123], a[216:219], v[28:31], a[120:123]\n"
    "v_exp_f32 v78, v218\n"
    "ds_read_b128 a[188:191], v109 offset:15360\n"
    "v_mfma_f32_16x16x32_bf16 a[88:91], a[248:251], v[32:35], a[88:91]\n"
    "v_exp_f32 v79, v219\n"
    "v_mfma_f32_16x16x32_bf16 a[120:123], a[248:251], v[36:39], a[120:123]\n"
    "s_sub_u32 s78, s78, 1\n"
    "v_cvt_pk_bf16_f32 v20, v76, v77\n"
    "v_mfma_f32_16x16x32_bf16 a[92:95], a[220:223], v[24:27], a[92:95]\n"
    "v_or3_b32 v108, v108, v16, v17\n"
    "s_cmp_lg_u32 s78, 0\n"
    "v_mfma_f32_16x16x32_bf16 a[124:127], a[220:223], v[28:31], a[124:127]\n"
    "v_exp_f32 v72, v196\n"
    "v_mfma_f32_16x16x32_bf16 a[92:95], a[252:255], v[32:35], a[92:95]\n"
    "v_exp_f32 v73, v197\n"
    "v_mfma_f32_16x16x32_bf16 a[124:127], a[252:255], v[36:39], a[124:127]\n"
    "v_cvt_pk_bf16_f32 v21, v78, v79\n"
    "v_mfma_f32_16x16x32_bf16 v[96:99], v[80:83], v[36:39], v[96:99]\n"
    "v_exp_f32 v74, v198\n"
    "v_exp_f32 v75, v199\n"
    "v_cvt_pk_bf16_f32 v10, v72, v73\n"
    "v_or3_b32 v108, v108, v20, v21\n"
    "v_exp_f32 v76, v204\n"
    "s_cbranch_scc1 LloopH\n"
    "LloopHe:\n"
    "s_waitcnt vmcnt(16)\n"
    "s_lshl_b32 s4, s57, 2\n"
    "s_add_u32 s4, s4, s58\n"
    "s_lshl_b32 s4, s4, 5\n"
    "s_add_u32 s4, s4, s56\n"
    "s_lshl_b32 s4, s4, 2\n"
    "s_add_u32 s4, s4, 0x5000\n"
    "v_mov_b32 v6, 0\n"
    "s_mov_b64 exec, 1\n"
    "buffer_store_dword v7, v6, s[8:11], s4 offen sc1\n"
    "s_mov_b64 exec, -1\n"
    "s_waitcnt lgkmcnt(0)\n"
    "v_mfma_f32_16x16x32_bf16 v[224:227], a[128:131], v[160:163], v[40:43]\n"
    "s_waitcnt vmcnt(12)\n"
    "s_barrier\n"
    "v_mfma_f32_16x16x32_bf16 v[232:235], a[128:131], v[176:179], v[48:51]\n"
    "ds_read_b128 a[192:195], v110\n"
    "buffer_load_dwordx4 v2, s[24:27], s75 offen sc1 lds\n"
    "v_mfma_f32_16x16x32_bf16 v[240:243], a[160:163], v[160:163], v[56:59]\n"
    "v_exp_f32 v77, v205\n"
    "v_mfma_f32_16x16x32_bf16 v[248:251], a[160:163], v[176:179], v[64:67]\n"
    "buffer_load_dwordx4 v2, s[24:27], s75 offen offset:1024 sc1 lds\n"
    "ds_read_b128 a[224:227], v110 offset:8192\n"
    "v_mfma_f32_16x16x32_bf16 v[228:231], a[144:147], v[160:163], v[44:47]\n"
    "buffer_load_dwordx4 v2, s[24:27], s75 offen offset:2048 sc1 lds\n"
    "v_cvt_pk_bf16_f32 v11, v74, v75\n"
    "v_mfma_f32_16x16x32_bf16 v[236:239], a[144:147], v[176:179], v[52:55]\n"
    "v_exp_f32 v78, v206\n"
    "v_mfma_f32_16x16x32_bf16 v[244:247], a[176:179], v[160:163], v[60:63]\n"
    "ds_read_b128 a[196:199], v110 offset:1024\n"
    "buffer_load_dwordx4 v2, s[24:27], s75 offen offset:3072 sc1 lds\n"
    "v_mfma_f32_16x16x32_bf16 v[252:255], a[176:179], v[176:179], v[68:71]\n"
    "ds_read_b128 a[228:231], v110 offset:9216\n"
    "s_add_u32 s76, s76, 0x8000\n"
    "v_mfma_f32_16x16x32_bf16 v[224:227], a[132:135], v[164:167], v[224:227]\n"
    "v_exp_f32 v79, v207\n"
    "v_mfma_f32_16x16x32_bf16 v[232:235], a[132:135], v[180:183], v[232:235]\n"
    "ds_read_b128 a[200:203], v110 offset:2048\n"
    "s_and_b32 s76, s76, 0x1ffff\n"
    "v_mfma_f32_16x16x32_bf16 v[240:243], a[164:167], v[164:167], v[240:243]\n"
    "v_add_u32 v109, s76, v115\n"
    "v_cvt_pk_bf16_f32 v14, v76, v77\n"
    "v_mfma_f32_16x16x32_bf16 v[248:251], a[164:167], v[180:183], v[248:251]\n"
    "v_and_b32 v5, 7, v1\n"
    "v_or3_b32 v108, v108, v10, v11\n"
    "v_mfma_f32_16x16x32_bf16 v[228:231], a[148:151], v[164:167], v[228:231]\n"
    "ds_read_b128 a[232:235], v110 offset:10240\n"
    "v_lshlrev_b32 v5, 2, v5\n"
    "v_mfma_f32_16x16x32_bf16 v[236:239], a[148:151], v[180:183], v[236:239]\n"
    "v_exp_f32 v72, v212\n"
    "v_mfma_f32_16x16x32_bf16 v[244:247], a[180:183], v[164:167], v[244:247]\n"
    "ds_read_b128 a[204:207], v110 offset:3072\n"
    "s_add_u32 s7, s68, 0x3000\n"
    "v_mfma_f32_16x16x32_bf16 v[252:255], a[180:183], v[180:183], v[252:255]\n"
    "v_exp_f32 v73, v213\n"
    "v_mfma_f32_16x16x32_bf16 v[224:227], a[136:139], v[168:171], v[224:227]\n"
    "buffer_load_dword v122, v5, s[8:11], s7 offen sc1\n"
    "ds_read_b128 a[236:239], v110 offset:11264\n"
    "v_mfma_f32_16x16x32_bf16 v[232:235], a[136:139], v[184:187], v[232:235]\n"
    "s_add_u32 s70, s67, 0x2000\n"
    "v_cvt_pk_bf16_f32 v15, v78, v79\n"
    "v_mfma_f32_16x16x32_bf16 v[240:243], a[168:171], v[168:171], v[240:243]\n"
    "ds_read_b128 a[208:211], v110 offset:4096\n"
    "s_add_u32 m0, s59, 0x20000\n"
    "v_mfma_f32_16x16x32_bf16 v[248:251], a[168:171], v[184:187], v[248:251]\n"
    "v_exp_f32 v74, v214\n"
    "v_mfma_f32_16x16x32_bf16 v[228:231], a[152:155], v[168:171], v[228:231]\n"
    "buffer_load_dwordx4 v121, s[16:19], s67 offen lds\n"
    "ds_read_b128 a[240:243], v110 offset:12288\n"
    "v_mfma_f32_16x16x32_bf16 v[236:239], a[152:155], v[184:187], v[236:239]\n"
    "v_exp_f32 v75, v215\n"
    "v_mfma_f32_16x16x32_bf16 v[244:247], a[184:187], v[168:171], v[244:247]\n"
    "ds_read_b128 a[212:215], v110 offset:5120\n"
    "s_add_u32 m0, s59, 0x20400\n"
    "v_mfma_f32_16x16x32_bf16 v[252:255], a[184:187], v[184:187], v[252:255]\n"
    "buffer_load_dwordx4 v123, s[16:19], s67 offen lds\n"
    "v_cvt_pk_bf16_f32 v18, v72, v73\n"
    "v_mfma_f32_16x16x32_bf16 v[224:227], a[140:143], v[172:175], v[224:227]\n"
    "ds_read_b128 a[244:247], v110 offset:13312\n"
    "s_add_u32 m0, s59, 0x20800\n"
    "v_mfma_f32_16x16x32_bf16 v[232:235], a[140:143], v[188:191], v[232:235]\n"
    "buffer_load_dwordx4 v121, s[16:19], s70 offen lds\n"
    "v_or3_b32 v108, v108, v14, v15\n"
    "ds_read_b128 a[216:219], v110 offset:6144\n"
    "ds_read_b128 a[248:251], v110 offset:14336\n"
    "ds_read_b128 a[220:223], v110 offset:7168\n"
    "ds_read_b128 a[252:255], v110 offset:15360\n"
    "v_mfma_f32_16x16x32_bf16 v[240:243], a[172:175], v[172:175], v[240:243]\n"
    "v_exp_f32 v76, v220\n"
    "v_mfma_f32_16x16x32_bf16 v[248:251], a[172:175], v[188:191], v[248:251]\n"
    "v_exp_f32 v77, v221\n"
    "v_mfma_f32_16x16x32_bf16 v[228:231], a[156:159], v[172:175], v[228:231]\n"
    "s_add_u32 m0, s59, 0x20c00\n"
    "v_cvt_pk_bf16_f32 v19, v74, v75\n"
    "v_mfma_f32_16x16x32_bf16 v[236:239], a[156:159], v[188:191], v[236:239]\n"
    "buffer_load_dwordx4 v123, s[16:19], s70 offen lds\n"
    "s_add_u32 s67, s67, 0x10000\n"
    "v_mfma_f32_16x16x32_bf16 v[244:247], a[188:191], v[172:175], v[244:247]\n"
    "s_add_u32 s70, s67, 0x2000\n"
    "s_add_u32 m0, s59, 0x24000\n"
    "v_exp_f32 v78, v222\n"
    "v_exp_f32 v79, v223\n"
    "v_cvt_pk_bf16_f32 v22, v76, v77\n"
    "v_or3_b32 v108, v108, v18, v19\n"
    "v_cvt_pk_bf16_f32 v23, v78, v79\n"
    "v_or3_b32 v108, v108, v22, v23\n"
    "v_and_b32 v3, 0x40004000, v108\n"
    "v_cmp_ne_u32 vcc, 0, v3\n"
    "buffer_load_dwordx4 v121, s[16:19], s67 offen lds\n"
    "s_add_u32 m0, s59, 0x24400\n"
    "s_nop 0\n"
    "buffer_load_dwordx4 v123, s[16:19], s67 offen lds\n"
    "s_add_u32 m0, s59, 0x24800\n"
    "s_nop 0\n"
    "buffer_load_dwordx4 v121, s[16:19], s70 offen lds\n"
    "s_add_u32 m0, s59, 0x24c00\n"
    "s_nop 0\n"
    "buffer_load_dwordx4 v123, s[16:19], s70 offen lds\n"
    "s_add_u32 s67, s67, 0x10000\n"
    "v_mfma_f32_16x16x32_bf16 v[252:255], a[188:191], v[188:191], v[252:255]\n"
    "s_cbranch_vccnz Lrs11\n"
    "Lrr11:\n"
    "s_waitcnt lgkmcnt(0)\n"
    "v_mfma_f32_16x16x32_bf16 a[64:67], a[192:195], v[8:11], a[64:67]\n"
    "ds_read_b128 a[128:131], v109\n"
    "s_add_u32 s75, s75, 0x8000\n"
    "v_mfma_f32_16x16x32_bf16 a[96:99], a[192:195], v[12:15], a[96:99]\n"
    "ds_read_b128 a[160:163], v109 offset:8192\n"
    "v_mov_b32 v112, v111\n"
    "v_mfma_f32_16x16x32_bf16 a[64:67], a[224:227], v[16:19], a[64:67]\n"
    "v_exp_f32 v72, v224\n"
    "v_mfma_f32_16x16x32_bf16 a[96:99], a[224:227], v[20:23], a[96:99]\n"
    "v_cmp_gt_i32 s[84:85], 0, v112\n"
    "v_cmp_gt_i32 s[86:87], 1, v112\n"
    "v_mfma_f32_16x16x32_bf16 a[68:71], a[196:199], v[8:11], a[68:71]\n"
    "v_exp_f32 v73, v225\n"
    "v_mfma_f32_16x16x32_bf16 a[100:103], a[196:199], v[12:15], a[100:103]\n"
    "ds_read_b128 a[144:147], v109 offset:4096\n"
    "v_cmp_gt_i32 s[92:93], 2, v112\n"
    "v_mfma_f32_16x16x32_bf16 a[68:71], a[228:231], v[16:19], a[68:71]\n"
    "v_exp_f32 v74, v226\n"
    "v_mfma_f32_16x16x32_bf16 a[100:103], a[228:231], v[20:23], a[100:103]\n"
    "ds_read_b128 a[176:179], v109 offset:12288\n"
    "v_cndmask_b32 v40, v40, v113, s[84:85]\n"
    "v_mfma_f32_16x16x32_bf16 v[92:95], v[80:83], v[8:11], v[92:95]\n"
    "v_exp_f32 v75, v227\n"
    "v_mfma_f32_16x16x32_bf16 a[72:75], a[200:203], v[8:11], a[72:75]\n"
    "v_cmp_gt_i32 s[94:95], 3, v112\n"
    "v_cndmask_b32 v41, v41, v113, s[86:87]\n"
    "v_mfma_f32_16x16x32_bf16 a[104:107], a[200:203], v[12:15], a[104:107]\n"
    "ds_read_b128 a[132:135], v109 offset:1024\n"
    "v_cvt_pk_bf16_f32 v24, v72, v73\n"
    "v_mfma_f32_16x16x32_bf16 a[72:75], a[232:235], v[16:19], a[72:75]\n"
    "v_exp_f32 v76, v232\n"
    "v_mfma_f32_16x16x32_bf16 a[104:107], a[232:235], v[20:23], a[104:107]\n"
    "v_cmp_gt_i32 s[84:85], 16, v112\n"
    "v_cndmask_b32 v42, v42, v113, s[92:93]\n"
    "v_mfma_f32_16x16x32_bf16 a[76:79], a[204:207], v[8:11], a[76:79]\n"
    "ds_read_b128 a[164:167], v109 offset:9216\n"
    "v_cmp_gt_i32 s[86:87], 17, v112\n"
    "v_mfma_f32_16x16x32_bf16 a[108:111], a[204:207], v[12:15], a[108:111]\n"
    "v_exp_f32 v77, v233\n"
    "v_mfma_f32_16x16x32_bf16 a[76:79], a[236:239], v[16:19], a[76:79]\n"
    "ds_read_b128 a[148:151], v109 offset:5120\n"
    "v_cvt_pk_bf16_f32 v25, v74, v75\n"
    "v_mfma_f32_16x16x32_bf16 a[108:111], a[236:239], v[20:23], a[108:111]\n"
    "v_exp_f32 v78, v234\n"
    "v_mfma_f32_16x16x32_bf16 v[96:99], v[80:83], v[12:15], v[96:99]\n"
    "v_cndmask_b32 v43, v43, v113, s[94:95]\n"
    "v_cmp_gt_i32 s[92:93], 18, v112\n"
    "v_mfma_f32_16x16x32_bf16 a[80:83], a[208:211], v[8:11], a[80:83]\n"
    "ds_read_b128 a[180:183], v109 offset:13312\n"
    "v_cndmask_b32 v44, v44, v113, s[84:85]\n"
    "v_mfma_f32_16x16x32_bf16 a[112:115], a[208:211], v[12:15], a[112:115]\n"
    "v_exp_f32 v79, v235\n"
    "v_mfma_f32_16x16x32_bf16 a[80:83], a[240:243], v[16:19], a[80:83]\n"
    "v_cmp_gt_i32 s[94:95], 19, v112\n"
    "v_cvt_pk_bf16_f32 v28, v76, v77\n"
    "v_mfma_f32_16x16x32_bf16 a[112:115], a[240:243], v[20:23], a[112:115]\n"
    "ds_read_b128 a[136:139], v109 offset:2048\n"
    "v_or_b32 v108, v24, v25\n"
    "v_mfma_f32_16x16x32_bf16 a[84:87], a[212:215], v[8:11], a[84:87]\n"
    "v_exp_f32 v72, v240\n"
    "v_mfma_f32_16x16x32_bf16 a[116:119], a[212:215], v[12:15], a[116:119]\n"
    "v_cndmask_b32 v45, v45, v113, s[86:87]\n"
    "v_cndmask_b32 v46, v46, v113, s[92:93]\n"
    "v_mfma_f32_16x16x32_bf16 a[84:87], a[244:247], v[16:19], a[84:87]\n"
    "ds_read_b128 a[168:171], v109 offset:10240\n"
    "v_cndmask_b32 v47, v47, v113, s[94:95]\n"
    "v_mfma_f32_16x16x32_bf16 a[116:119], a[244:247], v[20:23], a[116:119]\n"
    "v_exp_f32 v73, v241\n"
    "v_mfma_f32_16x16x32_bf16 v[92:95], v[80:83], v[16:19], v[92:95]\n"
    "ds_read_b128 a[152:155], v109 offset:6144\n"
    "v_subrev_u32 v112, 32, v111\n"
    "v_mfma_f32_16x16x32_bf16 a[88:91], a[216:219], v[8:11], a[88:91]\n"
    "v_cvt_pk_bf16_f32 v29, v78, v79\n"
    "v_cmp_gt_i32 s[84:85], 0, v112\n"
    "v_mfma_f32_16x16x32_bf16 a[120:123], a[216:219], v[12:15], a[120:123]\n"
    "v_exp_f32 v74, v242\n"
    "ds_read_b128 a[184:187], v109 offset:14336\n"
    "ds_read_b128 a[140:143], v109 offset:3072\n"
    "ds_read_b128 a[172:175], v109 offset:11264\n"
    "ds_read_b128 a[156:159], v109 offset:7168\n"
    "ds_read_b128 a[188:191], v109 offset:15360\n"
    "v_mfma_f32_16x16x32_bf16 a[88:91], a[248:251], v[16:19], a[88:91]\n"
    "v_exp_f32 v75, v243\n"
    "v_mfma_f32_16x16x32_bf16 a[120:123], a[248:251], v[20:23], a[120:123]\n"
    "v_cmp_gt_i32 s[86:87], 1, v112\n"
    "v_cmp_gt_i32 s[92:93], 2, v112\n"
    "v_mfma_f32_16x16x32_bf16 a[92:95], a[220:223], v[8:11], a[92:95]\n"
    "v_cvt_pk_bf16_f32 v32, v72, v73\n"
    "v_cndmask_b32 v56, v56, v113, s[84:85]\n"
    "v_mfma_f32_16x16x32_bf16 a[124:127], a[220:223], v[12:15], a[124:127]\n"
    "v_or3_b32 v108, v108, v28, v29\n"
    "v_cmp_gt_i32 s[94:95], 3, v112\n"
    "v_mfma_f32_16x16x32_bf16 a[92:95], a[252:255], v[16:19], a[92:95]\n"
    "v_exp_f32 v76, v248\n"
    "v_mfma_f32_16x16x32_bf16 a[124:127], a[252:255], v[20:23], a[124:127]\n"
    "v_exp_f32 v77, v249\n"
    "v_mfma_f32_16x16x32_bf16 v[96:99], v[80:83], v[20:23], v[96:99]\n"
    "v_cndmask_b32 v57, v57, v113, s[86:87]\n"
    "v_cmp_gt_i32 s[84:85], 16, v112\n"
    "v_cvt_pk_bf16_f32 v33, v74, v75\n"
    "v_exp_f32 v78, v250\n"
    "v_exp_f32 v79, v251\n"
    "v_cvt_pk_bf16_f32 v36, v76, v77\n"
    "v_or3_b32 v108, v108, v32, v33\n"
    "v_exp_f32 v72, v228\n"
    "v_exp_f32 v73, v229\n"
    "v_cvt_pk_bf16_f32 v37, v78, v79\n"
    "v_exp_f32 v74, v230\n"
    "v_exp_f32 v75, v231\n"
    "v_cvt_pk_bf16_f32 v26, v72, v73\n"
    "v_or3_b32 v108, v108, v36, v37\n"
    "v_exp_f32 v76, v236\n"
    "v_cndmask_b32 v58, v58, v113, s[92:93]\n"
    "v_cmp_gt_i32 s[86:87], 17, v112\n"
    "v_cndmask_b32 v59, v59, v113, s[94:95]\n"
    "v_cmp_gt_i32 s[92:93], 18, v112\n"
    "v_cndmask_b32 v60, v60, v113, s[84:85]\n"
    "v_cmp_gt_i32 s[94:95], 19, v112\n"
    "v_cndmask_b32 v61, v61, v113, s[86:87]\n"
    "v_cndmask_b32 v62, v62, v113, s[92:93]\n"
    "v_cndmask_b32 v63, v63, v113, s[94:95]\n"
    "s_waitcnt vmcnt(8)\n"
    "v_cmp_ne_u32 vcc, v122, v7\n"
    "s_nop 0\n"
    "s_cbranch_vccnz Lspin4\n"
    "Lspinret4:\n"
    "s_add_u32 s77, s77, 0x8000\n"
    "s_and_b32 s77, s77, 0x1ffff\n"
    "v_add_u32 v110, s77, v115\n"
    "s_waitcnt lgkmcnt(0)\n"
    "v_mfma_f32_16x16x32_bf16 v[192:195], a[128:131], v[160:163], v[40:43]\n"
    "s_waitcnt vmcnt(12)\n"
    "s_barrier\n"
    "v_mfma_f32_16x16x32_bf16 v[200:203], a[128:131], v[176:179], v[48:51]\n"
    "ds_read_b128 a[192:195], v110\n"
    "s_add_u32 s76, s76, 0x8000\n"
    "v_mfma_f32_16x16x32_bf16 v[208:211], a[160:163], v[160:163], v[56:59]\n"
    "v_exp_f32 v77, v237\n"
    "v_mfma_f32_16x16x32_bf16 v[216:219], a[160:163], v[176:179], v[64:67]\n"
    "ds_read_b128 a[224:227], v110 offset:8192\n"
    "v_cvt_pk_bf16_f32 v27, v74, v75\n"
    "v_mfma_f32_16x16x32_bf16 v[196:199], a[144:147], v[160:163], v[44:47]\n"
    "ds_read_b128 a[196:199], v110 offset:1024\n"
    "s_and_b32 s76, s76, 0x1ffff\n"
    "v_mfma_f32_16x16x32_bf16 v[204:207], a[144:147], v[176:179], v[52:55]\n"
    "v_exp_f32 v78, v238\n"
    "v_mfma_f32_16x16x32_bf16 v[212:215], a[176:179], v[160:163], v[60:63]\n"
    "ds_read_b128 a[228:231], v110 offset:9216\n"
    "v_add_u32 v109, s76, v115\n"
    "v_mfma_f32_16x16x32_bf16 v[220:223], a[176:179], v[176:179], v[68:71]\n"
    "v_exp_f32 v79, v239\n"
    "v_mfma_f32_16x16x32_bf16 v[192:195], a[132:135], v[164:167], v[192:195]\n"
    "buffer_load_dwordx4 v[128:131], v2, s[8:11], s66 offen sc1\n"
    "ds_read_b128 a[200:203], v110 offset:2048\n"
    "v_mfma_f32_16x16x32_bf16 v[200:203], a[132:135], v[180:183], v[200:203]\n"
    "v_cvt_pk_bf16_f32 v30, v76, v77\n"
    "v_or3_b32 v108, v108, v26, v27\n"
    "v_mfma_f32_16x16x32_bf16 v[208:211], a[164:167], v[164:167], v[208:211]\n"
    "ds_read_b128 a[232:235], v110 offset:10240\n"
    "buffer_load_dwordx4 v[132:135], v120, s[8:11], s66 offen sc1\n"
    "v_mfma_f32_16x16x32_bf16 v[216:219], a[164:167], v[180:183], v[216:219]\n"
    "v_exp_f32 v72, v244\n"
    "v_mfma_f32_16x16x32_bf16 v[196:199], a[148:151], v[164:167], v[196:199]\n"
    "ds_read_b128 a[204:207], v110 offset:3072\n"
    "buffer_load_dwordx4 v[136:139], v2, s[8:11], s66 offen offset:1024 sc1\n"
    "v_mfma_f32_16x16x32_bf16 v[204:207], a[148:151], v[180:183], v[204:207]\n"
    "v_exp_f32 v73, v245\n"
    "v_mfma_f32_16x16x32_bf16 v[212:215], a[180:183], v[164:167], v[212:215]\n"
    "ds_read_b128 a[236:239], v110 offset:11264\n"
    "v_cvt_pk_bf16_f32 v31, v78, v79\n"
    "v_mfma_f32_16x16x32_bf16 v[220:223], a[180:183], v[180:183], v[220:223]\n"
    "buffer_load_dwordx4 v[140:143], v120, s[8:11], s66 offen offset:1024 sc1\n"
    "ds_read_b128 a[208:211], v110 offset:4096\n"
    "v_mfma_f32_16x16x32_bf16 v[192:195], a[136:139], v[168:171], v[192:195]\n"
    "v_exp_f32 v74, v246\n"
    "v_mfma_f32_16x16x32_bf16 v[200:203], a[136:139], v[184:187], v[200:203]\n"
    "buffer_load_dwordx4 v[144:147], v2, s[8:11], s66 offen offset:2048 sc1\n"
    "ds_read_b128 a[240:243], v110 offset:12288\n"
    "v_mfma_f32_16x16x32_bf16 v[208:211], a[168:171], v[168:171], v[208:211]\n"
    "v_exp_f32 v75, v247\n"
    "v_mfma_f32_16x16x32_bf16 v[216:219], a[168:171], v[184:187], v[216:219]\n"
    "ds_read_b128 a[212:215], v110 offset:5120\n"
    "buffer_load_dwordx4 v[148:151], v120, s[8:11], s66 offen offset:2048 sc1\n"
    "v_mfma_f32_16x16x32_bf16 v[196:199], a[152:155], v[168:171], v[196:199]\n"
    "v_cvt_pk_bf16_f32 v34, v72, v73\n"
    "v_or3_b32 v108, v108, v30, v31\n"
    "v_mfma_f32_16x16x32_bf16 v[204:207], a[152:155], v[184:187], v[204:207]\n"
    "ds_read_b128 a[244:247], v110 offset:13312\n"
    "buffer_load_dwordx4 v[152:155], v2, s[8:11], s66 offen offset:3072 sc1\n"
    "v_mfma_f32_16x16x32_bf16 v[212:215], a[184:187], v[168:171], v[212:215]\n"
    "v_exp_f32 v76, v252\n"
    "v_mfma_f32_16x16x32_bf16 v[220:223], a[184:187], v[184:187], v[220:223]\n"
    "ds_read_b128 a[216:219], v110 offset:6144\n"
    "buffer_load_dwordx4 v[156:159], v120, s[8:11], s66 offen offset:3072 sc1\n"
    "v_mfma_f32_16x16x32_bf16 v[192:195], a[140:143], v[172:175], v[192:195]\n"
    "v_exp_f32 v77, v253\n"
    "v_mfma_f32_16x16x32_bf16 v[200:203], a[140:143], v[188:191], v[200:203]\n"
    "ds_read_b128 a[248:251], v110 offset:14336\n"
    "v_cvt_pk_bf16_f32 v35, v74, v75\n"
    "ds_read_b128 a[220:223], v110 offset:7168\n"
    "ds_read_b128 a[252:255], v110 offset:15360\n"
    "v_mfma_f32_16x16x32_bf16 v[208:211], a[172:175], v[172:175], v[208:211]\n"
    "v_exp_f32 v78, v254\n"
    "v_mfma_f32_16x16x32_bf16 v[216:219], a[172:175], v[188:191], v[216:219]\n"
    "v_exp_f32 v79, v255\n"
    "v_mfma_f32_16x16x32_bf16 v[196:199], a[156:159], v[172:175], v[196:199]\n"
    "s_add_u32 s66, s66, 0x1000\n"
    "v_cvt_pk_bf16_f32 v38, v76, v77\n"
    "v_mfma_f32_16x16x32_bf16 v[204:207], a[156:159], v[188:191], v[204:207]\n"
    "buffer_load_dwordx4 a[0:3], v2, s[8:11], s66 offen sc1\n"
    "buffer_load_dwordx4 a[4:7], v120, s[8:11], s66 offen sc1\n"
    "v_mfma_f32_16x16x32_bf16 v[212:215], a[188:191], v[172:175], v[212:215]\n"
    "buffer_load_dwordx4 a[8:11], v2, s[8:11], s66 offen offset:1024 sc1\n"
    "v_or3_b32 v108, v108, v34, v35\n"
    "v_cvt_pk_bf16_f32 v39, v78, v79\n"
    "v_or3_b32 v108, v108, v38, v39\n"
    "v_and_b32 v3, 0x40004000, v108\n"
    "v_cmp_ne_u32 vcc, 0, v3\n"
    "buffer_load_dwordx4 a[12:15], v120, s[8:11], s66 offen offset:1024 sc1\n"
    "v_mfma_f32_16x16x32_bf16 v[220:223], a[188:191], v[188:191], v[220:223]\n"
    "s_cbranch_vccnz Lrs12\n"
    "Lrr12:\n"
    "s_waitcnt lgkmcnt(0)\n"
    "v_mfma_f32_16x16x32_bf16 a[64:67], a[192:195], v[24:27], a[64:67]\n"
    "ds_read_b128 a[128:131], v109\n"
    "s_add_u32 s75, s75, 0x8000\n"
    "v_mfma_f32_16x16x32_bf16 a[96:99], a[192:195], v[28:31], a[96:99]\n"
    "s_lshr_b32 s6, s62, 10\n"
    "s_sub_u32 s6, s6, 64\n"
    "v_mfma_f32_16x16x32_bf16 a[64:67], a[224:227], v[32:35], a[64:67]\n"
    "v_exp_f32 v72, v192\n"
    "v_mfma_f32_16x16x32_bf16 a[96:99], a[224:227], v[36:39], a[96:99]\n"
    "ds_read_b128 a[160:163], v109 offset:8192\n"
    "v_add_u32 v112, s6, v111\n"
    "v_mfma_f32_16x16x32_bf16 a[68:71], a[196:199], v[24:27], a[68:71]\n"
    "v_exp_f32 v73, v193\n"
    "v_mfma_f32_16x16x32_bf16 a[100:103], a[196:199], v[28:31], a[100:103]\n"
    "ds_read_b128 a[144:147], v109 offset:4096\n"
    "v_cmp_gt_i32 s[84:85], 0, v112\n"
    "v_mfma_f32_16x16x32_bf16 a[68:71], a[228:231], v[32:35], a[68:71]\n"
    "v_exp_f32 v74, v194\n"
    "v_mfma_f32_16x16x32_bf16 a[100:103], a[228:231], v[36:39], a[100:103]\n"
    "ds_read_b128 a[176:179], v109 offset:12288\n"
    "v_cmp_gt_i32 s[86:87], 1, v112\n"
    "v_mfma_f32_16x16x32_bf16 v[92:95], v[80:83], v[24:27], v[92:95]\n"
    "v_exp_f32 v75, v195\n"
    "v_mfma_f32_16x16x32_bf16 a[72:75], a[200:203], v[24:27], a[72:75]\n"
    "v_cmp_gt_i32 s[92:93], 2, v112\n"
    "v_cndmask_b32 v48, v48, v113, s[84:85]\n"
    "v_mfma_f32_16x16x32_bf16 a[104:107], a[200:203], v[28:31], a[104:107]\n"
    "ds_read_b128 a[132:135], v109 offset:1024\n"
    "v_cvt_pk_bf16_f32 v8, v72, v73\n"
    "v_mfma_f32_16x16x32_bf16 a[72:75], a[232:235], v[32:35], a[72:75]\n"
    "v_cmp_gt_i32 s[94:95], 3, v112\n"
    "v_cndmask_b32 v49, v49, v113, s[86:87]\n"
    "v_mfma_f32_16x16x32_bf16 a[104:107], a[232:235], v[36:39], a[104:107]\n"
    "v_exp_f32 v76, v200\n"
    "v_mfma_f32_16x16x32_bf16 a[76:79], a[204:207], v[24:27], a[76:79]\n"
    "ds_read_b128 a[164:167], v109 offset:9216\n"
    "v_cmp_gt_i32 s[84:85], 16, v112\n"
    "v_mfma_f32_16x16x32_bf16 a[108:111], a[204:207], v[28:31], a[108:111]\n"
    "v_exp_f32 v77, v201\n"
    "v_mfma_f32_16x16x32_bf16 a[76:79], a[236:239], v[32:35], a[76:79]\n"
    "ds_read_b128 a[148:151], v109 offset:5120\n"
    "v_cndmask_b32 v50, v50, v113, s[92:93]\n"
    "v_mfma_f32_16x16x32_bf16 a[108:111], a[236:239], v[36:39], a[108:111]\n"
    "v_cvt_pk_bf16_f32 v9, v74, v75\n"
    "v_cmp_gt_i32 s[86:87], 17, v112\n"
    "v_mfma_f32_16x16x32_bf16 v[96:99], v[80:83], v[28:31], v[96:99]\n"
    "v_exp_f32 v78, v202\n"
    "v_mfma_f32_16x16x32_bf16 a[80:83], a[208:211], v[24:27], a[80:83]\n"
    "ds_read_b128 a[180:183], v109 offset:13312\n"
    "v_cndmask_b32 v51, v51, v113, s[94:95]\n"
    "v_mfma_f32_16x16x32_bf16 a[112:115], a[208:211], v[28:31], a[112:115]\n"
    "v_exp_f32 v79, v203\n"
    "v_mfma_f32_16x16x32_bf16 a[80:83], a[240:243], v[32:35], a[80:83]\n"
    "v_cmp_gt_i32 s[92:93], 18, v112\n"
    "v_cvt_pk_bf16_f32 v12, v76, v77\n"
    "v_mfma_f32_16x16x32_bf16 a[112:115], a[240:243], v[36:39], a[112:115]\n"
    "ds_read_b128 a[136:139], v109 offset:2048\n"
    "v_cndmask_b32 v52, v52, v113, s[84:85]\n"
    "v_mfma_f32_16x16x32_bf16 a[84:87], a[212:215], v[24:27], a[84:87]\n"
    "v_or_b32 v108, v8, v9\n"
    "v_cmp_gt_i32 s[94:95], 19, v112\n"
    "v_mfma_f32_16x16x32_bf16 a[116:119], a[212:215], v[28:31], a[116:119]\n"
    "v_exp_f32 v72, v208\n"
    "v_mfma_f32_16x16x32_bf16 a[84:87], a[244:247], v[32:35], a[84:87]\n"
    "ds_read_b128 a[168:171], v109 offset:10240\n"
    "v_cndmask_b32 v53, v53, v113, s[86:87]\n"
    "v_mfma_f32_16x16x32_bf16 a[116:119], a[244:247], v[36:39], a[116:119]\n"
    "v_exp_f32 v73, v209\n"
    "v_mfma_f32_16x16x32_bf16 v[92:95], v[80:83], v[32:35], v[92:95]\n"
    "ds_read_b128 a[152:155], v109 offset:6144\n"
    "v_cndmask_b32 v54, v54, v113, s[92:93]\n"
    "v_mfma_f32_16x16x32_bf16 a[88:91], a[216:219], v[24:27], a[88:91]\n"
    "v_cvt_pk_bf16_f32 v13, v78, v79\n"
    "v_cndmask_b32 v55, v55, v113, s[94:95]\n"
    "v_mfma_f32_16x16x32_bf16 a[120:123], a[216:219], v[28:31], a[120:123]\n"
    "v_exp_f32 v74, v210\n"
    "ds_read_b128 a[184:187], v109 offset:14336\n"
    "ds_read_b128 a[140:143], v109 offset:3072\n"
    "ds_read_b128 a[172:175], v109 offset:11264\n"
    "ds_read_b128 a[156:159], v109 offset:7168\n"
    "ds_read_b128 a[188:191], v109 offset:15360\n"
    "v_mfma_f32_16x16x32_bf16 a[88:91], a[248:251], v[32:35], a[88:91]\n"
    "s_lshr_b32 s6, s62, 10\n"
    "s_sub_u32 s6, s6, 0x60\n"
    "v_mfma_f32_16x16x32_bf16 a[120:123], a[248:251], v[36:39], a[120:123]\n"
    "v_exp_f32 v75, v211\n"
    "v_mfma_f32_16x16x32_bf16 a[92:95], a[220:223], v[24:27], a[92:95]\n"
    "v_add_u32 v112, s6, v111\n"
    "v_cvt_pk_bf16_f32 v16, v72, v73\n"
    "v_mfma_f32_16x16x32_bf16 a[124:127], a[220:223], v[28:31], a[124:127]\n"
    "v_cmp_gt_i32 s[84:85], 0, v112\n"
    "v_or3_b32 v108, v108, v12, v13\n"
    "v_mfma_f32_16x16x32_bf16 a[92:95], a[252:255], v[32:35], a[92:95]\n"
    "v_exp_f32 v76, v216\n"
    "v_mfma_f32_16x16x32_bf16 a[124:127], a[252:255], v[36:39], a[124:127]\n"
    "v_cmp_gt_i32 s[86:87], 1, v112\n"
    "v_cmp_gt_i32 s[92:93], 2, v112\n"
    "v_mfma_f32_16x16x32_bf16 v[96:99], v[80:83], v[36:39], v[96:99]\n"
    "v_exp_f32 v77, v217\n"
    "v_cvt_pk_bf16_f32 v17, v74, v75\n"
    "v_exp_f32 v78, v218\n"
    "v_exp_f32 v79, v219\n"
    "v_cvt_pk_bf16_f32 v20, v76, v77\n"
    "v_or3_b32 v108, v108, v16, v17\n"
    "v_exp_f32 v72, v196\n"
    "v_exp_f32 v73, v197\n"
    "v_cvt_pk_bf16_f32 v21, v78, v79\n"
    "v_exp_f32 v74, v198\n"
    "v_exp_f32 v75, v199\n"
    "v_cvt_pk_bf16_f32 v10, v72, v73\n"
    "v_or3_b32 v108, v108, v20, v21\n"
    "v_exp_f32 v76, v204\n"
    "v_cndmask_b32 v64, v64, v113, s[84:85]\n"
    "v_cmp_gt_i32 s[94:95], 3, v112\n"
    "v_cndmask_b32 v65, v65, v113, s[86:87]\n"
    "v_cmp_gt_i32 s[84:85], 16, v112\n"
    "v_cndmask_b32 v66, v66, v113, s[92:93]\n"
    "v_cmp_gt_i32 s[86:87], 17, v112\n"
    "v_cndmask_b32 v67, v67, v113, s[94:95]\n"
    "v_cmp_gt_i32 s[92:93], 18, v112\n"
    "v_cndmask_b32 v68, v68, v113, s[84:85]\n"
    "v_cmp_gt_i32 s[94:95], 19, v112\n"
    "v_cndmask_b32 v69, v69, v113, s[86:87]\n"
    "v_cndmask_b32 v70, v70, v113, s[92:93]\n"
    "v_cndmask_b32 v71, v71, v113, s[94:95]\n"
    "s_add_u32 s77, s77, 0x8000\n"
    "s_and_b32 s77, s77, 0x1ffff\n"
    "v_add_u32 v110, s77, v115\n"
    "LT1:\n"
    "s_waitcnt lgkmcnt(0)\n"
    "v_mfma_f32_16x16x32_bf16 v[232:235], a[128:131], v[176:179], v[48:51]\n"
    "s_waitcnt vmcnt(21)\n"
    "s_barrier\n"
    "v_mfma_f32_16x16x32_bf16 v[248:251], a[160:163], v[176:179], v[64:67]\n"
    "ds_read_b128 a[192:195], v110\n"
    "s_add_u32 s70, s67, 0x2000\n"
    "v_mfma_f32_16x16x32_bf16 v[236:239], a[144:147], v[176:179], v[52:55]\n"
    "v_exp_f32 v77, v205\n"
    "v_mfma_f32_16x16x32_bf16 v[252:255], a[176:179], v[176:179], v[68:71]\n"
    "ds_read_b128 a[224:227], v110 offset:8192\n"
    "ds_read_b128 a[196:199], v110 offset:1024\n"
    "v_mfma_f32_16x16x32_bf16 v[232:235], a[132:135], v[180:183], v[232:235]\n"
    "ds_read_b128 a[228:231], v110 offset:9216\n"
    "s_add_u32 m0, s59, 0\n"
    "v_mfma_f32_16x16x32_bf16 v[248:251], a[164:167], v[180:183], v[248:251]\n"
    "ds_read_b128 a[200:203], v110 offset:2048\n"
    "v_cvt_pk_bf16_f32 v11, v74, v75\n"
    "v_mfma_f32_16x16x32_bf16 v[236:239], a[148:151], v[180:183], v[236:239]\n"
    "v_exp_f32 v78, v206\n"
    "v_mfma_f32_16x16x32_bf16 v[252:255], a[180:183], v[180:183], v[252:255]\n"
    "ds_read_b128 a[232:235], v110 offset:10240\n"
    "ds_read_b128 a[204:207], v110 offset:3072\n"
    "v_mfma_f32_16x16x32_bf16 v[232:235], a[136:139], v[184:187], v[232:235]\n"
    "buffer_load_dwordx4 v121, s[16:19], s67 offen lds\n"
    "ds_read_b128 a[236:239], v110 offset:11264\n"
    "v_mfma_f32_16x16x32_bf16 v[248:251], a[168:171], v[184:187], v[248:251]\n"
    "v_exp_f32 v79, v207\n"
    "ds_read_b128 a[208:211], v110 offset:4096\n"
    "ds_read_b128 a[240:243], v110 offset:12288\n"
    "ds_read_b128 a[212:215], v110 offset:5120\n"
    "ds_read_b128 a[244:247], v110 offset:13312\n"
    "ds_read_b128 a[216:219], v110 offset:6144\n"
    "ds_read_b128 a[248:251], v110 offset:14336\n"
    "ds_read_b128 a[220:223], v110 offset:7168\n"
    "ds_read_b128 a[252:255], v110 offset:15360\n"
    "v_mfma_f32_16x16x32_bf16 v[236:239], a[152:155], v[184:187], v[236:239]\n"
    "s_add_u32 m0, s59, 0x400\n"
    "v_cvt_pk_bf16_f32 v14, v76, v77\n"
    "v_mfma_f32_16x16x32_bf16 v[252:255], a[184:187], v[184:187], v[252:255]\n"
    "buffer_load_dwordx4 v123, s[16:19], s67 offen lds\n"
    "v_or3_b32 v108, v108, v10, v11\n"
    "v_mfma_f32_16x16x32_bf16 v[232:235], a[140:143], v[188:191], v[232:235]\n"
    "v_exp_f32 v72, v212\n"
    "v_mfma_f32_16x16x32_bf16 v[248:251], a[172:175], v[188:191], v[248:251]\n"
    "v_exp_f32 v73, v213\n"
    "v_mfma_f32_16x16x32_bf16 v[236:239], a[156:159], v[188:191], v[236:239]\n"
    "s_add_u32 m0, s59, 0x800\n"
    "v_cvt_pk_bf16_f32 v15, v78, v79\n"
    "v_exp_f32 v74, v214\n"
    "v_exp_f32 v75, v215\n"
    "v_cvt_pk_bf16_f32 v18, v72, v73\n"
    "v_or3_b32 v108, v108, v14, v15\n"
    "v_exp_f32 v76, v220\n"
    "v_exp_f32 v77, v221\n"
    "v_cvt_pk_bf16_f32 v19, v74, v75\n"
    "v_exp_f32 v78, v222\n"
    "v_exp_f32 v79, v223\n"
    "v_cvt_pk_bf16_f32 v22, v76, v77\n"
    "v_or3_b32 v108, v108, v18, v19\n"
    "v_cvt_pk_bf16_f32 v23, v78, v79\n"
    "v_or3_b32 v108, v108, v22, v23\n"
    "v_and_b32 v3, 0x40004000, v108\n"
    "v_cmp_ne_u32 vcc, 0, v3\n"
    "buffer_load_dwordx4 v121, s[16:19], s70 offen lds\n"
    "s_add_u32 m0, s59, 0xc00\n"
    "s_nop 0\n"
    "buffer_load_dwordx4 v123, s[16:19], s70 offen lds\n"
    "s_add_u32 s67, s67, 0x10000\n"
    "s_add_u32 s70, s67, 0x2000\n"
    "s_add_u32 m0, s59, 0x8000\n"
    "s_nop 0\n"
    "buffer_load_dwordx4 v121, s[16:19], s67 offen lds\n"
    "s_add_u32 m0, s59, 0x8400\n"
    "s_nop 0\n"
    "buffer_load_dwordx4 v123, s[16:19], s67 offen lds\n"
    "s_add_u32 m0, s59, 0x8800\n"
    "s_nop 0\n"
    "buffer_load_dwordx4 v121, s[16:19], s70 offen lds\n"
    "s_add_u32 m0, s59, 0x8c00\n"
    "s_nop 0\n"
    "buffer_load_dwordx4 v123, s[16:19], s70 offen lds\n"
    "s_add_u32 s67, s67, 0x10000\n"
    "v_mfma_f32_16x16x32_bf16 v[252:255], a[188:191], v[188:191], v[252:255]\n"
    "s_cbranch_vccnz Lrs13\n"
    "Lrr13:\n"
    "s_waitcnt lgkmcnt(0)\n"
    "v_mfma_f32_16x16x32_bf16 a[64:67], a[192:195], v[8:11], a[64:67]\n"
    "s_add_u32 s77, s77, 0x8000\n"
    "s_add_u32 s70, s67, 0x2000\n"
    "v_mfma_f32_16x16x32_bf16 a[96:99], a[192:195], v[12:15], a[96:99]\n"
    "s_and_b32 s77, s77, 0x1ffff\n"
    "s_add_u32 m0, s59, 0x10000\n"
    "v_mfma_f32_16x16x32_bf16 a[64:67], a[224:227], v[16:19], a[64:67]\n"
    "v_exp_f32 v72, v232\n"
    "v_mfma_f32_16x16x32_bf16 a[96:99], a[224:227], v[20:23], a[96:99]\n"
    "buffer_load_dwordx4 v121, s[16:19], s67 offen lds\n"
    "v_add_u32 v110, s77, v115\n"
    "v_mfma_f32_16x16x32_bf16 a[68:71], a[196:199], v[8:11], a[68:71]\n"
    "v_exp_f32 v73, v233\n"
    "v_mfma_f32_16x16x32_bf16 a[100:103], a[196:199], v[12:15], a[100:103]\n"
    "v_exp_f32 v74, v234\n"
    "v_mfma_f32_16x16x32_bf16 a[68:71], a[228:231], v[16:19], a[68:71]\n"
    "ds_read_b128 a[128:131], v110\n"
    "s_add_u32 m0, s59, 0x10400\n"
    "v_mfma_f32_16x16x32_bf16 a[100:103], a[228:231], v[20:23], a[100:103]\n"
    "v_exp_f32 v75, v235\n"
    "v_mfma_f32_16x16x32_bf16 v[92:95], v[80:83], v[8:11], v[92:95]\n"
    "ds_read_b128 a[160:163], v110 offset:8192\n"
    "buffer_load_dwordx4 v123, s[16:19], s67 offen lds\n"
    "v_mfma_f32_16x16x32_bf16 a[72:75], a[200:203], v[8:11], a[72:75]\n"
    "ds_read_b128 a[132:135], v110 offset:1024\n"
    "v_cvt_pk_bf16_f32 v28, v72, v73\n"
    "v_mfma_f32_16x16x32_bf16 a[104:107], a[200:203], v[12:15], a[104:107]\n"
    "v_exp_f32 v76, v248\n"
    "v_mfma_f32_16x16x32_bf16 a[72:75], a[232:235], v[16:19], a[72:75]\n"
    "ds_read_b128 a[164:167], v110 offset:9216\n"
    "s_add_u32 m0, s59, 0x10800\n"
    "v_mfma_f32_16x16x32_bf16 a[104:107], a[232:235], v[20:23], a[104:107]\n"
    "v_exp_f32 v77, v249\n"
    "v_mfma_f32_16x16x32_bf16 a[76:79], a[204:207], v[8:11], a[76:79]\n"
    "buffer_load_dwordx4 v121, s[16:19], s70 offen lds\n"
    "ds_read_b128 a[136:139], v110 offset:2048\n"
    "v_mfma_f32_16x16x32_bf16 a[108:111], a[204:207], v[12:15], a[108:111]\n"
    "v_cvt_pk_bf16_f32 v29, v74, v75\n"
    "s_add_u32 m0, s59, 0x10c00\n"
    "v_mfma_f32_16x16x32_bf16 a[76:79], a[236:239], v[16:19], a[76:79]\n"
    "v_exp_f32 v78, v250\n"
    "v_mfma_f32_16x16x32_bf16 a[108:111], a[236:239], v[20:23], a[108:111]\n"
    "ds_read_b128 a[168:171], v110 offset:10240\n"
    "ds_read_b128 a[140:143], v110 offset:3072\n"
    "v_mfma_f32_16x16x32_bf16 v[96:99], v[80:83], v[12:15], v[96:99]\n"
    "v_exp_f32 v79, v251\n"
    "v_mfma_f32_16x16x32_bf16 a[80:83], a[208:211], v[8:11], a[80:83]\n"
    "buffer_load_dwordx4 v123, s[16:19], s70 offen lds\n"
    "v_cvt_pk_bf16_f32 v36, v76, v77\n"
    "v_mfma_f32_16x16x32_bf16 a[112:115], a[208:211], v[12:15], a[112:115]\n"
    "ds_read_b128 a[172:175], v110 offset:11264\n"
    "s_add_u32 s67, s67, 0x10000\n"
    "v_mfma_f32_16x16x32_bf16 a[80:83], a[240:243], v[16:19], a[80:83]\n"
    "ds_read_b128 a[144:147], v110 offset:4096\n"
    "v_or_b32 v108, v28, v29\n"
    "v_mfma_f32_16x16x32_bf16 a[112:115], a[240:243], v[20:23], a[112:115]\n"
    "v_exp_f32 v72, v236\n"
    "v_mfma_f32_16x16x32_bf16 a[84:87], a[212:215], v[8:11], a[84:87]\n"
    "ds_read_b128 a[176:179], v110 offset:12288\n"
    "s_add_u32 s70, s67, 0x2000\n"
    "v_mfma_f32_16x16x32_bf16 a[116:119], a[212:215], v[12:15], a[116:119]\n"
    "v_exp_f32 v73, v237\n"
    "v_mfma_f32_16x16x32_bf16 a[84:87], a[244:247], v[16:19], a[84:87]\n"
    "s_add_u32 m0, s59, 0x18000\n"
    "v_cvt_pk_bf16_f32 v37, v78, v79\n"
    "v_mfma_f32_16x16x32_bf16 a[116:119], a[244:247], v[20:23], a[116:119]\n"
    "ds_read_b128 a[148:151], v110 offset:5120\n"
    "buffer_load_dwordx4 v121, s[16:19], s67 offen lds\n"
    "v_mfma_f32_16x16x32_bf16 v[92:95], v[80:83], v[16:19], v[92:95]\n"
    "v_exp_f32 v74, v238\n"
    "v_mfma_f32_16x16x32_bf16 a[88:91], a[216:219], v[8:11], a[88:91]\n"
    "v_exp_f32 v75, v239\n"
    "v_mfma_f32_16x16x32_bf16 a[120:123], a[216:219], v[12:15], a[120:123]\n"
    "ds_read_b128 a[180:183], v110 offset:13312\n"
    "s_add_u32 m0, s59, 0x18400\n"
    "v_mfma_f32_16x16x32_bf16 a[88:91], a[248:251], v[16:19], a[88:91]\n"
    "ds_read_b128 a[152:155], v110 offset:6144\n"
    "v_cvt_pk_bf16_f32 v30, v72, v73\n"
    "v_mfma_f32_16x16x32_bf16 a[120:123], a[248:251], v[20:23], a[120:123]\n"
    "buffer_load_dwordx4 v123, s[16:19], s67 offen lds\n"
    "v_or3_b32 v108, v108, v36, v37\n"
    "ds_read_b128 a[184:187], v110 offset:14336\n"
    "ds_read_b128 a[156:159], v110 offset:7168\n"
    "ds_read_b128 a[188:191], v110 offset:15360\n"
    "v_mfma_f32_16x16x32_bf16 a[92:95], a[220:223], v[8:11], a[92:95]\n"
    "v_exp_f32 v76, v252\n"
    "v_mfma_f32_16x16x32_bf16 a[124:127], a[220:223], v[12:15], a[124:127]\n"
    "v_exp_f32 v77, v253\n"
    "v_mfma_f32_16x16x32_bf16 a[92:95], a[252:255], v[16:19], a[92:95]\n"
    "s_add_u32 m0, s59, 0x18800\n"
    "v_cvt_pk_bf16_f32 v31, v74, v75\n"
    "v_mfma_f32_16x16x32_bf16 a[124:127], a[252:255], v[20:23], a[124:127]\n"
    "v_exp_f32 v78, v254\n"
    "v_mfma_f32_16x16x32_bf16 v[96:99], v[80:83], v[20:23], v[96:99]\n"
    "buffer_load_dwordx4 v121, s[16:19], s70 offen lds\n"
    "s_add_u32 m0, s59, 0x18c00\n"
    "v_exp_f32 v79, v255\n"
    "v_cvt_pk_bf16_f32 v38, v76, v77\n"
    "v_or3_b32 v108, v108, v30, v31\n"
    "v_cvt_pk_bf16_f32 v39, v78, v79\n"
    "v_or3_b32 v108, v108, v38, v39\n"
    "buffer_load_dwordx4 v123, s[16:19], s70 offen lds\n"
    "s_add_u32 s67, s67, 0x10000\n"
    "s_waitcnt lgkmcnt(0)\n"
    "v_and_b32 v3, 0x40004000, v108\n"
    "v_cmp_ne_u32 vcc, 0, v3\n"
    "s_nop 0\n"
    "s_cbranch_vccnz Lrs14\n"
    "Lrr14:\n"
    "s_waitcnt lgkmcnt(0)\n"
    "v_mfma_f32_16x16x32_bf16 v[96:99], v[80:83], v[28:31], v[96:99]\n"
    "s_sub_u32 s4, 15, s57\n"
    "s_barrier\n"
    "v_mfma_f32_16x16x32_bf16 v[96:99], v[80:83], v[36:39], v[96:99]\n"
    "s_lshl_b32 s4, s4, 2\n"
    "s_add_u32 s4, s4, s58\n"
    "v_mfma_f32_16x16x32_bf16 a[96:99], a[128:131], v[28:31], a[96:99]\n"
    "buffer_load_dwordx4 a[16:19], v2, s[8:11], s66 offen offset:2048 sc1\n"
    "s_mul_i32 s4, s4, 0x58000\n"
    "v_mfma_f32_16x16x32_bf16 a[96:99], a[160:163], v[36:39], a[96:99]\n"
    "buffer_load_dwordx4 a[20:23], v120, s[8:11], s66 offen offset:2048 sc1\n"
    "s_lshl_b32 s5, s56, 12\n"
    "v_mfma_f32_16x16x32_bf16 a[100:103], a[132:135], v[28:31], a[100:103]\n"
    "s_add_u32 s4, s4, s5\n"
    "s_add_u32 s4, s4, 0x2220000\n"
    "v_mfma_f32_16x16x32_bf16 a[100:103], a[164:167], v[36:39], a[100:103]\n"
    "buffer_load_dwordx4 a[24:27], v2, s[8:11], s66 offen offset:3072 sc1\n"
    "buffer_load_dwordx4 a[28:31], v120, s[8:11], s66 offen offset:3072 sc1\n"
    "v_mfma_f32_16x16x32_bf16 a[104:107], a[136:139], v[28:31], a[104:107]\n"
    "v_rcp_f32 v124, v92\n"
    "v_mfma_f32_16x16x32_bf16 a[104:107], a[168:171], v[36:39], a[104:107]\n"
    "v_accvgpr_read_b32 v52, a64\n"
    "v_accvgpr_read_b32 v53, a65\n"
    "v_mfma_f32_16x16x32_bf16 a[108:111], a[140:143], v[28:31], a[108:111]\n"
    "s_add_u32 s66, s66, 0x1000\n"
    "v_accvgpr_read_b32 v54, a66\n"
    "v_mfma_f32_16x16x32_bf16 a[108:111], a[172:175], v[36:39], a[108:111]\n"
    "buffer_load_dwordx4 a[32:35], v2, s[8:11], s66 offen sc1\n"
    "v_accvgpr_read_b32 v55, a67\n"
    "v_mfma_f32_16x16x32_bf16 a[112:115], a[144:147], v[28:31], a[112:115]\n"
    "v_accvgpr_read_b32 v56, a68\n"
    "v_accvgpr_read_b32 v57, a69\n"
    "v_mfma_f32_16x16x32_bf16 a[112:115], a[176:179], v[36:39], a[112:115]\n"
    "buffer_load_dwordx4 a[36:39], v120, s[8:11], s66 offen sc1\n"
    "v_accvgpr_read_b32 v58, a70\n"
    "v_mfma_f32_16x16x32_bf16 a[116:119], a[148:151], v[28:31], a[116:119]\n"
    "buffer_load_dwordx4 a[40:43], v2, s[8:11], s66 offen offset:1024 sc1\n"
    "v_accvgpr_read_b32 v59, a71\n"
    "v_mfma_f32_16x16x32_bf16 a[116:119], a[180:183], v[36:39], a[116:119]\n"
    "v_mul_f32 v52, v52, v124\n"
    "v_mul_f32 v53, v53, v124\n"
    "v_mfma_f32_16x16x32_bf16 a[120:123], a[152:155], v[28:31], a[120:123]\n"
    "buffer_load_dwordx4 a[44:47], v120, s[8:11], s66 offen offset:1024 sc1\n"
    "v_mul_f32 v54, v54, v124\n"
    "v_mfma_f32_16x16x32_bf16 a[120:123], a[184:187], v[36:39], a[120:123]\n"
    "buffer_load_dwordx4 a[48:51], v2, s[8:11], s66 offen offset:2048 sc1\n"
    "v_mul_f32 v55, v55, v124\n"
    "v_mfma_f32_16x16x32_bf16 a[124:127], a[156:159], v[28:31], a[124:127]\n"
    "v_mul_f32 v56, v56, v124\n"
    "v_mul_f32 v57, v57, v124\n"
    "v_mfma_f32_16x16x32_bf16 a[124:127], a[188:191], v[36:39], a[124:127]\n"
    "buffer_load_dwordx4 a[52:55], v120, s[8:11], s66 offen offset:2048 sc1\n"
    "v_mul_f32 v58, v58, v124\n"
    "buffer_load_dwordx4 a[56:59], v2, s[8:11], s66 offen offset:3072 sc1\n"
    "buffer_load_dwordx4 a[60:63], v120, s[8:11], s66 offen offset:3072 sc1\n"
    "s_add_u32 s66, s66, 0x1000\n"
    "v_mul_f32 v59, v59, v124\n"
    "v_cvt_pk_bf16_f32 v192, v52, v53\n"
    "v_cvt_pk_bf16_f32 v193, v54, v55\n"
    "v_cvt_pk_bf16_f32 v194, v56, v57\n"
    "v_cvt_pk_bf16_f32 v195, v58, v59\n"
    "buffer_store_dwordx4 v[192:195], v2, s[8:11], s4 offen sc1\n"
    "v_rcp_f32 v126, v96\n"
    "s_nop 0\n"
    "v_mov_b32 v127, v126\n"
    "v_mov_b32 v125, v124\n"
    ".p2align 3\n"
    "v_accvgpr_read_b32 v44, a96\n"
    "v_accvgpr_read_b32 v45, a97\n"
    "v_accvgpr_read_b32 v46, a98\n"
    "v_accvgpr_read_b32 v47, a99\n"
    "v_accvgpr_read_b32 v48, a100\n"
    "v_accvgpr_read_b32 v49, a101\n"
    "v_accvgpr_read_b32 v50, a102\n"
    "v_accvgpr_read_b32 v51, a103\n"
    "v_pk_mul_f32 v[44:45], v[44:45], v[126:127]\n"
    "v_pk_mul_f32 v[46:47], v[46:47], v[126:127]\n"
    "v_pk_mul_f32 v[48:49], v[48:49], v[126:127]\n"
    "v_pk_mul_f32 v[50:51], v[50:51], v[126:127]\n"
    "v_cvt_pk_bf16_f32 v196, v44, v45\n"
    "v_cvt_pk_bf16_f32 v197, v46, v47\n"
    "v_cvt_pk_bf16_f32 v198, v48, v49\n"
    "v_cvt_pk_bf16_f32 v199, v50, v51\n"
    "buffer_store_dwordx4 v[196:199], v120, s[8:11], s4 offen sc1\n"
    "v_accvgpr_read_b32 v52, a72\n"
    "v_accvgpr_read_b32 v53, a73\n"
    "v_accvgpr_read_b32 v54, a74\n"
    "v_accvgpr_read_b32 v55, a75\n"
    "v_accvgpr_read_b32 v56, a76\n"
    "v_accvgpr_read_b32 v57, a77\n"
    "v_accvgpr_read_b32 v58, a78\n"
    "v_accvgpr_read_b32 v59, a79\n"
    "v_pk_mul_f32 v[52:53], v[52:53], v[124:125]\n"
    "v_pk_mul_f32 v[54:55], v[54:55], v[124:125]\n"
    "v_pk_mul_f32 v[56:57], v[56:57], v[124:125]\n"
    "v_pk_mul_f32 v[58:59], v[58:59], v[124:125]\n"
    "v_cvt_pk_bf16_f32 v200, v52, v53\n"
    "v_cvt_pk_bf16_f32 v201, v54, v55\n"
    "v_cvt_pk_bf16_f32 v202, v56, v57\n"
    "v_cvt_pk_bf16_f32 v203, v58, v59\n"
    "buffer_store_dwordx4 v[200:203], v2, s[8:11], s4 offen offset:1024 sc1\n"
    "v_accvgpr_read_b32 v44, a104\n"
    "v_accvgpr_read_b32 v45, a105\n"
    "v_accvgpr_read_b32 v46, a106\n"
    "v_accvgpr_read_b32 v47, a107\n"
    "v_accvgpr_read_b32 v48, a108\n"
    "v_accvgpr_read_b32 v49, a109\n"
    "v_accvgpr_read_b32 v50, a110\n"
    "v_accvgpr_read_b32 v51, a111\n"
    "v_pk_mul_f32 v[44:45], v[44:45], v[126:127]\n"
    "v_pk_mul_f32 v[46:47], v[46:47], v[126:127]\n"
    "v_pk_mul_f32 v[48:49], v[48:49], v[126:127]\n"
    "v_pk_mul_f32 v[50:51], v[50:51], v[126:127]\n"
    "v_cvt_pk_bf16_f32 v204, v44, v45\n"
    "v_cvt_pk_bf16_f32 v205, v46, v47\n"
    "v_cvt_pk_bf16_f32 v206, v48, v49\n"
    "v_cvt_pk_bf16_f32 v207, v50, v51\n"
    "buffer_store_dwordx4 v[204:207], v120, s[8:11], s4 offen offset:1024 sc1\n"
    "v_accvgpr_read_b32 v52, a80\n"
    "v_accvgpr_read_b32 v53, a81\n"
    "v_accvgpr_read_b32 v54, a82\n"
    "v_accvgpr_read_b32 v55, a83\n"
    "v_accvgpr_read_b32 v56, a84\n"
    "v_accvgpr_read_b32 v57, a85\n"
    "v_accvgpr_read_b32 v58, a86\n"
    "v_accvgpr_read_b32 v59, a87\n"
    "v_pk_mul_f32 v[52:53], v[52:53], v[124:125]\n"
    "v_pk_mul_f32 v[54:55], v[54:55], v[124:125]\n"
    "v_pk_mul_f32 v[56:57], v[56:57], v[124:125]\n"
    "v_pk_mul_f32 v[58:59], v[58:59], v[124:125]\n"
    "v_cvt_pk_bf16_f32 v208, v52, v53\n"
    "v_cvt_pk_bf16_f32 v209, v54, v55\n"
    "v_cvt_pk_bf16_f32 v210, v56, v57\n"
    "v_cvt_pk_bf16_f32 v211, v58, v59\n"
    "buffer_store_dwordx4 v[208:211], v2, s[8:11], s4 offen offset:2048 sc1\n"
    "v_accvgpr_read_b32 v44, a112\n"
    "v_accvgpr_read_b32 v45, a113\n"
    "v_accvgpr_read_b32 v46, a114\n"
    "v_accvgpr_read_b32 v47, a115\n"
    "v_accvgpr_read_b32 v48, a116\n"
    "v_accvgpr_read_b32 v49, a117\n"
    "v_accvgpr_read_b32 v50, a118\n"
    "v_accvgpr_read_b32 v51, a119\n"
    "v_pk_mul_f32 v[44:45], v[44:45], v[126:127]\n"
    "v_pk_mul_f32 v[46:47], v[46:47], v[126:127]\n"
    "v_pk_mul_f32 v[48:49], v[48:49], v[126:127]\n"
    "v_pk_mul_f32 v[50:51], v[50:51], v[126:127]\n"
    "v_cvt_pk_bf16_f32 v212, v44, v45\n"
    "v_cvt_pk_bf16_f32 v213, v46, v47\n"
    "v_cvt_pk_bf16_f32 v214, v48, v49\n"
    "v_cvt_pk_bf16_f32 v215, v50, v51\n"
    "buffer_store_dwordx4 v[212:215], v120, s[8:11], s4 offen offset:2048 sc1\n"
    "v_accvgpr_read_b32 v52, a88\n"
    "v_accvgpr_read_b32 v53, a89\n"
    "v_accvgpr_read_b32 v54, a90\n"
    "v_accvgpr_read_b32 v55, a91\n"
    "v_accvgpr_read_b32 v56, a92\n"
    "v_accvgpr_read_b32 v57, a93\n"
    "v_accvgpr_read_b32 v58, a94\n"
    "v_accvgpr_read_b32 v59, a95\n"
    "v_pk_mul_f32 v[52:53], v[52:53], v[124:125]\n"
    "v_pk_mul_f32 v[54:55], v[54:55], v[124:125]\n"
    "v_pk_mul_f32 v[56:57], v[56:57], v[124:125]\n"
    "v_pk_mul_f32 v[58:59], v[58:59], v[124:125]\n"
    "v_cvt_pk_bf16_f32 v216, v52, v53\n"
    "v_cvt_pk_bf16_f32 v217, v54, v55\n"
    "v_cvt_pk_bf16_f32 v218, v56, v57\n"
    "v_cvt_pk_bf16_f32 v219, v58, v59\n"
    "buffer_store_dwordx4 v[216:219], v2, s[8:11], s4 offen offset:3072 sc1\n"
    "v_accvgpr_read_b32 v44, a120\n"
    "v_accvgpr_read_b32 v45, a121\n"
    "v_accvgpr_read_b32 v46, a122\n"
    "v_accvgpr_read_b32 v47, a123\n"
    "v_accvgpr_read_b32 v48, a124\n"
    "v_accvgpr_read_b32 v49, a125\n"
    "v_accvgpr_read_b32 v50, a126\n"
    "v_accvgpr_read_b32 v51, a127\n"
    "v_pk_mul_f32 v[44:45], v[44:45], v[126:127]\n"
    "v_pk_mul_f32 v[46:47], v[46:47], v[126:127]\n"
    "v_pk_mul_f32 v[48:49], v[48:49], v[126:127]\n"
    "v_pk_mul_f32 v[50:51], v[50:51], v[126:127]\n"
    "v_cvt_pk_bf16_f32 v220, v44, v45\n"
    "v_cvt_pk_bf16_f32 v221, v46, v47\n"
    "v_cvt_pk_bf16_f32 v222, v48, v49\n"
    "v_cvt_pk_bf16_f32 v223, v50, v51\n"
    "buffer_store_dwordx4 v[220:223], v120, s[8:11], s4 offen offset:3072 sc1\n"
    "s_add_u32 s67, s3, 7\n"
    "s_and_b32 s67, s67, 7\n"
    "s_lshl_b32 s67, s67, 17\n"
    "s_add_u32 s67, s67, s90\n"
    "s_add_u32 s70, s67, 0x2000\n"
    "s_add_u32 m0, s59, 0x4000\n"
    "s_nop 0\n"
    "buffer_load_dwordx4 v121, s[12:15], s67 offen lds\n"
    "s_add_u32 m0, s59, 0x4400\n"
    "s_nop 0\n"
    "buffer_load_dwordx4 v123, s[12:15], s67 offen lds\n"
    "s_add_u32 m0, s59, 0x4800\n"
    "s_nop 0\n"
    "buffer_load_dwordx4 v121, s[12:15], s70 offen lds\n"
    "s_add_u32 m0, s59, 0x4c00\n"
    "s_nop 0\n"
    "buffer_load_dwordx4 v123, s[12:15], s70 offen lds\n"
    "s_add_u32 s67, s67, 0x10000\n"
    "v_lshrrev_b32 v3, 5, v1\n"
    "v_lshlrev_b32 v3, 11, v3\n"
    "v_bfe_u32 v4, v1, 2, 3\n"
    "v_lshl_add_u32 v3, v4, 7, v3\n"
    "v_bfe_u32 v4, v1, 3, 2\n"
    "v_lshl_add_u32 v3, v4, 5, v3\n"
    "v_and_b32 v4, 3, v1\n"
    "v_lshl_add_u32 v108, v4, 3, v3\n"
    "s_waitcnt vmcnt(36)\n"
    "s_barrier\n"
    "v_add_u32 v104, 0x20000, v108\n"
    "v_xor_b32 v105, 32, v104\n"
    "v_xor_b32 v106, 64, v104\n"
    "v_xor_b32 v107, 0x60, v104\n"
    "ds_read_b64_tr_b16 v[160:161], v104\n"
    "ds_read_b64_tr_b16 v[162:163], v104 offset:4096\n"
    "ds_read_b64_tr_b16 v[164:165], v105\n"
    "ds_read_b64_tr_b16 v[166:167], v105 offset:4096\n"
    "ds_read_b64_tr_b16 v[168:169], v106\n"
    "ds_read_b64_tr_b16 v[170:171], v106 offset:4096\n"
    "ds_read_b64_tr_b16 v[172:173], v107\n"
    "ds_read_b64_tr_b16 v[174:175], v107 offset:4096\n"
    "ds_read_b64_tr_b16 v[176:177], v104 offset:1024\n"
    "ds_read_b64_tr_b16 v[178:179], v104 offset:5120\n"
    "ds_read_b64_tr_b16 v[180:181], v105 offset:1024\n"
    "ds_read_b64_tr_b16 v[182:183], v105 offset:5120\n"
    "ds_read_b64_tr_b16 v[184:185], v106 offset:1024\n"
    "ds_read_b64_tr_b16 v[186:187], v106 offset:5120\n"
    "ds_read_b64_tr_b16 v[188:189], v107 offset:1024\n"
    "ds_read_b64_tr_b16 v[190:191], v107 offset:5120\n"
    "ds_read_b64_tr_b16 v[224:225], v104 offset:8192\n"
    "ds_read_b64_tr_b16 v[226:227], v104 offset:12288\n"
    "ds_read_b64_tr_b16 v[228:229], v105 offset:8192\n"
    "ds_read_b64_tr_b16 v[230:231], v105 offset:12288\n"
    "ds_read_b64_tr_b16 v[232:233], v106 offset:8192\n"
    "ds_read_b64_tr_b16 v[234:235], v106 offset:12288\n"
    "ds_read_b64_tr_b16 v[236:237], v107 offset:8192\n"
    "ds_read_b64_tr_b16 v[238:239], v107 offset:12288\n"
    "ds_read_b64_tr_b16 v[240:241], v104 offset:9216\n"
    "ds_read_b64_tr_b16 v[242:243], v104 offset:13312\n"
    "ds_read_b64_tr_b16 v[244:245], v105 offset:9216\n"
    "ds_read_b64_tr_b16 v[246:247], v105 offset:13312\n"
    "ds_read_b64_tr_b16 v[248:249], v106 offset:9216\n"
    "ds_read_b64_tr_b16 v[250:251], v106 offset:13312\n"
    "ds_read_b64_tr_b16 v[252:253], v107 offset:9216\n"
    "ds_read_b64_tr_b16 v[254:255], v107 offset:13312\n"
    "s_waitcnt lgkmcnt(15)\n"
    "v_mfma_f32_16x16x32_bf16 v[8:11], v[160:163], v[128:131], 0\n"
    "v_add_u32 v104, 0x24000, v108\n"
    "s_add_u32 s7, s68, 0x5000\n"
    "v_mfma_f32_16x16x32_bf16 v[16:19], v[160:163], v[132:135], 0\n"
    "v_xor_b32 v105, 32, v104\n"
    "v_xor_b32 v106, 64, v104\n"
    "v_mfma_f32_16x16x32_bf16 v[12:15], v[164:167], v[128:131], 0\n"
    "v_xor_b32 v107, 0x60, v104\n"
    "s_mov_b32 s82, 0\n"
    "v_mfma_f32_16x16x32_bf16 v[20:23], v[164:167], v[132:135], 0\n"
    "ds_read_b64_tr_b16 v[72:73], v104\n"
    "buffer_load_dword v122, v5, s[8:11], s7 offen sc1\n"
    "v_mfma_f32_16x16x32_bf16 v[24:27], v[168:171], v[128:131], 0\n"
    "ds_read_b64_tr_b16 v[74:75], v104 offset:4096\n"
    "ds_read_b64_tr_b16 v[76:77], v105\n"
    "v_mfma_f32_16x16x32_bf16 v[32:35], v[168:171], v[132:135], 0\n"
    "ds_read_b64_tr_b16 v[78:79], v105 offset:4096\n"
    "s_add_u32 s70, s67, 0x2000\n"
    "v_mfma_f32_16x16x32_bf16 v[28:31], v[172:175], v[128:131], 0\n"
    "ds_read_b64_tr_b16 v[80:81], v106\n"
    "s_add_u32 m0, s59, 0xc000\n"
    "v_mfma_f32_16x16x32_bf16 v[36:39], v[172:175], v[132:135], 0\n"
    "ds_read_b64_tr_b16 v[82:83], v106 offset:4096\n"
    "buffer_load_dwordx4 v121, s[12:15], s67 offen lds\n"
    "v_mfma_f32_16x16x32_bf16 v[40:43], v[176:179], v[128:131], 0\n"
    "ds_read_b64_tr_b16 v[84:85], v107\n"
    "ds_read_b64_tr_b16 v[86:87], v107 offset:4096\n"
    "v_mfma_f32_16x16x32_bf16 v[48:51], v[176:179], v[132:135], 0\n"
    "ds_read_b64_tr_b16 v[88:89], v104 offset:1024\n"
    "s_add_u32 m0, s59, 0xc400\n"
    "v_mfma_f32_16x16x32_bf16 v[44:47], v[180:183], v[128:131], 0\n"
    "ds_read_b64_tr_b16 v[90:91], v104 offset:5120\n"
    "ds_read_b64_tr_b16 v[92:93], v105 offset:1024\n"
    "v_mfma_f32_16x16x32_bf16 v[52:55], v[180:183], v[132:135], 0\n"
    "buffer_load_dwordx4 v123, s[12:15], s67 offen lds\n"
    "ds_read_b64_tr_b16 v[94:95], v105 offset:5120\n"
    "v_mfma_f32_16x16x32_bf16 v[56:59], v[184:187], v[128:131], 0\n"
    "ds_read_b64_tr_b16 v[96:97], v106 offset:1024\n"
    "s_add_u32 m0, s59, 0xc800\n"
    "v_mfma_f32_16x16x32_bf16 v[64:67], v[184:187], v[132:135], 0\n"
    "ds_read_b64_tr_b16 v[98:99], v106 offset:5120\n"
    "ds_read_b64_tr_b16 v[100:101], v107 offset:1024\n"
    "v_mfma_f32_16x16x32_bf16 v[60:63], v[188:191], v[128:131], 0\n"
    "buffer_load_dwordx4 v121, s[12:15], s70 offen lds\n"
    "ds_read_b64_tr_b16 v[102:103], v107 offset:5120\n"
    "v_mfma_f32_16x16x32_bf16 v[68:71], v[188:191], v[132:135], 0\n"
    "s_add_u32 m0, s59, 0xcc00\n"
    "s_waitcnt lgkmcnt(15)\n"
    "v_mfma_f32_16x16x32_bf16 v[8:11], v[224:227], v[136:139], v[8:11]\n"
    "ds_read_b64_tr_b16 v[160:161], v104 offset:8192\n"
    "ds_read_b64_tr_b16 v[162:163], v104 offset:12288\n"
    "v_mfma_f32_16x16x32_bf16 v[16:19], v[224:227], v[140:143], v[16:19]\n"
    "buffer_load_dwordx4 v123, s[12:15], s70 offen lds\n"
    "ds_read_b64_tr_b16 v[164:165], v105 offset:8192\n"
    "v_mfma_f32_16x16x32_bf16 v[12:15], v[228:231], v[136:139], v[12:15]\n"
    "ds_read_b64_tr_b16 v[166:167], v105 offset:12288\n"
    "s_add_u32 s67, s67, 0x10000\n"
    "v_mfma_f32_16x16x32_bf16 v[20:23], v[228:231], v[140:143], v[20:23]\n"
    "ds_read_b64_tr_b16 v[168:169], v106 offset:8192\n"
    "ds_read_b64_tr_b16 v[170:171], v106 offset:12288\n"
    "v_mfma_f32_16x16x32_bf16 v[24:27], v[232:235], v[136:139], v[24:27]\n"
    "s_add_u32 s67, s3, s69\n"
    "s_and_b32 s67, s67, 7\n"
    "s_add_u32 s69, s69, 1\n"
    "s_cmp_eq_u32 s69, 7\n"
    "s_cselect_b32 s69, 0, s69\n"
    "v_mfma_f32_16x16x32_bf16 v[32:35], v[232:235], v[140:143], v[32:35]\n"
    "ds_read_b64_tr_b16 v[172:173], v107 offset:8192\n"
    "ds_read_b64_tr_b16 v[174:175], v107 offset:12288\n"
    "v_mfma_f32_16x16x32_bf16 v[28:31], v[236:239], v[136:139], v[28:31]\n"
    "ds_read_b64_tr_b16 v[176:177], v104 offset:9216\n"
    "ds_read_b64_tr_b16 v[178:179], v104 offset:13312\n"
    "v_mfma_f32_16x16x32_bf16 v[36:39], v[236:239], v[140:143], v[36:39]\n"
    "ds_read_b64_tr_b16 v[180:181], v105 offset:9216\n"
    "ds_read_b64_tr_b16 v[182:183], v105 offset:13312\n"
    "v_mfma_f32_16x16x32_bf16 v[40:43], v[240:243], v[136:139], v[40:43]\n"
    "ds_read_b64_tr_b16 v[184:185], v106 offset:9216\n"
    "ds_read_b64_tr_b16 v[186:187], v106 offset:13312\n"
    "v_mfma_f32_16x16x32_bf16 v[48:51], v[240:243], v[140:143], v[48:51]\n"
    "ds_read_b64_tr_b16 v[188:189], v107 offset:9216\n"
    "ds_read_b64_tr_b16 v[190:191], v107 offset:13312\n"
    "v_mfma_f32_16x16x32_bf16 v[44:47], v[244:247], v[136:139], v[44:47]\n"
    "v_mfma_f32_16x16x32_bf16 v[52:55], v[244:247], v[140:143], v[52:55]\n"
    "v_mfma_f32_16x16x32_bf16 v[56:59], v[248:251], v[136:139], v[56:59]\n"
    "s_lshl_b32 s67, s67, 17\n"
    "v_mfma_f32_16x16x32_bf16 v[64:67], v[248:251], v[140:143], v[64:67]\n"
    "s_add_u32 s67, s67, s90\n"
    "v_mfma_f32_16x16x32_bf16 v[60:63], v[252:255], v[136:139], v[60:63]\n"
    "v_mfma_f32_16x16x32_bf16 v[68:71], v[252:255], v[140:143], v[68:71]\n"
    "s_add_u32 s70, s67, 0x2000\n"
    "s_waitcnt lgkmcnt(15)\n"
    "v_mfma_f32_16x16x32_bf16 v[8:11], v[72:75], v[144:147], v[8:11]\n"
    "v_add_u32 v104, 0, v108\n"
    "v_xor_b32 v105, 32, v104\n"
    "v_mfma_f32_16x16x32_bf16 v[16:19], v[72:75], v[148:151], v[16:19]\n"
    "v_xor_b32 v106, 64, v104\n"
    "s_add_u32 m0, s59, 0x14000\n"
    "v_mfma_f32_16x16x32_bf16 v[12:15], v[76:79], v[144:147], v[12:15]\n"
    "buffer_load_dwordx4 v121, s[12:15], s67 offen lds\n"
    "v_xor_b32 v107, 0x60, v104\n"
    "v_mfma_f32_16x16x32_bf16 v[20:23], v[76:79], v[148:151], v[20:23]\n"
    "ds_read_b64_tr_b16 v[224:225], v104\n"
    "ds_read_b64_tr_b16 v[226:227], v104 offset:4096\n"
    "v_mfma_f32_16x16x32_bf16 v[24:27], v[80:83], v[144:147], v[24:27]\n"
    "ds_read_b64_tr_b16 v[228:229], v105\n"
    "s_add_u32 m0, s59, 0x14400\n"
    "v_mfma_f32_16x16x32_bf16 v[32:35], v[80:83], v[148:151], v[32:35]\n"
    "ds_read_b64_tr_b16 v[230:231], v105 offset:4096\n"
    "buffer_load_dwordx4 v123, s[12:15], s67 offen lds\n"
    "v_mfma_f32_16x16x32_bf16 v[28:31], v[84:87], v[144:147], v[28:31]\n"
    "ds_read_b64_tr_b16 v[232:233], v106\n"
    "ds_read_b64_tr_b16 v[234:235], v106 offset:4096\n"
    "v_mfma_f32_16x16x32_bf16 v[36:39], v[84:87], v[148:151], v[36:39]\n"
    "ds_read_b64_tr_b16 v[236:237], v107\n"
    "s_add_u32 m0, s59, 0x14800\n"
    "v_mfma_f32_16x16x32_bf16 v[40:43], v[88:91], v[144:147], v[40:43]\n"
    "ds_read_b64_tr_b16 v[238:239], v107 offset:4096\n"
    "buffer_load_dwordx4 v121, s[12:15], s70 offen lds\n"
    "v_mfma_f32_16x16x32_bf16 v[48:51], v[88:91], v[148:151], v[48:51]\n"
    "ds_read_b64_tr_b16 v[240:241], v104 offset:1024\n"
    "ds_read_b64_tr_b16 v[242:243], v104 offset:5120\n"
    "v_mfma_f32_16x16x32_bf16 v[44:47], v[92:95], v[144:147], v[44:47]\n"
    "ds_read_b64_tr_b16 v[244:245], v105 offset:1024\n"
    "s_add_u32 m0, s59, 0x14c00\n"
    "v_mfma_f32_16x16x32_bf16 v[52:55], v[92:95], v[148:151], v[52:55]\n"
    "ds_read_b64_tr_b16 v[246:247], v105 offset:5120\n"
    "ds_read_b64_tr_b16 v[248:249], v106 offset:1024\n"
    "v_mfma_f32_16x16x32_bf16 v[56:59], v[96:99], v[144:147], v[56:59]\n"
    "buffer_load_dwordx4 v123, s[12:15], s70 offen lds\n"
    "ds_read_b64_tr_b16 v[250:251], v106 offset:5120\n"
    "v_mfma_f32_16x16x32_bf16 v[64:67], v[96:99], v[148:151], v[64:67]\n"
    "ds_read_b64_tr_b16 v[252:253], v107 offset:1024\n"
    "s_add_u32 s67, s67, 0x10000\n"
    "v_mfma_f32_16x16x32_bf16 v[60:63], v[100:103], v[144:147], v[60:63]\n"
    "ds_read_b64_tr_b16 v[254:255], v107 offset:5120\n"
    "v_mfma_f32_16x16x32_bf16 v[68:71], v[100:103], v[148:151], v[68:71]\n"
    "s_add_u32 s70, s67, 0x2000\n"
    "s_waitcnt lgkmcnt(15)\n"
    "v_mfma_f32_16x16x32_bf16 v[8:11], v[160:163], v[152:155], v[8:11]\n"
    "s_waitcnt vmcnt(13)\n"
    "s_sub_u32 s4, 15, s57\n"
    "s_lshl_b32 s4, s4, 2\n"
    "s_add_u32 s4, s4, s58\n"
    "s_lshl_b32 s4, s4, 5\n"
    "s_add_u32 s4, s4, s56\n"
    "s_lshl_b32 s4, s4, 2\n"
    "s_add_u32 s4, s4, 0x5000\n"
    "v_mov_b32 v6, 0\n"
    "s_mov_b64 exec, 1\n"
    "buffer_store_dword v7, v6, s[8:11], s4 offen sc1\n"
    "s_mov_b64 exec, -1\n"
    "s_cmp_lg_u32 s57, 7\n"
    "s_cbranch_scc1 Lnfa\n"
    "s_lshl_b32 s4, s57, 2\n"
    "s_add_u32 s4, s4, s58\n"
    "s_lshl_b32 s4, s4, 5\n"
    "s_add_u32 s4, s4, s56\n"
    "s_lshl_b32 s4, s4, 2\n"
    "s_add_u32 s4, s4, 0x5000\n"
    "v_mov_b32 v6, 0\n"
    "s_mov_b64 exec, 1\n"
    "buffer_store_dword v7, v6, s[8:11], s4 offen sc1\n"
    "s_mov_b64 exec, -1\n"
    "Lnfa:\n"
    "v_mfma_f32_16x16x32_bf16 v[16:19], v[160:163], v[156:159], v[16:19]\n"
    "ds_read_b64_tr_b16 v[72:73], v104 offset:8192\n"
    "ds_read_b64_tr_b16 v[74:75], v104 offset:12288\n"
    "v_mfma_f32_16x16x32_bf16 v[12:15], v[164:167], v[152:155], v[12:15]\n"
    "ds_read_b64_tr_b16 v[76:77], v105 offset:8192\n"
    "s_add_u32 m0, s59, 0x1c000\n"
    "v_mfma_f32_16x16x32_bf16 v[20:23], v[164:167], v[156:159], v[20:23]\n"
    "ds_read_b64_tr_b16 v[78:79], v105 offset:12288\n"
    "buffer_load_dwordx4 v121, s[12:15], s67 offen lds\n"
    "v_mfma_f32_16x16x32_bf16 v[24:27], v[168:171], v[152:155], v[24:27]\n"
    "ds_read_b64_tr_b16 v[80:81], v106 offset:8192\n"
    "s_add_u32 m0, s59, 0x1c400\n"
    "v_mfma_f32_16x16x32_bf16 v[32:35], v[168:171], v[156:159], v[32:35]\n"
    "ds_read_b64_tr_b16 v[82:83], v106 offset:12288\n"
    "buffer_load_dwordx4 v123, s[12:15], s67 offen lds\n"
    "v_mfma_f32_16x16x32_bf16 v[28:31], v[172:175], v[152:155], v[28:31]\n"
    "ds_read_b64_tr_b16 v[84:85], v107 offset:8192\n"
    "ds_read_b64_tr_b16 v[86:87], v107 offset:12288\n"
    "v_mfma_f32_16x16x32_bf16 v[36:39], v[172:175], v[156:159], v[36:39]\n"
    "ds_read_b64_tr_b16 v[88:89], v104 offset:9216\n"
    "s_add_u32 m0, s59, 0x1c800\n"
    "v_mfma_f32_16x16x32_bf16 v[40:43], v[176:179], v[152:155], v[40:43]\n"
    "buffer_load_dwordx4 v121, s[12:15], s70 offen lds\n"
    "ds_read_b64_tr_b16 v[90:91], v104 offset:13312\n"
    "v_mfma_f32_16x16x32_bf16 v[48:51], v[176:179], v[156:159], v[48:51]\n"
    "ds_read_b64_tr_b16 v[92:93], v105 offset:9216\n"
    "ds_read_b64_tr_b16 v[94:95], v105 offset:13312\n"
    "v_mfma_f32_16x16x32_bf16 v[44:47], v[180:183], v[152:155], v[44:47]\n"
    "ds_read_b64_tr_b16 v[96:97], v106 offset:9216\n"
    "s_add_u32 m0, s59, 0x1cc00\n"
    "v_mfma_f32_16x16x32_bf16 v[52:55], v[180:183], v[156:159], v[52:55]\n"
    "buffer_load_dwordx4 v123, s[12:15], s70 offen lds\n"
    "ds_read_b64_tr_b16 v[98:99], v106 offset:13312\n"
    "v_mfma_f32_16x16x32_bf16 v[56:59], v[184:187], v[152:155], v[56:59]\n"
    "ds_read_b64_tr_b16 v[100:101], v107 offset:9216\n"
    "ds_read_b64_tr_b16 v[102:103], v107 offset:13312\n"
    "v_mfma_f32_16x16x32_bf16 v[64:67], v[184:187], v[156:159], v[64:67]\n"
    "s_add_u32 s67, s67, 0x10000\n"
    "v_mfma_f32_16x16x32_bf16 v[60:63], v[188:191], v[152:155], v[60:63]\n"
    "v_mfma_f32_16x16x32_bf16 v[68:71], v[188:191], v[156:159], v[68:71]\n"
    "s_barrier\n"
    "s_waitcnt lgkmcnt(15)\n"
    "v_mfma_f32_16x16x32_bf16 v[8:11], v[224:227], a[0:3], v[8:11]\n"
    "v_add_u32 v104, 0x8000, v108\n"
    "v_xor_b32 v105, 32, v104\n"
    "v_mfma_f32_16x16x32_bf16 v[16:19], v[224:227], a[4:7], v[16:19]\n"
    "s_waitcnt vmcnt(13)\n"
    "v_cmp_ne_u32 s[86:87], v122, v7\n"
    "v_mfma_f32_16x16x32_bf16 v[12:15], v[228:231], a[0:3], v[12:15]\n"
    "v_xor_b32 v106, 64, v104\n"
    "v_xor_b32 v107, 0x60, v104\n"
    "v_mfma_f32_16x16x32_bf16 v[20:23], v[228:231], a[4:7], v[20:23]\n"
    "s_add_u32 s67, s3, s69\n"
    "s_and_b32 s67, s67, 7\n"
    "s_add_u32 s69, s69, 1\n"
    "s_cmp_eq_u32 s69, 7\n"
    "s_cselect_b32 s69, 0, s69\n"
    "v_mfma_f32_16x16x32_bf16 v[24:27], v[232:235], a[0:3], v[24:27]\n"
    "ds_read_b64_tr_b16 v[160:161], v104\n"
    "ds_read_b64_tr_b16 v[162:163], v104 offset:4096\n"
    "v_mfma_f32_16x16x32_bf16 v[32:35], v[232:235], a[4:7], v[32:35]\n"
    "ds_read_b64_tr_b16 v[164:165], v105\n"
    "ds_read_b64_tr_b16 v[166:167], v105 offset:4096\n"
    "v_mfma_f32_16x16x32_bf16 v[28:31], v[236:239], a[0:3], v[28:31]\n"
    "s_bitcmp1_b32 s60, 0\n"
    "s_cselect_b64 s[84:85], -1, 0\n"
    "s_or_b64 vcc, s[86:87], s[84:85]\n"
    "s_cselect_b32 s4, 0, 1\n"
    "s_or_b32 s82, s82, s4\n"
    "v_mfma_f32_16x16x32_bf16 v[36:39], v[236:239], a[4:7], v[36:39]\n"
    "ds_read_b64_tr_b16 v[168:169], v106\n"
    "ds_read_b64_tr_b16 v[170:171], v106 offset:4096\n"
    "v_mfma_f32_16x16x32_bf16 v[40:43], v[240:243], a[0:3], v[40:43]\n"
    "ds_read_b64_tr_b16 v[172:173], v107\n"
    "ds_read_b64_tr_b16 v[174:175], v107 offset:4096\n"
    "v_mfma_f32_16x16x32_bf16 v[48:51], v[240:243], a[4:7], v[48:51]\n"
    "ds_read_b64_tr_b16 v[176:177], v104 offset:1024\n"
    "ds_read_b64_tr_b16 v[178:179], v104 offset:5120\n"
    "v_mfma_f32_16x16x32_bf16 v[44:47], v[244:247], a[0:3], v[44:47]\n"
    "ds_read_b64_tr_b16 v[180:181], v105 offset:1024\n"
    "ds_read_b64_tr_b16 v[182:183], v105 offset:5120\n"
    "v_mfma_f32_16x16x32_bf16 v[52:55], v[244:247], a[4:7], v[52:55]\n"
    "ds_read_b64_tr_b16 v[184:185], v106 offset:1024\n"
    "s_lshl_b32 s67, s67, 17\n"
    "v_mfma_f32_16x16x32_bf16 v[56:59], v[248:251], a[0:3], v[56:59]\n"
    "ds_read_b64_tr_b16 v[186:187], v106 offset:5120\n"
    "ds_read_b64_tr_b16 v[188:189], v107 offset:1024\n"
    "v_mfma_f32_16x16x32_bf16 v[64:67], v[248:251], a[4:7], v[64:67]\n"
    "ds_read_b64_tr_b16 v[190:191], v107 offset:5120\n"
    "v_mfma_f32_16x16x32_bf16 v[60:63], v[252:255], a[0:3], v[60:63]\n"
    "s_add_u32 s67, s67, s90\n"
    "v_mfma_f32_16x16x32_bf16 v[68:71], v[252:255], a[4:7], v[68:71]\n"
    "s_waitcnt lgkmcnt(15)\n"
    "v_mfma_f32_16x16x32_bf16 v[8:11], v[72:75], a[8:11], v[8:11]\n"
    "ds_read_b64_tr_b16 v[224:225], v104 offset:8192\n"
    "s_add_u32 s70, s67, 0x2000\n"
    "v_mfma_f32_16x16x32_bf16 v[16:19], v[72:75], a[12:15], v[16:19]\n"
    "ds_read_b64_tr_b16 v[226:227], v104 offset:12288\n"
    "ds_read_b64_tr_b16 v[228:229], v105 offset:8192\n"
    "v_mfma_f32_16x16x32_bf16 v[12:15], v[76:79], a[8:11], v[12:15]\n"
    "ds_read_b64_tr_b16 v[230:231], v105 offset:12288\n"
    "s_add_u32 m0, s59, 0x20000\n"
    "v_mfma_f32_16x16x32_bf16 v[20:23], v[76:79], a[12:15], v[20:23]\n"
    "s_cbranch_vccnz Lc1_0\n"
    "s_add_u32 s66, s3, 7\n"
    "s_and_b32 s66, s66, 7\n"
    "s_lshl_b32 s66, s66, 12\n"
    "s_add_u32 s66, s66, s74\n"
    "buffer_load_dwordx4 v[192:195], v2, s[8:11], s66 offen sc1\n"
    "buffer_load_dwordx4 v[196:199], v120, s[8:11], s66 offen sc1\n"
    "buffer_load_dwordx4 v[200:203], v2, s[8:11], s66 offen offset:1024 sc1\n"
    "buffer_load_dwordx4 v[204:207], v120, s[8:11], s66 offen offset:1024 sc1\n"
    "Lc1_0:\n"
    "v_mfma_f32_16x16x32_bf16 v[24:27], v[80:83], a[8:11], v[24:27]\n"
    "ds_read_b64_tr_b16 v[232:233], v106 offset:8192\n"
    "ds_read_b64_tr_b16 v[234:235], v106 offset:12288\n"
    "v_mfma_f32_16x16x32_bf16 v[32:35], v[80:83], a[12:15], v[32:35]\n"
    "buffer_load_dwordx4 v121, s[12:15], s67 offen lds\n"
    "ds_read_b64_tr_b16 v[236:237], v107 offset:8192\n"
    "v_mfma_f32_16x16x32_bf16 v[28:31], v[84:87], a[8:11], v[28:31]\n"
    "ds_read_b64_tr_b16 v[238:239], v107 offset:12288\n"
    "ds_read_b64_tr_b16 v[240:241], v104 offset:9216\n"
    "v_mfma_f32_16x16x32_bf16 v[36:39], v[84:87], a[12:15], v[36:39]\n"
    "ds_read_b64_tr_b16 v[242:243], v104 offset:13312\n"
    "s_add_u32 m0, s59, 0x20400\n"
    "v_mfma_f32_16x16x32_bf16 v[40:43], v[88:91], a[8:11], v[40:43]\n"
    "ds_read_b64_tr_b16 v[244:245], v105 offset:9216\n"
    "ds_read_b64_tr_b16 v[246:247], v105 offset:13312\n"
    "v_mfma_f32_16x16x32_bf16 v[48:51], v[88:91], a[12:15], v[48:51]\n"
    "buffer_load_dwordx4 v123, s[12:15], s67 offen lds\n"
    "ds_read_b64_tr_b16 v[248:249], v106 offset:9216\n"
    "v_mfma_f32_16x16x32_bf16 v[44:47], v[92:95], a[8:11], v[44:47]\n"
    "ds_read_b64_tr_b16 v[250:251], v106 offset:13312\n"
    "ds_read_b64_tr_b16 v[252:253], v107 offset:9216\n"
    "v_mfma_f32_16x16x32_bf16 v[52:55], v[92:95], a[12:15], v[52:55]\n"
    "ds_read_b64_tr_b16 v[254:255], v107 offset:13312\n"
    "v_mfma_f32_16x16x32_bf16 v[56:59], v[96:99], a[8:11], v[56:59]\n"
    "s_add_u32 m0, s59, 0x20800\n"
    "v_mfma_f32_16x16x32_bf16 v[64:67], v[96:99], a[12:15], v[64:67]\n"
    "v_mfma_f32_16x16x32_bf16 v[60:63], v[100:103], a[8:11], v[60:63]\n"
    "buffer_load_dwordx4 v121, s[12:15], s70 offen lds\n"
    "v_mfma_f32_16x16x32_bf16 v[68:71], v[100:103], a[12:15], v[68:71]\n"
    "s_waitcnt lgkmcnt(15)\n"
    "v_mfma_f32_16x16x32_bf16 v[8:11], v[160:163], a[16:19], v[8:11]\n"
    "v_add_u32 v104, 0x10000, v108\n"
    "v_xor_b32 v105, 32, v104\n"
    "v_mfma_f32_16x16x32_bf16 v[16:19], v[160:163], a[20:23], v[16:19]\n"
    "v_xor_b32 v106, 64, v104\n"
    "s_add_u32 m0, s59, 0x20c00\n"
    "v_mfma_f32_16x16x32_bf16 v[12:15], v[164:167], a[16:19], v[12:15]\n"
    "v_xor_b32 v107, 0x60, v104\n"
    "v_mfma_f32_16x16x32_bf16 v[20:23], v[164:167], a[20:23], v[20:23]\n"
    "ds_read_b64_tr_b16 v[72:73], v104\n"
    "buffer_load_dwordx4 v123, s[12:15], s70 offen lds\n"
    "v_mfma_f32_16x16x32_bf16 v[24:27], v[168:171], a[16:19], v[24:27]\n"
    "ds_read_b64_tr_b16 v[74:75], v104 offset:4096\n"
    "ds_read_b64_tr_b16 v[76:77], v105\n"
    "v_mfma_f32_16x16x32_bf16 v[32:35], v[168:171], a[20:23], v[32:35]\n"
    "ds_read_b64_tr_b16 v[78:79], v105 offset:4096\n"
    "s_add_u32 s67, s67, 0x10000\n"
    "v_mfma_f32_16x16x32_bf16 v[28:31], v[172:175], a[16:19], v[28:31]\n"
    "ds_read_b64_tr_b16 v[80:81], v106\n"
    "ds_read_b64_tr_b16 v[82:83], v106 offset:4096\n"
    "v_mfma_f32_16x16x32_bf16 v[36:39], v[172:175], a[20:23], v[36:39]\n"
    "ds_read_b64_tr_b16 v[84:85], v107\n"
    "s_add_u32 s70, s67, 0x2000\n"
    "v_mfma_f32_16x16x32_bf16 v[40:43], v[176:179], a[16:19], v[40:43]\n"
    "ds_read_b64_tr_b16 v[86:87], v107 offset:4096\n"
    "ds_read_b64_tr_b16 v[88:89], v104 offset:1024\n"
    "v_mfma_f32_16x16x32_bf16 v[48:51], v[176:179], a[20:23], v[48:51]\n"
    "ds_read_b64_tr_b16 v[90:91], v104 offset:5120\n"
    "ds_read_b64_tr_b16 v[92:93], v105 offset:1024\n"
    "v_mfma_f32_16x16x32_bf16 v[44:47], v[180:183], a[16:19], v[44:47]\n"
    "ds_read_b64_tr_b16 v[94:95], v105 offset:5120\n"
    "s_add_u32 m0, s59, 0x24000\n"
    "v_mfma_f32_16x16x32_bf16 v[52:55], v[180:183], a[20:23], v[52:55]\n"
    "s_cbranch_vccnz Lc1_1\n"
    "buffer_load_dwordx4 v[208:211], v2, s[8:11], s66 offen offset:2048 sc1\n"
    "buffer_load_dwordx4 v[212:215], v120, s[8:11], s66 offen offset:2048 sc1\n"
    "buffer_load_dwordx4 v[216:219], v2, s[8:11], s66 offen offset:3072 sc1\n"
    "buffer_load_dwordx4 v[220:223], v120, s[8:11], s66 offen offset:3072 sc1\n"
    "Lc1_1:\n"
    "v_mfma_f32_16x16x32_bf16 v[56:59], v[184:187], a[16:19], v[56:59]\n"
    "ds_read_b64_tr_b16 v[96:97], v106 offset:1024\n"
    "buffer_load_dwordx4 v121, s[12:15], s67 offen lds\n"
    "v_mfma_f32_16x16x32_bf16 v[64:67], v[184:187], a[20:23], v[64:67]\n"
    "ds_read_b64_tr_b16 v[98:99], v106 offset:5120\n"
    "ds_read_b64_tr_b16 v[100:101], v107 offset:1024\n"
    "v_mfma_f32_16x16x32_bf16 v[60:63], v[188:191], a[16:19], v[60:63]\n"
    "ds_read_b64_tr_b16 v[102:103], v107 offset:5120\n"
    "s_add_u32 m0, s59, 0x24400\n"
    "v_mfma_f32_16x16x32_bf16 v[68:71], v[188:191], a[20:23], v[68:71]\n"
    "s_waitcnt lgkmcnt(15)\n"
    "v_mfma_f32_16x16x32_bf16 v[8:11], v[224:227], a[24:27], v[8:11]\n"
    "buffer_load_dword v122, v5, s[8:11], s7 offen sc1\n"
    "ds_read_b64_tr_b16 v[160:161], v104 offset:8192\n"
    "v_mfma_f32_16x16x32_bf16 v[16:19], v[224:227], a[28:31], v[16:19]\n"
    "ds_read_b64_tr_b16 v[162:163], v104 offset:12288\n"
    "buffer_load_dwordx4 v123, s[12:15], s67 offen lds\n"
    "v_mfma_f32_16x16x32_bf16 v[12:15], v[228:231], a[24:27], v[12:15]\n"
    "ds_read_b64_tr_b16 v[164:165], v105 offset:8192\n"
    "ds_read_b64_tr_b16 v[166:167], v105 offset:12288\n"
    "v_mfma_f32_16x16x32_bf16 v[20:23], v[228:231], a[28:31], v[20:23]\n"
    "ds_read_b64_tr_b16 v[168:169], v106 offset:8192\n"
    "s_add_u32 m0, s59, 0x24800\n"
    "v_mfma_f32_16x16x32_bf16 v[24:27], v[232:235], a[24:27], v[24:27]\n"
    "ds_read_b64_tr_b16 v[170:171], v106 offset:12288\n"
    "ds_read_b64_tr_b16 v[172:173], v107 offset:8192\n"
    "v_mfma_f32_16x16x32_bf16 v[32:35], v[232:235], a[28:31], v[32:35]\n"
    "buffer_load_dwordx4 v121, s[12:15], s70 offen lds\n"
    "ds_read_b64_tr_b16 v[174:175], v107 offset:12288\n"
    "v_mfma_f32_16x16x32_bf16 v[28:31], v[236:239], a[24:27], v[28:31]\n"
    "ds_read_b64_tr_b16 v[176:177], v104 offset:9216\n"
    "ds_read_b64_tr_b16 v[178:179], v104 offset:13312\n"
    "v_mfma_f32_16x16x32_bf16 v[36:39], v[236:239], a[28:31], v[36:39]\n"
    "ds_read_b64_tr_b16 v[180:181], v105 offset:9216\n"
    "ds_read_b64_tr_b16 v[182:183], v105 offset:13312\n"
    "v_mfma_f32_16x16x32_bf16 v[40:43], v[240:243], a[24:27], v[40:43]\n"
    "ds_read_b64_tr_b16 v[184:185], v106 offset:9216\n"
    "s_add_u32 m0, s59, 0x24c00\n"
    "v_mfma_f32_16x16x32_bf16 v[48:51], v[240:243], a[28:31], v[48:51]\n"
    "ds_read_b64_tr_b16 v[186:187], v106 offset:13312\n"
    "ds_read_b64_tr_b16 v[188:189], v107 offset:9216\n"
    "v_mfma_f32_16x16x32_bf16 v[44:47], v[244:247], a[24:27], v[44:47]\n"
    "buffer_load_dwordx4 v123, s[12:15], s70 offen lds\n"
    "ds_read_b64_tr_b16 v[190:191], v107 offset:13312\n"
    "v_mfma_f32_16x16x32_bf16 v[52:55], v[244:247], a[28:31], v[52:55]\n"
    "v_mfma_f32_16x16x32_bf16 v[56:59], v[248:251], a[24:27], v[56:59]\n"
    "s_add_u32 s67, s67, 0x10000\n"
    "v_mfma_f32_16x16x32_bf16 v[64:67], v[248:251], a[28:31], v[64:67]\n"
    "v_mfma_f32_16x16x32_bf16 v[60:63], v[252:255], a[24:27], v[60:63]\n"
    "v_mfma_f32_16x16x32_bf16 v[68:71], v[252:255], a[28:31], v[68:71]\n"
    "s_barrier\n"
    "s_waitcnt lgkmcnt(15)\n"
    "v_mfma_f32_16x16x32_bf16 v[8:11], v[72:75], a[32:35], v[8:11]\n"
    "v_add_u32 v104, 0x18000, v108\n"
    "v_xor_b32 v105, 32, v104\n"
    "v_mfma_f32_16x16x32_bf16 v[16:19], v[72:75], a[36:39], v[16:19]\n"
    "s_add_u32 s67, s3, s69\n"
    "s_and_b32 s67, s67, 7\n"
    "s_add_u32 s69, s69, 1\n"
    "s_cmp_eq_u32 s69, 7\n"
    "s_cselect_b32 s69, 0, s69\n"
    "v_mfma_f32_16x16x32_bf16 v[12:15], v[76:79], a[32:35], v[12:15]\n"
    "v_xor_b32 v106, 64, v104\n"
    "v_xor_b32 v107, 0x60, v104\n"
    "v_mfma_f32_16x16x32_bf16 v[20:23], v[76:79], a[36:39], v[20:23]\n"
    "ds_read_b64_tr_b16 v[224:225], v104\n"
    "ds_read_b64_tr_b16 v[226:227], v104 offset:4096\n"
    "v_mfma_f32_16x16x32_bf16 v[24:27], v[80:83], a[32:35], v[24:27]\n"
    "ds_read_b64_tr_b16 v[228:229], v105\n"
    "ds_read_b64_tr_b16 v[230:231], v105 offset:4096\n"
    "v_mfma_f32_16x16x32_bf16 v[32:35], v[80:83], a[36:39], v[32:35]\n"
    "ds_read_b64_tr_b16 v[232:233], v106\n"
    "ds_read_b64_tr_b16 v[234:235], v106 offset:4096\n"
    "v_mfma_f32_16x16x32_bf16 v[28:31], v[84:87], a[32:35], v[28:31]\n"
    "ds_read_b64_tr_b16 v[236:237], v107\n"
    "ds_read_b64_tr_b16 v[238:239], v107 offset:4096\n"
    "v_mfma_f32_16x16x32_bf16 v[36:39], v[84:87], a[36:39], v[36:39]\n"
    "ds_read_b64_tr_b16 v[240:241], v104 offset:1024\n"
    "s_lshl_b32 s67, s67, 17\n"
    "v_mfma_f32_16x16x32_bf16 v[40:43], v[88:91], a[32:35], v[40:43]\n"
    "ds_read_b64_tr_b16 v[242:243], v104 offset:5120\n"
    "s_add_u32 s67, s67, s90\n"
    "v_mfma_f32_16x16x32_bf16 v[48:51], v[88:91], a[36:39], v[48:51]\n"
    "ds_read_b64_tr_b16 v[244:245], v105 offset:1024\n"
    "ds_read_b64_tr_b16 v[246:247], v105 offset:5120\n"
    "v_mfma_f32_16x16x32_bf16 v[44:47], v[92:95], a[32:35], v[44:47]\n"
    "ds_read_b64_tr_b16 v[248:249], v106 offset:1024\n"
    "s_add_u32 s70, s67, 0x2000\n"
    "v_mfma_f32_16x16x32_bf16 v[52:55], v[92:95], a[36:39], v[52:55]\n"
    "ds_read_b64_tr_b16 v[250:251], v106 offset:5120\n"
    "s_add_u32 m0, s59, 0\n"
    "v_mfma_f32_16x16x32_bf16 v[56:59], v[96:99], a[32:35], v[56:59]\n"
    "buffer_load_dwordx4 v121, s[12:15], s67 offen lds\n"
    "ds_read_b64_tr_b16 v[252:253], v107 offset:1024\n"
    "v_mfma_f32_16x16x32_bf16 v[64:67], v[96:99], a[36:39], v[64:67]\n"
    "ds_read_b64_tr_b16 v[254:255], v107 offset:5120\n"
    "v_mfma_f32_16x16x32_bf16 v[60:63], v[100:103], a[32:35], v[60:63]\n"
    "s_add_u32 m0, s59, 0x400\n"
    "v_mfma_f32_16x16x32_bf16 v[68:71], v[100:103], a[36:39], v[68:71]\n"
    "buffer_load_dwordx4 v123, s[12:15], s67 offen lds\n"
    "s_waitcnt lgkmcnt(15)\n"
    "v_mfma_f32_16x16x32_bf16 v[8:11], v[160:163], a[40:43], v[8:11]\n"
    "ds_read_b64_tr_b16 v[72:73], v104 offset:8192\n"
    "ds_read_b64_tr_b16 v[74:75], v104 offset:12288\n"
    "v_mfma_f32_16x16x32_bf16 v[16:19], v[160:163], a[44:47], v[16:19]\n"
    "ds_read_b64_tr_b16 v[76:77], v105 offset:8192\n"
    "s_add_u32 m0, s59, 0x800\n"
    "v_mfma_f32_16x16x32_bf16 v[12:15], v[164:167], a[40:43], v[12:15]\n"
    "buffer_load_dwordx4 v121, s[12:15], s70 offen lds\n"
    "ds_read_b64_tr_b16 v[78:79], v105 offset:12288\n"
    "v_mfma_f32_16x16x32_bf16 v[20:23], v[164:167], a[44:47], v[20:23]\n"
    "ds_read_b64_tr_b16 v[80:81], v106 offset:8192\n"
    "ds_read_b64_tr_b16 v[82:83], v106 offset:12288\n"
    "v_mfma_f32_16x16x32_bf16 v[24:27], v[168:171], a[40:43], v[24:27]\n"
    "s_waitcnt vmcnt(6)\n"
    "v_cmp_ne_u32 s[86:87], v122, v7\n"
    "v_mfma_f32_16x16x32_bf16 v[32:35], v[168:171], a[44:47], v[32:35]\n"
    "ds_read_b64_tr_b16 v[84:85], v107 offset:8192\n"
    "s_add_u32 m0, s59, 0xc00\n"
    "v_mfma_f32_16x16x32_bf16 v[28:31], v[172:175], a[40:43], v[28:31]\n"
    "buffer_load_dwordx4 v123, s[12:15], s70 offen lds\n"
    "s_add_u32 s67, s67, 0x10000\n"
    "v_mfma_f32_16x16x32_bf16 v[36:39], v[172:175], a[44:47], v[36:39]\n"
    "ds_read_b64_tr_b16 v[86:87], v107 offset:12288\n"
    "ds_read_b64_tr_b16 v[88:89], v104 offset:9216\n"
    "v_mfma_f32_16x16x32_bf16 v[40:43], v[176:179], a[40:43], v[40:43]\n"
    "s_bitcmp1_b32 s82, 1\n"
    "s_cselect_b64 s[84:85], -1, 0\n"
    "s_or_b64 vcc, s[86:87], s[84:85]\n"
    "s_cselect_b32 s4, 0, 2\n"
    "s_or_b32 s82, s82, s4\n"
    "v_mfma_f32_16x16x32_bf16 v[48:51], v[176:179], a[44:47], v[48:51]\n"
    "ds_read_b64_tr_b16 v[90:91], v104 offset:13312\n"
    "s_add_u32 s70, s67, 0x2000\n"
    "v_mfma_f32_16x16x32_bf16 v[44:47], v[180:183], a[40:43], v[44:47]\n"
    "ds_read_b64_tr_b16 v[92:93], v105 offset:9216\n"
    "s_add_u32 m0, s59, 0x8000\n"
    "v_mfma_f32_16x16x32_bf16 v[52:55], v[180:183], a[44:47], v[52:55]\n"
    "ds_read_b64_tr_b16 v[94:95], v105 offset:13312\n"
    "buffer_load_dwordx4 v121, s[12:15], s67 offen lds\n"
    "v_mfma_f32_16x16x32_bf16 v[56:59], v[184:187], a[40:43], v[56:59]\n"
    "ds_read_b64_tr_b16 v[96:97], v106 offset:9216\n"
    "s_add_u32 m0, s59, 0x8400\n"
    "v_mfma_f32_16x16x32_bf16 v[64:67], v[184:187], a[44:47], v[64:67]\n"
    "ds_read_b64_tr_b16 v[98:99], v106 offset:13312\n"
    "ds_read_b64_tr_b16 v[100:101], v107 offset:9216\n"
    "v_mfma_f32_16x16x32_bf16 v[60:63], v[188:191], a[40:43], v[60:63]\n"
    "buffer_load_dwordx4 v123, s[12:15], s67 offen lds\n"
    "ds_read_b64_tr_b16 v[102:103], v107 offset:13312\n"
    "v_mfma_f32_16x16x32_bf16 v[68:71], v[188:191], a[44:47], v[68:71]\n"
    "s_cbranch_vccnz Lc2a_0\n"
    "s_add_u32 s66, s3, s71\n"
    "s_and_b32 s66, s66, 7\n"
    "s_add_u32 s71, s71, 1\n"
    "s_cmp_eq_u32 s71, 7\n"
    "s_cselect_b32 s71, 0, s71\n"
    "s_lshl_b32 s66, s66, 12\n"
    "s_add_u32 s66, s66, s74\n"
    "buffer_load_dwordx4 a[64:67], v2, s[8:11], s66 offen sc1\n"
    "buffer_load_dwordx4 a[68:71], v120, s[8:11], s66 offen sc1\n"
    "buffer_load_dwordx4 a[72:75], v2, s[8:11], s66 offen offset:1024 sc1\n"
    "buffer_load_dwordx4 a[76:79], v120, s[8:11], s66 offen offset:1024 sc1\n"
    "Lc2a_0:\n"
    "s_waitcnt lgkmcnt(15)\n"
    "v_mfma_f32_16x16x32_bf16 v[8:11], v[224:227], a[48:51], v[8:11]\n"
    "v_add_u32 v104, 0x4000, v108\n"
    "s_add_u32 m0, s59, 0x8800\n"
    "v_mfma_f32_16x16x32_bf16 v[16:19], v[224:227], a[52:55], v[16:19]\n"
    "v_xor_b32 v105, 32, v104\n"
    "v_xor_b32 v106, 64, v104\n"
    "v_mfma_f32_16x16x32_bf16 v[12:15], v[228:231], a[48:51], v[12:15]\n"
    "buffer_load_dwordx4 v121, s[12:15], s70 offen lds\n"
    "v_xor_b32 v107, 0x60, v104\n"
    "v_mfma_f32_16x16x32_bf16 v[20:23], v[228:231], a[52:55], v[20:23]\n"
    "ds_read_b64_tr_b16 v[160:161], v104\n"
    "s_add_u32 m0, s59, 0x8c00\n"
    "v_mfma_f32_16x16x32_bf16 v[24:27], v[232:235], a[48:51], v[24:27]\n"
    "ds_read_b64_tr_b16 v[162:163], v104 offset:4096\n"
    "buffer_load_dwordx4 v123, s[12:15], s70 offen lds\n"
    "v_mfma_f32_16x16x32_bf16 v[32:35], v[232:235], a[52:55], v[32:35]\n"
    "ds_read_b64_tr_b16 v[164:165], v105\n"
    "ds_read_b64_tr_b16 v[166:167], v105 offset:4096\n"
    "v_mfma_f32_16x16x32_bf16 v[28:31], v[236:239], a[48:51], v[28:31]\n"
    "ds_read_b64_tr_b16 v[168:169], v106\n"
    "s_add_u32 s67, s67, 0x10000\n"
    "v_mfma_f32_16x16x32_bf16 v[36:39], v[236:239], a[52:55], v[36:39]\n"
    "ds_read_b64_tr_b16 v[170:171], v106 offset:4096\n"
    "v_and_b32 v3, 15, v1\n"
    "v_mfma_f32_16x16x32_bf16 v[40:43], v[240:243], a[48:51], v[40:43]\n"
    "ds_read_b64_tr_b16 v[172:173], v107\n"
    "v_lshlrev_b32 v3, 10, v3\n"
    "v_mfma_f32_16x16x32_bf16 v[48:51], v[240:243], a[52:55], v[48:51]\n"
    "ds_read_b64_tr_b16 v[174:175], v107 offset:4096\n"
    "ds_read_b64_tr_b16 v[176:177], v104 offset:1024\n"
    "v_mfma_f32_16x16x32_bf16 v[44:47], v[244:247], a[48:51], v[44:47]\n"
    "ds_read_b64_tr_b16 v[178:179], v104 offset:5120\n"
    "v_lshrrev_b32 v4, 4, v1\n"
    "v_mfma_f32_16x16x32_bf16 v[52:55], v[244:247], a[52:55], v[52:55]\n"
    "ds_read_b64_tr_b16 v[180:181], v105 offset:1024\n"
    "v_lshl_add_u32 v110, v4, 3, v3\n"
    "v_mfma_f32_16x16x32_bf16 v[56:59], v[248:251], a[48:51], v[56:59]\n"
    "ds_read_b64_tr_b16 v[182:183], v105 offset:5120\n"
    "ds_read_b64_tr_b16 v[184:185], v106 offset:1024\n"
    "v_mfma_f32_16x16x32_bf16 v[64:67], v[248:251], a[52:55], v[64:67]\n"
    "ds_read_b64_tr_b16 v[186:187], v106 offset:5120\n"
    "v_add_u32 v6, s62, v110\n"
    "v_mfma_f32_16x16x32_bf16 v[60:63], v[252:255], a[48:51], v[60:63]\n"
    "ds_read_b64_tr_b16 v[188:189], v107 offset:1024\n"
    "s_lshr_b32 s75, s65, 2\n"
    "v_mfma_f32_16x16x32_bf16 v[68:71], v[252:255], a[52:55], v[68:71]\n"
    "ds_read_b64_tr_b16 v[190:191], v107 offset:5120\n"
    "s_lshl_b32 s75, s75, 3\n"
    "s_waitcnt lgkmcnt(15)\n"
    "v_mfma_f32_16x16x32_bf16 v[8:11], v[72:75], a[56:59], v[8:11]\n"
    "ds_read_b64_tr_b16 v[224:225], v104 offset:8192\n"
    "s_add_u32 s75, s75, s58\n"
    "v_mfma_f32_16x16x32_bf16 v[16:19], v[72:75], a[60:63], v[16:19]\n"
    "ds_read_b64_tr_b16 v[226:227], v104 offset:12288\n"
    "ds_read_b64_tr_b16 v[228:229], v105 offset:8192\n"
    "v_mfma_f32_16x16x32_bf16 v[12:15], v[76:79], a[56:59], v[12:15]\n"
    "s_cbranch_vccnz Lc2a_1\n"
    "buffer_load_dwordx4 a[80:83], v2, s[8:11], s66 offen offset:2048 sc1\n"
    "buffer_load_dwordx4 a[84:87], v120, s[8:11], s66 offen offset:2048 sc1\n"
    "buffer_load_dwordx4 a[88:91], v2, s[8:11], s66 offen offset:3072 sc1\n"
    "buffer_load_dwordx4 a[92:95], v120, s[8:11], s66 offen offset:3072 sc1\n"
    "Lc2a_1:\n"
    "v_mfma_f32_16x16x32_bf16 v[20:23], v[76:79], a[60:63], v[20:23]\n"
    "ds_read_b64_tr_b16 v[230:231], v105 offset:12288\n"
    "s_lshl_b32 s75, s75, 14\n"
    "v_mfma_f32_16x16x32_bf16 v[24:27], v[80:83], a[56:59], v[24:27]\n"
    "ds_read_b64_tr_b16 v[232:233], v106 offset:8192\n"
    "s_lshl_b32 s4, s64, 8\n"
    "v_mfma_f32_16x16x32_bf16 v[32:35], v[80:83], a[60:63], v[32:35]\n"
    "ds_read_b64_tr_b16 v[234:235], v106 offset:12288\n"
    "s_add_u32 s75, s75, s4\n"
    "v_mfma_f32_16x16x32_bf16 v[28:31], v[84:87], a[56:59], v[28:31]\n"
    "ds_read_b64_tr_b16 v[236:237], v107 offset:8192\n"
    "s_lshl_b32 s4, s63, 6\n"
    "v_mfma_f32_16x16x32_bf16 v[36:39], v[84:87], a[60:63], v[36:39]\n"
    "ds_read_b64_tr_b16 v[238:239], v107 offset:12288\n"
    "s_add_u32 s75, s75, s4\n"
    "v_mfma_f32_16x16x32_bf16 v[40:43], v[88:91], a[56:59], v[40:43]\n"
    "ds_read_b64_tr_b16 v[240:241], v104 offset:9216\n"
    "ds_read_b64_tr_b16 v[242:243], v104 offset:13312\n"
    "v_mfma_f32_16x16x32_bf16 v[48:51], v[88:91], a[60:63], v[48:51]\n"
    "buffer_load_dwordx2 v[112:113], v110, s[20:23], s75 offen\n"
    "ds_read_b64_tr_b16 v[244:245], v105 offset:9216\n"
    "v_mfma_f32_16x16x32_bf16 v[44:47], v[92:95], a[56:59], v[44:47]\n"
    "buffer_load_dwordx2 v[114:115], v110, s[20:23], s75 offen offset:32\n"
    "ds_read_b64_tr_b16 v[246:247], v105 offset:13312\n"
    "v_mfma_f32_16x16x32_bf16 v[52:55], v[92:95], a[60:63], v[52:55]\n"
    "ds_read_b64_tr_b16 v[248:249], v106 offset:9216\n"
    "ds_read_b64_tr_b16 v[250:251], v106 offset:13312\n"
    "v_mfma_f32_16x16x32_bf16 v[56:59], v[96:99], a[56:59], v[56:59]\n"
    "buffer_load_dwordx2 v[116:117], v6, s[20:23], s75 offen\n"
    "ds_read_b64_tr_b16 v[252:253], v107 offset:9216\n"
    "v_mfma_f32_16x16x32_bf16 v[64:67], v[96:99], a[60:63], v[64:67]\n"
    "buffer_load_dwordx2 v[118:119], v6, s[20:23], s75 offen offset:32\n"
    "ds_read_b64_tr_b16 v[254:255], v107 offset:13312\n"
    "v_mfma_f32_16x16x32_bf16 v[60:63], v[100:103], a[56:59], v[60:63]\n"
    "v_mfma_f32_16x16x32_bf16 v[68:71], v[100:103], a[60:63], v[68:71]\n"
    "buffer_load_dword v122, v5, s[8:11], s7 offen sc1\n"
    "s_barrier\n"
    "s_or_b32 s4, s60, s82\n"
    "s_bitcmp1_b32 s4, 0\n"
    "s_cbranch_scc1 Lcl3\n"
    "s_waitcnt vmcnt(0)\n"
    "v_cmp_ne_u32 vcc, v122, v7\n"
    "s_nop 0\n"
    "s_cbranch_vccnz Lspin5\n"
    "Lspinret5:\n"
    "s_add_u32 s66, s3, 7\n"
    "s_and_b32 s66, s66, 7\n"
    "s_lshl_b32 s66, s66, 12\n"
    "s_add_u32 s66, s66, s74\n"
    "buffer_load_dwordx4 v[192:195], v2, s[8:11], s66 offen sc1\n"
    "buffer_load_dwordx4 v[196:199], v120, s[8:11], s66 offen sc1\n"
    "buffer_load_dwordx4 v[200:203], v2, s[8:11], s66 offen offset:1024 sc1\n"
    "buffer_load_dwordx4 v[204:207], v120, s[8:11], s66 offen offset:1024 sc1\n"
    "buffer_load_dwordx4 v[208:211], v2, s[8:11], s66 offen offset:2048 sc1\n"
    "buffer_load_dwordx4 v[212:215], v120, s[8:11], s66 offen offset:2048 sc1\n"
    "buffer_load_dwordx4 v[216:219], v2, s[8:11], s66 offen offset:3072 sc1\n"
    "buffer_load_dwordx4 v[220:223], v120, s[8:11], s66 offen offset:3072 sc1\n"
    "Lcl3:\n"
    "s_waitcnt lgkmcnt(15)\n"
    "s_waitcnt vmcnt(7)\n"
    "v_mfma_f32_16x16x32_bf16 v[8:11], v[160:163], v[192:195], v[8:11]\n"
    "v_add_u32 v104, 0xc000, v108\n"
    "v_xor_b32 v105, 32, v104\n"
    "s_waitcnt vmcnt(6)\n"
    "v_mfma_f32_16x16x32_bf16 v[16:19], v[160:163], v[196:199], v[16:19]\n"
    "s_add_u32 s67, s3, s69\n"
    "s_and_b32 s67, s67, 7\n"
    "s_add_u32 s69, s69, 1\n"
    "s_cmp_eq_u32 s69, 7\n"
    "s_cselect_b32 s69, 0, s69\n"
    "v_mfma_f32_16x16x32_bf16 v[12:15], v[164:167], v[192:195], v[12:15]\n"
    "v_xor_b32 v106, 64, v104\n"
    "v_xor_b32 v107, 0x60, v104\n"
    "v_mfma_f32_16x16x32_bf16 v[20:23], v[164:167], v[196:199], v[20:23]\n"
    "ds_read_b64_tr_b16 v[72:73], v104\n"
    "ds_read_b64_tr_b16 v[74:75], v104 offset:4096\n"
    "v_mfma_f32_16x16x32_bf16 v[24:27], v[168:171], v[192:195], v[24:27]\n"
    "ds_read_b64_tr_b16 v[76:77], v105\n"
    "ds_read_b64_tr_b16 v[78:79], v105 offset:4096\n"
    "v_mfma_f32_16x16x32_bf16 v[32:35], v[168:171], v[196:199], v[32:35]\n"
    "ds_read_b64_tr_b16 v[80:81], v106\n"
    "ds_read_b64_tr_b16 v[82:83], v106 offset:4096\n"
    "v_mfma_f32_16x16x32_bf16 v[28:31], v[172:175], v[192:195], v[28:31]\n"
    "ds_read_b64_tr_b16 v[84:85], v107\n"
    "ds_read_b64_tr_b16 v[86:87], v107 offset:4096\n"
    "v_mfma_f32_16x16x32_bf16 v[36:39], v[172:175], v[196:199], v[36:39]\n"
    "ds_read_b64_tr_b16 v[88:89], v104 offset:1024\n"
    "ds_read_b64_tr_b16 v[90:91], v104 offset:5120\n"
    "v_mfma_f32_16x16x32_bf16 v[40:43], v[176:179], v[192:195], v[40:43]\n"
    "ds_read_b64_tr_b16 v[92:93], v105 offset:1024\n"
    "ds_read_b64_tr_b16 v[94:95], v105 offset:5120\n"
    "v_mfma_f32_16x16x32_bf16 v[48:51], v[176:179], v[196:199], v[48:51]\n"
    "ds_read_b64_tr_b16 v[96:97], v106 offset:1024\n"
    "ds_read_b64_tr_b16 v[98:99], v106 offset:5120\n"
    "v_mfma_f32_16x16x32_bf16 v[44:47], v[180:183], v[192:195], v[44:47]\n"
    "ds_read_b64_tr_b16 v[100:101], v107 offset:1024\n"
    "ds_read_b64_tr_b16 v[102:103], v107 offset:5120\n"
    "v_mfma_f32_16x16x32_bf16 v[52:55], v[180:183], v[196:199], v[52:55]\n"
    "s_lshl_b32 s67, s67, 17\n"
    "v_mfma_f32_16x16x32_bf16 v[56:59], v[184:187], v[192:195], v[56:59]\n"
    "v_mfma_f32_16x16x32_bf16 v[64:67], v[184:187], v[196:199], v[64:67]\n"
    "v_mfma_f32_16x16x32_bf16 v[60:63], v[188:191], v[192:195], v[60:63]\n"
    "s_add_u32 s67, s67, s90\n"
    "v_mfma_f32_16x16x32_bf16 v[68:71], v[188:191], v[196:199], v[68:71]\n"
    "s_waitcnt lgkmcnt(15)\n"
    "s_waitcnt vmcnt(5)\n"
    "v_mfma_f32_16x16x32_bf16 v[8:11], v[224:227], v[200:203], v[8:11]\n"
    "ds_read_b64_tr_b16 v[160:161], v104 offset:8192\n"
    "s_add_u32 s70, s67, 0x2000\n"
    "s_waitcnt vmcnt(4)\n"
    "v_mfma_f32_16x16x32_bf16 v[16:19], v[224:227], v[204:207], v[16:19]\n"
    "ds_read_b64_tr_b16 v[162:163], v104 offset:12288\n"
    "ds_read_b64_tr_b16 v[164:165], v105 offset:8192\n"
    "v_mfma_f32_16x16x32_bf16 v[12:15], v[228:231], v[200:203], v[12:15]\n"
    "ds_read_b64_tr_b16 v[166:167], v105 offset:12288\n"
    "s_add_u32 m0, s59, 0x10000\n"
    "v_mfma_f32_16x16x32_bf16 v[20:23], v[228:231], v[204:207], v[20:23]\n"
    "ds_read_b64_tr_b16 v[168:169], v106 offset:8192\n"
    "ds_read_b64_tr_b16 v[170:171], v106 offset:12288\n"
    "v_mfma_f32_16x16x32_bf16 v[24:27], v[232:235], v[200:203], v[24:27]\n"
    "s_waitcnt vmcnt(0)\n"
    "v_cmp_ne_u32 s[86:87], v122, v7\n"
    "v_mfma_f32_16x16x32_bf16 v[32:35], v[232:235], v[204:207], v[32:35]\n"
    "s_bitcmp1_b32 s82, 1\n"
    "s_cselect_b64 s[84:85], -1, 0\n"
    "s_or_b64 vcc, s[86:87], s[84:85]\n"
    "s_cselect_b32 s4, 0, 2\n"
    "s_or_b32 s82, s82, s4\n"
    "v_mfma_f32_16x16x32_bf16 v[28:31], v[236:239], v[200:203], v[28:31]\n"
    "ds_read_b64_tr_b16 v[172:173], v107 offset:8192\n"
    "buffer_load_dwordx4 v121, s[12:15], s67 offen lds\n"
    "v_mfma_f32_16x16x32_bf16 v[36:39], v[236:239], v[204:207], v[36:39]\n"
    "ds_read_b64_tr_b16 v[174:175], v107 offset:12288\n"
    "s_add_u32 m0, s59, 0x10400\n"
    "v_mfma_f32_16x16x32_bf16 v[40:43], v[240:243], v[200:203], v[40:43]\n"
    "ds_read_b64_tr_b16 v[176:177], v104 offset:9216\n"
    "ds_read_b64_tr_b16 v[178:179], v104 offset:13312\n"
    "v_mfma_f32_16x16x32_bf16 v[48:51], v[240:243], v[204:207], v[48:51]\n"
    "s_cbranch_vccnz Lc3a_0\n"
    "s_add_u32 s66, s3, s71\n"
    "s_and_b32 s66, s66, 7\n"
    "s_add_u32 s71, s71, 1\n"
    "s_cmp_eq_u32 s71, 7\n"
    "s_cselect_b32 s71, 0, s71\n"
    "s_lshl_b32 s66, s66, 12\n"
    "s_add_u32 s66, s66, s74\n"
    "buffer_load_dwordx4 a[64:67], v2, s[8:11], s66 offen sc1\n"
    "buffer_load_dwordx4 a[68:71], v120, s[8:11], s66 offen sc1\n"
    "buffer_load_dwordx4 a[72:75], v2, s[8:11], s66 offen offset:1024 sc1\n"
    "buffer_load_dwordx4 a[76:79], v120, s[8:11], s66 offen offset:1024 sc1\n"
    "Lc3a_0:\n"
    "v_mfma_f32_16x16x32_bf16 v[44:47], v[244:247], v[200:203], v[44:47]\n"
    "ds_read_b64_tr_b16 v[180:181], v105 offset:9216\n"
    "buffer_load_dwordx4 v123, s[12:15], s67 offen lds\n"
    "v_mfma_f32_16x16x32_bf16 v[52:55], v[244:247], v[204:207], v[52:55]\n"
    "ds_read_b64_tr_b16 v[182:183], v105 offset:13312\n"
    "ds_read_b64_tr_b16 v[184:185], v106 offset:9216\n"
    "v_mfma_f32_16x16x32_bf16 v[56:59], v[248:251], v[200:203], v[56:59]\n"
    "ds_read_b64_tr_b16 v[186:187], v106 offset:13312\n"
    "s_add_u32 m0, s59, 0x10800\n"
    "v_mfma_f32_16x16x32_bf16 v[64:67], v[248:251], v[204:207], v[64:67]\n"
    "ds_read_b64_tr_b16 v[188:189], v107 offset:9216\n"
    "ds_read_b64_tr_b16 v[190:191], v107 offset:13312\n"
    "v_mfma_f32_16x16x32_bf16 v[60:63], v[252:255], v[200:203], v[60:63]\n"
    "buffer_load_dwordx4 v121, s[12:15], s70 offen lds\n"
    "v_mfma_f32_16x16x32_bf16 v[68:71], v[252:255], v[204:207], v[68:71]\n"
    "s_waitcnt lgkmcnt(15)\n"
    "v_mfma_f32_16x16x32_bf16 v[8:11], v[72:75], v[208:211], v[8:11]\n"
    "v_add_u32 v104, 0x14000, v108\n"
    "v_xor_b32 v105, 32, v104\n"
    "v_mfma_f32_16x16x32_bf16 v[16:19], v[72:75], v[212:215], v[16:19]\n"
    "v_xor_b32 v106, 64, v104\n"
    "s_add_u32 m0, s59, 0x10c00\n"
    "v_mfma_f32_16x16x32_bf16 v[12:15], v[76:79], v[208:211], v[12:15]\n"
    "v_xor_b32 v107, 0x60, v104\n"
    "v_mfma_f32_16x16x32_bf16 v[20:23], v[76:79], v[212:215], v[20:23]\n"
    "ds_read_b64_tr_b16 v[224:225], v104\n"
    "buffer_load_dwordx4 v123, s[12:15], s70 offen lds\n"
    "v_mfma_f32_16x16x32_bf16 v[24:27], v[80:83], v[208:211], v[24:27]\n"
    "s_cbranch_vccnz Lc3a_1\n"
    "buffer_load_dwordx4 a[80:83], v2, s[8:11], s66 offen offset:2048 sc1\n"
    "buffer_load_dwordx4 a[84:87], v120, s[8:11], s66 offen offset:2048 sc1\n"
    "buffer_load_dwordx4 a[88:91], v2, s[8:11], s66 offen offset:3072 sc1\n"
    "buffer_load_dwordx4 a[92:95], v120, s[8:11], s66 offen offset:3072 sc1\n"
    "Lc3a_1:\n"
    "v_mfma_f32_16x16x32_bf16 v[32:35], v[80:83], v[212:215], v[32:35]\n"
    "ds_read_b64_tr_b16 v[226:227], v104 offset:4096\n"
    "ds_read_b64_tr_b16 v[228:229], v105\n"
    "v_mfma_f32_16x16x32_bf16 v[28:31], v[84:87], v[208:211], v[28:31]\n"
    "ds_read_b64_tr_b16 v[230:231], v105 offset:4096\n"
    "ds_read_b64_tr_b16 v[232:233], v106\n"
    "v_mfma_f32_16x16x32_bf16 v[36:39], v[84:87], v[212:215], v[36:39]\n"
    "ds_read_b64_tr_b16 v[234:235], v106 offset:4096\n"
    "s_add_u32 s67, s67, 0x10000\n"
    "v_mfma_f32_16x16x32_bf16 v[40:43], v[88:91], v[208:211], v[40:43]\n"
    "s_bitcmp1_b32 s82, 2\n"
    "s_cselect_b64 s[84:85], -1, 0\n"
    "s_or_b64 vcc, s[86:87], s[84:85]\n"
    "s_cselect_b32 s4, 0, 4\n"
    "s_or_b32 s82, s82, s4\n"
    "v_mfma_f32_16x16x32_bf16 v[48:51], v[88:91], v[212:215], v[48:51]\n"
    "ds_read_b64_tr_b16 v[236:237], v107\n"
    "ds_read_b64_tr_b16 v[238:239], v107 offset:4096\n"
    "v_mfma_f32_16x16x32_bf16 v[44:47], v[92:95], v[208:211], v[44:47]\n"
    "ds_read_b64_tr_b16 v[240:241], v104 offset:1024\n"
    "s_add_u32 s70, s67, 0x2000\n"
    "v_mfma_f32_16x16x32_bf16 v[52:55], v[92:95], v[212:215], v[52:55]\n"
    "ds_read_b64_tr_b16 v[242:243], v104 offset:5120\n"
    "ds_read_b64_tr_b16 v[244:245], v105 offset:1024\n"
    "v_mfma_f32_16x16x32_bf16 v[56:59], v[96:99], v[208:211], v[56:59]\n"
    "ds_read_b64_tr_b16 v[246:247], v105 offset:5120\n"
    "s_add_u32 m0, s59, 0x18000\n"
    "v_mfma_f32_16x16x32_bf16 v[64:67], v[96:99], v[212:215], v[64:67]\n"
    "s_cbranch_vccnz Lc3b_0\n"
    "s_add_u32 s66, s3, s71\n"
    "s_and_b32 s66, s66, 7\n"
    "s_add_u32 s71, s71, 1\n"
    "s_cmp_eq_u32 s71, 7\n"
    "s_cselect_b32 s71, 0, s71\n"
    "s_lshl_b32 s66, s66, 12\n"
    "s_add_u32 s66, s66, s74\n"
    "buffer_load_dwordx4 a[96:99], v2, s[8:11], s66 offen sc1\n"
    "buffer_load_dwordx4 a[100:103], v120, s[8:11], s66 offen sc1\n"
    "buffer_load_dwordx4 a[104:107], v2, s[8:11], s66 offen offset:1024 sc1\n"
    "buffer_load_dwordx4 a[108:111], v120, s[8:11], s66 offen offset:1024 sc1\n"
    "Lc3b_0:\n"
    "v_mfma_f32_16x16x32_bf16 v[60:63], v[100:103], v[208:211], v[60:63]\n"
    "ds_read_b64_tr_b16 v[248:249], v106 offset:1024\n"
    "ds_read_b64_tr_b16 v[250:251], v106 offset:5120\n"
    "v_mfma_f32_16x16x32_bf16 v[68:71], v[100:103], v[212:215], v[68:71]\n"
    "ds_read_b64_tr_b16 v[252:253], v107 offset:1024\n"
    "buffer_load_dwordx4 v121, s[12:15], s67 offen lds\n"
    "ds_read_b64_tr_b16 v[254:255], v107 offset:5120\n"
    "s_waitcnt lgkmcnt(15)\n"
    "v_mfma_f32_16x16x32_bf16 v[8:11], v[160:163], v[216:219], v[8:11]\n"
    "ds_read_b64_tr_b16 v[72:73], v104 offset:8192\n"
    "s_add_u32 m0, s59, 0x18400\n"
    "v_mfma_f32_16x16x32_bf16 v[16:19], v[160:163], v[220:223], v[16:19]\n"
    "ds_read_b64_tr_b16 v[74:75], v104 offset:12288\n"
    "buffer_load_dwordx4 v123, s[12:15], s67 offen lds\n"
    "v_mfma_f32_16x16x32_bf16 v[12:15], v[164:167], v[216:219], v[12:15]\n"
    "ds_read_b64_tr_b16 v[76:77], v105 offset:8192\n"
    "ds_read_b64_tr_b16 v[78:79], v105 offset:12288\n"
    "v_mfma_f32_16x16x32_bf16 v[20:23], v[164:167], v[220:223], v[20:23]\n"
    "ds_read_b64_tr_b16 v[80:81], v106 offset:8192\n"
    "s_add_u32 m0, s59, 0x18800\n"
    "v_mfma_f32_16x16x32_bf16 v[24:27], v[168:171], v[216:219], v[24:27]\n"
    "s_cbranch_vccnz Lc3b_1\n"
    "buffer_load_dwordx4 a[112:115], v2, s[8:11], s66 offen offset:2048 sc1\n"
    "buffer_load_dwordx4 a[116:119], v120, s[8:11], s66 offen offset:2048 sc1\n"
    "buffer_load_dwordx4 a[120:123], v2, s[8:11], s66 offen offset:3072 sc1\n"
    "buffer_load_dwordx4 a[124:127], v120, s[8:11], s66 offen offset:3072 sc1\n"
    "Lc3b_1:\n"
    "v_mfma_f32_16x16x32_bf16 v[32:35], v[168:171], v[220:223], v[32:35]\n"
    "ds_read_b64_tr_b16 v[82:83], v106 offset:12288\n"
    "buffer_load_dwordx4 v121, s[12:15], s70 offen lds\n"
    "v_mfma_f32_16x16x32_bf16 v[28:31], v[172:175], v[216:219], v[28:31]\n"
    "ds_read_b64_tr_b16 v[84:85], v107 offset:8192\n"
    "ds_read_b64_tr_b16 v[86:87], v107 offset:12288\n"
    "v_mfma_f32_16x16x32_bf16 v[36:39], v[172:175], v[220:223], v[36:39]\n"
    "ds_read_b64_tr_b16 v[88:89], v104 offset:9216\n"
    "ds_read_b64_tr_b16 v[90:91], v104 offset:13312\n"
    "v_mfma_f32_16x16x32_bf16 v[40:43], v[176:179], v[216:219], v[40:43]\n"
    "ds_read_b64_tr_b16 v[92:93], v105 offset:9216\n"
    "s_add_u32 m0, s59, 0x18c00\n"
    "v_mfma_f32_16x16x32_bf16 v[48:51], v[176:179], v[220:223], v[48:51]\n"
    "ds_read_b64_tr_b16 v[94:95], v105 offset:13312\n"
    "ds_read_b64_tr_b16 v[96:97], v106 offset:9216\n"
    "v_mfma_f32_16x16x32_bf16 v[44:47], v[180:183], v[216:219], v[44:47]\n"
    "buffer_load_dwordx4 v123, s[12:15], s70 offen lds\n"
    "ds_read_b64_tr_b16 v[98:99], v106 offset:13312\n"
    "v_mfma_f32_16x16x32_bf16 v[52:55], v[180:183], v[220:223], v[52:55]\n"
    "ds_read_b64_tr_b16 v[100:101], v107 offset:9216\n"
    "ds_read_b64_tr_b16 v[102:103], v107 offset:13312\n"
    "v_mfma_f32_16x16x32_bf16 v[56:59], v[184:187], v[216:219], v[56:59]\n"
    "s_add_u32 s67, s67, 0x10000\n"
    "v_mfma_f32_16x16x32_bf16 v[64:67], v[184:187], v[220:223], v[64:67]\n"
    "v_mfma_f32_16x16x32_bf16 v[60:63], v[188:191], v[216:219], v[60:63]\n"
    "v_mfma_f32_16x16x32_bf16 v[68:71], v[188:191], v[220:223], v[68:71]\n"
    "buffer_load_dword v122, v5, s[8:11], s7 offen sc1\n"
    "s_barrier\n"
    "s_bitcmp1_b32 s82, 2\n"
    "s_cbranch_scc1 Lcl4\n"
    "s_waitcnt vmcnt(0)\n"
    "v_cmp_ne_u32 vcc, v122, v7\n"
    "s_nop 0\n"
    "s_cbranch_vccnz Lspin6\n"
    "Lspinret6:\n"
    "s_add_u32 s66, s3, s71\n"
    "s_and_b32 s66, s66, 7\n"
    "s_add_u32 s71, s71, 1\n"
    "s_cmp_eq_u32 s71, 7\n"
    "s_cselect_b32 s71, 0, s71\n"
    "s_lshl_b32 s66, s66, 12\n"
    "s_add_u32 s66, s66, s74\n"
    "buffer_load_dwordx4 a[64:67], v2, s[8:11], s66 offen sc1\n"
    "buffer_load_dwordx4 a[68:71], v120, s[8:11], s66 offen sc1\n"
    "buffer_load_dwordx4 a[72:75], v2, s[8:11], s66 offen offset:1024 sc1\n"
    "buffer_load_dwordx4 a[76:79], v120, s[8:11], s66 offen offset:1024 sc1\n"
    "buffer_load_dwordx4 a[80:83], v2, s[8:11], s66 offen offset:2048 sc1\n"
    "buffer_load_dwordx4 a[84:87], v120, s[8:11], s66 offen offset:2048 sc1\n"
    "buffer_load_dwordx4 a[88:91], v2, s[8:11], s66 offen offset:3072 sc1\n"
    "buffer_load_dwordx4 a[92:95], v120, s[8:11], s66 offen offset:3072 sc1\n"
    "s_add_u32 s66, s3, s71\n"
    "s_and_b32 s66, s66, 7\n"
    "s_add_u32 s71, s71, 1\n"
    "s_cmp_eq_u32 s71, 7\n"
    "s_cselect_b32 s71, 0, s71\n"
    "s_lshl_b32 s66, s66, 12\n"
    "s_add_u32 s66, s66, s74\n"
    "buffer_load_dwordx4 a[96:99], v2, s[8:11], s66 offen sc1\n"
    "buffer_load_dwordx4 a[100:103], v120, s[8:11], s66 offen sc1\n"
    "buffer_load_dwordx4 a[104:107], v2, s[8:11], s66 offen offset:1024 sc1\n"
    "buffer_load_dwordx4 a[108:111], v120, s[8:11], s66 offen offset:1024 sc1\n"
    "buffer_load_dwordx4 a[112:115], v2, s[8:11], s66 offen offset:2048 sc1\n"
    "buffer_load_dwordx4 a[116:119], v120, s[8:11], s66 offen offset:2048 sc1\n"
    "buffer_load_dwordx4 a[120:123], v2, s[8:11], s66 offen offset:3072 sc1\n"
    "buffer_load_dwordx4 a[124:127], v120, s[8:11], s66 offen offset:3072 sc1\n"
    "Lcl4:\n"
    "s_waitcnt lgkmcnt(15)\n"
    "s_waitcnt vmcnt(15)\n"
    "v_mfma_f32_16x16x32_bf16 v[8:11], v[224:227], a[64:67], v[8:11]\n"
    "v_add_u32 v104, 0x1c000, v108\n"
    "v_xor_b32 v105, 32, v104\n"
    "s_waitcnt vmcnt(14)\n"
    "v_mfma_f32_16x16x32_bf16 v[16:19], v[224:227], a[68:71], v[16:19]\n"
    "s_add_u32 s66, s3, s71\n"
    "s_and_b32 s66, s66, 7\n"
    "s_add_u32 s71, s71, 1\n"
    "s_cmp_eq_u32 s71, 7\n"
    "s_cselect_b32 s71, 0, s71\n"
    "v_mfma_f32_16x16x32_bf16 v[12:15], v[228:231], a[64:67], v[12:15]\n"
    "v_xor_b32 v106, 64, v104\n"
    "v_xor_b32 v107, 0x60, v104\n"
    "v_mfma_f32_16x16x32_bf16 v[20:23], v[228:231], a[68:71], v[20:23]\n"
    "s_add_u32 s67, s3, s69\n"
    "s_and_b32 s67, s67, 7\n"
    "s_add_u32 s69, s69, 1\n"
    "s_cmp_eq_u32 s69, 7\n"
    "s_cselect_b32 s69, 0, s69\n"
    "v_mfma_f32_16x16x32_bf16 v[24:27], v[232:235], a[64:67], v[24:27]\n"
    "ds_read_b64_tr_b16 v[160:161], v104\n"
    "ds_read_b64_tr_b16 v[162:163], v104 offset:4096\n"
    "v_mfma_f32_16x16x32_bf16 v[32:35], v[232:235], a[68:71], v[32:35]\n"
    "ds_read_b64_tr_b16 v[164:165], v105\n"
    "ds_read_b64_tr_b16 v[166:167], v105 offset:4096\n"
    "v_mfma_f32_16x16x32_bf16 v[28:31], v[236:239], a[64:67], v[28:31]\n"
    "ds_read_b64_tr_b16 v[168:169], v106\n"
    "ds_read_b64_tr_b16 v[170:171], v106 offset:4096\n"
    "v_mfma_f32_16x16x32_bf16 v[36:39], v[236:239], a[68:71], v[36:39]\n"
    "ds_read_b64_tr_b16 v[172:173], v107\n"
    "ds_read_b64_tr_b16 v[174:175], v107 offset:4096\n"
    "v_mfma_f32_16x16x32_bf16 v[40:43], v[240:243], a[64:67], v[40:43]\n"
    "ds_read_b64_tr_b16 v[176:177], v104 offset:1024\n"
    "ds_read_b64_tr_b16 v[178:179], v104 offset:5120\n"
    "v_mfma_f32_16x16x32_bf16 v[48:51], v[240:243], a[68:71], v[48:51]\n"
    "ds_read_b64_tr_b16 v[180:181], v105 offset:1024\n"
    "ds_read_b64_tr_b16 v[182:183], v105 offset:5120\n"
    "v_mfma_f32_16x16x32_bf16 v[44:47], v[244:247], a[64:67], v[44:47]\n"
    "ds_read_b64_tr_b16 v[184:185], v106 offset:1024\n"
    "ds_read_b64_tr_b16 v[186:187], v106 offset:5120\n"
    "v_mfma_f32_16x16x32_bf16 v[52:55], v[244:247], a[68:71], v[52:55]\n"
    "s_lshl_b32 s67, s67, 17\n"
    "s_lshl_b32 s66, s66, 12\n"
    "v_mfma_f32_16x16x32_bf16 v[56:59], v[248:251], a[64:67], v[56:59]\n"
    "ds_read_b64_tr_b16 v[188:189], v107 offset:1024\n"
    "ds_read_b64_tr_b16 v[190:191], v107 offset:5120\n"
    "v_mfma_f32_16x16x32_bf16 v[64:67], v[248:251], a[68:71], v[64:67]\n"
    "v_mfma_f32_16x16x32_bf16 v[60:63], v[252:255], a[64:67], v[60:63]\n"
    "s_add_u32 s67, s67, s90\n"
    "s_add_u32 s66, s66, s74\n"
    "v_mfma_f32_16x16x32_bf16 v[68:71], v[252:255], a[68:71], v[68:71]\n"
    "s_waitcnt lgkmcnt(15)\n"
    "s_waitcnt vmcnt(13)\n"
    "v_mfma_f32_16x16x32_bf16 v[8:11], v[72:75], a[72:75], v[8:11]\n"
    "ds_read_b64_tr_b16 v[224:225], v104 offset:8192\n"
    "s_add_u32 s70, s67, 0x2000\n"
    "s_waitcnt vmcnt(12)\n"
    "v_mfma_f32_16x16x32_bf16 v[16:19], v[72:75], a[76:79], v[16:19]\n"
    "ds_read_b64_tr_b16 v[226:227], v104 offset:12288\n"
    "buffer_load_dwordx4 a[128:131], v2, s[8:11], s66 offen sc1\n"
    "v_mfma_f32_16x16x32_bf16 v[12:15], v[76:79], a[72:75], v[12:15]\n"
    "ds_read_b64_tr_b16 v[228:229], v105 offset:8192\n"
    "s_add_u32 m0, s59, 0x4000\n"
    "v_mfma_f32_16x16x32_bf16 v[20:23], v[76:79], a[76:79], v[20:23]\n"
    "ds_read_b64_tr_b16 v[230:231], v105 offset:12288\n"
    "buffer_load_dwordx4 a[132:135], v120, s[8:11], s66 offen sc1\n"
    "v_mfma_f32_16x16x32_bf16 v[24:27], v[80:83], a[72:75], v[24:27]\n"
    "ds_read_b64_tr_b16 v[232:233], v106 offset:8192\n"
    "ds_read_b64_tr_b16 v[234:235], v106 offset:12288\n"
    "v_mfma_f32_16x16x32_bf16 v[32:35], v[80:83], a[76:79], v[32:35]\n"
    "buffer_load_dwordx4 v121, s[12:15], s67 offen lds\n"
    "buffer_load_dwordx4 a[136:139], v2, s[8:11], s66 offen offset:1024 sc1\n"
    "v_mfma_f32_16x16x32_bf16 v[28:31], v[84:87], a[72:75], v[28:31]\n"
    "ds_read_b64_tr_b16 v[236:237], v107 offset:8192\n"
    "ds_read_b64_tr_b16 v[238:239], v107 offset:12288\n"
    "v_mfma_f32_16x16x32_bf16 v[36:39], v[84:87], a[76:79], v[36:39]\n"
    "buffer_load_dwordx4 a[140:143], v120, s[8:11], s66 offen offset:1024 sc1\n"
    "s_add_u32 m0, s59, 0x4400\n"
    "v_mfma_f32_16x16x32_bf16 v[40:43], v[88:91], a[72:75], v[40:43]\n"
    "ds_read_b64_tr_b16 v[240:241], v104 offset:9216\n"
    "ds_read_b64_tr_b16 v[242:243], v104 offset:13312\n"
    "v_mfma_f32_16x16x32_bf16 v[48:51], v[88:91], a[76:79], v[48:51]\n"
    "buffer_load_dwordx4 v123, s[12:15], s67 offen lds\n"
    "ds_read_b64_tr_b16 v[244:245], v105 offset:9216\n"
    "v_mfma_f32_16x16x32_bf16 v[44:47], v[92:95], a[72:75], v[44:47]\n"
    "buffer_load_dwordx4 a[144:147], v2, s[8:11], s66 offen offset:2048 sc1\n"
    "ds_read_b64_tr_b16 v[246:247], v105 offset:13312\n"
    "v_mfma_f32_16x16x32_bf16 v[52:55], v[92:95], a[76:79], v[52:55]\n"
    "ds_read_b64_tr_b16 v[248:249], v106 offset:9216\n"
    "ds_read_b64_tr_b16 v[250:251], v106 offset:13312\n"
    "v_mfma_f32_16x16x32_bf16 v[56:59], v[96:99], a[72:75], v[56:59]\n"
    "buffer_load_dwordx4 a[148:151], v120, s[8:11], s66 offen offset:2048 sc1\n"
    "s_add_u32 m0, s59, 0x4800\n"
    "v_mfma_f32_16x16x32_bf16 v[64:67], v[96:99], a[76:79], v[64:67]\n"
    "ds_read_b64_tr_b16 v[252:253], v107 offset:9216\n"
    "ds_read_b64_tr_b16 v[254:255], v107 offset:13312\n"
    "v_mfma_f32_16x16x32_bf16 v[60:63], v[100:103], a[72:75], v[60:63]\n"
    "buffer_load_dwordx4 v121, s[12:15], s70 offen lds\n"
    "buffer_load_dwordx4 a[152:155], v2, s[8:11], s66 offen offset:3072 sc1\n"
    "v_mfma_f32_16x16x32_bf16 v[68:71], v[100:103], a[76:79], v[68:71]\n"
    "s_waitcnt lgkmcnt(15)\n"
    "s_waitcnt vmcnt(21)\n"
    "v_mfma_f32_16x16x32_bf16 v[8:11], v[160:163], a[80:83], v[8:11]\n"
    "v_add_u32 v104, 0x20000, v108\n"
    "v_xor_b32 v105, 32, v104\n"
    "s_waitcnt vmcnt(20)\n"
    "v_mfma_f32_16x16x32_bf16 v[16:19], v[160:163], a[84:87], v[16:19]\n"
    "v_xor_b32 v106, 64, v104\n"
    "s_add_u32 m0, s59, 0x4c00\n"
    "v_mfma_f32_16x16x32_bf16 v[12:15], v[164:167], a[80:83], v[12:15]\n"
    "buffer_load_dwordx4 a[156:159], v120, s[8:11], s66 offen offset:3072 sc1\n"
    "v_xor_b32 v107, 0x60, v104\n"
    "v_mfma_f32_16x16x32_bf16 v[20:23], v[164:167], a[84:87], v[20:23]\n"
    "ds_read_b64_tr_b16 v[72:73], v104\n"
    "buffer_load_dwordx4 v123, s[12:15], s70 offen lds\n"
    "v_mfma_f32_16x16x32_bf16 v[24:27], v[168:171], a[80:83], v[24:27]\n"
    "ds_read_b64_tr_b16 v[74:75], v104 offset:4096\n"
    "ds_read_b64_tr_b16 v[76:77], v105\n"
    "v_mfma_f32_16x16x32_bf16 v[32:35], v[168:171], a[84:87], v[32:35]\n"
    "s_add_u32 s66, s3, s71\n"
    "s_and_b32 s66, s66, 7\n"
    "s_add_u32 s71, s71, 1\n"
    "s_cmp_eq_u32 s71, 7\n"
    "s_cselect_b32 s71, 0, s71\n"
    "v_mfma_f32_16x16x32_bf16 v[28:31], v[172:175], a[80:83], v[28:31]\n"
    "ds_read_b64_tr_b16 v[78:79], v105 offset:4096\n"
    "ds_read_b64_tr_b16 v[80:81], v106\n"
    "v_mfma_f32_16x16x32_bf16 v[36:39], v[172:175], a[84:87], v[36:39]\n"
    "ds_read_b64_tr_b16 v[82:83], v106 offset:4096\n"
    "s_add_u32 s67, s67, 0x10000\n"
    "v_mfma_f32_16x16x32_bf16 v[40:43], v[176:179], a[80:83], v[40:43]\n"
    "ds_read_b64_tr_b16 v[84:85], v107\n"
    "s_add_u32 s70, s67, 0x2000\n"
    "v_mfma_f32_16x16x32_bf16 v[48:51], v[176:179], a[84:87], v[48:51]\n"
    "ds_read_b64_tr_b16 v[86:87], v107 offset:4096\n"
    "ds_read_b64_tr_b16 v[88:89], v104 offset:1024\n"
    "v_mfma_f32_16x16x32_bf16 v[44:47], v[180:183], a[80:83], v[44:47]\n"
    "ds_read_b64_tr_b16 v[90:91], v104 offset:5120\n"
    "s_add_u32 m0, s59, 0xc000\n"
    "v_mfma_f32_16x16x32_bf16 v[52:55], v[180:183], a[84:87], v[52:55]\n"
    "ds_read_b64_tr_b16 v[92:93], v105 offset:1024\n"
    "ds_read_b64_tr_b16 v[94:95], v105 offset:5120\n"
    "v_mfma_f32_16x16x32_bf16 v[56:59], v[184:187], a[80:83], v[56:59]\n"
    "ds_read_b64_tr_b16 v[96:97], v106 offset:1024\n"
    "buffer_load_dwordx4 v121, s[12:15], s67 offen lds\n"
    "v_mfma_f32_16x16x32_bf16 v[64:67], v[184:187], a[84:87], v[64:67]\n"
    "ds_read_b64_tr_b16 v[98:99], v106 offset:5120\n"
    "ds_read_b64_tr_b16 v[100:101], v107 offset:1024\n"
    "v_mfma_f32_16x16x32_bf16 v[60:63], v[188:191], a[80:83], v[60:63]\n"
    "ds_read_b64_tr_b16 v[102:103], v107 offset:5120\n"
    "s_add_u32 m0, s59, 0xc400\n"
    "v_mfma_f32_16x16x32_bf16 v[68:71], v[188:191], a[84:87], v[68:71]\n"
    "s_lshl_b32 s66, s66, 12\n"
    "s_waitcnt lgkmcnt(15)\n"
    "s_waitcnt vmcnt(22)\n"
    "v_mfma_f32_16x16x32_bf16 v[8:11], v[224:227], a[88:91], v[8:11]\n"
    "ds_read_b64_tr_b16 v[160:161], v104 offset:8192\n"
    "ds_read_b64_tr_b16 v[162:163], v104 offset:12288\n"
    "s_waitcnt vmcnt(21)\n"
    "v_mfma_f32_16x16x32_bf16 v[16:19], v[224:227], a[92:95], v[16:19]\n"
    "buffer_load_dwordx4 v123, s[12:15], s67 offen lds\n"
    "s_add_u32 s66, s66, s74\n"
    "v_mfma_f32_16x16x32_bf16 v[12:15], v[228:231], a[88:91], v[12:15]\n"
    "ds_read_b64_tr_b16 v[164:165], v105 offset:8192\n"
    "ds_read_b64_tr_b16 v[166:167], v105 offset:12288\n"
    "v_mfma_f32_16x16x32_bf16 v[20:23], v[228:231], a[92:95], v[20:23]\n"
    "ds_read_b64_tr_b16 v[168:169], v106 offset:8192\n"
    "s_add_u32 m0, s59, 0xc800\n"
    "v_mfma_f32_16x16x32_bf16 v[24:27], v[232:235], a[88:91], v[24:27]\n"
    "buffer_load_dwordx4 a[160:163], v2, s[8:11], s66 offen sc1\n"
    "ds_read_b64_tr_b16 v[170:171], v106 offset:12288\n"
    "v_mfma_f32_16x16x32_bf16 v[32:35], v[232:235], a[92:95], v[32:35]\n"
    "buffer_load_dwordx4 v121, s[12:15], s70 offen lds\n"
    "ds_read_b64_tr_b16 v[172:173], v107 offset:8192\n"
    "v_mfma_f32_16x16x32_bf16 v[28:31], v[236:239], a[88:91], v[28:31]\n"
    "buffer_load_dwordx4 a[164:167], v120, s[8:11], s66 offen sc1\n"
    "ds_read_b64_tr_b16 v[174:175], v107 offset:12288\n"
    "v_mfma_f32_16x16x32_bf16 v[36:39], v[236:239], a[92:95], v[36:39]\n"
    "ds_read_b64_tr_b16 v[176:177], v104 offset:9216\n"
    "ds_read_b64_tr_b16 v[178:179], v104 offset:13312\n"
    "v_mfma_f32_16x16x32_bf16 v[40:43], v[240:243], a[88:91], v[40:43]\n"
    "ds_read_b64_tr_b16 v[180:181], v105 offset:9216\n"
    "s_add_u32 m0, s59, 0xcc00\n"
    "v_mfma_f32_16x16x32_bf16 v[48:51], v[240:243], a[92:95], v[48:51]\n"
    "ds_read_b64_tr_b16 v[182:183], v105 offset:13312\n"
    "ds_read_b64_tr_b16 v[184:185], v106 offset:9216\n"
    "v_mfma_f32_16x16x32_bf16 v[44:47], v[244:247], a[88:91], v[44:47]\n"
    "buffer_load_dwordx4 v123, s[12:15], s70 offen lds\n"
    "ds_read_b64_tr_b16 v[186:187], v106 offset:13312\n"
    "v_mfma_f32_16x16x32_bf16 v[52:55], v[244:247], a[92:95], v[52:55]\n"
    "ds_read_b64_tr_b16 v[188:189], v107 offset:9216\n"
    "ds_read_b64_tr_b16 v[190:191], v107 offset:13312\n"
    "v_mfma_f32_16x16x32_bf16 v[56:59], v[248:251], a[88:91], v[56:59]\n"
    "s_add_u32 s67, s67, 0x10000\n"
    "v_mfma_f32_16x16x32_bf16 v[64:67], v[248:251], a[92:95], v[64:67]\n"
    "v_mfma_f32_16x16x32_bf16 v[60:63], v[252:255], a[88:91], v[60:63]\n"
    "v_mfma_f32_16x16x32_bf16 v[68:71], v[252:255], a[92:95], v[68:71]\n"
    "s_barrier\n"
    "s_waitcnt lgkmcnt(15)\n"
    "s_waitcnt vmcnt(25)\n"
    "v_mfma_f32_16x16x32_bf16 v[8:11], v[72:75], a[96:99], v[8:11]\n"
    "buffer_load_dwordx4 a[168:171], v2, s[8:11], s66 offen offset:1024 sc1\n"
    "v_add_u32 v104, 0x24000, v108\n"
    "s_waitcnt vmcnt(25)\n"
    "v_mfma_f32_16x16x32_bf16 v[16:19], v[72:75], a[100:103], v[16:19]\n"
    "v_xor_b32 v105, 32, v104\n"
    "v_xor_b32 v106, 64, v104\n"
    "v_mfma_f32_16x16x32_bf16 v[12:15], v[76:79], a[96:99], v[12:15]\n"
    "s_add_u32 s67, s3, s69\n"
    "s_and_b32 s67, s67, 7\n"
    "s_add_u32 s69, s69, 1\n"
    "s_cmp_eq_u32 s69, 7\n"
    "s_cselect_b32 s69, 0, s69\n"
    "v_mfma_f32_16x16x32_bf16 v[20:23], v[76:79], a[100:103], v[20:23]\n"
    "buffer_load_dwordx4 a[172:175], v120, s[8:11], s66 offen offset:1024 sc1\n"
    "v_xor_b32 v107, 0x60, v104\n"
    "v_mfma_f32_16x16x32_bf16 v[24:27], v[80:83], a[96:99], v[24:27]\n"
    "ds_read_b64_tr_b16 v[224:225], v104\n"
    "ds_read_b64_tr_b16 v[226:227], v104 offset:4096\n"
    "v_mfma_f32_16x16x32_bf16 v[32:35], v[80:83], a[100:103], v[32:35]\n"
    "ds_read_b64_tr_b16 v[228:229], v105\n"
    "ds_read_b64_tr_b16 v[230:231], v105 offset:4096\n"
    "v_mfma_f32_16x16x32_bf16 v[28:31], v[84:87], a[96:99], v[28:31]\n"
    "ds_read_b64_tr_b16 v[232:233], v106\n"
    "buffer_load_dwordx4 a[176:179], v2, s[8:11], s66 offen offset:2048 sc1\n"
    "v_mfma_f32_16x16x32_bf16 v[36:39], v[84:87], a[100:103], v[36:39]\n"
    "ds_read_b64_tr_b16 v[234:235], v106 offset:4096\n"
    "ds_read_b64_tr_b16 v[236:237], v107\n"
    "v_mfma_f32_16x16x32_bf16 v[40:43], v[88:91], a[96:99], v[40:43]\n"
    "ds_read_b64_tr_b16 v[238:239], v107 offset:4096\n"
    "ds_read_b64_tr_b16 v[240:241], v104 offset:1024\n"
    "v_mfma_f32_16x16x32_bf16 v[48:51], v[88:91], a[100:103], v[48:51]\n"
    "ds_read_b64_tr_b16 v[242:243], v104 offset:5120\n"
    "ds_read_b64_tr_b16 v[244:245], v105 offset:1024\n"
    "v_mfma_f32_16x16x32_bf16 v[44:47], v[92:95], a[96:99], v[44:47]\n"
    "buffer_load_dwordx4 a[180:183], v120, s[8:11], s66 offen offset:2048 sc1\n"
    "ds_read_b64_tr_b16 v[246:247], v105 offset:5120\n"
    "v_mfma_f32_16x16x32_bf16 v[52:55], v[92:95], a[100:103], v[52:55]\n"
    "ds_read_b64_tr_b16 v[248:249], v106 offset:1024\n"
    "s_lshl_b32 s67, s67, 17\n"
    "v_mfma_f32_16x16x32_bf16 v[56:59], v[96:99], a[96:99], v[56:59]\n"
    "ds_read_b64_tr_b16 v[250:251], v106 offset:5120\n"
    "ds_read_b64_tr_b16 v[252:253], v107 offset:1024\n"
    "v_mfma_f32_16x16x32_bf16 v[64:67], v[96:99], a[100:103], v[64:67]\n"
    "buffer_load_dwordx4 a[184:187], v2, s[8:11], s66 offen offset:3072 sc1\n"
    "ds_read_b64_tr_b16 v[254:255], v107 offset:5120\n"
    "v_mfma_f32_16x16x32_bf16 v[60:63], v[100:103], a[96:99], v[60:63]\n"
    "s_add_u32 s67, s67, s90\n"
    "v_mfma_f32_16x16x32_bf16 v[68:71], v[100:103], a[100:103], v[68:71]\n"
    "s_waitcnt lgkmcnt(15)\n"
    "s_waitcnt vmcnt(28)\n"
    "v_mfma_f32_16x16x32_bf16 v[8:11], v[160:163], a[104:107], v[8:11]\n"
    "ds_read_b64_tr_b16 v[72:73], v104 offset:8192\n"
    "s_add_u32 s70, s67, 0x2000\n"
    "s_waitcnt vmcnt(27)\n"
    "v_mfma_f32_16x16x32_bf16 v[16:19], v[160:163], a[108:111], v[16:19]\n"
    "ds_read_b64_tr_b16 v[74:75], v104 offset:12288\n"
    "buffer_load_dwordx4 a[188:191], v120, s[8:11], s66 offen offset:3072 sc1\n"
    "v_mfma_f32_16x16x32_bf16 v[12:15], v[164:167], a[104:107], v[12:15]\n"
    "ds_read_b64_tr_b16 v[76:77], v105 offset:8192\n"
    "s_add_u32 m0, s59, 0x14000\n"
    "v_mfma_f32_16x16x32_bf16 v[20:23], v[164:167], a[108:111], v[20:23]\n"
    "ds_read_b64_tr_b16 v[78:79], v105 offset:12288\n"
    "ds_read_b64_tr_b16 v[80:81], v106 offset:8192\n"
    "v_mfma_f32_16x16x32_bf16 v[24:27], v[168:171], a[104:107], v[24:27]\n"
    "s_add_u32 s66, s3, s71\n"
    "s_and_b32 s66, s66, 7\n"
    "s_add_u32 s71, s71, 1\n"
    "s_cmp_eq_u32 s71, 7\n"
    "s_cselect_b32 s71, 0, s71\n"
    "v_mfma_f32_16x16x32_bf16 v[32:35], v[168:171], a[108:111], v[32:35]\n"
    "ds_read_b64_tr_b16 v[82:83], v106 offset:12288\n"
    "buffer_load_dwordx4 v121, s[12:15], s67 offen lds\n"
    "v_mfma_f32_16x16x32_bf16 v[28:31], v[172:175], a[104:107], v[28:31]\n"
    "ds_read_b64_tr_b16 v[84:85], v107 offset:8192\n"
    "ds_read_b64_tr_b16 v[86:87], v107 offset:12288\n"
    "v_mfma_f32_16x16x32_bf16 v[36:39], v[172:175], a[108:111], v[36:39]\n"
    "ds_read_b64_tr_b16 v[88:89], v104 offset:9216\n"
    "s_add_u32 m0, s59, 0x14400\n"
    "v_mfma_f32_16x16x32_bf16 v[40:43], v[176:179], a[104:107], v[40:43]\n"
    "ds_read_b64_tr_b16 v[90:91], v104 offset:13312\n"
    "ds_read_b64_tr_b16 v[92:93], v105 offset:9216\n"
    "v_mfma_f32_16x16x32_bf16 v[48:51], v[176:179], a[108:111], v[48:51]\n"
    "buffer_load_dwordx4 v123, s[12:15], s67 offen lds\n"
    "ds_read_b64_tr_b16 v[94:95], v105 offset:13312\n"
    "v_mfma_f32_16x16x32_bf16 v[44:47], v[180:183], a[104:107], v[44:47]\n"
    "ds_read_b64_tr_b16 v[96:97], v106 offset:9216\n"
    "ds_read_b64_tr_b16 v[98:99], v106 offset:13312\n"
    "v_mfma_f32_16x16x32_bf16 v[52:55], v[180:183], a[108:111], v[52:55]\n"
    "ds_read_b64_tr_b16 v[100:101], v107 offset:9216\n"
    "ds_read_b64_tr_b16 v[102:103], v107 offset:13312\n"
    "v_mfma_f32_16x16x32_bf16 v[56:59], v[184:187], a[104:107], v[56:59]\n"
    "s_add_u32 m0, s59, 0x14800\n"
    "v_mfma_f32_16x16x32_bf16 v[64:67], v[184:187], a[108:111], v[64:67]\n"
    "v_mfma_f32_16x16x32_bf16 v[60:63], v[188:191], a[104:107], v[60:63]\n"
    "buffer_load_dwordx4 v121, s[12:15], s70 offen lds\n"
    "v_mfma_f32_16x16x32_bf16 v[68:71], v[188:191], a[108:111], v[68:71]\n"
    "s_waitcnt lgkmcnt(15)\n"
    "s_waitcnt vmcnt(30)\n"
    "v_mfma_f32_16x16x32_bf16 v[8:11], v[224:227], a[112:115], v[8:11]\n"
    "v_add_u32 v104, 0, v108\n"
    "s_add_u32 m0, s59, 0x14c00\n"
    "s_waitcnt vmcnt(29)\n"
    "v_mfma_f32_16x16x32_bf16 v[16:19], v[224:227], a[116:119], v[16:19]\n"
    "v_xor_b32 v105, 32, v104\n"
    "v_xor_b32 v106, 64, v104\n"
    "v_mfma_f32_16x16x32_bf16 v[12:15], v[228:231], a[112:115], v[12:15]\n"
    "v_xor_b32 v107, 0x60, v104\n"
    "v_mfma_f32_16x16x32_bf16 v[20:23], v[228:231], a[116:119], v[20:23]\n"
    "buffer_load_dwordx4 v123, s[12:15], s70 offen lds\n"
    "ds_read_b64_tr_b16 v[168:169], v106\n"
    "v_mfma_f32_16x16x32_bf16 v[24:27], v[232:235], a[112:115], v[24:27]\n"
    "ds_read_b64_tr_b16 v[170:171], v106 offset:4096\n"
    "s_lshl_b32 s66, s66, 12\n"
    "v_mfma_f32_16x16x32_bf16 v[32:35], v[232:235], a[116:119], v[32:35]\n"
    "ds_read_b64_tr_b16 v[172:173], v107\n"
    "s_add_u32 s67, s67, 0x10000\n"
    "v_mfma_f32_16x16x32_bf16 v[28:31], v[236:239], a[112:115], v[28:31]\n"
    "ds_read_b64_tr_b16 v[174:175], v107 offset:4096\n"
    "ds_read_b64_tr_b16 v[176:177], v104 offset:1024\n"
    "v_mfma_f32_16x16x32_bf16 v[36:39], v[236:239], a[116:119], v[36:39]\n"
    "ds_read_b64_tr_b16 v[178:179], v104 offset:5120\n"
    "s_add_u32 s70, s67, 0x2000\n"
    "v_mfma_f32_16x16x32_bf16 v[40:43], v[240:243], a[112:115], v[40:43]\n"
    "ds_read_b64_tr_b16 v[180:181], v105 offset:1024\n"
    "s_add_u32 s66, s66, s74\n"
    "v_mfma_f32_16x16x32_bf16 v[48:51], v[240:243], a[116:119], v[48:51]\n"
    "ds_read_b64_tr_b16 v[182:183], v105 offset:5120\n"
    "ds_read_b64_tr_b16 v[184:185], v106 offset:1024\n"
    "v_mfma_f32_16x16x32_bf16 v[44:47], v[244:247], a[112:115], v[44:47]\n"
    "ds_read_b64_tr_b16 v[186:187], v106 offset:5120\n"
    "s_add_u32 m0, s59, 0x1c000\n"
    "v_mfma_f32_16x16x32_bf16 v[52:55], v[244:247], a[116:119], v[52:55]\n"
    "buffer_load_dwordx4 a[192:195], v2, s[8:11], s66 offen sc1\n"
    "ds_read_b64_tr_b16 v[188:189], v107 offset:1024\n"
    "v_mfma_f32_16x16x32_bf16 v[56:59], v[248:251], a[112:115], v[56:59]\n"
    "buffer_load_dwordx4 v121, s[12:15], s67 offen lds\n"
    "ds_read_b64_tr_b16 v[190:191], v107 offset:5120\n"
    "v_mfma_f32_16x16x32_bf16 v[64:67], v[248:251], a[116:119], v[64:67]\n"
    "v_mfma_f32_16x16x32_bf16 v[60:63], v[252:255], a[112:115], v[60:63]\n"
    "buffer_load_dwordx4 a[196:199], v120, s[8:11], s66 offen sc1\n"
    "s_add_u32 m0, s59, 0x1c400\n"
    "v_mfma_f32_16x16x32_bf16 v[68:71], v[252:255], a[116:119], v[68:71]\n"
    "s_waitcnt lgkmcnt(15)\n"
    "s_waitcnt vmcnt(32)\n"
    "v_mfma_f32_16x16x32_bf16 v[8:11], v[72:75], a[120:123], v[8:11]\n"
    "ds_read_b64_tr_b16 v[232:233], v106 offset:8192\n"
    "ds_read_b64_tr_b16 v[234:235], v106 offset:12288\n"
    "s_waitcnt vmcnt(31)\n"
    "v_mfma_f32_16x16x32_bf16 v[16:19], v[72:75], a[124:127], v[16:19]\n"
    "buffer_load_dwordx4 v123, s[12:15], s67 offen lds\n"
    "ds_read_b64_tr_b16 v[236:237], v107 offset:8192\n"
    "v_mfma_f32_16x16x32_bf16 v[12:15], v[76:79], a[120:123], v[12:15]\n"
    "buffer_load_dwordx4 a[200:203], v2, s[8:11], s66 offen offset:1024 sc1\n"
    "ds_read_b64_tr_b16 v[238:239], v107 offset:12288\n"
    "v_mfma_f32_16x16x32_bf16 v[20:23], v[76:79], a[124:127], v[20:23]\n"
    "ds_read_b64_tr_b16 v[240:241], v104 offset:9216\n"
    "s_add_u32 m0, s59, 0x1c800\n"
    "v_mfma_f32_16x16x32_bf16 v[24:27], v[80:83], a[120:123], v[24:27]\n"
    "ds_read_b64_tr_b16 v[242:243], v104 offset:13312\n"
    "ds_read_b64_tr_b16 v[244:245], v105 offset:9216\n"
    "v_mfma_f32_16x16x32_bf16 v[32:35], v[80:83], a[124:127], v[32:35]\n"
    "buffer_load_dwordx4 a[204:207], v120, s[8:11], s66 offen offset:1024 sc1\n"
    "buffer_load_dwordx4 v121, s[12:15], s70 offen lds\n"
    "v_mfma_f32_16x16x32_bf16 v[28:31], v[84:87], a[120:123], v[28:31]\n"
    "ds_read_b64_tr_b16 v[246:247], v105 offset:13312\n"
    "ds_read_b64_tr_b16 v[248:249], v106 offset:9216\n"
    "v_mfma_f32_16x16x32_bf16 v[36:39], v[84:87], a[124:127], v[36:39]\n"
    "ds_read_b64_tr_b16 v[250:251], v106 offset:13312\n"
    "ds_read_b64_tr_b16 v[252:253], v107 offset:9216\n"
    "v_mfma_f32_16x16x32_bf16 v[40:43], v[88:91], a[120:123], v[40:43]\n"
    "ds_read_b64_tr_b16 v[254:255], v107 offset:13312\n"
    "s_add_u32 m0, s59, 0x1cc00\n"
    "v_mfma_f32_16x16x32_bf16 v[48:51], v[88:91], a[124:127], v[48:51]\n"
    "v_mfma_f32_16x16x32_bf16 v[44:47], v[92:95], a[120:123], v[44:47]\n"
    "buffer_load_dwordx4 v123, s[12:15], s70 offen lds\n"
    "v_mfma_f32_16x16x32_bf16 v[52:55], v[92:95], a[124:127], v[52:55]\n"
    "s_waitcnt lgkmcnt(15)\n"
    "v_mfma_f32_16x16x32_bf16 v[56:59], v[96:99], a[120:123], v[56:59]\n"
    "s_add_u32 s67, s67, 0x10000\n"
    "v_mfma_f32_16x16x32_bf16 v[64:67], v[96:99], a[124:127], v[64:67]\n"
    "v_mfma_f32_16x16x32_bf16 v[60:63], v[100:103], a[120:123], v[60:63]\n"
    "v_mfma_f32_16x16x32_bf16 v[68:71], v[100:103], a[124:127], v[68:71]\n"
    "s_barrier\n"
    "s_waitcnt vmcnt(35)\n"
    "v_mfma_f32_16x16x32_bf16 v[24:27], v[168:171], a[128:131], v[24:27]\n"
    "buffer_load_dwordx4 a[208:211], v2, s[8:11], s66 offen offset:2048 sc1\n"
    "v_add_u32 v104, 0x8000, v108\n"
    "s_waitcnt vmcnt(35)\n"
    "v_mfma_f32_16x16x32_bf16 v[32:35], v[168:171], a[132:135], v[32:35]\n"
    "v_xor_b32 v105, 32, v104\n"
    "v_xor_b32 v106, 64, v104\n"
    "v_mfma_f32_16x16x32_bf16 v[28:31], v[172:175], a[128:131], v[28:31]\n"
    "s_add_u32 s67, s3, s69\n"
    "s_and_b32 s67, s67, 7\n"
    "s_add_u32 s69, s69, 1\n"
    "s_cmp_eq_u32 s69, 7\n"
    "s_cselect_b32 s69, 0, s69\n"
    "v_mfma_f32_16x16x32_bf16 v[36:39], v[172:175], a[132:135], v[36:39]\n"
    "buffer_load_dwordx4 a[212:215], v120, s[8:11], s66 offen offset:2048 sc1\n"
    "v_xor_b32 v107, 0x60, v104\n"
    "v_mfma_f32_16x16x32_bf16 v[40:43], v[176:179], a[128:131], v[40:43]\n"
    "ds_read_b64_tr_b16 v[80:81], v106\n"
    "ds_read_b64_tr_b16 v[82:83], v106 offset:4096\n"
    "v_mfma_f32_16x16x32_bf16 v[48:51], v[176:179], a[132:135], v[48:51]\n"
    "ds_read_b64_tr_b16 v[84:85], v107\n"
    "ds_read_b64_tr_b16 v[86:87], v107 offset:4096\n"
    "v_mfma_f32_16x16x32_bf16 v[44:47], v[180:183], a[128:131], v[44:47]\n"
    "ds_read_b64_tr_b16 v[88:89], v104 offset:1024\n"
    "buffer_load_dwordx4 a[216:219], v2, s[8:11], s66 offen offset:3072 sc1\n"
    "v_mfma_f32_16x16x32_bf16 v[52:55], v[180:183], a[132:135], v[52:55]\n"
    "ds_read_b64_tr_b16 v[90:91], v104 offset:5120\n"
    "ds_read_b64_tr_b16 v[92:93], v105 offset:1024\n"
    "s_waitcnt lgkmcnt(15)\n"
    "v_mfma_f32_16x16x32_bf16 v[56:59], v[184:187], a[128:131], v[56:59]\n"
    "ds_read_b64_tr_b16 v[94:95], v105 offset:5120\n"
    "buffer_load_dwordx4 a[220:223], v120, s[8:11], s66 offen offset:3072 sc1\n"
    "v_mfma_f32_16x16x32_bf16 v[64:67], v[184:187], a[132:135], v[64:67]\n"
    "ds_read_b64_tr_b16 v[96:97], v106 offset:1024\n"
    "s_lshl_b32 s67, s67, 17\n"
    "v_mfma_f32_16x16x32_bf16 v[60:63], v[188:191], a[128:131], v[60:63]\n"
    "ds_read_b64_tr_b16 v[98:99], v106 offset:5120\n"
    "ds_read_b64_tr_b16 v[100:101], v107 offset:1024\n"
    "v_mfma_f32_16x16x32_bf16 v[68:71], v[188:191], a[132:135], v[68:71]\n"
    "s_add_u32 s66, s3, s71\n"
    "s_and_b32 s66, s66, 7\n"
    "s_add_u32 s71, s71, 1\n"
    "s_cmp_eq_u32 s71, 7\n"
    "s_cselect_b32 s71, 0, s71\n"
    "ds_read_b64_tr_b16 v[102:103], v107 offset:5120\n"
    "s_waitcnt vmcnt(36)\n"
    "v_mfma_f32_16x16x32_bf16 v[24:27], v[232:235], a[136:139], v[24:27]\n"
    "ds_read_b64_tr_b16 v[168:169], v106 offset:8192\n"
    "s_add_u32 s67, s67, s90\n"
    "s_waitcnt vmcnt(35)\n"
    "v_mfma_f32_16x16x32_bf16 v[32:35], v[232:235], a[140:143], v[32:35]\n"
    "ds_read_b64_tr_b16 v[170:171], v106 offset:12288\n"
    "s_add_u32 s70, s67, 0x2000\n"
    "v_mfma_f32_16x16x32_bf16 v[28:31], v[236:239], a[136:139], v[28:31]\n"
    "ds_read_b64_tr_b16 v[172:173], v107 offset:8192\n"
    "s_add_u32 m0, s59, 0x20000\n"
    "v_mfma_f32_16x16x32_bf16 v[36:39], v[236:239], a[140:143], v[36:39]\n"
    "ds_read_b64_tr_b16 v[174:175], v107 offset:12288\n"
    "buffer_load_dwordx4 v121, s[12:15], s67 offen lds\n"
    "s_waitcnt lgkmcnt(15)\n"
    "v_mfma_f32_16x16x32_bf16 v[40:43], v[240:243], a[136:139], v[40:43]\n"
    "ds_read_b64_tr_b16 v[176:177], v104 offset:9216\n"
    "ds_read_b64_tr_b16 v[178:179], v104 offset:13312\n"
    "v_mfma_f32_16x16x32_bf16 v[48:51], v[240:243], a[140:143], v[48:51]\n"
    "ds_read_b64_tr_b16 v[180:181], v105 offset:9216\n"
    "s_add_u32 m0, s59, 0x20400\n"
    "v_mfma_f32_16x16x32_bf16 v[44:47], v[244:247], a[136:139], v[44:47]\n"
    "ds_read_b64_tr_b16 v[182:183], v105 offset:13312\n"
    "ds_read_b64_tr_b16 v[184:185], v106 offset:9216\n"
    "v_mfma_f32_16x16x32_bf16 v[52:55], v[244:247], a[140:143], v[52:55]\n"
    "buffer_load_dwordx4 v123, s[12:15], s67 offen lds\n"
    "ds_read_b64_tr_b16 v[186:187], v106 offset:13312\n"
    "v_mfma_f32_16x16x32_bf16 v[56:59], v[248:251], a[136:139], v[56:59]\n"
    "ds_read_b64_tr_b16 v[188:189], v107 offset:9216\n"
    "s_add_u32 m0, s59, 0x20800\n"
    "v_mfma_f32_16x16x32_bf16 v[64:67], v[248:251], a[140:143], v[64:67]\n"
    "ds_read_b64_tr_b16 v[190:191], v107 offset:13312\n"
    "s_lshl_b32 s66, s66, 12\n"
    "v_mfma_f32_16x16x32_bf16 v[60:63], v[252:255], a[136:139], v[60:63]\n"
    "buffer_load_dwordx4 v121, s[12:15], s70 offen lds\n"
    "v_mfma_f32_16x16x32_bf16 v[68:71], v[252:255], a[140:143], v[68:71]\n"
    "s_waitcnt lgkmcnt(15)\n"
    "s_waitcnt vmcnt(36)\n"
    "v_mfma_f32_16x16x32_bf16 v[24:27], v[80:83], a[144:147], v[24:27]\n"
    "v_add_u32 v104, 0x10000, v108\n"
    "s_add_u32 s66, s66, s74\n"
    "s_waitcnt vmcnt(35)\n"
    "v_mfma_f32_16x16x32_bf16 v[32:35], v[80:83], a[148:151], v[32:35]\n"
    "v_xor_b32 v105, 32, v104\n"
    "v_xor_b32 v106, 64, v104\n"
    "v_mfma_f32_16x16x32_bf16 v[28:31], v[84:87], a[144:147], v[28:31]\n"
    "v_xor_b32 v107, 0x60, v104\n"
    "s_add_u32 m0, s59, 0x20c00\n"
    "v_mfma_f32_16x16x32_bf16 v[36:39], v[84:87], a[148:151], v[36:39]\n"
    "ds_read_b64_tr_b16 v[232:233], v106\n"
    "buffer_load_dwordx4 v123, s[12:15], s70 offen lds\n"
    "v_mfma_f32_16x16x32_bf16 v[40:43], v[88:91], a[144:147], v[40:43]\n"
    "ds_read_b64_tr_b16 v[234:235], v106 offset:4096\n"
    "buffer_load_dwordx4 a[224:227], v2, s[8:11], s66 offen sc1\n"
    "v_mfma_f32_16x16x32_bf16 v[48:51], v[88:91], a[148:151], v[48:51]\n"
    "ds_read_b64_tr_b16 v[236:237], v107\n"
    "ds_read_b64_tr_b16 v[238:239], v107 offset:4096\n"
    "v_mfma_f32_16x16x32_bf16 v[44:47], v[92:95], a[144:147], v[44:47]\n"
    "ds_read_b64_tr_b16 v[240:241], v104 offset:1024\n"
    "s_add_u32 s67, s67, 0x10000\n"
    "v_mfma_f32_16x16x32_bf16 v[52:55], v[92:95], a[148:151], v[52:55]\n"
    "ds_read_b64_tr_b16 v[242:243], v104 offset:5120\n"
    "buffer_load_dwordx4 a[228:231], v120, s[8:11], s66 offen sc1\n"
    "s_waitcnt lgkmcnt(15)\n"
    "v_mfma_f32_16x16x32_bf16 v[56:59], v[96:99], a[144:147], v[56:59]\n"
    "ds_read_b64_tr_b16 v[244:245], v105 offset:1024\n"
    "s_add_u32 s70, s67, 0x2000\n"
    "v_mfma_f32_16x16x32_bf16 v[64:67], v[96:99], a[148:151], v[64:67]\n"
    "ds_read_b64_tr_b16 v[246:247], v105 offset:5120\n"
    "ds_read_b64_tr_b16 v[248:249], v106 offset:1024\n"
    "v_mfma_f32_16x16x32_bf16 v[60:63], v[100:103], a[144:147], v[60:63]\n"
    "ds_read_b64_tr_b16 v[250:251], v106 offset:5120\n"
    "buffer_load_dwordx4 a[232:235], v2, s[8:11], s66 offen offset:1024 sc1\n"
    "v_mfma_f32_16x16x32_bf16 v[68:71], v[100:103], a[148:151], v[68:71]\n"
    "ds_read_b64_tr_b16 v[252:253], v107 offset:1024\n"
    "s_add_u32 m0, s59, 0x24000\n"
    "ds_read_b64_tr_b16 v[254:255], v107 offset:5120\n"
    "s_waitcnt vmcnt(37)\n"
    "v_mfma_f32_16x16x32_bf16 v[24:27], v[168:171], a[152:155], v[24:27]\n"
    "buffer_load_dwordx4 v121, s[12:15], s67 offen lds\n"
    "ds_read_b64_tr_b16 v[80:81], v106 offset:8192\n"
    "s_waitcnt vmcnt(37)\n"
    "v_mfma_f32_16x16x32_bf16 v[32:35], v[168:171], a[156:159], v[32:35]\n"
    "buffer_load_dwordx4 a[236:239], v120, s[8:11], s66 offen offset:1024 sc1\n"
    "ds_read_b64_tr_b16 v[82:83], v106 offset:12288\n"
    "s_waitcnt lgkmcnt(15)\n"
    "v_mfma_f32_16x16x32_bf16 v[28:31], v[172:175], a[152:155], v[28:31]\n"
    "ds_read_b64_tr_b16 v[84:85], v107 offset:8192\n"
    "s_add_u32 m0, s59, 0x24400\n"
    "v_mfma_f32_16x16x32_bf16 v[36:39], v[172:175], a[156:159], v[36:39]\n"
    "buffer_load_dwordx4 a[240:243], v2, s[8:11], s66 offen offset:2048 sc1\n"
    "ds_read_b64_tr_b16 v[86:87], v107 offset:12288\n"
    "v_mfma_f32_16x16x32_bf16 v[40:43], v[176:179], a[152:155], v[40:43]\n"
    "buffer_load_dwordx4 v123, s[12:15], s67 offen lds\n"
    "ds_read_b64_tr_b16 v[88:89], v104 offset:9216\n"
    "buffer_load_dwordx4 a[244:247], v120, s[8:11], s66 offen offset:2048 sc1\n"
    "v_mfma_f32_16x16x32_bf16 v[48:51], v[176:179], a[156:159], v[48:51]\n"
    "ds_read_b64_tr_b16 v[90:91], v104 offset:13312\n"
    "s_add_u32 m0, s59, 0x24800\n"
    "v_mfma_f32_16x16x32_bf16 v[44:47], v[180:183], a[152:155], v[44:47]\n"
    "ds_read_b64_tr_b16 v[92:93], v105 offset:9216\n"
    "buffer_load_dwordx4 v121, s[12:15], s70 offen lds\n"
    "v_mfma_f32_16x16x32_bf16 v[52:55], v[180:183], a[156:159], v[52:55]\n"
    "ds_read_b64_tr_b16 v[94:95], v105 offset:13312\n"
    "s_add_u32 m0, s59, 0x24c00\n"
    "v_mfma_f32_16x16x32_bf16 v[56:59], v[184:187], a[152:155], v[56:59]\n"
    "ds_read_b64_tr_b16 v[96:97], v106 offset:9216\n"
    "buffer_load_dwordx4 v123, s[12:15], s70 offen lds\n"
    "v_mfma_f32_16x16x32_bf16 v[64:67], v[184:187], a[156:159], v[64:67]\n"
    "ds_read_b64_tr_b16 v[98:99], v106 offset:13312\n"
    "s_add_u32 s67, s67, 0x10000\n"
    "s_waitcnt lgkmcnt(15)\n"
    "v_mfma_f32_16x16x32_bf16 v[60:63], v[188:191], a[152:155], v[60:63]\n"
    "ds_read_b64_tr_b16 v[100:101], v107 offset:9216\n"
    "ds_read_b64_tr_b16 v[102:103], v107 offset:13312\n"
    "v_mfma_f32_16x16x32_bf16 v[68:71], v[188:191], a[156:159], v[68:71]\n"
    "s_waitcnt vmcnt(42)\n"
    "s_barrier\n"
    "s_waitcnt vmcnt(39)\n"
    "v_mfma_f32_16x16x32_bf16 v[24:27], v[232:235], a[160:163], v[24:27]\n"
    "buffer_load_dwordx4 a[248:251], v2, s[8:11], s66 offen offset:3072 sc1\n"
    "v_add_u32 v104, 0x18000, v108\n"
    "s_waitcnt vmcnt(38)\n"
    "v_mfma_f32_16x16x32_bf16 v[32:35], v[232:235], a[164:167], v[32:35]\n"
    "v_xor_b32 v105, 32, v104\n"
    "s_lshr_b32 s4, s65, 2\n"
    "v_mfma_f32_16x16x32_bf16 v[28:31], v[236:239], a[160:163], v[28:31]\n"
    "v_xor_b32 v106, 64, v104\n"
    "v_xor_b32 v107, 0x60, v104\n"
    "v_mfma_f32_16x16x32_bf16 v[36:39], v[236:239], a[164:167], v[36:39]\n"
    "ds_read_b64_tr_b16 v[168:169], v106\n"
    "s_lshl_b32 s4, s4, 2\n"
    "v_mfma_f32_16x16x32_bf16 v[40:43], v[240:243], a[160:163], v[40:43]\n"
    "ds_read_b64_tr_b16 v[170:171], v106 offset:4096\n"
    "buffer_load_dwordx4 a[252:255], v120, s[8:11], s66 offen offset:3072 sc1\n"
    "v_mfma_f32_16x16x32_bf16 v[48:51], v[240:243], a[164:167], v[48:51]\n"
    "ds_read_b64_tr_b16 v[172:173], v107\n"
    "s_add_u32 s4, s4, s64\n"
    "s_waitcnt lgkmcnt(15)\n"
    "v_mfma_f32_16x16x32_bf16 v[44:47], v[244:247], a[160:163], v[44:47]\n"
    "ds_read_b64_tr_b16 v[174:175], v107 offset:4096\n"
    "ds_read_b64_tr_b16 v[176:177], v104 offset:1024\n"
    "v_mfma_f32_16x16x32_bf16 v[52:55], v[244:247], a[164:167], v[52:55]\n"
    "ds_read_b64_tr_b16 v[178:179], v104 offset:5120\n"
    "s_lshl_b32 s76, s4, 18\n"
    "v_mfma_f32_16x16x32_bf16 v[56:59], v[248:251], a[160:163], v[56:59]\n"
    "ds_read_b64_tr_b16 v[180:181], v105 offset:1024\n"
    "s_add_u32 s76, s76, s59\n"
    "v_mfma_f32_16x16x32_bf16 v[64:67], v[248:251], a[164:167], v[64:67]\n"
    "s_add_u32 s66, s3, s71\n"
    "s_and_b32 s66, s66, 7\n"
    "s_add_u32 s71, s71, 1\n"
    "s_cmp_eq_u32 s71, 7\n"
    "s_cselect_b32 s71, 0, s71\n"
    "v_mfma_f32_16x16x32_bf16 v[60:63], v[252:255], a[160:163], v[60:63]\n"
    "ds_read_b64_tr_b16 v[182:183], v105 offset:5120\n"
    "ds_read_b64_tr_b16 v[184:185], v106 offset:1024\n"
    "v_mfma_f32_16x16x32_bf16 v[68:71], v[252:255], a[164:167], v[68:71]\n"
    "ds_read_b64_tr_b16 v[186:187], v106 offset:5120\n"
    "ds_read_b64_tr_b16 v[188:189], v107 offset:1024\n"
    "ds_read_b64_tr_b16 v[190:191], v107 offset:5120\n"
    "s_waitcnt lgkmcnt(15)\n"
    "s_waitcnt vmcnt(37)\n"
    "v_mfma_f32_16x16x32_bf16 v[24:27], v[80:83], a[168:171], v[24:27]\n"
    "s_add_u32 s76, s76, 0x3820000\n"
    "s_xor_b32 s5, s63, 1\n"
    "s_waitcnt vmcnt(36)\n"
    "v_mfma_f32_16x16x32_bf16 v[32:35], v[80:83], a[172:175], v[32:35]\n"
    "ds_read_b64_tr_b16 v[232:233], v106 offset:8192\n"
    "s_lshl_b32 s6, s5, 16\n"
    "v_mfma_f32_16x16x32_bf16 v[28:31], v[84:87], a[168:171], v[28:31]\n"
    "ds_read_b64_tr_b16 v[234:235], v106 offset:12288\n"
    "s_lshl_b32 s7, s63, 14\n"
    "v_mfma_f32_16x16x32_bf16 v[36:39], v[84:87], a[172:175], v[36:39]\n"
    "ds_read_b64_tr_b16 v[236:237], v107 offset:8192\n"
    "s_add_u32 s6, s6, s7\n"
    "v_mfma_f32_16x16x32_bf16 v[40:43], v[88:91], a[168:171], v[40:43]\n"
    "ds_read_b64_tr_b16 v[238:239], v107 offset:12288\n"
    "s_add_u32 s85, s6, s76\n"
    "v_mfma_f32_16x16x32_bf16 v[48:51], v[88:91], a[172:175], v[48:51]\n"
    "ds_read_b64_tr_b16 v[240:241], v104 offset:9216\n"
    "s_lshl_b32 s6, s5, 14\n"
    "v_mfma_f32_16x16x32_bf16 v[44:47], v[92:95], a[168:171], v[44:47]\n"
    "ds_read_b64_tr_b16 v[242:243], v104 offset:13312\n"
    "s_lshl_b32 s7, s63, 16\n"
    "v_mfma_f32_16x16x32_bf16 v[52:55], v[92:95], a[172:175], v[52:55]\n"
    "ds_read_b64_tr_b16 v[244:245], v105 offset:9216\n"
    "s_add_u32 s6, s6, s7\n"
    "s_waitcnt lgkmcnt(15)\n"
    "v_mfma_f32_16x16x32_bf16 v[56:59], v[96:99], a[168:171], v[56:59]\n"
    "ds_read_b64_tr_b16 v[246:247], v105 offset:13312\n"
    "s_lshl_b32 s66, s66, 12\n"
    "v_mfma_f32_16x16x32_bf16 v[64:67], v[96:99], a[172:175], v[64:67]\n"
    "ds_read_b64_tr_b16 v[248:249], v106 offset:9216\n"
    "s_add_u32 s92, s6, s76\n"
    "v_mfma_f32_16x16x32_bf16 v[60:63], v[100:103], a[168:171], v[60:63]\n"
    "ds_read_b64_tr_b16 v[250:251], v106 offset:13312\n"
    "s_xor_b32 s5, s63, 2\n"
    "v_mfma_f32_16x16x32_bf16 v[68:71], v[100:103], a[172:175], v[68:71]\n"
    "ds_read_b64_tr_b16 v[252:253], v107 offset:9216\n"
    "s_add_u32 s66, s66, s74\n"
    "ds_read_b64_tr_b16 v[254:255], v107 offset:13312\n"
    "s_waitcnt vmcnt(35)\n"
    "v_mfma_f32_16x16x32_bf16 v[24:27], v[168:171], a[176:179], v[24:27]\n"
    "s_lshl_b32 s6, s5, 16\n"
    "s_lshl_b32 s7, s63, 14\n"
    "s_waitcnt vmcnt(34)\n"
    "v_mfma_f32_16x16x32_bf16 v[32:35], v[168:171], a[180:183], v[32:35]\n"
    "buffer_load_dwordx4 a[0:3], v2, s[8:11], s66 offen sc1\n"
    "v_add_u32 v104, 0x4000, v108\n"
    "v_mfma_f32_16x16x32_bf16 v[28:31], v[172:175], a[176:179], v[28:31]\n"
    "v_xor_b32 v105, 32, v104\n"
    "v_xor_b32 v106, 64, v104\n"
    "v_mfma_f32_16x16x32_bf16 v[36:39], v[172:175], a[180:183], v[36:39]\n"
    "v_xor_b32 v107, 0x60, v104\n"
    "s_add_u32 s6, s6, s7\n"
    "s_waitcnt lgkmcnt(15)\n"
    "v_mfma_f32_16x16x32_bf16 v[40:43], v[176:179], a[176:179], v[40:43]\n"
    "ds_read_b64_tr_b16 v[80:81], v106\n"
    "buffer_load_dwordx4 a[4:7], v120, s[8:11], s66 offen sc1\n"
    "v_mfma_f32_16x16x32_bf16 v[48:51], v[176:179], a[180:183], v[48:51]\n"
    "ds_read_b64_tr_b16 v[82:83], v106 offset:4096\n"
    "s_add_u32 s86, s6, s76\n"
    "v_mfma_f32_16x16x32_bf16 v[44:47], v[180:183], a[176:179], v[44:47]\n"
    "ds_read_b64_tr_b16 v[84:85], v107\n"
    "ds_read_b64_tr_b16 v[86:87], v107 offset:4096\n"
    "v_mfma_f32_16x16x32_bf16 v[52:55], v[180:183], a[180:183], v[52:55]\n"
    "ds_read_b64_tr_b16 v[88:89], v104 offset:1024\n"
    "s_lshl_b32 s6, s5, 14\n"
    "s_waitcnt lgkmcnt(15)\n"
    "v_mfma_f32_16x16x32_bf16 v[56:59], v[184:187], a[176:179], v[56:59]\n"
    "ds_read_b64_tr_b16 v[90:91], v104 offset:5120\n"
    "buffer_load_dwordx4 a[8:11], v2, s[8:11], s66 offen offset:1024 sc1\n"
    "v_mfma_f32_16x16x32_bf16 v[64:67], v[184:187], a[180:183], v[64:67]\n"
    "ds_read_b64_tr_b16 v[92:93], v105 offset:1024\n"
    "s_lshl_b32 s7, s63, 16\n"
    "v_mfma_f32_16x16x32_bf16 v[60:63], v[188:191], a[176:179], v[60:63]\n"
    "ds_read_b64_tr_b16 v[94:95], v105 offset:5120\n"
    "ds_read_b64_tr_b16 v[96:97], v106 offset:1024\n"
    "v_mfma_f32_16x16x32_bf16 v[68:71], v[188:191], a[180:183], v[68:71]\n"
    "ds_read_b64_tr_b16 v[98:99], v106 offset:5120\n"
    "s_add_u32 s6, s6, s7\n"
    "ds_read_b64_tr_b16 v[100:101], v107 offset:1024\n"
    "ds_read_b64_tr_b16 v[102:103], v107 offset:5120\n"
    "s_waitcnt vmcnt(36)\n"
    "v_mfma_f32_16x16x32_bf16 v[24:27], v[232:235], a[184:187], v[24:27]\n"
    "buffer_load_dwordx4 a[12:15], v120, s[8:11], s66 offen offset:1024 sc1\n"
    "s_add_u32 s93, s6, s76\n"
    "s_waitcnt vmcnt(36)\n"
    "v_mfma_f32_16x16x32_bf16 v[32:35], v[232:235], a[188:191], v[32:35]\n"
    "ds_read_b64_tr_b16 v[168:169], v106 offset:8192\n"
    "s_xor_b32 s5, s63, 3\n"
    "s_waitcnt lgkmcnt(15)\n"
    "v_mfma_f32_16x16x32_bf16 v[28:31], v[236:239], a[184:187], v[28:31]\n"
    "buffer_load_dwordx4 a[16:19], v2, s[8:11], s66 offen offset:2048 sc1\n"
    "s_lshl_b32 s6, s5, 16\n"
    "v_mfma_f32_16x16x32_bf16 v[36:39], v[236:239], a[188:191], v[36:39]\n"
    "ds_read_b64_tr_b16 v[170:171], v106 offset:12288\n"
    "s_lshl_b32 s7, s63, 14\n"
    "v_mfma_f32_16x16x32_bf16 v[40:43], v[240:243], a[184:187], v[40:43]\n"
    "ds_read_b64_tr_b16 v[172:173], v107 offset:8192\n"
    "buffer_load_dwordx4 a[20:23], v120, s[8:11], s66 offen offset:2048 sc1\n"
    "buffer_load_dwordx4 a[24:27], v2, s[8:11], s66 offen offset:3072 sc1\n"
    "buffer_load_dwordx4 a[28:31], v120, s[8:11], s66 offen offset:3072 sc1\n"
    "v_mfma_f32_16x16x32_bf16 v[48:51], v[240:243], a[188:191], v[48:51]\n"
    "ds_read_b64_tr_b16 v[174:175], v107 offset:12288\n"
    "s_add_u32 s6, s6, s7\n"
    "v_mfma_f32_16x16x32_bf16 v[44:47], v[244:247], a[184:187], v[44:47]\n"
    "ds_read_b64_tr_b16 v[176:177], v104 offset:9216\n"
    "s_add_u32 s87, s6, s76\n"
    "v_mfma_f32_16x16x32_bf16 v[52:55], v[244:247], a[188:191], v[52:55]\n"
    "ds_read_b64_tr_b16 v[178:179], v104 offset:13312\n"
    "s_lshl_b32 s6, s5, 14\n"
    "v_mfma_f32_16x16x32_bf16 v[56:59], v[248:251], a[184:187], v[56:59]\n"
    "ds_read_b64_tr_b16 v[180:181], v105 offset:9216\n"
    "s_lshl_b32 s7, s63, 16\n"
    "v_mfma_f32_16x16x32_bf16 v[64:67], v[248:251], a[188:191], v[64:67]\n"
    "ds_read_b64_tr_b16 v[182:183], v105 offset:13312\n"
    "ds_read_b64_tr_b16 v[184:185], v106 offset:9216\n"
    "s_waitcnt lgkmcnt(15)\n"
    "v_mfma_f32_16x16x32_bf16 v[60:63], v[252:255], a[184:187], v[60:63]\n"
    "ds_read_b64_tr_b16 v[186:187], v106 offset:13312\n"
    "s_add_u32 s6, s6, s7\n"
    "s_add_u32 s94, s6, s76\n"
    "s_add_u32 s24, s52, 0\n"
    "s_addc_u32 s25, s53, 0\n"
    "s_and_b32 s25, s25, 0xffff\n"
    "s_mov_b32 s26, 0x200000\n"
    "s_mov_b32 s27, 0x20000\n"
    "v_mfma_f32_16x16x32_bf16 v[68:71], v[252:255], a[188:191], v[68:71]\n"
    "ds_read_b64_tr_b16 v[188:189], v107 offset:9216\n"
    "ds_read_b64_tr_b16 v[190:191], v107 offset:13312\n"
    "s_waitcnt vmcnt(36)\n"
    "s_barrier\n"
    "s_waitcnt vmcnt(35)\n"
    "v_mfma_f32_16x16x32_bf16 v[24:27], v[80:83], a[192:195], v[24:27]\n"
    "v_add_u32 v104, 0xc000, v108\n"
    "v_lshlrev_b32 v144, 16, v112\n"
    "s_waitcnt vmcnt(33)\n"
    "v_mfma_f32_16x16x32_bf16 v[32:35], v[80:83], a[196:199], v[32:35]\n"
    "v_xor_b32 v105, 32, v104\n"
    "v_xor_b32 v106, 64, v104\n"
    "v_mfma_f32_16x16x32_bf16 v[28:31], v[84:87], a[192:195], v[28:31]\n"
    "v_xor_b32 v107, 0x60, v104\n"
    "v_and_b32 v145, 0xffff0000, v112\n"
    "v_mfma_f32_16x16x32_bf16 v[36:39], v[84:87], a[196:199], v[36:39]\n"
    "ds_read_b64_tr_b16 v[232:233], v106\n"
    "ds_read_b64_tr_b16 v[234:235], v106 offset:4096\n"
    "v_mfma_f32_16x16x32_bf16 v[40:43], v[88:91], a[192:195], v[40:43]\n"
    "ds_read_b64_tr_b16 v[236:237], v107\n"
    "v_lshlrev_b32 v146, 16, v113\n"
    "v_mfma_f32_16x16x32_bf16 v[48:51], v[88:91], a[196:199], v[48:51]\n"
    "ds_read_b64_tr_b16 v[238:239], v107 offset:4096\n"
    "ds_read_b64_tr_b16 v[240:241], v104 offset:1024\n"
    "s_waitcnt lgkmcnt(15)\n"
    "v_mfma_f32_16x16x32_bf16 v[44:47], v[92:95], a[192:195], v[44:47]\n"
    "ds_read_b64_tr_b16 v[242:243], v104 offset:5120\n"
    "v_and_b32 v147, 0xffff0000, v113\n"
    "v_mfma_f32_16x16x32_bf16 v[52:55], v[92:95], a[196:199], v[52:55]\n"
    "ds_read_b64_tr_b16 v[244:245], v105 offset:1024\n"
    "ds_read_b64_tr_b16 v[246:247], v105 offset:5120\n"
    "v_mfma_f32_16x16x32_bf16 v[56:59], v[96:99], a[192:195], v[56:59]\n"
    "ds_read_b64_tr_b16 v[248:249], v106 offset:1024\n"
    "v_lshlrev_b32 v148, 16, v114\n"
    "v_mfma_f32_16x16x32_bf16 v[64:67], v[96:99], a[196:199], v[64:67]\n"
    "ds_read_b64_tr_b16 v[250:251], v106 offset:5120\n"
    "ds_read_b64_tr_b16 v[252:253], v107 offset:1024\n"
    "v_mfma_f32_16x16x32_bf16 v[60:63], v[100:103], a[192:195], v[60:63]\n"
    "ds_read_b64_tr_b16 v[254:255], v107 offset:5120\n"
    "v_and_b32 v149, 0xffff0000, v114\n"
    "v_mfma_f32_16x16x32_bf16 v[68:71], v[100:103], a[196:199], v[68:71]\n"
    "v_lshlrev_b32 v150, 16, v115\n"
    "v_and_b32 v151, 0xffff0000, v115\n"
    "s_waitcnt vmcnt(31)\n"
    "v_mfma_f32_16x16x32_bf16 v[24:27], v[168:171], a[200:203], v[24:27]\n"
    "ds_read_b64_tr_b16 v[80:81], v106 offset:8192\n"
    "ds_read_b64_tr_b16 v[82:83], v106 offset:12288\n"
    "s_waitcnt vmcnt(30)\n"
    "v_mfma_f32_16x16x32_bf16 v[32:35], v[168:171], a[204:207], v[32:35]\n"
    "ds_read_b64_tr_b16 v[84:85], v107 offset:8192\n"
    "ds_read_b64_tr_b16 v[86:87], v107 offset:12288\n"
    "s_waitcnt lgkmcnt(15)\n"
    "v_mfma_f32_16x16x32_bf16 v[28:31], v[172:175], a[200:203], v[28:31]\n"
    "ds_read_b64_tr_b16 v[88:89], v104 offset:9216\n"
    "ds_read_b64_tr_b16 v[90:91], v104 offset:13312\n"
    "v_mfma_f32_16x16x32_bf16 v[36:39], v[172:175], a[204:207], v[36:39]\n"
    "ds_read_b64_tr_b16 v[92:93], v105 offset:9216\n"
    "v_lshlrev_b32 v152, 16, v116\n"
    "v_mfma_f32_16x16x32_bf16 v[40:43], v[176:179], a[200:203], v[40:43]\n"
    "ds_read_b64_tr_b16 v[94:95], v105 offset:13312\n"
    "v_and_b32 v153, 0xffff0000, v116\n"
    "v_mfma_f32_16x16x32_bf16 v[48:51], v[176:179], a[204:207], v[48:51]\n"
    "ds_read_b64_tr_b16 v[96:97], v106 offset:9216\n"
    "v_lshlrev_b32 v154, 16, v117\n"
    "v_mfma_f32_16x16x32_bf16 v[44:47], v[180:183], a[200:203], v[44:47]\n"
    "ds_read_b64_tr_b16 v[98:99], v106 offset:13312\n"
    "ds_read_b64_tr_b16 v[100:101], v107 offset:9216\n"
    "v_mfma_f32_16x16x32_bf16 v[52:55], v[180:183], a[204:207], v[52:55]\n"
    "ds_read_b64_tr_b16 v[102:103], v107 offset:13312\n"
    "v_and_b32 v155, 0xffff0000, v117\n"
    "v_mfma_f32_16x16x32_bf16 v[56:59], v[184:187], a[200:203], v[56:59]\n"
    "v_lshlrev_b32 v156, 16, v118\n"
    "v_and_b32 v157, 0xffff0000, v118\n"
    "v_mfma_f32_16x16x32_bf16 v[64:67], v[184:187], a[204:207], v[64:67]\n"
    "v_lshlrev_b32 v158, 16, v119\n"
    "v_and_b32 v159, 0xffff0000, v119\n"
    "v_mfma_f32_16x16x32_bf16 v[60:63], v[188:191], a[200:203], v[60:63]\n"
    "v_mfma_f32_16x16x32_bf16 v[68:71], v[188:191], a[204:207], v[68:71]\n"
    "s_waitcnt lgkmcnt(15)\n"
    "s_waitcnt vmcnt(27)\n"
    "v_mfma_f32_16x16x32_bf16 v[24:27], v[232:235], a[208:211], v[24:27]\n"
    "s_waitcnt vmcnt(26)\n"
    "v_mfma_f32_16x16x32_bf16 v[32:35], v[232:235], a[212:215], v[32:35]\n"
    "v_mfma_f32_16x16x32_bf16 v[28:31], v[236:239], a[208:211], v[28:31]\n"
    "v_mfma_f32_16x16x32_bf16 v[36:39], v[236:239], a[212:215], v[36:39]\n"
    "v_mfma_f32_16x16x32_bf16 v[40:43], v[240:243], a[208:211], v[40:43]\n"
    "v_mfma_f32_16x16x32_bf16 v[48:51], v[240:243], a[212:215], v[48:51]\n"
    "v_mfma_f32_16x16x32_bf16 v[44:47], v[244:247], a[208:211], v[44:47]\n"
    "v_mfma_f32_16x16x32_bf16 v[52:55], v[244:247], a[212:215], v[52:55]\n"
    "s_waitcnt lgkmcnt(14)\n"
    "v_mfma_f32_16x16x32_bf16 v[56:59], v[248:251], a[208:211], v[56:59]\n"
    "v_mfma_f32_16x16x32_bf16 v[64:67], v[248:251], a[212:215], v[64:67]\n"
    "s_waitcnt lgkmcnt(12)\n"
    "v_mfma_f32_16x16x32_bf16 v[60:63], v[252:255], a[208:211], v[60:63]\n"
    "v_mfma_f32_16x16x32_bf16 v[68:71], v[252:255], a[212:215], v[68:71]\n"
    "s_waitcnt lgkmcnt(10)\n"
    "s_waitcnt vmcnt(25)\n"
    "v_mfma_f32_16x16x32_bf16 v[24:27], v[80:83], a[216:219], v[24:27]\n"
    "s_waitcnt vmcnt(24)\n"
    "v_mfma_f32_16x16x32_bf16 v[32:35], v[80:83], a[220:223], v[32:35]\n"
    "s_waitcnt lgkmcnt(8)\n"
    "v_mfma_f32_16x16x32_bf16 v[28:31], v[84:87], a[216:219], v[28:31]\n"
    "v_mfma_f32_16x16x32_bf16 v[36:39], v[84:87], a[220:223], v[36:39]\n"
    "s_waitcnt lgkmcnt(6)\n"
    "v_mfma_f32_16x16x32_bf16 v[40:43], v[88:91], a[216:219], v[40:43]\n"
    "v_mfma_f32_16x16x32_bf16 v[48:51], v[88:91], a[220:223], v[48:51]\n"
    "s_waitcnt lgkmcnt(4)\n"
    "v_mfma_f32_16x16x32_bf16 v[44:47], v[92:95], a[216:219], v[44:47]\n"
    "v_mfma_f32_16x16x32_bf16 v[52:55], v[92:95], a[220:223], v[52:55]\n"
    "s_waitcnt lgkmcnt(2)\n"
    "v_mfma_f32_16x16x32_bf16 v[56:59], v[96:99], a[216:219], v[56:59]\n"
    "v_mfma_f32_16x16x32_bf16 v[64:67], v[96:99], a[220:223], v[64:67]\n"
    "s_waitcnt lgkmcnt(0)\n"
    "v_mfma_f32_16x16x32_bf16 v[60:63], v[100:103], a[216:219], v[60:63]\n"
    "v_mfma_f32_16x16x32_bf16 v[68:71], v[100:103], a[220:223], v[68:71]\n"
    "s_waitcnt vmcnt(10)\n"
    "s_barrier\n"
    "v_add_u32 v104, 0x14000, v108\n"
    "v_xor_b32 v106, 64, v104\n"
    "v_xor_b32 v107, 0x60, v104\n"
    "ds_read_b64_tr_b16 v[160:161], v106 offset:1024\n"
    "ds_read_b64_tr_b16 v[162:163], v106 offset:5120\n"
    "ds_read_b64_tr_b16 v[164:165], v107 offset:1024\n"
    "ds_read_b64_tr_b16 v[166:167], v107 offset:5120\n"
    "ds_read_b64_tr_b16 v[168:169], v106 offset:9216\n"
    "ds_read_b64_tr_b16 v[170:171], v106 offset:13312\n"
    "ds_read_b64_tr_b16 v[172:173], v107 offset:9216\n"
    "ds_read_b64_tr_b16 v[174:175], v107 offset:13312\n"
    "v_add_u32 v104, 0x1c000, v108\n"
    "v_xor_b32 v106, 64, v104\n"
    "v_xor_b32 v107, 0x60, v104\n"
    "ds_read_b64_tr_b16 v[176:177], v106 offset:1024\n"
    "ds_read_b64_tr_b16 v[178:179], v106 offset:5120\n"
    "ds_read_b64_tr_b16 v[180:181], v107 offset:1024\n"
    "ds_read_b64_tr_b16 v[182:183], v107 offset:5120\n"
    "ds_read_b64_tr_b16 v[184:185], v106 offset:9216\n"
    "ds_read_b64_tr_b16 v[186:187], v106 offset:13312\n"
    "ds_read_b64_tr_b16 v[188:189], v107 offset:9216\n"
    "ds_read_b64_tr_b16 v[190:191], v107 offset:13312\n"
    "s_waitcnt lgkmcnt(14)\n"
    "v_mfma_f32_16x16x32_bf16 v[56:59], v[160:163], a[224:227], v[56:59]\n"
    "v_mfma_f32_16x16x32_bf16 v[64:67], v[160:163], a[228:231], v[64:67]\n"
    "v_add_u32 v104, 0x20000, v108\n"
    "v_xor_b32 v106, 64, v104\n"
    "s_waitcnt lgkmcnt(12)\n"
    "v_mfma_f32_16x16x32_bf16 v[60:63], v[164:167], a[224:227], v[60:63]\n"
    "v_xor_b32 v107, 0x60, v104\n"
    "v_mfma_f32_16x16x32_bf16 v[68:71], v[164:167], a[228:231], v[68:71]\n"
    "ds_read_b64_tr_b16 v[192:193], v106 offset:1024\n"
    "ds_read_b64_tr_b16 v[194:195], v106 offset:5120\n"
    "ds_read_b64_tr_b16 v[196:197], v107 offset:1024\n"
    "ds_read_b64_tr_b16 v[198:199], v107 offset:5120\n"
    "s_waitcnt lgkmcnt(14)\n"
    "v_mfma_f32_16x16x32_bf16 v[56:59], v[168:171], a[232:235], v[56:59]\n"
    "v_mfma_f32_16x16x32_bf16 v[64:67], v[168:171], a[236:239], v[64:67]\n"
    "ds_read_b64_tr_b16 v[200:201], v106 offset:9216\n"
    "ds_read_b64_tr_b16 v[202:203], v106 offset:13312\n"
    "s_waitcnt lgkmcnt(14)\n"
    "v_mfma_f32_16x16x32_bf16 v[60:63], v[172:175], a[232:235], v[60:63]\n"
    "ds_read_b64_tr_b16 v[204:205], v107 offset:9216\n"
    "ds_read_b64_tr_b16 v[206:207], v107 offset:13312\n"
    "v_mfma_f32_16x16x32_bf16 v[68:71], v[172:175], a[236:239], v[68:71]\n"
    "s_waitcnt lgkmcnt(14)\n"
    "v_mfma_f32_16x16x32_bf16 v[56:59], v[176:179], a[240:243], v[56:59]\n"
    "v_mfma_f32_16x16x32_bf16 v[64:67], v[176:179], a[244:247], v[64:67]\n"
    "v_add_u32 v104, 0x24000, v108\n"
    "v_xor_b32 v106, 64, v104\n"
    "s_waitcnt lgkmcnt(12)\n"
    "v_mfma_f32_16x16x32_bf16 v[60:63], v[180:183], a[240:243], v[60:63]\n"
    "v_xor_b32 v107, 0x60, v104\n"
    "v_mfma_f32_16x16x32_bf16 v[68:71], v[180:183], a[244:247], v[68:71]\n"
    "ds_read_b64_tr_b16 v[160:161], v106 offset:1024\n"
    "ds_read_b64_tr_b16 v[162:163], v106 offset:5120\n"
    "ds_read_b64_tr_b16 v[164:165], v107 offset:1024\n"
    "ds_read_b64_tr_b16 v[166:167], v107 offset:5120\n"
    "s_waitcnt lgkmcnt(14)\n"
    "s_waitcnt vmcnt(9)\n"
    "v_mfma_f32_16x16x32_bf16 v[56:59], v[184:187], a[248:251], v[56:59]\n"
    "s_waitcnt vmcnt(8)\n"
    "v_mfma_f32_16x16x32_bf16 v[64:67], v[184:187], a[252:255], v[64:67]\n"
    "ds_read_b64_tr_b16 v[168:169], v106 offset:9216\n"
    "ds_read_b64_tr_b16 v[170:171], v106 offset:13312\n"
    "s_waitcnt lgkmcnt(14)\n"
    "v_mfma_f32_16x16x32_bf16 v[60:63], v[188:191], a[248:251], v[60:63]\n"
    "ds_read_b64_tr_b16 v[172:173], v107 offset:9216\n"
    "ds_read_b64_tr_b16 v[174:175], v107 offset:13312\n"
    "v_mfma_f32_16x16x32_bf16 v[68:71], v[188:191], a[252:255], v[68:71]\n"
    "s_waitcnt lgkmcnt(14)\n"
    "s_waitcnt vmcnt(7)\n"
    "v_mfma_f32_16x16x32_bf16 v[56:59], v[192:195], a[0:3], v[56:59]\n"
    "s_waitcnt vmcnt(6)\n"
    "v_mfma_f32_16x16x32_bf16 v[64:67], v[192:195], a[4:7], v[64:67]\n"
    "v_add_u32 v104, 0x14000, v108\n"
    "v_xor_b32 v105, 32, v104\n"
    "s_waitcnt lgkmcnt(12)\n"
    "v_mfma_f32_16x16x32_bf16 v[60:63], v[196:199], a[0:3], v[60:63]\n"
    "ds_read_b64_tr_b16 v[176:177], v104 offset:1024\n"
    "ds_read_b64_tr_b16 v[178:179], v104 offset:5120\n"
    "v_mfma_f32_16x16x32_bf16 v[68:71], v[196:199], a[4:7], v[68:71]\n"
    "ds_read_b64_tr_b16 v[180:181], v105 offset:1024\n"
    "ds_read_b64_tr_b16 v[182:183], v105 offset:5120\n"
    "s_waitcnt lgkmcnt(14)\n"
    "s_waitcnt vmcnt(5)\n"
    "v_mfma_f32_16x16x32_bf16 v[56:59], v[200:203], a[8:11], v[56:59]\n"
    "s_waitcnt vmcnt(4)\n"
    "v_mfma_f32_16x16x32_bf16 v[64:67], v[200:203], a[12:15], v[64:67]\n"
    "ds_read_b64_tr_b16 v[184:185], v104 offset:9216\n"
    "ds_read_b64_tr_b16 v[186:187], v104 offset:13312\n"
    "s_waitcnt lgkmcnt(14)\n"
    "v_mfma_f32_16x16x32_bf16 v[60:63], v[204:207], a[8:11], v[60:63]\n"
    "ds_read_b64_tr_b16 v[188:189], v105 offset:9216\n"
    "ds_read_b64_tr_b16 v[190:191], v105 offset:13312\n"
    "v_mfma_f32_16x16x32_bf16 v[68:71], v[204:207], a[12:15], v[68:71]\n"
    "s_waitcnt lgkmcnt(14)\n"
    "s_waitcnt vmcnt(3)\n"
    "v_mfma_f32_16x16x32_bf16 v[56:59], v[160:163], a[16:19], v[56:59]\n"
    "s_waitcnt vmcnt(2)\n"
    "v_mfma_f32_16x16x32_bf16 v[64:67], v[160:163], a[20:23], v[64:67]\n"
    "v_add_u32 v104, 0x1c000, v108\n"
    "v_xor_b32 v105, 32, v104\n"
    "s_waitcnt lgkmcnt(12)\n"
    "v_mfma_f32_16x16x32_bf16 v[60:63], v[164:167], a[16:19], v[60:63]\n"
    "ds_read_b64_tr_b16 v[192:193], v104 offset:1024\n"
    "ds_read_b64_tr_b16 v[194:195], v104 offset:5120\n"
    "v_mfma_f32_16x16x32_bf16 v[68:71], v[164:167], a[20:23], v[68:71]\n"
    "ds_read_b64_tr_b16 v[196:197], v105 offset:1024\n"
    "ds_read_b64_tr_b16 v[198:199], v105 offset:5120\n"
    "s_waitcnt lgkmcnt(14)\n"
    "s_waitcnt vmcnt(1)\n"
    "v_mfma_f32_16x16x32_bf16 v[56:59], v[168:171], a[24:27], v[56:59]\n"
    "s_waitcnt vmcnt(0)\n"
    "v_mfma_f32_16x16x32_bf16 v[64:67], v[168:171], a[28:31], v[64:67]\n"
    "ds_read_b64_tr_b16 v[200:201], v104 offset:9216\n"
    "ds_read_b64_tr_b16 v[202:203], v104 offset:13312\n"
    "s_waitcnt lgkmcnt(14)\n"
    "v_mfma_f32_16x16x32_bf16 v[60:63], v[172:175], a[24:27], v[60:63]\n"
    "ds_read_b64_tr_b16 v[204:205], v105 offset:9216\n"
    "ds_read_b64_tr_b16 v[206:207], v105 offset:13312\n"
    "v_mfma_f32_16x16x32_bf16 v[68:71], v[172:175], a[28:31], v[68:71]\n"
    "s_waitcnt lgkmcnt(14)\n"
    "v_mfma_f32_16x16x32_bf16 v[40:43], v[176:179], a[224:227], v[40:43]\n"
    "v_mfma_f32_16x16x32_bf16 v[48:51], v[176:179], a[228:231], v[48:51]\n"
    "v_add_u32 v104, 0x20000, v108\n"
    "v_xor_b32 v105, 32, v104\n"
    "s_waitcnt lgkmcnt(12)\n"
    "v_mfma_f32_16x16x32_bf16 v[44:47], v[180:183], a[224:227], v[44:47]\n"
    "ds_read_b64_tr_b16 v[160:161], v104 offset:1024\n"
    "ds_read_b64_tr_b16 v[162:163], v104 offset:5120\n"
    "v_mfma_f32_16x16x32_bf16 v[52:55], v[180:183], a[228:231], v[52:55]\n"
    "ds_read_b64_tr_b16 v[164:165], v105 offset:1024\n"
    "ds_read_b64_tr_b16 v[166:167], v105 offset:5120\n"
    "s_waitcnt lgkmcnt(14)\n"
    "v_mfma_f32_16x16x32_bf16 v[40:43], v[184:187], a[232:235], v[40:43]\n"
    "v_mfma_f32_16x16x32_bf16 v[48:51], v[184:187], a[236:239], v[48:51]\n"
    "ds_read_b64_tr_b16 v[168:169], v104 offset:9216\n"
    "ds_read_b64_tr_b16 v[170:171], v104 offset:13312\n"
    "s_waitcnt lgkmcnt(14)\n"
    "v_mfma_f32_16x16x32_bf16 v[44:47], v[188:191], a[232:235], v[44:47]\n"
    "ds_read_b64_tr_b16 v[172:173], v105 offset:9216\n"
    "v_mov_b32 v129, v7\n"
    "v_mfma_f32_16x16x32_bf16 v[52:55], v[188:191], a[236:239], v[52:55]\n"
    "ds_read_b64_tr_b16 v[174:175], v105 offset:13312\n"
    "v_mov_b32 v131, v7\n"
    "s_waitcnt lgkmcnt(14)\n"
    "v_mfma_f32_16x16x32_bf16 v[40:43], v[192:195], a[240:243], v[40:43]\n"
    "v_cvt_pk_bf16_f32 v128, v56, v57\n"
    "v_cvt_pk_bf16_f32 v130, v58, v59\n"
    "v_mfma_f32_16x16x32_bf16 v[48:51], v[192:195], a[244:247], v[48:51]\n"
    "v_add_u32 v104, 0x24000, v108\n"
    "v_xor_b32 v105, 32, v104\n"
    "s_waitcnt lgkmcnt(12)\n"
    "v_mfma_f32_16x16x32_bf16 v[44:47], v[196:199], a[240:243], v[44:47]\n"
    "ds_read_b64_tr_b16 v[176:177], v104 offset:1024\n"
    "ds_read_b64_tr_b16 v[178:179], v104 offset:5120\n"
    "v_mfma_f32_16x16x32_bf16 v[52:55], v[196:199], a[244:247], v[52:55]\n"
    "ds_read_b64_tr_b16 v[180:181], v105 offset:1024\n"
    "ds_read_b64_tr_b16 v[182:183], v105 offset:5120\n"
    "s_waitcnt lgkmcnt(14)\n"
    "v_mfma_f32_16x16x32_bf16 v[40:43], v[200:203], a[248:251], v[40:43]\n"
    "buffer_store_dwordx4 v[128:131], v2, s[8:11], s87 offen sc1\n"
    "v_mov_b32 v133, v7\n"
    "v_mfma_f32_16x16x32_bf16 v[48:51], v[200:203], a[252:255], v[48:51]\n"
    "ds_read_b64_tr_b16 v[184:185], v104 offset:9216\n"
    "ds_read_b64_tr_b16 v[186:187], v104 offset:13312\n"
    "s_waitcnt lgkmcnt(14)\n"
    "v_mfma_f32_16x16x32_bf16 v[44:47], v[204:207], a[248:251], v[44:47]\n"
    "ds_read_b64_tr_b16 v[188:189], v105 offset:9216\n"
    "v_mov_b32 v135, v7\n"
    "v_mfma_f32_16x16x32_bf16 v[52:55], v[204:207], a[252:255], v[52:55]\n"
    "ds_read_b64_tr_b16 v[190:191], v105 offset:13312\n"
    "v_cvt_pk_bf16_f32 v132, v60, v61\n"
    "s_waitcnt lgkmcnt(14)\n"
    "v_mfma_f32_16x16x32_bf16 v[40:43], v[160:163], a[0:3], v[40:43]\n"
    "v_cvt_pk_bf16_f32 v134, v62, v63\n"
    "v_mfma_f32_16x16x32_bf16 v[48:51], v[160:163], a[4:7], v[48:51]\n"
    "v_add_u32 v104, 0x14000, v108\n"
    "v_xor_b32 v106, 64, v104\n"
    "s_waitcnt lgkmcnt(12)\n"
    "v_mfma_f32_16x16x32_bf16 v[44:47], v[164:167], a[0:3], v[44:47]\n"
    "buffer_store_dwordx4 v[132:135], v2, s[8:11], s87 offen offset:1024 sc1\n"
    "v_xor_b32 v107, 0x60, v104\n"
    "v_mfma_f32_16x16x32_bf16 v[52:55], v[164:167], a[4:7], v[52:55]\n"
    "ds_read_b64_tr_b16 v[192:193], v106\n"
    "ds_read_b64_tr_b16 v[194:195], v106 offset:4096\n"
    "ds_read_b64_tr_b16 v[196:197], v107\n"
    "ds_read_b64_tr_b16 v[198:199], v107 offset:4096\n"
    "s_waitcnt lgkmcnt(14)\n"
    "v_mfma_f32_16x16x32_bf16 v[40:43], v[168:171], a[8:11], v[40:43]\n"
    "v_mov_b32 v137, v7\n"
    "v_mov_b32 v139, v7\n"
    "v_mfma_f32_16x16x32_bf16 v[48:51], v[168:171], a[12:15], v[48:51]\n"
    "ds_read_b64_tr_b16 v[200:201], v106 offset:8192\n"
    "ds_read_b64_tr_b16 v[202:203], v106 offset:12288\n"
    "s_waitcnt lgkmcnt(14)\n"
    "v_mfma_f32_16x16x32_bf16 v[44:47], v[172:175], a[8:11], v[44:47]\n"
    "ds_read_b64_tr_b16 v[204:205], v107 offset:8192\n"
    "v_cvt_pk_bf16_f32 v136, v64, v65\n"
    "v_mfma_f32_16x16x32_bf16 v[52:55], v[172:175], a[12:15], v[52:55]\n"
    "ds_read_b64_tr_b16 v[206:207], v107 offset:12288\n"
    "v_cvt_pk_bf16_f32 v138, v66, v67\n"
    "s_waitcnt lgkmcnt(14)\n"
    "v_mfma_f32_16x16x32_bf16 v[40:43], v[176:179], a[16:19], v[40:43]\n"
    "buffer_store_dwordx4 v[136:139], v2, s[8:11], s87 offen offset:2048 sc1\n"
    "v_mov_b32 v141, v7\n"
    "v_mfma_f32_16x16x32_bf16 v[48:51], v[176:179], a[20:23], v[48:51]\n"
    "v_add_u32 v104, 0x1c000, v108\n"
    "v_xor_b32 v106, 64, v104\n"
    "s_waitcnt lgkmcnt(12)\n"
    "v_mfma_f32_16x16x32_bf16 v[44:47], v[180:183], a[16:19], v[44:47]\n"
    "v_xor_b32 v107, 0x60, v104\n"
    "v_mov_b32 v143, v7\n"
    "v_mfma_f32_16x16x32_bf16 v[52:55], v[180:183], a[20:23], v[52:55]\n"
    "ds_read_b64_tr_b16 v[160:161], v106\n"
    "ds_read_b64_tr_b16 v[162:163], v106 offset:4096\n"
    "ds_read_b64_tr_b16 v[164:165], v107\n"
    "ds_read_b64_tr_b16 v[166:167], v107 offset:4096\n"
    "s_waitcnt lgkmcnt(14)\n"
    "v_mfma_f32_16x16x32_bf16 v[40:43], v[184:187], a[24:27], v[40:43]\n"
    "v_cvt_pk_bf16_f32 v140, v68, v69\n"
    "v_cvt_pk_bf16_f32 v142, v70, v71\n"
    "v_mfma_f32_16x16x32_bf16 v[48:51], v[184:187], a[28:31], v[48:51]\n"
    "ds_read_b64_tr_b16 v[168:169], v106 offset:8192\n"
    "ds_read_b64_tr_b16 v[170:171], v106 offset:12288\n"
    "s_waitcnt lgkmcnt(14)\n"
    "v_mfma_f32_16x16x32_bf16 v[44:47], v[188:191], a[24:27], v[44:47]\n"
    "ds_read_b64_tr_b16 v[172:173], v107 offset:8192\n"
    "ds_read_b64_tr_b16 v[174:175], v107 offset:12288\n"
    "v_mfma_f32_16x16x32_bf16 v[52:55], v[188:191], a[28:31], v[52:55]\n"
    "buffer_store_dwordx4 v[140:143], v2, s[8:11], s87 offen offset:3072 sc1\n"
    "s_waitcnt lgkmcnt(14)\n"
    "v_mfma_f32_16x16x32_bf16 v[24:27], v[192:195], a[224:227], v[24:27]\n"
    "v_mfma_f32_16x16x32_bf16 v[32:35], v[192:195], a[228:231], v[32:35]\n"
    "v_add_u32 v104, 0x20000, v108\n"
    "v_xor_b32 v106, 64, v104\n"
    "s_waitcnt lgkmcnt(12)\n"
    "v_mfma_f32_16x16x32_bf16 v[28:31], v[196:199], a[224:227], v[28:31]\n"
    "v_xor_b32 v107, 0x60, v104\n"
    "v_mfma_f32_16x16x32_bf16 v[36:39], v[196:199], a[228:231], v[36:39]\n"
    "ds_read_b64_tr_b16 v[176:177], v106\n"
    "ds_read_b64_tr_b16 v[178:179], v106 offset:4096\n"
    "ds_read_b64_tr_b16 v[180:181], v107\n"
    "ds_read_b64_tr_b16 v[182:183], v107 offset:4096\n"
    "s_waitcnt lgkmcnt(14)\n"
    "v_mfma_f32_16x16x32_bf16 v[24:27], v[200:203], a[232:235], v[24:27]\n"
    "v_mfma_f32_16x16x32_bf16 v[32:35], v[200:203], a[236:239], v[32:35]\n"
    "ds_read_b64_tr_b16 v[184:185], v106 offset:8192\n"
    "ds_read_b64_tr_b16 v[186:187], v106 offset:12288\n"
    "s_waitcnt lgkmcnt(14)\n"
    "v_mfma_f32_16x16x32_bf16 v[28:31], v[204:207], a[232:235], v[28:31]\n"
    "ds_read_b64_tr_b16 v[188:189], v107 offset:8192\n"
    "v_mov_b32 v89, v7\n"
    "v_mfma_f32_16x16x32_bf16 v[36:39], v[204:207], a[236:239], v[36:39]\n"
    "ds_read_b64_tr_b16 v[190:191], v107 offset:12288\n"
    "v_mov_b32 v91, v7\n"
    "s_waitcnt lgkmcnt(14)\n"
    "v_mfma_f32_16x16x32_bf16 v[24:27], v[160:163], a[240:243], v[24:27]\n"
    "v_cvt_pk_bf16_f32 v88, v40, v41\n"
    "v_cvt_pk_bf16_f32 v90, v42, v43\n"
    "v_mfma_f32_16x16x32_bf16 v[32:35], v[160:163], a[244:247], v[32:35]\n"
    "v_add_u32 v104, 0x24000, v108\n"
    "v_xor_b32 v106, 64, v104\n"
    "s_waitcnt lgkmcnt(12)\n"
    "v_mfma_f32_16x16x32_bf16 v[28:31], v[164:167], a[240:243], v[28:31]\n"
    "buffer_store_dwordx4 v[88:91], v2, s[8:11], s86 offen sc1\n"
    "v_xor_b32 v107, 0x60, v104\n"
    "v_mfma_f32_16x16x32_bf16 v[36:39], v[164:167], a[244:247], v[36:39]\n"
    "ds_read_b64_tr_b16 v[192:193], v106\n"
    "ds_read_b64_tr_b16 v[194:195], v106 offset:4096\n"
    "ds_read_b64_tr_b16 v[196:197], v107\n"
    "ds_read_b64_tr_b16 v[198:199], v107 offset:4096\n"
    "s_waitcnt lgkmcnt(14)\n"
    "v_mfma_f32_16x16x32_bf16 v[24:27], v[168:171], a[248:251], v[24:27]\n"
    "v_mov_b32 v93, v7\n"
    "v_mov_b32 v95, v7\n"
    "v_mfma_f32_16x16x32_bf16 v[32:35], v[168:171], a[252:255], v[32:35]\n"
    "ds_read_b64_tr_b16 v[200:201], v106 offset:8192\n"
    "ds_read_b64_tr_b16 v[202:203], v106 offset:12288\n"
    "s_waitcnt lgkmcnt(14)\n"
    "v_mfma_f32_16x16x32_bf16 v[28:31], v[172:175], a[248:251], v[28:31]\n"
    "ds_read_b64_tr_b16 v[204:205], v107 offset:8192\n"
    "v_cvt_pk_bf16_f32 v92, v44, v45\n"
    "v_mfma_f32_16x16x32_bf16 v[36:39], v[172:175], a[252:255], v[36:39]\n"
    "ds_read_b64_tr_b16 v[206:207], v107 offset:12288\n"
    "v_cvt_pk_bf16_f32 v94, v46, v47\n"
    "s_waitcnt lgkmcnt(14)\n"
    "v_mfma_f32_16x16x32_bf16 v[24:27], v[176:179], a[0:3], v[24:27]\n"
    "buffer_store_dwordx4 v[92:95], v2, s[8:11], s86 offen offset:1024 sc1\n"
    "v_mov_b32 v97, v7\n"
    "v_mfma_f32_16x16x32_bf16 v[32:35], v[176:179], a[4:7], v[32:35]\n"
    "v_add_u32 v104, 0, v108\n"
    "v_xor_b32 v105, 32, v104\n"
    "s_waitcnt lgkmcnt(12)\n"
    "v_mfma_f32_16x16x32_bf16 v[28:31], v[180:183], a[0:3], v[28:31]\n"
    "ds_read_b64_tr_b16 v[160:161], v104\n"
    "ds_read_b64_tr_b16 v[162:163], v104 offset:4096\n"
    "v_mfma_f32_16x16x32_bf16 v[36:39], v[180:183], a[4:7], v[36:39]\n"
    "ds_read_b64_tr_b16 v[164:165], v105\n"
    "ds_read_b64_tr_b16 v[166:167], v105 offset:4096\n"
    "s_waitcnt lgkmcnt(14)\n"
    "v_mfma_f32_16x16x32_bf16 v[24:27], v[184:187], a[8:11], v[24:27]\n"
    "v_mov_b32 v99, v7\n"
    "v_cvt_pk_bf16_f32 v96, v48, v49\n"
    "v_mfma_f32_16x16x32_bf16 v[32:35], v[184:187], a[12:15], v[32:35]\n"
    "ds_read_b64_tr_b16 v[168:169], v104 offset:8192\n"
    "ds_read_b64_tr_b16 v[170:171], v104 offset:12288\n"
    "s_waitcnt lgkmcnt(14)\n"
    "v_mfma_f32_16x16x32_bf16 v[28:31], v[188:191], a[8:11], v[28:31]\n"
    "ds_read_b64_tr_b16 v[172:173], v105 offset:8192\n"
    "ds_read_b64_tr_b16 v[174:175], v105 offset:12288\n"
    "v_mfma_f32_16x16x32_bf16 v[36:39], v[188:191], a[12:15], v[36:39]\n"
    "v_cvt_pk_bf16_f32 v98, v50, v51\n"
    "s_waitcnt lgkmcnt(14)\n"
    "v_mfma_f32_16x16x32_bf16 v[24:27], v[192:195], a[16:19], v[24:27]\n"
    "buffer_store_dwordx4 v[96:99], v2, s[8:11], s86 offen offset:2048 sc1\n"
    "v_mov_b32 v101, v7\n"
    "v_mfma_f32_16x16x32_bf16 v[32:35], v[192:195], a[20:23], v[32:35]\n"
    "v_add_u32 v104, 0x8000, v108\n"
    "v_xor_b32 v105, 32, v104\n"
    "s_waitcnt lgkmcnt(12)\n"
    "v_mfma_f32_16x16x32_bf16 v[28:31], v[196:199], a[16:19], v[28:31]\n"
    "ds_read_b64_tr_b16 v[176:177], v104\n"
    "ds_read_b64_tr_b16 v[178:179], v104 offset:4096\n"
    "v_mfma_f32_16x16x32_bf16 v[36:39], v[196:199], a[20:23], v[36:39]\n"
    "ds_read_b64_tr_b16 v[180:181], v105\n"
    "ds_read_b64_tr_b16 v[182:183], v105 offset:4096\n"
    "s_waitcnt lgkmcnt(14)\n"
    "v_mfma_f32_16x16x32_bf16 v[24:27], v[200:203], a[24:27], v[24:27]\n"
    "v_mov_b32 v103, v7\n"
    "v_cvt_pk_bf16_f32 v100, v52, v53\n"
    "v_mfma_f32_16x16x32_bf16 v[32:35], v[200:203], a[28:31], v[32:35]\n"
    "ds_read_b64_tr_b16 v[184:185], v104 offset:8192\n"
    "ds_read_b64_tr_b16 v[186:187], v104 offset:12288\n"
    "s_waitcnt lgkmcnt(14)\n"
    "v_mfma_f32_16x16x32_bf16 v[28:31], v[204:207], a[24:27], v[28:31]\n"
    "ds_read_b64_tr_b16 v[188:189], v105 offset:8192\n"
    "ds_read_b64_tr_b16 v[190:191], v105 offset:12288\n"
    "v_mfma_f32_16x16x32_bf16 v[36:39], v[204:207], a[28:31], v[36:39]\n"
    "v_cvt_pk_bf16_f32 v102, v54, v55\n"
    "s_waitcnt lgkmcnt(14)\n"
    "v_mfma_f32_16x16x32_bf16 v[8:11], v[160:163], a[128:131], v[8:11]\n"
    "buffer_store_dwordx4 v[100:103], v2, s[8:11], s86 offen offset:3072 sc1\n"
    "v_mfma_f32_16x16x32_bf16 v[16:19], v[160:163], a[132:135], v[16:19]\n"
    "v_add_u32 v104, 0x10000, v108\n"
    "v_xor_b32 v105, 32, v104\n"
    "s_waitcnt lgkmcnt(12)\n"
    "v_mfma_f32_16x16x32_bf16 v[12:15], v[164:167], a[128:131], v[12:15]\n"
    "ds_read_b64_tr_b16 v[192:193], v104\n"
    "ds_read_b64_tr_b16 v[194:195], v104 offset:4096\n"
    "v_mfma_f32_16x16x32_bf16 v[20:23], v[164:167], a[132:135], v[20:23]\n"
    "ds_read_b64_tr_b16 v[196:197], v105\n"
    "ds_read_b64_tr_b16 v[198:199], v105 offset:4096\n"
    "s_waitcnt lgkmcnt(14)\n"
    "v_mfma_f32_16x16x32_bf16 v[8:11], v[168:171], a[136:139], v[8:11]\n"
    "v_mfma_f32_16x16x32_bf16 v[16:19], v[168:171], a[140:143], v[16:19]\n"
    "ds_read_b64_tr_b16 v[200:201], v104 offset:8192\n"
    "ds_read_b64_tr_b16 v[202:203], v104 offset:12288\n"
    "s_waitcnt lgkmcnt(14)\n"
    "v_mfma_f32_16x16x32_bf16 v[12:15], v[172:175], a[136:139], v[12:15]\n"
    "ds_read_b64_tr_b16 v[204:205], v105 offset:8192\n"
    "v_mov_b32 v73, v7\n"
    "v_mfma_f32_16x16x32_bf16 v[20:23], v[172:175], a[140:143], v[20:23]\n"
    "ds_read_b64_tr_b16 v[206:207], v105 offset:12288\n"
    "v_mov_b32 v75, v7\n"
    "s_waitcnt lgkmcnt(14)\n"
    "v_mfma_f32_16x16x32_bf16 v[8:11], v[176:179], a[144:147], v[8:11]\n"
    "v_cvt_pk_bf16_f32 v72, v24, v25\n"
    "v_cvt_pk_bf16_f32 v74, v26, v27\n"
    "v_mfma_f32_16x16x32_bf16 v[16:19], v[176:179], a[148:151], v[16:19]\n"
    "v_add_u32 v104, 0x18000, v108\n"
    "v_xor_b32 v105, 32, v104\n"
    "s_waitcnt lgkmcnt(12)\n"
    "v_mfma_f32_16x16x32_bf16 v[12:15], v[180:183], a[144:147], v[12:15]\n"
    "ds_read_b64_tr_b16 v[160:161], v104\n"
    "ds_read_b64_tr_b16 v[162:163], v104 offset:4096\n"
    "v_mfma_f32_16x16x32_bf16 v[20:23], v[180:183], a[148:151], v[20:23]\n"
    "ds_read_b64_tr_b16 v[164:165], v105\n"
    "ds_read_b64_tr_b16 v[166:167], v105 offset:4096\n"
    "s_waitcnt lgkmcnt(14)\n"
    "v_mfma_f32_16x16x32_bf16 v[8:11], v[184:187], a[152:155], v[8:11]\n"
    "buffer_store_dwordx4 v[72:75], v2, s[8:11], s85 offen sc1\n"
    "v_mov_b32 v77, v7\n"
    "v_mfma_f32_16x16x32_bf16 v[16:19], v[184:187], a[156:159], v[16:19]\n"
    "ds_read_b64_tr_b16 v[168:169], v104 offset:8192\n"
    "ds_read_b64_tr_b16 v[170:171], v104 offset:12288\n"
    "s_waitcnt lgkmcnt(14)\n"
    "v_mfma_f32_16x16x32_bf16 v[12:15], v[188:191], a[152:155], v[12:15]\n"
    "ds_read_b64_tr_b16 v[172:173], v105 offset:8192\n"
    "ds_read_b64_tr_b16 v[174:175], v105 offset:12288\n"
    "v_mfma_f32_16x16x32_bf16 v[20:23], v[188:191], a[156:159], v[20:23]\n"
    "v_mov_b32 v79, v7\n"
    "v_cvt_pk_bf16_f32 v76, v28, v29\n"
    "s_waitcnt lgkmcnt(14)\n"
    "v_mfma_f32_16x16x32_bf16 v[8:11], v[192:195], a[160:163], v[8:11]\n"
    "v_cvt_pk_bf16_f32 v78, v30, v31\n"
    "v_mfma_f32_16x16x32_bf16 v[16:19], v[192:195], a[164:167], v[16:19]\n"
    "v_add_u32 v104, 0x4000, v108\n"
    "v_xor_b32 v105, 32, v104\n"
    "s_waitcnt lgkmcnt(12)\n"
    "v_mfma_f32_16x16x32_bf16 v[12:15], v[196:199], a[160:163], v[12:15]\n"
    "ds_read_b64_tr_b16 v[176:177], v104\n"
    "ds_read_b64_tr_b16 v[178:179], v104 offset:4096\n"
    "v_mfma_f32_16x16x32_bf16 v[20:23], v[196:199], a[164:167], v[20:23]\n"
    "ds_read_b64_tr_b16 v[180:181], v105\n"
    "ds_read_b64_tr_b16 v[182:183], v105 offset:4096\n"
    "s_waitcnt lgkmcnt(14)\n"
    "v_mfma_f32_16x16x32_bf16 v[8:11], v[200:203], a[168:171], v[8:11]\n"
    "buffer_store_dwordx4 v[76:79], v2, s[8:11], s85 offen offset:1024 sc1\n"
    "v_mov_b32 v81, v7\n"
    "v_mfma_f32_16x16x32_bf16 v[16:19], v[200:203], a[172:175], v[16:19]\n"
    "ds_read_b64_tr_b16 v[184:185], v104 offset:8192\n"
    "ds_read_b64_tr_b16 v[186:187], v104 offset:12288\n"
    "s_waitcnt lgkmcnt(14)\n"
    "v_mfma_f32_16x16x32_bf16 v[12:15], v[204:207], a[168:171], v[12:15]\n"
    "ds_read_b64_tr_b16 v[188:189], v105 offset:8192\n"
    "ds_read_b64_tr_b16 v[190:191], v105 offset:12288\n"
    "v_mfma_f32_16x16x32_bf16 v[20:23], v[204:207], a[172:175], v[20:23]\n"
    "v_mov_b32 v83, v7\n"
    "v_cvt_pk_bf16_f32 v80, v32, v33\n"
    "s_waitcnt lgkmcnt(14)\n"
    "v_mfma_f32_16x16x32_bf16 v[8:11], v[160:163], a[176:179], v[8:11]\n"
    "v_cvt_pk_bf16_f32 v82, v34, v35\n"
    "v_mfma_f32_16x16x32_bf16 v[16:19], v[160:163], a[180:183], v[16:19]\n"
    "v_add_u32 v104, 0xc000, v108\n"
    "v_xor_b32 v105, 32, v104\n"
    "s_waitcnt lgkmcnt(12)\n"
    "v_mfma_f32_16x16x32_bf16 v[12:15], v[164:167], a[176:179], v[12:15]\n"
    "ds_read_b64_tr_b16 v[192:193], v104\n"
    "ds_read_b64_tr_b16 v[194:195], v104 offset:4096\n"
    "v_mfma_f32_16x16x32_bf16 v[20:23], v[164:167], a[180:183], v[20:23]\n"
    "ds_read_b64_tr_b16 v[196:197], v105\n"
    "ds_read_b64_tr_b16 v[198:199], v105 offset:4096\n"
    "s_waitcnt lgkmcnt(14)\n"
    "v_mfma_f32_16x16x32_bf16 v[8:11], v[168:171], a[184:187], v[8:11]\n"
    "buffer_store_dwordx4 v[80:83], v2, s[8:11], s85 offen offset:2048 sc1\n"
    "v_mov_b32 v85, v7\n"
    "v_mfma_f32_16x16x32_bf16 v[16:19], v[168:171], a[188:191], v[16:19]\n"
    "ds_read_b64_tr_b16 v[200:201], v104 offset:8192\n"
    "ds_read_b64_tr_b16 v[202:203], v104 offset:12288\n"
    "s_waitcnt lgkmcnt(14)\n"
    "v_mfma_f32_16x16x32_bf16 v[12:15], v[172:175], a[184:187], v[12:15]\n"
    "ds_read_b64_tr_b16 v[204:205], v105 offset:8192\n"
    "ds_read_b64_tr_b16 v[206:207], v105 offset:12288\n"
    "v_mfma_f32_16x16x32_bf16 v[20:23], v[172:175], a[188:191], v[20:23]\n"
    "v_mov_b32 v87, v7\n"
    "v_cvt_pk_bf16_f32 v84, v36, v37\n"
    "s_waitcnt lgkmcnt(14)\n"
    "v_mfma_f32_16x16x32_bf16 v[8:11], v[176:179], a[192:195], v[8:11]\n"
    "v_cvt_pk_bf16_f32 v86, v38, v39\n"
    "v_mfma_f32_16x16x32_bf16 v[16:19], v[176:179], a[196:199], v[16:19]\n"
    "v_add_u32 v104, 0x14000, v108\n"
    "v_xor_b32 v105, 32, v104\n"
    "s_waitcnt lgkmcnt(12)\n"
    "v_mfma_f32_16x16x32_bf16 v[12:15], v[180:183], a[192:195], v[12:15]\n"
    "ds_read_b64_tr_b16 v[160:161], v104\n"
    "ds_read_b64_tr_b16 v[162:163], v104 offset:4096\n"
    "v_mfma_f32_16x16x32_bf16 v[20:23], v[180:183], a[196:199], v[20:23]\n"
    "ds_read_b64_tr_b16 v[164:165], v105\n"
    "ds_read_b64_tr_b16 v[166:167], v105 offset:4096\n"
    "s_waitcnt lgkmcnt(14)\n"
    "v_mfma_f32_16x16x32_bf16 v[8:11], v[184:187], a[200:203], v[8:11]\n"
    "buffer_store_dwordx4 v[84:87], v2, s[8:11], s85 offen offset:3072 sc1\n"
    "v_mfma_f32_16x16x32_bf16 v[16:19], v[184:187], a[204:207], v[16:19]\n"
    "ds_read_b64_tr_b16 v[168:169], v104 offset:8192\n"
    "ds_read_b64_tr_b16 v[170:171], v104 offset:12288\n"
    "s_waitcnt lgkmcnt(14)\n"
    "v_mfma_f32_16x16x32_bf16 v[12:15], v[188:191], a[200:203], v[12:15]\n"
    "ds_read_b64_tr_b16 v[172:173], v105 offset:8192\n"
    "ds_read_b64_tr_b16 v[174:175], v105 offset:12288\n"
    "v_mfma_f32_16x16x32_bf16 v[20:23], v[188:191], a[204:207], v[20:23]\n"
    "s_waitcnt lgkmcnt(14)\n"
    "v_mfma_f32_16x16x32_bf16 v[8:11], v[192:195], a[208:211], v[8:11]\n"
    "v_mfma_f32_16x16x32_bf16 v[16:19], v[192:195], a[212:215], v[16:19]\n"
    "v_add_u32 v104, 0x1c000, v108\n"
    "v_xor_b32 v105, 32, v104\n"
    "s_waitcnt lgkmcnt(12)\n"
    "v_mfma_f32_16x16x32_bf16 v[12:15], v[196:199], a[208:211], v[12:15]\n"
    "ds_read_b64_tr_b16 v[176:177], v104\n"
    "ds_read_b64_tr_b16 v[178:179], v104 offset:4096\n"
    "v_mfma_f32_16x16x32_bf16 v[20:23], v[196:199], a[212:215], v[20:23]\n"
    "ds_read_b64_tr_b16 v[180:181], v105\n"
    "ds_read_b64_tr_b16 v[182:183], v105 offset:4096\n"
    "s_waitcnt lgkmcnt(14)\n"
    "v_mfma_f32_16x16x32_bf16 v[8:11], v[200:203], a[216:219], v[8:11]\n"
    "v_mfma_f32_16x16x32_bf16 v[16:19], v[200:203], a[220:223], v[16:19]\n"
    "ds_read_b64_tr_b16 v[184:185], v104 offset:8192\n"
    "ds_read_b64_tr_b16 v[186:187], v104 offset:12288\n"
    "s_waitcnt lgkmcnt(14)\n"
    "v_mfma_f32_16x16x32_bf16 v[12:15], v[204:207], a[216:219], v[12:15]\n"
    "ds_read_b64_tr_b16 v[188:189], v105 offset:8192\n"
    "ds_read_b64_tr_b16 v[190:191], v105 offset:12288\n"
    "v_mfma_f32_16x16x32_bf16 v[20:23], v[204:207], a[220:223], v[20:23]\n"
    "s_waitcnt lgkmcnt(14)\n"
    "v_mfma_f32_16x16x32_bf16 v[8:11], v[160:163], a[224:227], v[8:11]\n"
    "v_mfma_f32_16x16x32_bf16 v[16:19], v[160:163], a[228:231], v[16:19]\n"
    "v_add_u32 v104, 0x20000, v108\n"
    "v_xor_b32 v105, 32, v104\n"
    "s_waitcnt lgkmcnt(12)\n"
    "v_mfma_f32_16x16x32_bf16 v[12:15], v[164:167], a[224:227], v[12:15]\n"
    "ds_read_b64_tr_b16 v[192:193], v104\n"
    "ds_read_b64_tr_b16 v[194:195], v104 offset:4096\n"
    "v_mfma_f32_16x16x32_bf16 v[20:23], v[164:167], a[228:231], v[20:23]\n"
    "ds_read_b64_tr_b16 v[196:197], v105\n"
    "ds_read_b64_tr_b16 v[198:199], v105 offset:4096\n"
    "s_waitcnt lgkmcnt(14)\n"
    "v_mfma_f32_16x16x32_bf16 v[8:11], v[168:171], a[232:235], v[8:11]\n"
    "v_mfma_f32_16x16x32_bf16 v[16:19], v[168:171], a[236:239], v[16:19]\n"
    "ds_read_b64_tr_b16 v[200:201], v104 offset:8192\n"
    "ds_read_b64_tr_b16 v[202:203], v104 offset:12288\n"
    "s_waitcnt lgkmcnt(14)\n"
    "v_mfma_f32_16x16x32_bf16 v[12:15], v[172:175], a[232:235], v[12:15]\n"
    "ds_read_b64_tr_b16 v[204:205], v105 offset:8192\n"
    "ds_read_b64_tr_b16 v[206:207], v105 offset:12288\n"
    "v_mfma_f32_16x16x32_bf16 v[20:23], v[172:175], a[236:239], v[20:23]\n"
    "s_waitcnt lgkmcnt(14)\n"
    "v_mfma_f32_16x16x32_bf16 v[8:11], v[176:179], a[240:243], v[8:11]\n"
    "v_mfma_f32_16x16x32_bf16 v[16:19], v[176:179], a[244:247], v[16:19]\n"
    "v_add_u32 v104, 0x24000, v108\n"
    "v_xor_b32 v105, 32, v104\n"
    "s_waitcnt lgkmcnt(12)\n"
    "v_mfma_f32_16x16x32_bf16 v[12:15], v[180:183], a[240:243], v[12:15]\n"
    "ds_read_b64_tr_b16 v[160:161], v104\n"
    "ds_read_b64_tr_b16 v[162:163], v104 offset:4096\n"
    "v_mfma_f32_16x16x32_bf16 v[20:23], v[180:183], a[244:247], v[20:23]\n"
    "ds_read_b64_tr_b16 v[164:165], v105\n"
    "ds_read_b64_tr_b16 v[166:167], v105 offset:4096\n"
    "s_waitcnt lgkmcnt(14)\n"
    "v_mfma_f32_16x16x32_bf16 v[8:11], v[184:187], a[248:251], v[8:11]\n"
    "v_mfma_f32_16x16x32_bf16 v[16:19], v[184:187], a[252:255], v[16:19]\n"
    "ds_read_b64_tr_b16 v[168:169], v104 offset:8192\n"
    "ds_read_b64_tr_b16 v[170:171], v104 offset:12288\n"
    "s_waitcnt lgkmcnt(14)\n"
    "v_mfma_f32_16x16x32_bf16 v[12:15], v[188:191], a[248:251], v[12:15]\n"
    "ds_read_b64_tr_b16 v[172:173], v105 offset:8192\n"
    "ds_read_b64_tr_b16 v[174:175], v105 offset:12288\n"
    "v_mfma_f32_16x16x32_bf16 v[20:23], v[188:191], a[252:255], v[20:23]\n"
    "s_waitcnt lgkmcnt(14)\n"
    "v_mfma_f32_16x16x32_bf16 v[8:11], v[192:195], a[0:3], v[8:11]\n"
    "v_mfma_f32_16x16x32_bf16 v[16:19], v[192:195], a[4:7], v[16:19]\n"
    "s_waitcnt lgkmcnt(12)\n"
    "v_mfma_f32_16x16x32_bf16 v[12:15], v[196:199], a[0:3], v[12:15]\n"
    "v_mfma_f32_16x16x32_bf16 v[20:23], v[196:199], a[4:7], v[20:23]\n"
    "s_waitcnt lgkmcnt(10)\n"
    "v_mfma_f32_16x16x32_bf16 v[8:11], v[200:203], a[8:11], v[8:11]\n"
    "v_mfma_f32_16x16x32_bf16 v[16:19], v[200:203], a[12:15], v[16:19]\n"
    "s_waitcnt lgkmcnt(8)\n"
    "v_mfma_f32_16x16x32_bf16 v[12:15], v[204:207], a[8:11], v[12:15]\n"
    "v_mfma_f32_16x16x32_bf16 v[20:23], v[204:207], a[12:15], v[20:23]\n"
    "s_waitcnt lgkmcnt(6)\n"
    "v_mfma_f32_16x16x32_bf16 v[8:11], v[160:163], a[16:19], v[8:11]\n"
    "v_mfma_f32_16x16x32_bf16 v[16:19], v[160:163], a[20:23], v[16:19]\n"
    "s_waitcnt lgkmcnt(4)\n"
    "v_mfma_f32_16x16x32_bf16 v[12:15], v[164:167], a[16:19], v[12:15]\n"
    "v_mfma_f32_16x16x32_bf16 v[20:23], v[164:167], a[20:23], v[20:23]\n"
    "s_waitcnt lgkmcnt(2)\n"
    "v_mfma_f32_16x16x32_bf16 v[8:11], v[168:171], a[24:27], v[8:11]\n"
    "v_mfma_f32_16x16x32_bf16 v[16:19], v[168:171], a[28:31], v[16:19]\n"
    "s_waitcnt lgkmcnt(0)\n"
    "v_mfma_f32_16x16x32_bf16 v[12:15], v[172:175], a[24:27], v[12:15]\n"
    "v_mfma_f32_16x16x32_bf16 v[20:23], v[172:175], a[28:31], v[20:23]\n"
    "buffer_load_dwordx4 v[240:243], v2, s[8:11], s94 offen sc1\n"
    "buffer_load_dwordx4 v[244:247], v2, s[8:11], s94 offen offset:1024 sc1\n"
    "buffer_load_dwordx4 v[248:251], v2, s[8:11], s94 offen offset:2048 sc1\n"
    "buffer_load_dwordx4 v[252:255], v2, s[8:11], s94 offen offset:3072 sc1\n"
    "buffer_load_dwordx4 v[224:227], v2, s[8:11], s93 offen sc1\n"
    "buffer_load_dwordx4 v[228:231], v2, s[8:11], s93 offen offset:1024 sc1\n"
    "buffer_load_dwordx4 v[232:235], v2, s[8:11], s93 offen offset:2048 sc1\n"
    "buffer_load_dwordx4 v[236:239], v2, s[8:11], s93 offen offset:3072 sc1\n"
    "buffer_load_dwordx4 v[208:211], v2, s[8:11], s92 offen sc1\n"
    "buffer_load_dwordx4 v[212:215], v2, s[8:11], s92 offen offset:1024 sc1\n"
    "buffer_load_dwordx4 v[216:219], v2, s[8:11], s92 offen offset:2048 sc1\n"
    "buffer_load_dwordx4 v[220:223], v2, s[8:11], s92 offen offset:3072 sc1\n"
    "v_pk_add_f32 v[8:9], v[8:9], v[144:145]\n"
    "v_pk_add_f32 v[10:11], v[10:11], v[146:147]\n"
    "v_pk_add_f32 v[12:13], v[12:13], v[148:149]\n"
    "v_pk_add_f32 v[14:15], v[14:15], v[150:151]\n"
    "v_pk_add_f32 v[16:17], v[16:17], v[152:153]\n"
    "v_pk_add_f32 v[18:19], v[18:19], v[154:155]\n"
    "v_pk_add_f32 v[20:21], v[20:21], v[156:157]\n"
    "v_pk_add_f32 v[22:23], v[22:23], v[158:159]\n"
    "s_mov_b32 s61, 0\n"
    "s_waitcnt vmcnt(0)\n"
    "Lpx:\n"
    "v_xor_b32 v24, v209, v7\n"
    "v_xor_b32 v25, v211, v7\n"
    "v_xor_b32 v26, v213, v7\n"
    "v_xor_b32 v27, v215, v7\n"
    "v_xor_b32 v28, v217, v7\n"
    "v_xor_b32 v29, v219, v7\n"
    "v_xor_b32 v30, v221, v7\n"
    "v_xor_b32 v31, v223, v7\n"
    "v_xor_b32 v32, v225, v7\n"
    "v_xor_b32 v33, v227, v7\n"
    "v_xor_b32 v34, v229, v7\n"
    "v_xor_b32 v35, v231, v7\n"
    "v_xor_b32 v36, v233, v7\n"
    "v_xor_b32 v37, v235, v7\n"
    "v_xor_b32 v38, v237, v7\n"
    "v_xor_b32 v39, v239, v7\n"
    "v_xor_b32 v40, v241, v7\n"
    "v_xor_b32 v41, v243, v7\n"
    "v_xor_b32 v42, v245, v7\n"
    "v_xor_b32 v43, v247, v7\n"
    "v_xor_b32 v44, v249, v7\n"
    "v_xor_b32 v45, v251, v7\n"
    "v_xor_b32 v46, v253, v7\n"
    "v_xor_b32 v47, v255, v7\n"
    "v_or3_b32 v3, v24, v25, v26\n"
    "v_or3_b32 v3, v3, v27, v28\n"
    "v_or3_b32 v3, v3, v29, v30\n"
    "v_or3_b32 v3, v3, v31, v32\n"
    "v_or3_b32 v3, v3, v33, v34\n"
    "v_or3_b32 v3, v3, v35, v36\n"
    "v_or3_b32 v3, v3, v37, v38\n"
    "v_or3_b32 v3, v3, v39, v40\n"
    "v_or3_b32 v3, v3, v41, v42\n"
    "v_or3_b32 v3, v3, v43, v44\n"
    "v_or3_b32 v3, v3, v45, v46\n"
    "v_or_b32 v3, v3, v47\n"
    "v_cmp_ne_u32 vcc, 0, v3\n"
    "s_nop 0\n"
    "s_cbranch_vccnz Lpxr\n"
    "Lpxd:\n"
    "v_and_b32 v209, 0xffff0000, v208\n"
    "v_lshlrev_b32 v208, 16, v208\n"
    "v_and_b32 v211, 0xffff0000, v210\n"
    "v_lshlrev_b32 v210, 16, v210\n"
    "v_and_b32 v213, 0xffff0000, v212\n"
    "v_lshlrev_b32 v212, 16, v212\n"
    "v_and_b32 v215, 0xffff0000, v214\n"
    "v_lshlrev_b32 v214, 16, v214\n"
    "v_and_b32 v217, 0xffff0000, v216\n"
    "v_lshlrev_b32 v216, 16, v216\n"
    "v_and_b32 v219, 0xffff0000, v218\n"
    "v_lshlrev_b32 v218, 16, v218\n"
    "v_and_b32 v221, 0xffff0000, v220\n"
    "v_lshlrev_b32 v220, 16, v220\n"
    "v_and_b32 v223, 0xffff0000, v222\n"
    "v_lshlrev_b32 v222, 16, v222\n"
    "v_pk_add_f32 v[8:9], v[8:9], v[208:209]\n"
    "v_pk_add_f32 v[10:11], v[10:11], v[210:211]\n"
    "v_pk_add_f32 v[12:13], v[12:13], v[212:213]\n"
    "v_pk_add_f32 v[14:15], v[14:15], v[214:215]\n"
    "v_pk_add_f32 v[16:17], v[16:17], v[216:217]\n"
    "v_pk_add_f32 v[18:19], v[18:19], v[218:219]\n"
    "v_pk_add_f32 v[20:21], v[20:21], v[220:221]\n"
    "v_pk_add_f32 v[22:23], v[22:23], v[222:223]\n"
    "v_and_b32 v225, 0xffff0000, v224\n"
    "v_lshlrev_b32 v224, 16, v224\n"
    "v_and_b32 v227, 0xffff0000, v226\n"
    "v_lshlrev_b32 v226, 16, v226\n"
    "v_and_b32 v229, 0xffff0000, v228\n"
    "v_lshlrev_b32 v228, 16, v228\n"
    "v_and_b32 v231, 0xffff0000, v230\n"
    "v_lshlrev_b32 v230, 16, v230\n"
    "v_and_b32 v233, 0xffff0000, v232\n"
    "v_lshlrev_b32 v232, 16, v232\n"
    "v_and_b32 v235, 0xffff0000, v234\n"
    "v_lshlrev_b32 v234, 16, v234\n"
    "v_and_b32 v237, 0xffff0000, v236\n"
    "v_lshlrev_b32 v236, 16, v236\n"
    "v_and_b32 v239, 0xffff0000, v238\n"
    "v_lshlrev_b32 v238, 16, v238\n"
    "v_pk_add_f32 v[8:9], v[8:9], v[224:225]\n"
    "v_pk_add_f32 v[10:11], v[10:11], v[226:227]\n"
    "v_pk_add_f32 v[12:13], v[12:13], v[228:229]\n"
    "v_pk_add_f32 v[14:15], v[14:15], v[230:231]\n"
    "v_pk_add_f32 v[16:17], v[16:17], v[232:233]\n"
    "v_pk_add_f32 v[18:19], v[18:19], v[234:235]\n"
    "v_pk_add_f32 v[20:21], v[20:21], v[236:237]\n"
    "v_pk_add_f32 v[22:23], v[22:23], v[238:239]\n"
    "v_and_b32 v241, 0xffff0000, v240\n"
    "v_lshlrev_b32 v240, 16, v240\n"
    "v_and_b32 v243, 0xffff0000, v242\n"
    "v_lshlrev_b32 v242, 16, v242\n"
    "v_and_b32 v245, 0xffff0000, v244\n"
    "v_lshlrev_b32 v244, 16, v244\n"
    "v_and_b32 v247, 0xffff0000, v246\n"
    "v_lshlrev_b32 v246, 16, v246\n"
    "v_and_b32 v249, 0xffff0000, v248\n"
    "v_lshlrev_b32 v248, 16, v248\n"
    "v_and_b32 v251, 0xffff0000, v250\n"
    "v_lshlrev_b32 v250, 16, v250\n"
    "v_and_b32 v253, 0xffff0000, v252\n"
    "v_lshlrev_b32 v252, 16, v252\n"
    "v_and_b32 v255, 0xffff0000, v254\n"
    "v_lshlrev_b32 v254, 16, v254\n"
    "v_pk_add_f32 v[8:9], v[8:9], v[240:241]\n"
    "v_pk_add_f32 v[10:11], v[10:11], v[242:243]\n"
    "v_pk_add_f32 v[12:13], v[12:13], v[244:245]\n"
    "v_pk_add_f32 v[14:15], v[14:15], v[246:247]\n"
    "v_pk_add_f32 v[16:17], v[16:17], v[248:249]\n"
    "v_pk_add_f32 v[18:19], v[18:19], v[250:251]\n"
    "v_pk_add_f32 v[20:21], v[20:21], v[252:253]\n"
    "v_pk_add_f32 v[22:23], v[22:23], v[254:255]\n"
    "v_cvt_pk_bf16_f32 v72, v8, v9\n"
    "v_cvt_pk_bf16_f32 v73, v10, v11\n"
    "v_cvt_pk_bf16_f32 v74, v12, v13\n"
    "v_cvt_pk_bf16_f32 v75, v14, v15\n"
    "v_cvt_pk_bf16_f32 v76, v16, v17\n"
    "v_cvt_pk_bf16_f32 v77, v18, v19\n"
    "v_cvt_pk_bf16_f32 v78, v20, v21\n"
    "v_cvt_pk_bf16_f32 v79, v22, v23\n"
    "v_add_u32 v6, s62, v110\n"
    "buffer_store_dwordx2 v[72:73], v110, s[24:27], s75 offen\n"
    "buffer_store_dwordx2 v[74:75], v110, s[24:27], s75 offen offset:32\n"
    "buffer_store_dwordx2 v[76:77], v6, s[24:27], s75 offen\n"
    "buffer_store_dwordx2 v[78:79], v6, s[24:27], s75 offen offset:32\n"
    "s_endpgm\n"
    "Lspin0:\n"
    "s_mov_b32 s61, 0\n"
    "Lspinl0:\n"
    "s_sleep 1\n"
    "buffer_load_dwordx4 v[96:99], v2, s[8:11], s5 offen sc1\n"
    "s_waitcnt vmcnt(0)\n"
    "v_xor_b32 v3, v96, v7\n"
    "v_xor_b32 v4, v97, v7\n"
    "v_or_b32 v3, v3, v4\n"
    "v_xor_b32 v4, v98, v7\n"
    "v_or_b32 v3, v3, v4\n"
    "v_xor_b32 v4, v99, v7\n"
    "v_or_b32 v3, v3, v4\n"
    "v_cmp_ne_u32 vcc, 0, v3\n"
    "s_nop 0\n"
    "s_cbranch_vccz Lspinret0\n"
    "s_add_u32 s61, s61, 1\n"
    "s_cmp_lt_u32 s61, 0x30d40\n"
    "s_cbranch_scc1 Lspinl0\n"
    "s_branch Lspinret0\n"
    "Lspin1:\n"
    "s_mov_b32 s61, 0\n"
    "Lspinl1:\n"
    "s_sleep 1\n"
    "buffer_load_dword v122, v5, s[8:11], s7 offen sc1\n"
    "s_waitcnt vmcnt(0)\n"
    "v_cmp_ne_u32 vcc, v122, v7\n"
    "s_nop 0\n"
    "s_cbranch_vccz Lspinret1\n"
    "s_add_u32 s61, s61, 1\n"
    "s_cmp_lt_u32 s61, 0x30d40\n"
    "s_cbranch_scc1 Lspinl1\n"
    "s_branch Lspinret1\n"
    "Lhodd:\n"
    "buffer_store_dwordx2 v[8:9], v2, s[8:11], s85 offen offset:8 sc1\n"
    "buffer_store_dwordx4 v[10:13], v2, s[8:11], s85 offen offset:1024 sc1\n"
    "buffer_store_dwordx2 v[14:15], v120, s[8:11], s85 offen offset:8 sc1\n"
    "buffer_store_dwordx4 v[16:19], v120, s[8:11], s85 offen offset:1024 sc1\n"
    "buffer_store_dwordx2 v[20:21], v2, s[8:11], s86 offen offset:8 sc1\n"
    "buffer_store_dwordx4 v[22:25], v2, s[8:11], s86 offen offset:1024 sc1\n"
    "buffer_store_dwordx2 v[26:27], v120, s[8:11], s86 offen offset:8 sc1\n"
    "buffer_store_dwordx4 v[28:31], v120, s[8:11], s86 offen offset:1024 sc1\n"
    "s_branch Lhret\n"
    "Lspin2:\n"
    "s_mov_b32 s61, 0\n"
    "Lspinl2:\n"
    "s_sleep 1\n"
    "buffer_load_dword v122, v5, s[8:11], s7 offen sc1\n"
    "s_waitcnt vmcnt(0)\n"
    "v_cmp_ne_u32 vcc, v122, v7\n"
    "s_nop 0\n"
    "s_cbranch_vccz Lspinret2\n"
    "s_add_u32 s61, s61, 1\n"
    "s_cmp_lt_u32 s61, 0x30d40\n"
    "s_cbranch_scc1 Lspinl2\n"
    "s_branch Lspinret2\n"
    "Lrs1:\n"
    "s_mov_b32 s79, 1\n"
    "s_mov_b32 s80, 1\n"
    "s_mov_b32 s81, 1\n"
    "s_branch Lrare0\n"
    "Levs1:\n"
    "s_mov_b32 s79, 1\n"
    "s_branch Levt\n"
    "Lrs2:\n"
    "s_mov_b32 s79, 2\n"
    "s_mov_b32 s80, 1\n"
    "s_mov_b32 s81, 1\n"
    "s_branch Lrare1\n"
    "Lrs3:\n"
    "s_mov_b32 s79, 3\n"
    "s_mov_b32 s80, 1\n"
    "s_mov_b32 s81, 1\n"
    "s_branch Lrare0\n"
    "Levs3:\n"
    "s_mov_b32 s79, 3\n"
    "s_branch Levt\n"
    "Lrs4:\n"
    "s_mov_b32 s79, 4\n"
    "s_mov_b32 s80, 1\n"
    "s_mov_b32 s81, 1\n"
    "s_branch Lrare1\n"
    "Lrs5:\n"
    "s_mov_b32 s79, 5\n"
    "s_mov_b32 s80, 1\n"
    "s_mov_b32 s81, 1\n"
    "s_branch Lrare0\n"
    "Levs5:\n"
    "s_mov_b32 s79, 5\n"
    "s_branch Levt\n"
    "Lrs6:\n"
    "s_mov_b32 s79, 6\n"
    "s_mov_b32 s80, 1\n"
    "s_mov_b32 s81, 1\n"
    "s_branch Lrare1\n"
    "Lrs7:\n"
    "s_mov_b32 s79, 7\n"
    "s_mov_b32 s80, 1\n"
    "s_mov_b32 s81, 1\n"
    "s_branch Lrare0\n"
    "Levs7:\n"
    "s_mov_b32 s79, 7\n"
    "s_branch Levt\n"
    "Lrs8:\n"
    "s_mov_b32 s79, 8\n"
    "s_mov_b32 s80, 1\n"
    "s_mov_b32 s81, 0\n"
    "s_branch Lrare1\n"
    "Lspin3:\n"
    "s_mov_b32 s61, 0\n"
    "Lspinl3:\n"
    "s_sleep 1\n"
    "buffer_load_dword v122, v5, s[8:11], s7 offen sc1\n"
    "s_waitcnt vmcnt(0)\n"
    "v_cmp_ne_u32 vcc, v122, v7\n"
    "s_nop 0\n"
    "s_cbranch_vccz Lspinret3\n"
    "s_add_u32 s61, s61, 1\n"
    "s_cmp_lt_u32 s61, 0x30d40\n"
    "s_cbranch_scc1 Lspinl3\n"
    "s_branch Lspinret3\n"
    "Lrs9:\n"
    "s_mov_b32 s79, 9\n"
    "s_mov_b32 s80, 0\n"
    "s_mov_b32 s81, 0\n"
    "s_branch Lrare0\n"
    "Lrs10:\n"
    "s_mov_b32 s79, 10\n"
    "s_mov_b32 s80, 0\n"
    "s_mov_b32 s81, 0\n"
    "s_branch Lrare1\n"
    "Lspin4:\n"
    "s_mov_b32 s61, 0\n"
    "Lspinl4:\n"
    "s_sleep 1\n"
    "buffer_load_dword v122, v5, s[8:11], s7 offen sc1\n"
    "s_waitcnt vmcnt(0)\n"
    "v_cmp_ne_u32 vcc, v122, v7\n"
    "s_nop 0\n"
    "s_cbranch_vccz Lspinret4\n"
    "s_add_u32 s61, s61, 1\n"
    "s_cmp_lt_u32 s61, 0x30d40\n"
    "s_cbranch_scc1 Lspinl4\n"
    "s_branch Lspinret4\n"
    "Lrs11:\n"
    "s_mov_b32 s79, 11\n"
    "s_mov_b32 s80, 0\n"
    "s_mov_b32 s81, 0\n"
    "s_branch Lrare0\n"
    "Lrs12:\n"
    "s_mov_b32 s79, 12\n"
    "s_mov_b32 s80, 0\n"
    "s_mov_b32 s81, 0\n"
    "s_branch Lrare1\n"
    "Lrs13:\n"
    "s_mov_b32 s79, 13\n"
    "s_mov_b32 s80, 0\n"
    "s_mov_b32 s81, 0\n"
    "s_branch Lrare0\n"
    "Lrs14:\n"
    "s_mov_b32 s79, 14\n"
    "s_mov_b32 s80, 0\n"
    "s_mov_b32 s81, 0\n"
    "s_branch Lrare1\n"
    "Levt:\n"
    "s_waitcnt vmcnt(8)\n"
    "s_lshl_b32 s4, s57, 2\n"
    "s_add_u32 s4, s4, s58\n"
    "s_lshl_b32 s4, s4, 5\n"
    "s_add_u32 s4, s4, s56\n"
    "s_lshl_b32 s4, s4, 2\n"
    "s_add_u32 s4, s4, 0x3000\n"
    "v_mov_b32 v6, 0\n"
    "s_mov_b64 exec, 1\n"
    "buffer_store_dword v7, v6, s[8:11], s4 offen sc1\n"
    "s_mov_b64 exec, -1\n"
    "s_sub_u32 s4, 15, s57\n"
    "s_lshl_b32 s4, s4, 2\n"
    "s_add_u32 s4, s4, s58\n"
    "s_lshl_b32 s4, s4, 5\n"
    "s_add_u32 s4, s4, s56\n"
    "s_lshl_b32 s4, s4, 2\n"
    "s_add_u32 s4, s4, 0x3000\n"
    "v_mov_b32 v6, 0\n"
    "s_mov_b64 exec, 1\n"
    "buffer_store_dword v7, v6, s[8:11], s4 offen sc1\n"
    "s_mov_b64 exec, -1\n"
    "s_cmp_eq_u32 s79, 1\n"
    "s_cbranch_scc1 Levr1\n"
    "s_cmp_eq_u32 s79, 3\n"
    "s_cbranch_scc1 Levr3\n"
    "s_cmp_eq_u32 s79, 5\n"
    "s_cbranch_scc1 Levr5\n"
    "s_branch Levr7\n"
    "Lrare0:\n"
    "s_nop 15\n"
    "v_max3_f32 v116, v192, v193, v194\n"
    "v_max3_f32 v116, v116, v195, v196\n"
    "v_max3_f32 v116, v116, v197, v198\n"
    "v_max_f32 v116, v116, v199\n"
    "v_max3_f32 v117, v200, v201, v202\n"
    "v_max3_f32 v117, v117, v203, v204\n"
    "v_max3_f32 v117, v117, v205, v206\n"
    "v_max_f32 v117, v117, v207\n"
    "v_max3_f32 v118, v208, v209, v210\n"
    "v_max3_f32 v118, v118, v211, v212\n"
    "v_max3_f32 v118, v118, v213, v214\n"
    "v_max_f32 v118, v118, v215\n"
    "v_max3_f32 v119, v216, v217, v218\n"
    "v_max3_f32 v119, v119, v219, v220\n"
    "v_max3_f32 v119, v119, v221, v222\n"
    "v_max_f32 v119, v119, v223\n"
    "s_cmp_eq_u32 s80, 1\n"
    "s_cbranch_scc1 Lrare0a\n"
    "v_max_f32 v116, v116, v118\n"
    "v_max_f32 v117, v117, v119\n"
    "v_mov_b32 v118, v116\n"
    "v_mov_b32 v119, v117\n"
    "Lrare0a:\n"
    "v_mov_b32 v72, v116\n"
    "v_mov_b32 v73, v117\n"
    "v_mov_b32 v74, v118\n"
    "v_mov_b32 v75, v119\n"
    "v_permlane16_swap_b32 v72, v116\n"
    "v_permlane16_swap_b32 v73, v117\n"
    "v_permlane16_swap_b32 v74, v118\n"
    "v_permlane16_swap_b32 v75, v119\n"
    "v_max_f32 v116, v116, v72\n"
    "v_max_f32 v117, v117, v73\n"
    "v_max_f32 v118, v118, v74\n"
    "v_max_f32 v119, v119, v75\n"
    "v_mov_b32 v72, v116\n"
    "v_mov_b32 v73, v117\n"
    "v_mov_b32 v74, v118\n"
    "v_mov_b32 v75, v119\n"
    "v_permlane32_swap_b32 v72, v116\n"
    "v_permlane32_swap_b32 v73, v117\n"
    "v_permlane32_swap_b32 v74, v118\n"
    "v_permlane32_swap_b32 v75, v119\n"
    "v_max_f32 v116, v116, v72\n"
    "v_max_f32 v117, v117, v73\n"
    "v_max_f32 v118, v118, v74\n"
    "v_max_f32 v119, v119, v75\n"
    "v_add_f32 v116, 0x41000000, v116\n"
    "v_max_f32 v116, 0, v116\n"
    "v_sub_f32 v124, 0, v116\n"
    "v_exp_f32 v124, v124\n"
    "v_add_f32 v117, 0x41000000, v117\n"
    "v_max_f32 v117, 0, v117\n"
    "v_sub_f32 v125, 0, v117\n"
    "v_exp_f32 v125, v125\n"
    "v_add_f32 v118, 0x41000000, v118\n"
    "v_max_f32 v118, 0, v118\n"
    "v_sub_f32 v126, 0, v118\n"
    "v_exp_f32 v126, v126\n"
    "v_add_f32 v119, 0x41000000, v119\n"
    "v_max_f32 v119, 0, v119\n"
    "v_sub_f32 v127, 0, v119\n"
    "v_exp_f32 v127, v127\n"
    "v_sub_f32 v192, v192, v116\n"
    "v_sub_f32 v193, v193, v116\n"
    "v_sub_f32 v194, v194, v116\n"
    "v_sub_f32 v195, v195, v116\n"
    "v_sub_f32 v196, v196, v116\n"
    "v_sub_f32 v197, v197, v116\n"
    "v_sub_f32 v198, v198, v116\n"
    "v_sub_f32 v199, v199, v116\n"
    "v_sub_f32 v200, v200, v117\n"
    "v_sub_f32 v201, v201, v117\n"
    "v_sub_f32 v202, v202, v117\n"
    "v_sub_f32 v203, v203, v117\n"
    "v_sub_f32 v204, v204, v117\n"
    "v_sub_f32 v205, v205, v117\n"
    "v_sub_f32 v206, v206, v117\n"
    "v_sub_f32 v207, v207, v117\n"
    "v_sub_f32 v208, v208, v118\n"
    "v_sub_f32 v209, v209, v118\n"
    "v_sub_f32 v210, v210, v118\n"
    "v_sub_f32 v211, v211, v118\n"
    "v_sub_f32 v212, v212, v118\n"
    "v_sub_f32 v213, v213, v118\n"
    "v_sub_f32 v214, v214, v118\n"
    "v_sub_f32 v215, v215, v118\n"
    "v_sub_f32 v216, v216, v119\n"
    "v_sub_f32 v217, v217, v119\n"
    "v_sub_f32 v218, v218, v119\n"
    "v_sub_f32 v219, v219, v119\n"
    "v_sub_f32 v220, v220, v119\n"
    "v_sub_f32 v221, v221, v119\n"
    "v_sub_f32 v222, v222, v119\n"
    "v_sub_f32 v223, v223, v119\n"
    "v_sub_f32 v240, v240, v118\n"
    "v_sub_f32 v241, v241, v118\n"
    "v_sub_f32 v242, v242, v118\n"
    "v_sub_f32 v243, v243, v118\n"
    "v_sub_f32 v244, v244, v118\n"
    "v_sub_f32 v245, v245, v118\n"
    "v_sub_f32 v246, v246, v118\n"
    "v_sub_f32 v247, v247, v118\n"
    "v_sub_f32 v248, v248, v119\n"
    "v_sub_f32 v249, v249, v119\n"
    "v_sub_f32 v250, v250, v119\n"
    "v_sub_f32 v251, v251, v119\n"
    "v_sub_f32 v252, v252, v119\n"
    "v_sub_f32 v253, v253, v119\n"
    "v_sub_f32 v254, v254, v119\n"
    "v_sub_f32 v255, v255, v119\n"
    "v_sub_f32 v56, v56, v118\n"
    "v_sub_f32 v57, v57, v118\n"
    "v_sub_f32 v58, v58, v118\n"
    "v_sub_f32 v59, v59, v118\n"
    "v_sub_f32 v60, v60, v118\n"
    "v_sub_f32 v61, v61, v118\n"
    "v_sub_f32 v62, v62, v118\n"
    "v_sub_f32 v63, v63, v118\n"
    "v_sub_f32 v64, v64, v119\n"
    "v_sub_f32 v65, v65, v119\n"
    "v_sub_f32 v66, v66, v119\n"
    "v_sub_f32 v67, v67, v119\n"
    "v_sub_f32 v68, v68, v119\n"
    "v_sub_f32 v69, v69, v119\n"
    "v_sub_f32 v70, v70, v119\n"
    "v_sub_f32 v71, v71, v119\n"
    "s_cmp_eq_u32 s81, 1\n"
    "s_cbranch_scc1 Lrare0b\n"
    "v_mov_b32 v116, v118\n"
    "v_mov_b32 v117, v119\n"
    "Lrare0b:\n"
    "v_sub_f32 v224, v224, v116\n"
    "v_sub_f32 v225, v225, v116\n"
    "v_sub_f32 v226, v226, v116\n"
    "v_sub_f32 v227, v227, v116\n"
    "v_sub_f32 v228, v228, v116\n"
    "v_sub_f32 v229, v229, v116\n"
    "v_sub_f32 v230, v230, v116\n"
    "v_sub_f32 v231, v231, v116\n"
    "v_sub_f32 v232, v232, v117\n"
    "v_sub_f32 v233, v233, v117\n"
    "v_sub_f32 v234, v234, v117\n"
    "v_sub_f32 v235, v235, v117\n"
    "v_sub_f32 v236, v236, v117\n"
    "v_sub_f32 v237, v237, v117\n"
    "v_sub_f32 v238, v238, v117\n"
    "v_sub_f32 v239, v239, v117\n"
    "v_sub_f32 v40, v40, v116\n"
    "v_sub_f32 v41, v41, v116\n"
    "v_sub_f32 v42, v42, v116\n"
    "v_sub_f32 v43, v43, v116\n"
    "v_sub_f32 v44, v44, v116\n"
    "v_sub_f32 v45, v45, v116\n"
    "v_sub_f32 v46, v46, v116\n"
    "v_sub_f32 v47, v47, v116\n"
    "v_sub_f32 v48, v48, v117\n"
    "v_sub_f32 v49, v49, v117\n"
    "v_sub_f32 v50, v50, v117\n"
    "v_sub_f32 v51, v51, v117\n"
    "v_sub_f32 v52, v52, v117\n"
    "v_sub_f32 v53, v53, v117\n"
    "v_sub_f32 v54, v54, v117\n"
    "v_sub_f32 v55, v55, v117\n"
    "v_accvgpr_read_b32 v72, a64\n"
    "v_mul_f32 v72, v72, v126\n"
    "v_accvgpr_write_b32 a64, v72\n"
    "v_accvgpr_read_b32 v73, a65\n"
    "v_mul_f32 v73, v73, v126\n"
    "v_accvgpr_write_b32 a65, v73\n"
    "v_accvgpr_read_b32 v74, a66\n"
    "v_mul_f32 v74, v74, v126\n"
    "v_accvgpr_write_b32 a66, v74\n"
    "v_accvgpr_read_b32 v75, a67\n"
    "v_mul_f32 v75, v75, v126\n"
    "v_accvgpr_write_b32 a67, v75\n"
    "v_accvgpr_read_b32 v76, a68\n"
    "v_mul_f32 v76, v76, v126\n"
    "v_accvgpr_write_b32 a68, v76\n"
    "v_accvgpr_read_b32 v77, a69\n"
    "v_mul_f32 v77, v77, v126\n"
    "v_accvgpr_write_b32 a69, v77\n"
    "v_accvgpr_read_b32 v78, a70\n"
    "v_mul_f32 v78, v78, v126\n"
    "v_accvgpr_write_b32 a70, v78\n"
    "v_accvgpr_read_b32 v79, a71\n"
    "v_mul_f32 v79, v79, v126\n"
    "v_accvgpr_write_b32 a71, v79\n"
    "v_accvgpr_read_b32 v72, a72\n"
    "v_mul_f32 v72, v72, v126\n"
    "v_accvgpr_write_b32 a72, v72\n"
    "v_accvgpr_read_b32 v73, a73\n"
    "v_mul_f32 v73, v73, v126\n"
    "v_accvgpr_write_b32 a73, v73\n"
    "v_accvgpr_read_b32 v74, a74\n"
    "v_mul_f32 v74, v74, v126\n"
    "v_accvgpr_write_b32 a74, v74\n"
    "v_accvgpr_read_b32 v75, a75\n"
    "v_mul_f32 v75, v75, v126\n"
    "v_accvgpr_write_b32 a75, v75\n"
    "v_accvgpr_read_b32 v76, a76\n"
    "v_mul_f32 v76, v76, v126\n"
    "v_accvgpr_write_b32 a76, v76\n"
    "v_accvgpr_read_b32 v77, a77\n"
    "v_mul_f32 v77, v77, v126\n"
    "v_accvgpr_write_b32 a77, v77\n"
    "v_accvgpr_read_b32 v78, a78\n"
    "v_mul_f32 v78, v78, v126\n"
    "v_accvgpr_write_b32 a78, v78\n"
    "v_accvgpr_read_b32 v79, a79\n"
    "v_mul_f32 v79, v79, v126\n"
    "v_accvgpr_write_b32 a79, v79\n"
    "v_accvgpr_read_b32 v72, a80\n"
    "v_mul_f32 v72, v72, v126\n"
    "v_accvgpr_write_b32 a80, v72\n"
    "v_accvgpr_read_b32 v73, a81\n"
    "v_mul_f32 v73, v73, v126\n"
    "v_accvgpr_write_b32 a81, v73\n"
    "v_accvgpr_read_b32 v74, a82\n"
    "v_mul_f32 v74, v74, v126\n"
    "v_accvgpr_write_b32 a82, v74\n"
    "v_accvgpr_read_b32 v75, a83\n"
    "v_mul_f32 v75, v75, v126\n"
    "v_accvgpr_write_b32 a83, v75\n"
    "v_accvgpr_read_b32 v76, a84\n"
    "v_mul_f32 v76, v76, v126\n"
    "v_accvgpr_write_b32 a84, v76\n"
    "v_accvgpr_read_b32 v77, a85\n"
    "v_mul_f32 v77, v77, v126\n"
    "v_accvgpr_write_b32 a85, v77\n"
    "v_accvgpr_read_b32 v78, a86\n"
    "v_mul_f32 v78, v78, v126\n"
    "v_accvgpr_write_b32 a86, v78\n"
    "v_accvgpr_read_b32 v79, a87\n"
    "v_mul_f32 v79, v79, v126\n"
    "v_accvgpr_write_b32 a87, v79\n"
    "v_accvgpr_read_b32 v72, a88\n"
    "v_mul_f32 v72, v72, v126\n"
    "v_accvgpr_write_b32 a88, v72\n"
    "v_accvgpr_read_b32 v73, a89\n"
    "v_mul_f32 v73, v73, v126\n"
    "v_accvgpr_write_b32 a89, v73\n"
    "v_accvgpr_read_b32 v74, a90\n"
    "v_mul_f32 v74, v74, v126\n"
    "v_accvgpr_write_b32 a90, v74\n"
    "v_accvgpr_read_b32 v75, a91\n"
    "v_mul_f32 v75, v75, v126\n"
    "v_accvgpr_write_b32 a91, v75\n"
    "v_accvgpr_read_b32 v76, a92\n"
    "v_mul_f32 v76, v76, v126\n"
    "v_accvgpr_write_b32 a92, v76\n"
    "v_accvgpr_read_b32 v77, a93\n"
    "v_mul_f32 v77, v77, v126\n"
    "v_accvgpr_write_b32 a93, v77\n"
    "v_accvgpr_read_b32 v78, a94\n"
    "v_mul_f32 v78, v78, v126\n"
    "v_accvgpr_write_b32 a94, v78\n"
    "v_accvgpr_read_b32 v79, a95\n"
    "v_mul_f32 v79, v79, v126\n"
    "v_accvgpr_write_b32 a95, v79\n"
    "v_mul_f32 v92, v92, v126\n"
    "v_accvgpr_read_b32 v72, a96\n"
    "v_mul_f32 v72, v72, v127\n"
    "v_accvgpr_write_b32 a96, v72\n"
    "v_accvgpr_read_b32 v73, a97\n"
    "v_mul_f32 v73, v73, v127\n"
    "v_accvgpr_write_b32 a97, v73\n"
    "v_accvgpr_read_b32 v74, a98\n"
    "v_mul_f32 v74, v74, v127\n"
    "v_accvgpr_write_b32 a98, v74\n"
    "v_accvgpr_read_b32 v75, a99\n"
    "v_mul_f32 v75, v75, v127\n"
    "v_accvgpr_write_b32 a99, v75\n"
    "v_accvgpr_read_b32 v76, a100\n"
    "v_mul_f32 v76, v76, v127\n"
    "v_accvgpr_write_b32 a100, v76\n"
    "v_accvgpr_read_b32 v77, a101\n"
    "v_mul_f32 v77, v77, v127\n"
    "v_accvgpr_write_b32 a101, v77\n"
    "v_accvgpr_read_b32 v78, a102\n"
    "v_mul_f32 v78, v78, v127\n"
    "v_accvgpr_write_b32 a102, v78\n"
    "v_accvgpr_read_b32 v79, a103\n"
    "v_mul_f32 v79, v79, v127\n"
    "v_accvgpr_write_b32 a103, v79\n"
    "v_accvgpr_read_b32 v72, a104\n"
    "v_mul_f32 v72, v72, v127\n"
    "v_accvgpr_write_b32 a104, v72\n"
    "v_accvgpr_read_b32 v73, a105\n"
    "v_mul_f32 v73, v73, v127\n"
    "v_accvgpr_write_b32 a105, v73\n"
    "v_accvgpr_read_b32 v74, a106\n"
    "v_mul_f32 v74, v74, v127\n"
    "v_accvgpr_write_b32 a106, v74\n"
    "v_accvgpr_read_b32 v75, a107\n"
    "v_mul_f32 v75, v75, v127\n"
    "v_accvgpr_write_b32 a107, v75\n"
    "v_accvgpr_read_b32 v76, a108\n"
    "v_mul_f32 v76, v76, v127\n"
    "v_accvgpr_write_b32 a108, v76\n"
    "v_accvgpr_read_b32 v77, a109\n"
    "v_mul_f32 v77, v77, v127\n"
    "v_accvgpr_write_b32 a109, v77\n"
    "v_accvgpr_read_b32 v78, a110\n"
    "v_mul_f32 v78, v78, v127\n"
    "v_accvgpr_write_b32 a110, v78\n"
    "v_accvgpr_read_b32 v79, a111\n"
    "v_mul_f32 v79, v79, v127\n"
    "v_accvgpr_write_b32 a111, v79\n"
    "v_accvgpr_read_b32 v72, a112\n"
    "v_mul_f32 v72, v72, v127\n"
    "v_accvgpr_write_b32 a112, v72\n"
    "v_accvgpr_read_b32 v73, a113\n"
    "v_mul_f32 v73, v73, v127\n"
    "v_accvgpr_write_b32 a113, v73\n"
    "v_accvgpr_read_b32 v74, a114\n"
    "v_mul_f32 v74, v74, v127\n"
    "v_accvgpr_write_b32 a114, v74\n"
    "v_accvgpr_read_b32 v75, a115\n"
    "v_mul_f32 v75, v75, v127\n"
    "v_accvgpr_write_b32 a115, v75\n"
    "v_accvgpr_read_b32 v76, a116\n"
    "v_mul_f32 v76, v76, v127\n"
    "v_accvgpr_write_b32 a116, v76\n"
    "v_accvgpr_read_b32 v77, a117\n"
    "v_mul_f32 v77, v77, v127\n"
    "v_accvgpr_write_b32 a117, v77\n"
    "v_accvgpr_read_b32 v78, a118\n"
    "v_mul_f32 v78, v78, v127\n"
    "v_accvgpr_write_b32 a118, v78\n"
    "v_accvgpr_read_b32 v79, a119\n"
    "v_mul_f32 v79, v79, v127\n"
    "v_accvgpr_write_b32 a119, v79\n"
    "v_accvgpr_read_b32 v72, a120\n"
    "v_mul_f32 v72, v72, v127\n"
    "v_accvgpr_write_b32 a120, v72\n"
    "v_accvgpr_read_b32 v73, a121\n"
    "v_mul_f32 v73, v73, v127\n"
    "v_accvgpr_write_b32 a121, v73\n"
    "v_accvgpr_read_b32 v74, a122\n"
    "v_mul_f32 v74, v74, v127\n"
    "v_accvgpr_write_b32 a122, v74\n"
    "v_accvgpr_read_b32 v75, a123\n"
    "v_mul_f32 v75, v75, v127\n"
    "v_accvgpr_write_b32 a123, v75\n"
    "v_accvgpr_read_b32 v76, a124\n"
    "v_mul_f32 v76, v76, v127\n"
    "v_accvgpr_write_b32 a124, v76\n"
    "v_accvgpr_read_b32 v77, a125\n"
    "v_mul_f32 v77, v77, v127\n"
    "v_accvgpr_write_b32 a125, v77\n"
    "v_accvgpr_read_b32 v78, a126\n"
    "v_mul_f32 v78, v78, v127\n"
    "v_accvgpr_write_b32 a126, v78\n"
    "v_accvgpr_read_b32 v79, a127\n"
    "v_mul_f32 v79, v79, v127\n"
    "v_accvgpr_write_b32 a127, v79\n"
    "v_mul_f32 v96, v96, v127\n"
    "s_cmp_eq_u32 s80, 0\n"
    "s_cbranch_scc1 Lrare0c\n"
    "v_accvgpr_read_b32 v72, a0\n"
    "v_mul_f32 v72, v72, v124\n"
    "v_accvgpr_write_b32 a0, v72\n"
    "v_accvgpr_read_b32 v73, a1\n"
    "v_mul_f32 v73, v73, v124\n"
    "v_accvgpr_write_b32 a1, v73\n"
    "v_accvgpr_read_b32 v74, a2\n"
    "v_mul_f32 v74, v74, v124\n"
    "v_accvgpr_write_b32 a2, v74\n"
    "v_accvgpr_read_b32 v75, a3\n"
    "v_mul_f32 v75, v75, v124\n"
    "v_accvgpr_write_b32 a3, v75\n"
    "v_accvgpr_read_b32 v76, a4\n"
    "v_mul_f32 v76, v76, v124\n"
    "v_accvgpr_write_b32 a4, v76\n"
    "v_accvgpr_read_b32 v77, a5\n"
    "v_mul_f32 v77, v77, v124\n"
    "v_accvgpr_write_b32 a5, v77\n"
    "v_accvgpr_read_b32 v78, a6\n"
    "v_mul_f32 v78, v78, v124\n"
    "v_accvgpr_write_b32 a6, v78\n"
    "v_accvgpr_read_b32 v79, a7\n"
    "v_mul_f32 v79, v79, v124\n"
    "v_accvgpr_write_b32 a7, v79\n"
    "v_accvgpr_read_b32 v72, a8\n"
    "v_mul_f32 v72, v72, v124\n"
    "v_accvgpr_write_b32 a8, v72\n"
    "v_accvgpr_read_b32 v73, a9\n"
    "v_mul_f32 v73, v73, v124\n"
    "v_accvgpr_write_b32 a9, v73\n"
    "v_accvgpr_read_b32 v74, a10\n"
    "v_mul_f32 v74, v74, v124\n"
    "v_accvgpr_write_b32 a10, v74\n"
    "v_accvgpr_read_b32 v75, a11\n"
    "v_mul_f32 v75, v75, v124\n"
    "v_accvgpr_write_b32 a11, v75\n"
    "v_accvgpr_read_b32 v76, a12\n"
    "v_mul_f32 v76, v76, v124\n"
    "v_accvgpr_write_b32 a12, v76\n"
    "v_accvgpr_read_b32 v77, a13\n"
    "v_mul_f32 v77, v77, v124\n"
    "v_accvgpr_write_b32 a13, v77\n"
    "v_accvgpr_read_b32 v78, a14\n"
    "v_mul_f32 v78, v78, v124\n"
    "v_accvgpr_write_b32 a14, v78\n"
    "v_accvgpr_read_b32 v79, a15\n"
    "v_mul_f32 v79, v79, v124\n"
    "v_accvgpr_write_b32 a15, v79\n"
    "v_accvgpr_read_b32 v72, a16\n"
    "v_mul_f32 v72, v72, v124\n"
    "v_accvgpr_write_b32 a16, v72\n"
    "v_accvgpr_read_b32 v73, a17\n"
    "v_mul_f32 v73, v73, v124\n"
    "v_accvgpr_write_b32 a17, v73\n"
    "v_accvgpr_read_b32 v74, a18\n"
    "v_mul_f32 v74, v74, v124\n"
    "v_accvgpr_write_b32 a18, v74\n"
    "v_accvgpr_read_b32 v75, a19\n"
    "v_mul_f32 v75, v75, v124\n"
    "v_accvgpr_write_b32 a19, v75\n"
    "v_accvgpr_read_b32 v76, a20\n"
    "v_mul_f32 v76, v76, v124\n"
    "v_accvgpr_write_b32 a20, v76\n"
    "v_accvgpr_read_b32 v77, a21\n"
    "v_mul_f32 v77, v77, v124\n"
    "v_accvgpr_write_b32 a21, v77\n"
    "v_accvgpr_read_b32 v78, a22\n"
    "v_mul_f32 v78, v78, v124\n"
    "v_accvgpr_write_b32 a22, v78\n"
    "v_accvgpr_read_b32 v79, a23\n"
    "v_mul_f32 v79, v79, v124\n"
    "v_accvgpr_write_b32 a23, v79\n"
    "v_accvgpr_read_b32 v72, a24\n"
    "v_mul_f32 v72, v72, v124\n"
    "v_accvgpr_write_b32 a24, v72\n"
    "v_accvgpr_read_b32 v73, a25\n"
    "v_mul_f32 v73, v73, v124\n"
    "v_accvgpr_write_b32 a25, v73\n"
    "v_accvgpr_read_b32 v74, a26\n"
    "v_mul_f32 v74, v74, v124\n"
    "v_accvgpr_write_b32 a26, v74\n"
    "v_accvgpr_read_b32 v75, a27\n"
    "v_mul_f32 v75, v75, v124\n"
    "v_accvgpr_write_b32 a27, v75\n"
    "v_accvgpr_read_b32 v76, a28\n"
    "v_mul_f32 v76, v76, v124\n"
    "v_accvgpr_write_b32 a28, v76\n"
    "v_accvgpr_read_b32 v77, a29\n"
    "v_mul_f32 v77, v77, v124\n"
    "v_accvgpr_write_b32 a29, v77\n"
    "v_accvgpr_read_b32 v78, a30\n"
    "v_mul_f32 v78, v78, v124\n"
    "v_accvgpr_write_b32 a30, v78\n"
    "v_accvgpr_read_b32 v79, a31\n"
    "v_mul_f32 v79, v79, v124\n"
    "v_accvgpr_write_b32 a31, v79\n"
    "v_mul_f32 v84, v84, v124\n"
    "v_accvgpr_read_b32 v72, a32\n"
    "v_mul_f32 v72, v72, v125\n"
    "v_accvgpr_write_b32 a32, v72\n"
    "v_accvgpr_read_b32 v73, a33\n"
    "v_mul_f32 v73, v73, v125\n"
    "v_accvgpr_write_b32 a33, v73\n"
    "v_accvgpr_read_b32 v74, a34\n"
    "v_mul_f32 v74, v74, v125\n"
    "v_accvgpr_write_b32 a34, v74\n"
    "v_accvgpr_read_b32 v75, a35\n"
    "v_mul_f32 v75, v75, v125\n"
    "v_accvgpr_write_b32 a35, v75\n"
    "v_accvgpr_read_b32 v76, a36\n"
    "v_mul_f32 v76, v76, v125\n"
    "v_accvgpr_write_b32 a36, v76\n"
    "v_accvgpr_read_b32 v77, a37\n"
    "v_mul_f32 v77, v77, v125\n"
    "v_accvgpr_write_b32 a37, v77\n"
    "v_accvgpr_read_b32 v78, a38\n"
    "v_mul_f32 v78, v78, v125\n"
    "v_accvgpr_write_b32 a38, v78\n"
    "v_accvgpr_read_b32 v79, a39\n"
    "v_mul_f32 v79, v79, v125\n"
    "v_accvgpr_write_b32 a39, v79\n"
    "v_accvgpr_read_b32 v72, a40\n"
    "v_mul_f32 v72, v72, v125\n"
    "v_accvgpr_write_b32 a40, v72\n"
    "v_accvgpr_read_b32 v73, a41\n"
    "v_mul_f32 v73, v73, v125\n"
    "v_accvgpr_write_b32 a41, v73\n"
    "v_accvgpr_read_b32 v74, a42\n"
    "v_mul_f32 v74, v74, v125\n"
    "v_accvgpr_write_b32 a42, v74\n"
    "v_accvgpr_read_b32 v75, a43\n"
    "v_mul_f32 v75, v75, v125\n"
    "v_accvgpr_write_b32 a43, v75\n"
    "v_accvgpr_read_b32 v76, a44\n"
    "v_mul_f32 v76, v76, v125\n"
    "v_accvgpr_write_b32 a44, v76\n"
    "v_accvgpr_read_b32 v77, a45\n"
    "v_mul_f32 v77, v77, v125\n"
    "v_accvgpr_write_b32 a45, v77\n"
    "v_accvgpr_read_b32 v78, a46\n"
    "v_mul_f32 v78, v78, v125\n"
    "v_accvgpr_write_b32 a46, v78\n"
    "v_accvgpr_read_b32 v79, a47\n"
    "v_mul_f32 v79, v79, v125\n"
    "v_accvgpr_write_b32 a47, v79\n"
    "v_accvgpr_read_b32 v72, a48\n"
    "v_mul_f32 v72, v72, v125\n"
    "v_accvgpr_write_b32 a48, v72\n"
    "v_accvgpr_read_b32 v73, a49\n"
    "v_mul_f32 v73, v73, v125\n"
    "v_accvgpr_write_b32 a49, v73\n"
    "v_accvgpr_read_b32 v74, a50\n"
    "v_mul_f32 v74, v74, v125\n"
    "v_accvgpr_write_b32 a50, v74\n"
    "v_accvgpr_read_b32 v75, a51\n"
    "v_mul_f32 v75, v75, v125\n"
    "v_accvgpr_write_b32 a51, v75\n"
    "v_accvgpr_read_b32 v76, a52\n"
    "v_mul_f32 v76, v76, v125\n"
    "v_accvgpr_write_b32 a52, v76\n"
    "v_accvgpr_read_b32 v77, a53\n"
    "v_mul_f32 v77, v77, v125\n"
    "v_accvgpr_write_b32 a53, v77\n"
    "v_accvgpr_read_b32 v78, a54\n"
    "v_mul_f32 v78, v78, v125\n"
    "v_accvgpr_write_b32 a54, v78\n"
    "v_accvgpr_read_b32 v79, a55\n"
    "v_mul_f32 v79, v79, v125\n"
    "v_accvgpr_write_b32 a55, v79\n"
    "v_accvgpr_read_b32 v72, a56\n"
    "v_mul_f32 v72, v72, v125\n"
    "v_accvgpr_write_b32 a56, v72\n"
    "v_accvgpr_read_b32 v73, a57\n"
    "v_mul_f32 v73, v73, v125\n"
    "v_accvgpr_write_b32 a57, v73\n"
    "v_accvgpr_read_b32 v74, a58\n"
    "v_mul_f32 v74, v74, v125\n"
    "v_accvgpr_write_b32 a58, v74\n"
    "v_accvgpr_read_b32 v75, a59\n"
    "v_mul_f32 v75, v75, v125\n"
    "v_accvgpr_write_b32 a59, v75\n"
    "v_accvgpr_read_b32 v76, a60\n"
    "v_mul_f32 v76, v76, v125\n"
    "v_accvgpr_write_b32 a60, v76\n"
    "v_accvgpr_read_b32 v77, a61\n"
    "v_mul_f32 v77, v77, v125\n"
    "v_accvgpr_write_b32 a61, v77\n"
    "v_accvgpr_read_b32 v78, a62\n"
    "v_mul_f32 v78, v78, v125\n"
    "v_accvgpr_write_b32 a62, v78\n"
    "v_accvgpr_read_b32 v79, a63\n"
    "v_mul_f32 v79, v79, v125\n"
    "v_accvgpr_write_b32 a63, v79\n"
    "v_mul_f32 v88, v88, v125\n"
    "Lrare0c:\n"
    "v_exp_f32 v72, v192\n"
    "v_exp_f32 v73, v193\n"
    "v_exp_f32 v74, v194\n"
    "v_exp_f32 v75, v195\n"
    "v_cvt_pk_bf16_f32 v8, v72, v73\n"
    "v_exp_f32 v76, v200\n"
    "v_exp_f32 v77, v201\n"
    "v_cvt_pk_bf16_f32 v9, v74, v75\n"
    "v_exp_f32 v78, v202\n"
    "v_exp_f32 v79, v203\n"
    "v_cvt_pk_bf16_f32 v12, v76, v77\n"
    "v_or_b32 v108, v8, v9\n"
    "v_exp_f32 v72, v208\n"
    "v_exp_f32 v73, v209\n"
    "v_cvt_pk_bf16_f32 v13, v78, v79\n"
    "v_exp_f32 v74, v210\n"
    "v_exp_f32 v75, v211\n"
    "v_cvt_pk_bf16_f32 v16, v72, v73\n"
    "v_or3_b32 v108, v108, v12, v13\n"
    "v_exp_f32 v76, v216\n"
    "v_exp_f32 v77, v217\n"
    "v_cvt_pk_bf16_f32 v17, v74, v75\n"
    "v_exp_f32 v78, v218\n"
    "v_exp_f32 v79, v219\n"
    "v_cvt_pk_bf16_f32 v20, v76, v77\n"
    "v_or3_b32 v108, v108, v16, v17\n"
    "v_exp_f32 v72, v196\n"
    "v_exp_f32 v73, v197\n"
    "v_cvt_pk_bf16_f32 v21, v78, v79\n"
    "v_exp_f32 v74, v198\n"
    "v_exp_f32 v75, v199\n"
    "v_cvt_pk_bf16_f32 v10, v72, v73\n"
    "v_or3_b32 v108, v108, v20, v21\n"
    "v_exp_f32 v76, v204\n"
    "v_exp_f32 v77, v205\n"
    "v_cvt_pk_bf16_f32 v11, v74, v75\n"
    "v_exp_f32 v78, v206\n"
    "v_exp_f32 v79, v207\n"
    "v_cvt_pk_bf16_f32 v14, v76, v77\n"
    "v_or3_b32 v108, v108, v10, v11\n"
    "v_exp_f32 v72, v212\n"
    "v_exp_f32 v73, v213\n"
    "v_cvt_pk_bf16_f32 v15, v78, v79\n"
    "v_exp_f32 v74, v214\n"
    "v_exp_f32 v75, v215\n"
    "v_cvt_pk_bf16_f32 v18, v72, v73\n"
    "v_or3_b32 v108, v108, v14, v15\n"
    "v_exp_f32 v76, v220\n"
    "v_exp_f32 v77, v221\n"
    "v_cvt_pk_bf16_f32 v19, v74, v75\n"
    "v_exp_f32 v78, v222\n"
    "v_exp_f32 v79, v223\n"
    "v_cvt_pk_bf16_f32 v22, v76, v77\n"
    "v_or3_b32 v108, v108, v18, v19\n"
    "v_cvt_pk_bf16_f32 v23, v78, v79\n"
    "v_or3_b32 v108, v108, v22, v23\n"
    "s_nop 1\n"
    "s_cmp_eq_u32 s79, 1\n"
    "s_cbranch_scc1 Lrr1\n"
    "s_cmp_eq_u32 s79, 3\n"
    "s_cbranch_scc1 Lrr3\n"
    "s_cmp_eq_u32 s79, 5\n"
    "s_cbranch_scc1 Lrr5\n"
    "s_cmp_eq_u32 s79, 7\n"
    "s_cbranch_scc1 Lrr7\n"
    "s_cmp_eq_u32 s79, 9\n"
    "s_cbranch_scc1 Lrr9\n"
    "s_cmp_eq_u32 s79, 11\n"
    "s_cbranch_scc1 Lrr11\n"
    "s_branch Lrr13\n"
    "Lrare1:\n"
    "s_nop 15\n"
    "v_max3_f32 v116, v224, v225, v226\n"
    "v_max3_f32 v116, v116, v227, v228\n"
    "v_max3_f32 v116, v116, v229, v230\n"
    "v_max_f32 v116, v116, v231\n"
    "v_max3_f32 v117, v232, v233, v234\n"
    "v_max3_f32 v117, v117, v235, v236\n"
    "v_max3_f32 v117, v117, v237, v238\n"
    "v_max_f32 v117, v117, v239\n"
    "v_max3_f32 v118, v240, v241, v242\n"
    "v_max3_f32 v118, v118, v243, v244\n"
    "v_max3_f32 v118, v118, v245, v246\n"
    "v_max_f32 v118, v118, v247\n"
    "v_max3_f32 v119, v248, v249, v250\n"
    "v_max3_f32 v119, v119, v251, v252\n"
    "v_max3_f32 v119, v119, v253, v254\n"
    "v_max_f32 v119, v119, v255\n"
    "s_cmp_eq_u32 s80, 1\n"
    "s_cbranch_scc1 Lrare1a\n"
    "v_max_f32 v116, v116, v118\n"
    "v_max_f32 v117, v117, v119\n"
    "v_mov_b32 v118, v116\n"
    "v_mov_b32 v119, v117\n"
    "Lrare1a:\n"
    "v_mov_b32 v72, v116\n"
    "v_mov_b32 v73, v117\n"
    "v_mov_b32 v74, v118\n"
    "v_mov_b32 v75, v119\n"
    "v_permlane16_swap_b32 v72, v116\n"
    "v_permlane16_swap_b32 v73, v117\n"
    "v_permlane16_swap_b32 v74, v118\n"
    "v_permlane16_swap_b32 v75, v119\n"
    "v_max_f32 v116, v116, v72\n"
    "v_max_f32 v117, v117, v73\n"
    "v_max_f32 v118, v118, v74\n"
    "v_max_f32 v119, v119, v75\n"
    "v_mov_b32 v72, v116\n"
    "v_mov_b32 v73, v117\n"
    "v_mov_b32 v74, v118\n"
    "v_mov_b32 v75, v119\n"
    "v_permlane32_swap_b32 v72, v116\n"
    "v_permlane32_swap_b32 v73, v117\n"
    "v_permlane32_swap_b32 v74, v118\n"
    "v_permlane32_swap_b32 v75, v119\n"
    "v_max_f32 v116, v116, v72\n"
    "v_max_f32 v117, v117, v73\n"
    "v_max_f32 v118, v118, v74\n"
    "v_max_f32 v119, v119, v75\n"
    "v_add_f32 v116, 0x41000000, v116\n"
    "v_max_f32 v116, 0, v116\n"
    "v_sub_f32 v124, 0, v116\n"
    "v_exp_f32 v124, v124\n"
    "v_add_f32 v117, 0x41000000, v117\n"
    "v_max_f32 v117, 0, v117\n"
    "v_sub_f32 v125, 0, v117\n"
    "v_exp_f32 v125, v125\n"
    "v_add_f32 v118, 0x41000000, v118\n"
    "v_max_f32 v118, 0, v118\n"
    "v_sub_f32 v126, 0, v118\n"
    "v_exp_f32 v126, v126\n"
    "v_add_f32 v119, 0x41000000, v119\n"
    "v_max_f32 v119, 0, v119\n"
    "v_sub_f32 v127, 0, v119\n"
    "v_exp_f32 v127, v127\n"
    "v_sub_f32 v224, v224, v116\n"
    "v_sub_f32 v225, v225, v116\n"
    "v_sub_f32 v226, v226, v116\n"
    "v_sub_f32 v227, v227, v116\n"
    "v_sub_f32 v228, v228, v116\n"
    "v_sub_f32 v229, v229, v116\n"
    "v_sub_f32 v230, v230, v116\n"
    "v_sub_f32 v231, v231, v116\n"
    "v_sub_f32 v232, v232, v117\n"
    "v_sub_f32 v233, v233, v117\n"
    "v_sub_f32 v234, v234, v117\n"
    "v_sub_f32 v235, v235, v117\n"
    "v_sub_f32 v236, v236, v117\n"
    "v_sub_f32 v237, v237, v117\n"
    "v_sub_f32 v238, v238, v117\n"
    "v_sub_f32 v239, v239, v117\n"
    "v_sub_f32 v240, v240, v118\n"
    "v_sub_f32 v241, v241, v118\n"
    "v_sub_f32 v242, v242, v118\n"
    "v_sub_f32 v243, v243, v118\n"
    "v_sub_f32 v244, v244, v118\n"
    "v_sub_f32 v245, v245, v118\n"
    "v_sub_f32 v246, v246, v118\n"
    "v_sub_f32 v247, v247, v118\n"
    "v_sub_f32 v248, v248, v119\n"
    "v_sub_f32 v249, v249, v119\n"
    "v_sub_f32 v250, v250, v119\n"
    "v_sub_f32 v251, v251, v119\n"
    "v_sub_f32 v252, v252, v119\n"
    "v_sub_f32 v253, v253, v119\n"
    "v_sub_f32 v254, v254, v119\n"
    "v_sub_f32 v255, v255, v119\n"
    "v_sub_f32 v208, v208, v118\n"
    "v_sub_f32 v209, v209, v118\n"
    "v_sub_f32 v210, v210, v118\n"
    "v_sub_f32 v211, v211, v118\n"
    "v_sub_f32 v212, v212, v118\n"
    "v_sub_f32 v213, v213, v118\n"
    "v_sub_f32 v214, v214, v118\n"
    "v_sub_f32 v215, v215, v118\n"
    "v_sub_f32 v216, v216, v119\n"
    "v_sub_f32 v217, v217, v119\n"
    "v_sub_f32 v218, v218, v119\n"
    "v_sub_f32 v219, v219, v119\n"
    "v_sub_f32 v220, v220, v119\n"
    "v_sub_f32 v221, v221, v119\n"
    "v_sub_f32 v222, v222, v119\n"
    "v_sub_f32 v223, v223, v119\n"
    "v_sub_f32 v56, v56, v118\n"
    "v_sub_f32 v57, v57, v118\n"
    "v_sub_f32 v58, v58, v118\n"
    "v_sub_f32 v59, v59, v118\n"
    "v_sub_f32 v60, v60, v118\n"
    "v_sub_f32 v61, v61, v118\n"
    "v_sub_f32 v62, v62, v118\n"
    "v_sub_f32 v63, v63, v118\n"
    "v_sub_f32 v64, v64, v119\n"
    "v_sub_f32 v65, v65, v119\n"
    "v_sub_f32 v66, v66, v119\n"
    "v_sub_f32 v67, v67, v119\n"
    "v_sub_f32 v68, v68, v119\n"
    "v_sub_f32 v69, v69, v119\n"
    "v_sub_f32 v70, v70, v119\n"
    "v_sub_f32 v71, v71, v119\n"
    "s_cmp_eq_u32 s81, 1\n"
    "s_cbranch_scc1 Lrare1b\n"
    "v_mov_b32 v116, v118\n"
    "v_mov_b32 v117, v119\n"
    "Lrare1b:\n"
    "v_sub_f32 v192, v192, v116\n"
    "v_sub_f32 v193, v193, v116\n"
    "v_sub_f32 v194, v194, v116\n"
    "v_sub_f32 v195, v195, v116\n"
    "v_sub_f32 v196, v196, v116\n"
    "v_sub_f32 v197, v197, v116\n"
    "v_sub_f32 v198, v198, v116\n"
    "v_sub_f32 v199, v199, v116\n"
    "v_sub_f32 v200, v200, v117\n"
    "v_sub_f32 v201, v201, v117\n"
    "v_sub_f32 v202, v202, v117\n"
    "v_sub_f32 v203, v203, v117\n"
    "v_sub_f32 v204, v204, v117\n"
    "v_sub_f32 v205, v205, v117\n"
    "v_sub_f32 v206, v206, v117\n"
    "v_sub_f32 v207, v207, v117\n"
    "v_sub_f32 v40, v40, v116\n"
    "v_sub_f32 v41, v41, v116\n"
    "v_sub_f32 v42, v42, v116\n"
    "v_sub_f32 v43, v43, v116\n"
    "v_sub_f32 v44, v44, v116\n"
    "v_sub_f32 v45, v45, v116\n"
    "v_sub_f32 v46, v46, v116\n"
    "v_sub_f32 v47, v47, v116\n"
    "v_sub_f32 v48, v48, v117\n"
    "v_sub_f32 v49, v49, v117\n"
    "v_sub_f32 v50, v50, v117\n"
    "v_sub_f32 v51, v51, v117\n"
    "v_sub_f32 v52, v52, v117\n"
    "v_sub_f32 v53, v53, v117\n"
    "v_sub_f32 v54, v54, v117\n"
    "v_sub_f32 v55, v55, v117\n"
    "v_accvgpr_read_b32 v72, a64\n"
    "v_mul_f32 v72, v72, v126\n"
    "v_accvgpr_write_b32 a64, v72\n"
    "v_accvgpr_read_b32 v73, a65\n"
    "v_mul_f32 v73, v73, v126\n"
    "v_accvgpr_write_b32 a65, v73\n"
    "v_accvgpr_read_b32 v74, a66\n"
    "v_mul_f32 v74, v74, v126\n"
    "v_accvgpr_write_b32 a66, v74\n"
    "v_accvgpr_read_b32 v75, a67\n"
    "v_mul_f32 v75, v75, v126\n"
    "v_accvgpr_write_b32 a67, v75\n"
    "v_accvgpr_read_b32 v76, a68\n"
    "v_mul_f32 v76, v76, v126\n"
    "v_accvgpr_write_b32 a68, v76\n"
    "v_accvgpr_read_b32 v77, a69\n"
    "v_mul_f32 v77, v77, v126\n"
    "v_accvgpr_write_b32 a69, v77\n"
    "v_accvgpr_read_b32 v78, a70\n"
    "v_mul_f32 v78, v78, v126\n"
    "v_accvgpr_write_b32 a70, v78\n"
    "v_accvgpr_read_b32 v79, a71\n"
    "v_mul_f32 v79, v79, v126\n"
    "v_accvgpr_write_b32 a71, v79\n"
    "v_accvgpr_read_b32 v72, a72\n"
    "v_mul_f32 v72, v72, v126\n"
    "v_accvgpr_write_b32 a72, v72\n"
    "v_accvgpr_read_b32 v73, a73\n"
    "v_mul_f32 v73, v73, v126\n"
    "v_accvgpr_write_b32 a73, v73\n"
    "v_accvgpr_read_b32 v74, a74\n"
    "v_mul_f32 v74, v74, v126\n"
    "v_accvgpr_write_b32 a74, v74\n"
    "v_accvgpr_read_b32 v75, a75\n"
    "v_mul_f32 v75, v75, v126\n"
    "v_accvgpr_write_b32 a75, v75\n"
    "v_accvgpr_read_b32 v76, a76\n"
    "v_mul_f32 v76, v76, v126\n"
    "v_accvgpr_write_b32 a76, v76\n"
    "v_accvgpr_read_b32 v77, a77\n"
    "v_mul_f32 v77, v77, v126\n"
    "v_accvgpr_write_b32 a77, v77\n"
    "v_accvgpr_read_b32 v78, a78\n"
    "v_mul_f32 v78, v78, v126\n"
    "v_accvgpr_write_b32 a78, v78\n"
    "v_accvgpr_read_b32 v79, a79\n"
    "v_mul_f32 v79, v79, v126\n"
    "v_accvgpr_write_b32 a79, v79\n"
    "v_accvgpr_read_b32 v72, a80\n"
    "v_mul_f32 v72, v72, v126\n"
    "v_accvgpr_write_b32 a80, v72\n"
    "v_accvgpr_read_b32 v73, a81\n"
    "v_mul_f32 v73, v73, v126\n"
    "v_accvgpr_write_b32 a81, v73\n"
    "v_accvgpr_read_b32 v74, a82\n"
    "v_mul_f32 v74, v74, v126\n"
    "v_accvgpr_write_b32 a82, v74\n"
    "v_accvgpr_read_b32 v75, a83\n"
    "v_mul_f32 v75, v75, v126\n"
    "v_accvgpr_write_b32 a83, v75\n"
    "v_accvgpr_read_b32 v76, a84\n"
    "v_mul_f32 v76, v76, v126\n"
    "v_accvgpr_write_b32 a84, v76\n"
    "v_accvgpr_read_b32 v77, a85\n"
    "v_mul_f32 v77, v77, v126\n"
    "v_accvgpr_write_b32 a85, v77\n"
    "v_accvgpr_read_b32 v78, a86\n"
    "v_mul_f32 v78, v78, v126\n"
    "v_accvgpr_write_b32 a86, v78\n"
    "v_accvgpr_read_b32 v79, a87\n"
    "v_mul_f32 v79, v79, v126\n"
    "v_accvgpr_write_b32 a87, v79\n"
    "v_accvgpr_read_b32 v72, a88\n"
    "v_mul_f32 v72, v72, v126\n"
    "v_accvgpr_write_b32 a88, v72\n"
    "v_accvgpr_read_b32 v73, a89\n"
    "v_mul_f32 v73, v73, v126\n"
    "v_accvgpr_write_b32 a89, v73\n"
    "v_accvgpr_read_b32 v74, a90\n"
    "v_mul_f32 v74, v74, v126\n"
    "v_accvgpr_write_b32 a90, v74\n"
    "v_accvgpr_read_b32 v75, a91\n"
    "v_mul_f32 v75, v75, v126\n"
    "v_accvgpr_write_b32 a91, v75\n"
    "v_accvgpr_read_b32 v76, a92\n"
    "v_mul_f32 v76, v76, v126\n"
    "v_accvgpr_write_b32 a92, v76\n"
    "v_accvgpr_read_b32 v77, a93\n"
    "v_mul_f32 v77, v77, v126\n"
    "v_accvgpr_write_b32 a93, v77\n"
    "v_accvgpr_read_b32 v78, a94\n"
    "v_mul_f32 v78, v78, v126\n"
    "v_accvgpr_write_b32 a94, v78\n"
    "v_accvgpr_read_b32 v79, a95\n"
    "v_mul_f32 v79, v79, v126\n"
    "v_accvgpr_write_b32 a95, v79\n"
    "v_mul_f32 v92, v92, v126\n"
    "v_accvgpr_read_b32 v72, a96\n"
    "v_mul_f32 v72, v72, v127\n"
    "v_accvgpr_write_b32 a96, v72\n"
    "v_accvgpr_read_b32 v73, a97\n"
    "v_mul_f32 v73, v73, v127\n"
    "v_accvgpr_write_b32 a97, v73\n"
    "v_accvgpr_read_b32 v74, a98\n"
    "v_mul_f32 v74, v74, v127\n"
    "v_accvgpr_write_b32 a98, v74\n"
    "v_accvgpr_read_b32 v75, a99\n"
    "v_mul_f32 v75, v75, v127\n"
    "v_accvgpr_write_b32 a99, v75\n"
    "v_accvgpr_read_b32 v76, a100\n"
    "v_mul_f32 v76, v76, v127\n"
    "v_accvgpr_write_b32 a100, v76\n"
    "v_accvgpr_read_b32 v77, a101\n"
    "v_mul_f32 v77, v77, v127\n"
    "v_accvgpr_write_b32 a101, v77\n"
    "v_accvgpr_read_b32 v78, a102\n"
    "v_mul_f32 v78, v78, v127\n"
    "v_accvgpr_write_b32 a102, v78\n"
    "v_accvgpr_read_b32 v79, a103\n"
    "v_mul_f32 v79, v79, v127\n"
    "v_accvgpr_write_b32 a103, v79\n"
    "v_accvgpr_read_b32 v72, a104\n"
    "v_mul_f32 v72, v72, v127\n"
    "v_accvgpr_write_b32 a104, v72\n"
    "v_accvgpr_read_b32 v73, a105\n"
    "v_mul_f32 v73, v73, v127\n"
    "v_accvgpr_write_b32 a105, v73\n"
    "v_accvgpr_read_b32 v74, a106\n"
    "v_mul_f32 v74, v74, v127\n"
    "v_accvgpr_write_b32 a106, v74\n"
    "v_accvgpr_read_b32 v75, a107\n"
    "v_mul_f32 v75, v75, v127\n"
    "v_accvgpr_write_b32 a107, v75\n"
    "v_accvgpr_read_b32 v76, a108\n"
    "v_mul_f32 v76, v76, v127\n"
    "v_accvgpr_write_b32 a108, v76\n"
    "v_accvgpr_read_b32 v77, a109\n"
    "v_mul_f32 v77, v77, v127\n"
    "v_accvgpr_write_b32 a109, v77\n"
    "v_accvgpr_read_b32 v78, a110\n"
    "v_mul_f32 v78, v78, v127\n"
    "v_accvgpr_write_b32 a110, v78\n"
    "v_accvgpr_read_b32 v79, a111\n"
    "v_mul_f32 v79, v79, v127\n"
    "v_accvgpr_write_b32 a111, v79\n"
    "v_accvgpr_read_b32 v72, a112\n"
    "v_mul_f32 v72, v72, v127\n"
    "v_accvgpr_write_b32 a112, v72\n"
    "v_accvgpr_read_b32 v73, a113\n"
    "v_mul_f32 v73, v73, v127\n"
    "v_accvgpr_write_b32 a113, v73\n"
    "v_accvgpr_read_b32 v74, a114\n"
    "v_mul_f32 v74, v74, v127\n"
    "v_accvgpr_write_b32 a114, v74\n"
    "v_accvgpr_read_b32 v75, a115\n"
    "v_mul_f32 v75, v75, v127\n"
    "v_accvgpr_write_b32 a115, v75\n"
    "v_accvgpr_read_b32 v76, a116\n"
    "v_mul_f32 v76, v76, v127\n"
    "v_accvgpr_write_b32 a116, v76\n"
    "v_accvgpr_read_b32 v77, a117\n"
    "v_mul_f32 v77, v77, v127\n"
    "v_accvgpr_write_b32 a117, v77\n"
    "v_accvgpr_read_b32 v78, a118\n"
    "v_mul_f32 v78, v78, v127\n"
    "v_accvgpr_write_b32 a118, v78\n"
    "v_accvgpr_read_b32 v79, a119\n"
    "v_mul_f32 v79, v79, v127\n"
    "v_accvgpr_write_b32 a119, v79\n"
    "v_accvgpr_read_b32 v72, a120\n"
    "v_mul_f32 v72, v72, v127\n"
    "v_accvgpr_write_b32 a120, v72\n"
    "v_accvgpr_read_b32 v73, a121\n"
    "v_mul_f32 v73, v73, v127\n"
    "v_accvgpr_write_b32 a121, v73\n"
    "v_accvgpr_read_b32 v74, a122\n"
    "v_mul_f32 v74, v74, v127\n"
    "v_accvgpr_write_b32 a122, v74\n"
    "v_accvgpr_read_b32 v75, a123\n"
    "v_mul_f32 v75, v75, v127\n"
    "v_accvgpr_write_b32 a123, v75\n"
    "v_accvgpr_read_b32 v76, a124\n"
    "v_mul_f32 v76, v76, v127\n"
    "v_accvgpr_write_b32 a124, v76\n"
    "v_accvgpr_read_b32 v77, a125\n"
    "v_mul_f32 v77, v77, v127\n"
    "v_accvgpr_write_b32 a125, v77\n"
    "v_accvgpr_read_b32 v78, a126\n"
    "v_mul_f32 v78, v78, v127\n"
    "v_accvgpr_write_b32 a126, v78\n"
    "v_accvgpr_read_b32 v79, a127\n"
    "v_mul_f32 v79, v79, v127\n"
    "v_accvgpr_write_b32 a127, v79\n"
    "v_mul_f32 v96, v96, v127\n"
    "s_cmp_eq_u32 s80, 0\n"
    "s_cbranch_scc1 Lrare1c\n"
    "v_accvgpr_read_b32 v72, a0\n"
    "v_mul_f32 v72, v72, v124\n"
    "v_accvgpr_write_b32 a0, v72\n"
    "v_accvgpr_read_b32 v73, a1\n"
    "v_mul_f32 v73, v73, v124\n"
    "v_accvgpr_write_b32 a1, v73\n"
    "v_accvgpr_read_b32 v74, a2\n"
    "v_mul_f32 v74, v74, v124\n"
    "v_accvgpr_write_b32 a2, v74\n"
    "v_accvgpr_read_b32 v75, a3\n"
    "v_mul_f32 v75, v75, v124\n"
    "v_accvgpr_write_b32 a3, v75\n"
    "v_accvgpr_read_b32 v76, a4\n"
    "v_mul_f32 v76, v76, v124\n"
    "v_accvgpr_write_b32 a4, v76\n"
    "v_accvgpr_read_b32 v77, a5\n"
    "v_mul_f32 v77, v77, v124\n"
    "v_accvgpr_write_b32 a5, v77\n"
    "v_accvgpr_read_b32 v78, a6\n"
    "v_mul_f32 v78, v78, v124\n"
    "v_accvgpr_write_b32 a6, v78\n"
    "v_accvgpr_read_b32 v79, a7\n"
    "v_mul_f32 v79, v79, v124\n"
    "v_accvgpr_write_b32 a7, v79\n"
    "v_accvgpr_read_b32 v72, a8\n"
    "v_mul_f32 v72, v72, v124\n"
    "v_accvgpr_write_b32 a8, v72\n"
    "v_accvgpr_read_b32 v73, a9\n"
    "v_mul_f32 v73, v73, v124\n"
    "v_accvgpr_write_b32 a9, v73\n"
    "v_accvgpr_read_b32 v74, a10\n"
    "v_mul_f32 v74, v74, v124\n"
    "v_accvgpr_write_b32 a10, v74\n"
    "v_accvgpr_read_b32 v75, a11\n"
    "v_mul_f32 v75, v75, v124\n"
    "v_accvgpr_write_b32 a11, v75\n"
    "v_accvgpr_read_b32 v76, a12\n"
    "v_mul_f32 v76, v76, v124\n"
    "v_accvgpr_write_b32 a12, v76\n"
    "v_accvgpr_read_b32 v77, a13\n"
    "v_mul_f32 v77, v77, v124\n"
    "v_accvgpr_write_b32 a13, v77\n"
    "v_accvgpr_read_b32 v78, a14\n"
    "v_mul_f32 v78, v78, v124\n"
    "v_accvgpr_write_b32 a14, v78\n"
    "v_accvgpr_read_b32 v79, a15\n"
    "v_mul_f32 v79, v79, v124\n"
    "v_accvgpr_write_b32 a15, v79\n"
    "v_accvgpr_read_b32 v72, a16\n"
    "v_mul_f32 v72, v72, v124\n"
    "v_accvgpr_write_b32 a16, v72\n"
    "v_accvgpr_read_b32 v73, a17\n"
    "v_mul_f32 v73, v73, v124\n"
    "v_accvgpr_write_b32 a17, v73\n"
    "v_accvgpr_read_b32 v74, a18\n"
    "v_mul_f32 v74, v74, v124\n"
    "v_accvgpr_write_b32 a18, v74\n"
    "v_accvgpr_read_b32 v75, a19\n"
    "v_mul_f32 v75, v75, v124\n"
    "v_accvgpr_write_b32 a19, v75\n"
    "v_accvgpr_read_b32 v76, a20\n"
    "v_mul_f32 v76, v76, v124\n"
    "v_accvgpr_write_b32 a20, v76\n"
    "v_accvgpr_read_b32 v77, a21\n"
    "v_mul_f32 v77, v77, v124\n"
    "v_accvgpr_write_b32 a21, v77\n"
    "v_accvgpr_read_b32 v78, a22\n"
    "v_mul_f32 v78, v78, v124\n"
    "v_accvgpr_write_b32 a22, v78\n"
    "v_accvgpr_read_b32 v79, a23\n"
    "v_mul_f32 v79, v79, v124\n"
    "v_accvgpr_write_b32 a23, v79\n"
    "v_accvgpr_read_b32 v72, a24\n"
    "v_mul_f32 v72, v72, v124\n"
    "v_accvgpr_write_b32 a24, v72\n"
    "v_accvgpr_read_b32 v73, a25\n"
    "v_mul_f32 v73, v73, v124\n"
    "v_accvgpr_write_b32 a25, v73\n"
    "v_accvgpr_read_b32 v74, a26\n"
    "v_mul_f32 v74, v74, v124\n"
    "v_accvgpr_write_b32 a26, v74\n"
    "v_accvgpr_read_b32 v75, a27\n"
    "v_mul_f32 v75, v75, v124\n"
    "v_accvgpr_write_b32 a27, v75\n"
    "v_accvgpr_read_b32 v76, a28\n"
    "v_mul_f32 v76, v76, v124\n"
    "v_accvgpr_write_b32 a28, v76\n"
    "v_accvgpr_read_b32 v77, a29\n"
    "v_mul_f32 v77, v77, v124\n"
    "v_accvgpr_write_b32 a29, v77\n"
    "v_accvgpr_read_b32 v78, a30\n"
    "v_mul_f32 v78, v78, v124\n"
    "v_accvgpr_write_b32 a30, v78\n"
    "v_accvgpr_read_b32 v79, a31\n"
    "v_mul_f32 v79, v79, v124\n"
    "v_accvgpr_write_b32 a31, v79\n"
    "v_mul_f32 v84, v84, v124\n"
    "v_accvgpr_read_b32 v72, a32\n"
    "v_mul_f32 v72, v72, v125\n"
    "v_accvgpr_write_b32 a32, v72\n"
    "v_accvgpr_read_b32 v73, a33\n"
    "v_mul_f32 v73, v73, v125\n"
    "v_accvgpr_write_b32 a33, v73\n"
    "v_accvgpr_read_b32 v74, a34\n"
    "v_mul_f32 v74, v74, v125\n"
    "v_accvgpr_write_b32 a34, v74\n"
    "v_accvgpr_read_b32 v75, a35\n"
    "v_mul_f32 v75, v75, v125\n"
    "v_accvgpr_write_b32 a35, v75\n"
    "v_accvgpr_read_b32 v76, a36\n"
    "v_mul_f32 v76, v76, v125\n"
    "v_accvgpr_write_b32 a36, v76\n"
    "v_accvgpr_read_b32 v77, a37\n"
    "v_mul_f32 v77, v77, v125\n"
    "v_accvgpr_write_b32 a37, v77\n"
    "v_accvgpr_read_b32 v78, a38\n"
    "v_mul_f32 v78, v78, v125\n"
    "v_accvgpr_write_b32 a38, v78\n"
    "v_accvgpr_read_b32 v79, a39\n"
    "v_mul_f32 v79, v79, v125\n"
    "v_accvgpr_write_b32 a39, v79\n"
    "v_accvgpr_read_b32 v72, a40\n"
    "v_mul_f32 v72, v72, v125\n"
    "v_accvgpr_write_b32 a40, v72\n"
    "v_accvgpr_read_b32 v73, a41\n"
    "v_mul_f32 v73, v73, v125\n"
    "v_accvgpr_write_b32 a41, v73\n"
    "v_accvgpr_read_b32 v74, a42\n"
    "v_mul_f32 v74, v74, v125\n"
    "v_accvgpr_write_b32 a42, v74\n"
    "v_accvgpr_read_b32 v75, a43\n"
    "v_mul_f32 v75, v75, v125\n"
    "v_accvgpr_write_b32 a43, v75\n"
    "v_accvgpr_read_b32 v76, a44\n"
    "v_mul_f32 v76, v76, v125\n"
    "v_accvgpr_write_b32 a44, v76\n"
    "v_accvgpr_read_b32 v77, a45\n"
    "v_mul_f32 v77, v77, v125\n"
    "v_accvgpr_write_b32 a45, v77\n"
    "v_accvgpr_read_b32 v78, a46\n"
    "v_mul_f32 v78, v78, v125\n"
    "v_accvgpr_write_b32 a46, v78\n"
    "v_accvgpr_read_b32 v79, a47\n"
    "v_mul_f32 v79, v79, v125\n"
    "v_accvgpr_write_b32 a47, v79\n"
    "v_accvgpr_read_b32 v72, a48\n"
    "v_mul_f32 v72, v72, v125\n"
    "v_accvgpr_write_b32 a48, v72\n"
    "v_accvgpr_read_b32 v73, a49\n"
    "v_mul_f32 v73, v73, v125\n"
    "v_accvgpr_write_b32 a49, v73\n"
    "v_accvgpr_read_b32 v74, a50\n"
    "v_mul_f32 v74, v74, v125\n"
    "v_accvgpr_write_b32 a50, v74\n"
    "v_accvgpr_read_b32 v75, a51\n"
    "v_mul_f32 v75, v75, v125\n"
    "v_accvgpr_write_b32 a51, v75\n"
    "v_accvgpr_read_b32 v76, a52\n"
    "v_mul_f32 v76, v76, v125\n"
    "v_accvgpr_write_b32 a52, v76\n"
    "v_accvgpr_read_b32 v77, a53\n"
    "v_mul_f32 v77, v77, v125\n"
    "v_accvgpr_write_b32 a53, v77\n"
    "v_accvgpr_read_b32 v78, a54\n"
    "v_mul_f32 v78, v78, v125\n"
    "v_accvgpr_write_b32 a54, v78\n"
    "v_accvgpr_read_b32 v79, a55\n"
    "v_mul_f32 v79, v79, v125\n"
    "v_accvgpr_write_b32 a55, v79\n"
    "v_accvgpr_read_b32 v72, a56\n"
    "v_mul_f32 v72, v72, v125\n"
    "v_accvgpr_write_b32 a56, v72\n"
    "v_accvgpr_read_b32 v73, a57\n"
    "v_mul_f32 v73, v73, v125\n"
    "v_accvgpr_write_b32 a57, v73\n"
    "v_accvgpr_read_b32 v74, a58\n"
    "v_mul_f32 v74, v74, v125\n"
    "v_accvgpr_write_b32 a58, v74\n"
    "v_accvgpr_read_b32 v75, a59\n"
    "v_mul_f32 v75, v75, v125\n"
    "v_accvgpr_write_b32 a59, v75\n"
    "v_accvgpr_read_b32 v76, a60\n"
    "v_mul_f32 v76, v76, v125\n"
    "v_accvgpr_write_b32 a60, v76\n"
    "v_accvgpr_read_b32 v77, a61\n"
    "v_mul_f32 v77, v77, v125\n"
    "v_accvgpr_write_b32 a61, v77\n"
    "v_accvgpr_read_b32 v78, a62\n"
    "v_mul_f32 v78, v78, v125\n"
    "v_accvgpr_write_b32 a62, v78\n"
    "v_accvgpr_read_b32 v79, a63\n"
    "v_mul_f32 v79, v79, v125\n"
    "v_accvgpr_write_b32 a63, v79\n"
    "v_mul_f32 v88, v88, v125\n"
    "Lrare1c:\n"
    "v_exp_f32 v72, v224\n"
    "v_exp_f32 v73, v225\n"
    "v_exp_f32 v74, v226\n"
    "v_exp_f32 v75, v227\n"
    "v_cvt_pk_bf16_f32 v24, v72, v73\n"
    "v_exp_f32 v76, v232\n"
    "v_exp_f32 v77, v233\n"
    "v_cvt_pk_bf16_f32 v25, v74, v75\n"
    "v_exp_f32 v78, v234\n"
    "v_exp_f32 v79, v235\n"
    "v_cvt_pk_bf16_f32 v28, v76, v77\n"
    "v_or_b32 v108, v24, v25\n"
    "v_exp_f32 v72, v240\n"
    "v_exp_f32 v73, v241\n"
    "v_cvt_pk_bf16_f32 v29, v78, v79\n"
    "v_exp_f32 v74, v242\n"
    "v_exp_f32 v75, v243\n"
    "v_cvt_pk_bf16_f32 v32, v72, v73\n"
    "v_or3_b32 v108, v108, v28, v29\n"
    "v_exp_f32 v76, v248\n"
    "v_exp_f32 v77, v249\n"
    "v_cvt_pk_bf16_f32 v33, v74, v75\n"
    "v_exp_f32 v78, v250\n"
    "v_exp_f32 v79, v251\n"
    "v_cvt_pk_bf16_f32 v36, v76, v77\n"
    "v_or3_b32 v108, v108, v32, v33\n"
    "v_exp_f32 v72, v228\n"
    "v_exp_f32 v73, v229\n"
    "v_cvt_pk_bf16_f32 v37, v78, v79\n"
    "v_exp_f32 v74, v230\n"
    "v_exp_f32 v75, v231\n"
    "v_cvt_pk_bf16_f32 v26, v72, v73\n"
    "v_or3_b32 v108, v108, v36, v37\n"
    "v_exp_f32 v76, v236\n"
    "v_exp_f32 v77, v237\n"
    "v_cvt_pk_bf16_f32 v27, v74, v75\n"
    "v_exp_f32 v78, v238\n"
    "v_exp_f32 v79, v239\n"
    "v_cvt_pk_bf16_f32 v30, v76, v77\n"
    "v_or3_b32 v108, v108, v26, v27\n"
    "v_exp_f32 v72, v244\n"
    "v_exp_f32 v73, v245\n"
    "v_cvt_pk_bf16_f32 v31, v78, v79\n"
    "v_exp_f32 v74, v246\n"
    "v_exp_f32 v75, v247\n"
    "v_cvt_pk_bf16_f32 v34, v72, v73\n"
    "v_or3_b32 v108, v108, v30, v31\n"
    "v_exp_f32 v76, v252\n"
    "v_exp_f32 v77, v253\n"
    "v_cvt_pk_bf16_f32 v35, v74, v75\n"
    "v_exp_f32 v78, v254\n"
    "v_exp_f32 v79, v255\n"
    "v_cvt_pk_bf16_f32 v38, v76, v77\n"
    "v_or3_b32 v108, v108, v34, v35\n"
    "v_cvt_pk_bf16_f32 v39, v78, v79\n"
    "v_or3_b32 v108, v108, v38, v39\n"
    "s_nop 1\n"
    "s_cmp_eq_u32 s79, 2\n"
    "s_cbranch_scc1 Lrr2\n"
    "s_cmp_eq_u32 s79, 4\n"
    "s_cbranch_scc1 Lrr4\n"
    "s_cmp_eq_u32 s79, 6\n"
    "s_cbranch_scc1 Lrr6\n"
    "s_cmp_eq_u32 s79, 8\n"
    "s_cbranch_scc1 Lrr8\n"
    "s_cmp_eq_u32 s79, 10\n"
    "s_cbranch_scc1 Lrr10\n"
    "s_cmp_eq_u32 s79, 12\n"
    "s_cbranch_scc1 Lrr12\n"
    "s_branch Lrr14\n"
    "Lspin5:\n"
    "s_mov_b32 s61, 0\n"
    "Lspinl5:\n"
    "s_sleep 1\n"
    "buffer_load_dword v122, v5, s[8:11], s7 offen sc1\n"
    "s_waitcnt vmcnt(0)\n"
    "v_cmp_ne_u32 vcc, v122, v7\n"
    "s_nop 0\n"
    "s_cbranch_vccz Lspinret5\n"
    "s_add_u32 s61, s61, 1\n"
    "s_cmp_lt_u32 s61, 0x30d40\n"
    "s_cbranch_scc1 Lspinl5\n"
    "s_branch Lspinret5\n"
    "Lspin6:\n"
    "s_mov_b32 s61, 0\n"
    "Lspinl6:\n"
    "s_sleep 1\n"
    "buffer_load_dword v122, v5, s[8:11], s7 offen sc1\n"
    "s_waitcnt vmcnt(0)\n"
    "v_cmp_ne_u32 vcc, v122, v7\n"
    "s_nop 0\n"
    "s_cbranch_vccz Lspinret6\n"
    "s_add_u32 s61, s61, 1\n"
    "s_cmp_lt_u32 s61, 0x30d40\n"
    "s_cbranch_scc1 Lspinl6\n"
    "s_branch Lspinret6\n"
    "Lpxr:\n"
    "s_add_u32 s61, s61, 1\n"
    "s_cmp_gt_u32 s61, 0x30d40\n"
    "s_cbranch_scc1 Lpxd\n"
    "buffer_load_dwordx4 v[240:243], v2, s[8:11], s94 offen sc1\n"
    "buffer_load_dwordx4 v[244:247], v2, s[8:11], s94 offen offset:1024 sc1\n"
    "buffer_load_dwordx4 v[248:251], v2, s[8:11], s94 offen offset:2048 sc1\n"
    "buffer_load_dwordx4 v[252:255], v2, s[8:11], s94 offen offset:3072 sc1\n"
    "buffer_load_dwordx4 v[224:227], v2, s[8:11], s93 offen sc1\n"
    "buffer_load_dwordx4 v[228:231], v2, s[8:11], s93 offen offset:1024 sc1\n"
    "buffer_load_dwordx4 v[232:235], v2, s[8:11], s93 offen offset:2048 sc1\n"
    "buffer_load_dwordx4 v[236:239], v2, s[8:11], s93 offen offset:3072 sc1\n"
    "buffer_load_dwordx4 v[208:211], v2, s[8:11], s92 offen sc1\n"
    "buffer_load_dwordx4 v[212:215], v2, s[8:11], s92 offen offset:1024 sc1\n"
    "buffer_load_dwordx4 v[216:219], v2, s[8:11], s92 offen offset:2048 sc1\n"
    "buffer_load_dwordx4 v[220:223], v2, s[8:11], s92 offen offset:3072 sc1\n"
    "s_waitcnt vmcnt(0)\n"
    "s_branch Lpx\n"
    "Lcodeend:\n"
    :: "s"(__builtin_amdgcn_kernarg_segment_ptr()), "s"(blockIdx.x), "v"(threadIdx.x), "s"((unsigned)(size_t)lds)
    : "v1", "v2", "v3", "v4", "v5", "v6", "v7", "v8", "v9", "v10", "v11", "v12", "v13", "v14", "v15", "v16", "v17", "v18", "v19", "v20", "v21", "v22", "v23", "v24", "v25", "v26", "v27", "v28", "v29", "v30", "v31", "v32", "v33", "v34", "v35", "v36", "v37", "v38", "v39", "v40", "v41", "v42", "v43", "v44", "v45", "v46", "v47", "v48", "v49", "v50", "v51", "v52", "v53", "v54", "v55", "v56", "v57", "v58", "v59", "v60", "v61", "v62", "v63", "v64", "v65", "v66", "v67", "v68", "v69", "v70", "v71", "v72", "v73", "v74", "v75", "v76", "v77", "v78", "v79", "v80", "v81", "v82", "v83", "v84", "v85", "v86", "v87", "v88", "v89", "v90", "v91", "v92", "v93", "v94", "v95", "v96", "v97", "v98", "v99", "v100", "v101", "v102", "v103", "v104", "v105", "v106", "v107", "v108", "v109", "v110", "v111", "v112", "v113", "v114", "v115", "v116", "v117", "v118", "v119", "v120", "v121", "v122", "v123", "v124", "v125", "v126", "v127", "v128", "v129", "v130", "v131", "v132", "v133", "v134", "v135", "v136", "v137", "v138", "v139", "v140", "v141", "v142", "v143", "v144", "v145", "v146", "v147", "v148", "v149", "v150", "v151", "v152", "v153", "v154", "v155", "v156", "v157", "v158", "v159", "v160", "v161", "v162", "v163", "v164", "v165", "v166", "v167", "v168", "v169", "v170", "v171", "v172", "v173", "v174", "v175", "v176", "v177", "v178", "v179", "v180", "v181", "v182", "v183", "v184", "v185", "v186", "v187", "v188", "v189", "v190", "v191", "v192", "v193", "v194", "v195", "v196", "v197", "v198", "v199", "v200", "v201", "v202", "v203", "v204", "v205", "v206", "v207", "v208", "v209", "v210", "v211", "v212", "v213", "v214", "v215", "v216", "v217", "v218", "v219", "v220", "v221", "v222", "v223", "v224", "v225", "v226", "v227", "v228", "v229", "v230", "v231", "v232", "v233", "v234", "v235", "v236", "v237", "v238", "v239", "v240", "v241", "v242", "v243", "v244", "v245", "v246", "v247", "v248", "v249", "v250", "v251", "v252", "v253", "v254", "v255", "a0", "a1", "a2", "a3", "a4", "a5", "a6", "a7", "a8", "a9", "a10", "a11", "a12", "a13", "a14", "a15", "a16", "a17", "a18", "a19", "a20", "a21", "a22", "a23", "a24", "a25", "a26", "a27", "a28", "a29", "a30", "a31", "a32", "a33", "a34", "a35", "a36", "a37", "a38", "a39", "a40", "a41", "a42", "a43", "a44", "a45", "a46", "a47", "a48", "a49", "a50", "a51", "a52", "a53", "a54", "a55", "a56", "a57", "a58", "a59", "a60", "a61", "a62", "a63", "a64", "a65", "a66", "a67", "a68", "a69", "a70", "a71", "a72", "a73", "a74", "a75", "a76", "a77", "a78", "a79", "a80", "a81", "a82", "a83", "a84", "a85", "a86", "a87", "a88", "a89", "a90", "a91", "a92", "a93", "a94", "a95", "a96", "a97", "a98", "a99", "a100", "a101", "a102", "a103", "a104", "a105", "a106", "a107", "a108", "a109", "a110", "a111", "a112", "a113", "a114", "a115", "a116", "a117", "a118", "a119", "a120", "a121", "a122", "a123", "a124", "a125", "a126", "a127", "a128", "a129", "a130", "a131", "a132", "a133", "a134", "a135", "a136", "a137", "a138", "a139", "a140", "a141", "a142", "a143", "a144", "a145", "a146", "a147", "a148", "a149", "a150", "a151", "a152", "a153", "a154", "a155", "a156", "a157", "a158", "a159", "a160", "a161", "a162", "a163", "a164", "a165", "a166", "a167", "a168", "a169", "a170", "a171", "a172", "a173", "a174", "a175", "a176", "a177", "a178", "a179", "a180", "a181", "a182", "a183", "a184", "a185", "a186", "a187", "a188", "a189", "a190", "a191", "a192", "a193", "a194", "a195", "a196", "a197", "a198", "a199", "a200", "a201", "a202", "a203", "a204", "a205", "a206", "a207", "a208", "a209", "a210", "a211", "a212", "a213", "a214", "a215", "a216", "a217", "a218", "a219", "a220", "a221", "a222", "a223", "a224", "a225", "a226", "a227", "a228", "a229", "a230", "a231", "a232", "a233", "a234", "a235", "a236", "a237", "a238", "a239", "a240", "a241", "a242", "a243", "a244", "a245", "a246", "a247", "a248", "a249", "a250", "a251", "a252", "a253", "a254", "a255", "s4", "s5", "s6", "s7", "s8", "s9", "s10", "s11", "s12", "s13", "s14", "s15", "s16", "s17", "s18", "s19", "s20", "s21", "s22", "s23", "s24", "s25", "s26", "s27", "s28", "s29", "s30", "s31", "s33", "s34", "s35", "s36", "s37", "s38", "s39", "s40", "s41", "s42", "s43", "s44", "s45", "s46", "s47", "s48", "s49", "s50", "s51", "s52", "s53", "s54", "s55", "s56", "s57", "s58", "s59", "s60", "s61", "s62", "s63", "s64", "s65", "s66", "s67", "s68", "s69", "s70", "s71", "s72", "s73", "s74", "s75", "s76", "s77", "s78", "s79", "s80", "s81", "s82", "s83", "s84", "s85", "s86", "s87", "s88", "s89", "s90", "s91", "s92", "s93", "s94", "s95", "vcc", "memory");
}
extern "C" void kernel_launch(void* const* d_in, const int* in_sizes, int n_in, void* d_out, int out_size, void* d_ws, size_t ws_size, hipStream_t stream) {
  Params p;
  for (int i = 0; i < 10; ++i) p.in[i] = d_in[i];
  p.out = d_out; p.ws = d_ws;
  fused_block<<<256, 256, 0, stream>>>(p);
}
```
